```python
import torch

def _softmax(x, axis=-1):
    return torch.softmax(x, dim=axis)
def _gelu(x):
    return torch.nn.functional.gelu(x, approximate="tanh")
def _silu(x):
    return torch.nn.functional.silu(x)
def _sigmoid(x):
    return torch.sigmoid(x)
def _layernorm(x, axis=-1, eps=1e-5):
    m = x.mean(dim=axis, keepdim=True)
    v = x.var(dim=axis, keepdim=True, unbiased=False)
    return (x - m) / torch.sqrt(v + eps)
def _scatter_add(tgt, idx, src, axis=0):
    return tgt.clone().index_add_(axis, idx, src)
def _segment_sum(src, seg, n):
    out = torch.zeros((n,) + src.shape[1:], dtype=src.dtype)
    return out.index_add_(0, seg, src)
def _pad(x, pw):
    flat = tuple(v for pair in reversed(pw) for v in pair)
    return torch.nn.functional.pad(x, flat)

kernel_name = 'standalone__seq2seq_rnn_t_joint__speech'

def setup_inputs():

    _w = (lambda a: (lambda t: t.to(torch.bfloat16) if t.is_floating_point() else t)(torch.as_tensor(a)))
    B, T, D, K = 2, 16000, 256, 31
    U = 8
    sc = (1.0 / (K * D)) ** 0.5
    sd = (1.0 / D) ** 0.5
    return {
        "x": _w((lambda *s: torch.randn(*s, dtype=torch.bfloat16) * 0.2)(B, T, D) * 0.5),
        "w1": _w((lambda *s: torch.randn(*s, dtype=torch.bfloat16) * 3.2)(K, D, D) * sc),
        "w2": _w((lambda *s: torch.randn(*s, dtype=torch.bfloat16) * 3.2)(K, D, D) * sc),
        "g": _w((lambda *s: torch.randn(*s, dtype=torch.bfloat16) * 3.2)(B, U, D) * 0.5),
        "w_f": _w((lambda *s: torch.randn(*s, dtype=torch.bfloat16) * 3.2)(D, D) * sd),
        "w_g": _w((lambda *s: torch.randn(*s, dtype=torch.bfloat16) * 3.2)(D, D) * sd),
        "w_o": _w((lambda *s: torch.randn(*s, dtype=torch.bfloat16) * 3.2)(D, D) * sd),
    }

def reference(x, w1, w2, g, w_f, w_g, w_o):
    B, T, D = x.shape
    K = w1.shape[0]
    pad = K // 2
    xp = _pad(x, ((0,0),(pad,pad),(0,0)))
    h = torch.zeros_like(x)
    for i in range(K):
        h = h + torch.einsum("btc,cd->btd", xp[:, i:i+T, :], w1[i])
    h = _gelu(h)[:, ::2, :]
    T1 = h.shape[1]
    hp = _pad(h, ((0,0),(pad,pad),(0,0)))
    f = torch.zeros_like(h)
    for i in range(K):
        f = f + torch.einsum("btc,cd->btd", hp[:, i:i+T1, :], w2[i])
    f = _gelu(f)[:, ::2, :]
    fe = torch.einsum("btd,de->bte", f, w_f)
    ge = torch.einsum("bud,de->bue", g, w_g)
    j = torch.tanh(fe[:, :, None, :] + ge[:, None, :, :])
    return torch.einsum("btud,dv->btuv", j, w_o)
```

```cpp
#include <hip/hip_runtime.h>
#include <hip/hip_bf16.h>
#include <utility>
__device__ const unsigned TAB[4608] __attribute__((aligned(128))) = {
0x3f803f80,0x3f803f80,0x3f803f80,0x3f803f80,0x0,0x0,0x0,0x0,
0x3f7f3f80,0x3f753f7b,0x3f613f6c,0x3f443f54,0x3dcc0000,0x3e973e4b,0x3ef53ec7,0x3f253f10,
0x3f7b3f80,0x3f543f6c,0x3f0b3f33,0x3e323ebb,0x3e4b0000,0x3f103ec7,0x3f573f37,0x3f7c3f6e,
0x3f753f80,0x3f203f54,0x3d993ebb,0xbf00be64,0x3e970000,0x3f483f10,0x3f7f3f6e,0x3f5e3f7a,
0x3f6c3f80,0x3ebb3f33,0xbed3bccc,0xbf71bf3c,0x3ec70000,0x3f6e3f37,0x3f693f80,0x3eaf3f2e,
0x3f613f80,0x3d993f0b,0xbf4cbed3,0xbf71bf7d,0x3ef50000,0x3f7f3f57,0x3f1b3f69,0xbeaf3e19,
0x3f543f80,0xbe643ebb,0xbf7dbf3c,0xbf00bf67,0x3f100000,0x3f7a3f6e,0x3e193f2e,0xbf5ebede,
0x3f443f80,0xbf003e32,0xbf71bf71,0x3e32bf00,0x3f250000,0x3f5e3f7c,0xbeaf3eaf,0xbf7cbf5e,
0x3f803f80,0x3f803f80,0x3f803f80,0x3f803f80,0x0,0x0,0x0,0x0,
0x3f203f33,0x3eea3f0b,0x3e8b3ebb,0x3d993e32,0x3f483f37,0x3f643f57,0x3f763f6e,0x3f7f3f7c,
0xbe64bccc,0xbf15bed3,0xbf5abf3c,0xbf7dbf71,0x3f7a3f80,0x3f503f69,0x3f053f2e,0x3e193eaf,
0xbf67bf3c,0xbf7dbf7d,0xbf3cbf67,0xbe64bf00,0x3ede3f2e,0xbe193e19,0xbf2ebede,0xbf7abf5e,
0xbf67bf80,0xbea3bf29,0x3eea3d99,0x3f753f44,0xbedebd4c,0xbf73bf40,0xbf64bf7f,0xbe97bf25,
0xbe64bf29,0x3f333e8b,0x3f7b3f75,0x3ebb3f44,0xbf7abf40,0xbf37bf76,0x3e4bbe97,0x3f6e3f25,
0x3f203d99,0x3f753f75,0x3d993f20,0xbf67bf00,0xbf48bf7f,0x3e97be97,0x3f7f3f48,0x3ede3f5e,
0x3f803f44,0x3e323f44,0xbf71bf00,0xbf00bf71,0xbf25,0x3f7c3f25,0x3eaf3f5e,0xbf5ebeaf,
0x3f803f80,0x3f803f80,0x3f803f80,0x3f803f80,0x0,0x0,0x0,0x0,
0xbdffbccc,0xbea3be64,0xbf00bed3,0xbf29bf15,0x3f7e3f80,0x3f733f7a,0x3f5e3f69,0x3f403f50,
0xbf78bf80,0xbf4cbf67,0xbf00bf29,0xbdffbea3,0xbe7dbd4c,0xbf1bbede,0xbf5ebf40,0xbf7ebf73,
0x3ebb3d99,0x3f543f20,0x3f803f75,0x3f543f75,0xbf6ebf7f,0xbf10bf48,0xbe97,0x3f103e97,
0x3f613f7f,0x3e8b3f20,0xbf00bdff,0xbf78bf4c,0x3ef53dcc,0x3f763f48,0x3f5e3f7e,0x3e7d3f1b,
0xbf15bdff,0xbf80bf67,0xbf00bf5a,0x3eeabccc,0x3f503f7e,0xbd4c3ede,0xbf5ebf05,0xbf64bf80,
0xbf3cbf7d,0x3ebbbe64,0x3f803f54,0x3ebb3f54,0xbf2ebe19,0xbf6ebf7a,0xbf10,0x3f6e3f10,
0x3f443e32,0x3f443f80,0xbf003e32,0xbf71bf71,0xbf25bf7c,0x3f250000,0x3f5e3f7c,0xbeaf3eaf,
0x3f803f80,0x3f803f80,0x3f803f80,0x3f803f80,0x0,0x0,0x0,0x0,
0xbf4cbf3c,0xbf67bf5a,0xbf78bf71,0xbf80bf7d,0x3f1b3f2e,0x3ede3f05,0x3e7d3eaf,0x3d4c3e19,
0x3e8b3d99,0x3f203eea,0x3f613f44,0x3f7f3f75,0xbf76bf7f,0xbf48bf64,0xbef5bf25,0xbdccbe97,
0x3ebb3f20,0xbe643d99,0xbf3cbf00,0xbf7dbf67,0x3f6e3f48,0x3f7a3f7f,0x3f2e3f5e,0x3e193ede,
0xbf5abf7d,0xbe64bf15,0x3f0b3e32,0x3f7b3f54,0xbf05be19,0xbf7abf50,0xbf57bf7c,0xbe4bbf10,
0x3f7f3f54,0x3f203f6c,0xbea33e32,0xbf78bf3c,0xbdccbf10,0x3f483ec7,0x3f733f7c,0x3e7d3f2e,
0xbf3cbe64,0xbf67bf7d,0x3d99bf00,0x3f753f20,0x3f2e3f7a,0xbede3e19,0xbf7fbf5e,0xbe97bf48,
0x3e32bf00,0x3f803f44,0x3e323f44,0xbf71bf00,0xbf7cbf5e,0xbf25,0x3f7c3f25,0x3eaf3f5e,
0x3f803f80,0x3f803f80,0x3f803f80,0x3f803f80,0x0,0x0,0x0,0x0,
0xbf7dbf80,0xbf71bf78,0xbf5abf67,0xbf3cbf4c,0xbe19bd4c,0xbeafbe7d,0xbf05bede,0xbf2ebf1b,
0x3f753f7f,0x3f443f61,0x3eea3f20,0x3d993e8b,0x3e973dcc,0x3f253ef5,0x3f643f48,0x3f7f3f76,
0xbf67bf7d,0xbf00bf3c,0x3d99be64,0x3f203ebb,0xbedebe19,0xbf5ebf2e,0xbf7fbf7a,0xbf48bf6e,
0x3f543f7b,0x3e323f0b,0xbf15be64,0xbf7dbf5a,0x3f103e4b,0x3f7c3f57,0x3f503f7a,0x3e193f05,
0xbf3cbf78,0x3e32bea3,0x3f6c3f20,0x3f543f7f,0xbf2ebe7d,0xbf7cbf73,0xbec7bf48,0x3f103dcc,
0x3f203f75,0xbf003d99,0xbf7dbf67,0xbe64bf3c,0x3f483e97,0x3f5e3f7f,0xbe193ede,0xbf7abf2e,
0xbf00bf71,0x3f443e32,0x3f443f80,0xbf003e32,0xbf5ebeaf,0xbf25bf7c,0x3f250000,0x3f5e3f7c,
0x3f803f80,0x3f803f80,0x3f803f80,0x3f803f80,0x0,0x0,0x0,0x0,
0xbf15bf29,0xbed3bf00,0xbe64bea3,0xbcccbdff,0xbf50bf40,0xbf69bf5e,0xbf7abf73,0xbf80bf7e,
0xbea3bdff,0xbf29bf00,0xbf67bf4c,0xbf80bf78,0x3f733f7e,0x3f403f5e,0x3ede3f1b,0x3d4c3e7d,
0x3f753f54,0x3f753f80,0x3f203f54,0x3d993ebb,0xbe97bf10,0x3e970000,0x3f483f10,0x3f7f3f6e,
0xbf4cbf78,0xbdffbf00,0x3f203e8b,0x3f7f3f61,0xbf1bbe7d,0xbf7ebf5e,0xbf48bf76,0xbdccbef5,
0xbccc3eea,0xbf5abf00,0xbf67bf80,0xbdffbf15,0x3f803f64,0x3f053f5e,0xbede3d4c,0xbf7ebf50,
0x3f543ebb,0x3f543f80,0xbe643ebb,0xbf7dbf3c,0xbf10bf6e,0x3f100000,0x3f7a3f6e,0x3e193f2e,
0xbf71bf71,0x3e32bf00,0x3f803f44,0x3e323f44,0xbeaf3eaf,0xbf7cbf5e,0xbf25,0x3f7c3f25,
0x3f803f80,0x3f803f80,0x3f803f80,0x3f803f80,0x0,0x0,0x0,0x0,
0x3e323d99,0x3ebb3e8b,0x3f0b3eea,0x3f333f20,0xbf7cbf7f,0xbf6ebf76,0xbf57bf64,0xbf37bf48,
0xbf71bf7d,0xbf3cbf5a,0xbed3bf15,0xbcccbe64,0xbeafbe19,0xbf2ebf05,0xbf69bf50,0xbf80bf7a,
0xbf00be64,0xbf67bf3c,0xbf7dbf7d,0xbf3cbf67,0x3f5e3f7a,0x3ede3f2e,0xbe193e19,0xbf2ebede,
0x3f443f75,0x3d993eea,0xbf29bea3,0xbf80bf67,0x3f253e97,0x3f7f3f64,0x3f403f73,0x3d4c3ede,
0x3f443ebb,0x3f753f7b,0x3e8b3f33,0xbf29be64,0xbf25bf6e,0x3e97be4b,0x3f763f37,0x3f403f7a,
0xbf00bf67,0x3f203d99,0x3f753f75,0x3d993f20,0xbf5ebede,0xbf48bf7f,0x3e97be97,0x3f7f3f48,
0xbf71bf00,0xbf00bf71,0x3f443e32,0x3f443f80,0x3eaf3f5e,0xbf5ebeaf,0xbf25bf7c,0x3f250000,
0x3f803f80,0x3f803f80,0x3f803f80,0x3f80,0x0,0x0,0x0,0x0,
0x3f543f44,0x3f6c3f61,0x3f7b3f75,0x3f7f,0xbf10bf25,0xbec7bef5,0xbe4bbe97,0xbdcc,
0x3ebb3e32,0x3f333f0b,0x3f6c3f54,0x3f7b,0xbf6ebf7c,0xbf37bf57,0xbec7bf10,0xbe4b,
0xbe64bf00,0x3ebb3d99,0x3f543f20,0x3f75,0xbf7abf5e,0xbf6ebf7f,0xbf10bf48,0xbe97,
0xbf3cbf71,0xbcccbed3,0x3f333ebb,0x3f6c,0xbf2ebeaf,0xbf80bf69,0xbf37bf6e,0xbec7,
0xbf7dbf71,0xbed3bf4c,0x3f0b3d99,0x3f61,0xbe193eaf,0xbf69bf1b,0xbf57bf7f,0xbef5,
0xbf67bf00,0xbf3cbf7d,0x3ebbbe64,0x3f54,0x3ede3f5e,0xbf2ebe19,0xbf6ebf7a,0xbf10,
0xbf003e32,0xbf71bf71,0x3e32bf00,0x3f44,0x3f5e3f7c,0xbeaf3eaf,0xbf7cbf5e,0xbf25,
0x3f333f80,0xbf3cbccc,0xbf29bf80,0x3f443d99,0x3f370000,0x3f2e3f80,0xbf40bd4c,0xbf25bf7f,
0x3f203f80,0xbf67be64,0xbe64bf67,0x3f803f20,0x3f480000,0x3ede3f7a,0xbf7abede,0xbf48,
0x3f0b3f80,0xbf7dbed3,0x3e8bbf29,0x3f443f75,0x3f570000,0x3e193f69,0xbf76bf40,0x3f25be97,
0x3eea3f80,0xbf7dbf15,0x3f33bea3,0x3e323f75,0x3f640000,0xbe193f50,0xbf37bf73,0x3f7c3e97,
0x3ebb3f80,0xbf67bf3c,0x3f753d99,0xbf003f20,0x3f6e0000,0xbede3f2e,0xbe97bf7f,0x3f5e3f48,
0x3e8b3f80,0xbf3cbf5a,0x3f7b3eea,0xbf713d99,0x3f760000,0xbf2e3f05,0x3e4bbf64,0x3eaf3f7f,
0x3e323f80,0xbf00bf71,0x3f443f44,0xbf71bf00,0x3f7c0000,0xbf5e3eaf,0x3f25bf25,0xbeaf3f5e,
0x3d993f80,0xbe64bf7d,0x3ebb3f75,0xbf00bf67,0x3f7f0000,0xbf7a3e19,0x3f6ebe97,0xbf5e3ede,
0x3f203f7f,0xbf4cbdff,0xbf15bf7d,0x3f543e32,0x3f483dcc,0x3f1b3f7e,0xbf50be19,0xbf10bf7c,
0xbe643f20,0xbf67bf67,0x3f20be64,0x3f203f80,0x3f7a3f48,0xbede3ede,0xbf48bf7a,0x3f480000,
0xbf67bdff,0xbcccbf5a,0x3f6c3f54,0xbf3c3e32,0x3ede3f7e,0xbf80bf05,0x3ec7bf10,0x3f2e3f7c,
0xbf67bf4c,0x3f61bccc,0xbdff3f54,0xbf3cbf71,0xbede3f1b,0xbef5bf80,0x3f7e3f10,0xbf2e3eaf,
0xbe64bf7d,0x3f543f54,0xbf7dbe64,0x3f20bf00,0xbf7abe19,0x3f10bf10,0x3e193f7a,0xbf48bf5e,
0x3f20bf15,0xbdff3f6c,0xbed3bf7d,0x3f543f44,0xbf48bf50,0x3f7e3ec7,0xbf693e19,0x3f10bf25,
0x3f803e32,0xbf713e32,0x3f44bf00,0xbf003f44,0xbf7c,0x3eaf3f7c,0xbf25bf5e,0x3f5e3f25,
0x3f203f54,0xbf3cbf3c,0x3f543f20,0xbf67bf00,0x3f48bf10,0xbf2e3f2e,0x3f10bf48,0xbede3f5e,
0x3f0b3f7b,0xbf5abe64,0xbf00bf78,0x3f613e8b,0x3f573e4b,0x3f053f7a,0xbf5ebe7d,0xbef5bf76,
0xbf67be64,0xbe64bf67,0x3f803f20,0xbe643f20,0x3ede3f7a,0xbf7abede,0xbf48,0x3f7a3f48,
0xbea3bf78,0x3f7f3f20,0xbf003eea,0xbf15bf80,0xbf73be7d,0x3dccbf48,0x3f5e3f64,0xbf503d4c,
0x3f7b3e8b,0xbed33f20,0xbf00bf80,0x3f7f3f0b,0xbe4bbf76,0x3f693f48,0xbf5e3d4c,0x3dccbf57,
0x3d993f75,0xbf3cbf67,0x3f803ebb,0xbf3c3ebb,0x3f7f3e97,0xbf2e3ede,0xbf6e,0x3f2e3f6e,
0xbf80bea3,0x3f61be64,0xbf003f33,0xbcccbf78,0xbd4c3f73,0xbef5bf7a,0x3f5e3f37,0xbf80be7d,
0x3e32bf71,0x3e323f80,0xbf00bf71,0x3f443f44,0xbf7cbeaf,0x3f7c0000,0xbf5e3eaf,0x3f25bf25,
0x3f753ebb,0xbf7dbe64,0x3f803d99,0xbf7d3d99,0x3e97bf6e,0xbe193f7a,0xbf7f,0x3e193f7f,
0x3eea3f75,0xbf67bea3,0xbed3bf71,0x3f6c3ebb,0x3f643e97,0x3ede3f73,0xbf69beaf,0xbec7bf6e,
0xbf67bf67,0x3f20be64,0x3f203f80,0xbf67be64,0xbede3ede,0xbf48bf7a,0x3f480000,0x3ede3f7a,
0x3f7b3ebb,0xbe643f33,0xbf4cbf71,0x3f613d99,0xbe4bbf6e,0x3f7a3f37,0xbf1b3eaf,0xbef5bf7f,
0xbf293ebb,0xbe64bf78,0x3f6c3f44,0xbf5a3d99,0x3f403f6e,0xbf7abe7d,0x3ec7bf25,0x3f053f7f,
0x3d99bf67,0x3f203f75,0xbf7dbf00,0x3f54be64,0xbf7fbede,0x3f48be97,0xbe193f5e,0xbf10bf7a,
0x3f0b3f75,0xbf67bf29,0x3f7f3e32,0xbf4c3ebb,0x3f57be97,0xbede3f40,0xbdccbf7c,0x3f1b3f6e,
0xbf71bf00,0x3f803e32,0xbf713e32,0x3f44bf00,0xbeaf3f5e,0xbf7c,0x3eaf3f7c,0xbf25bf5e,
0x3f75be64,0xbf673ebb,0x3f54bf00,0xbf3c3f20,0xbe97bf7a,0x3ede3f6e,0xbf10bf5e,0x3f2e3f48,
0x3ebb3f6c,0xbf71bed3,0xbea3bf67,0x3f753eea,0x3f6e3ec7,0x3eaf3f69,0xbf73bede,0xbe97bf64,
0xbe64bf67,0x3f803f20,0xbe643f20,0xbf67bf67,0xbf7abede,0xbf48,0x3f7a3f48,0xbede3ede,
0x3d993f61,0xbf71bf4c,0x3f33be64,0x3ebb3f7b,0x3f7f3ef5,0xbeaf3f1b,0xbf37bf7a,0x3f6e3e4b,
0x3d99bf5a,0x3f443f6c,0xbf78be64,0x3ebbbf29,0xbf7fbf05,0x3f25bec7,0x3e7d3f7a,0xbf6ebf40,
0xbe643f54,0xbf00bf7d,0x3f753f20,0xbf673d99,0x3f7a3f10,0xbf5e3e19,0x3e97bf48,0x3ede3f7f,
0x3ebbbf4c,0x3e323f7f,0xbf29bf67,0x3f753f0b,0xbf6ebf1b,0x3f7c3dcc,0xbf403ede,0x3e97bf57,
0xbf003f44,0x3e32bf71,0x3e323f80,0xbf00bf71,0x3f5e3f25,0xbf7cbeaf,0x3f7c0000,0xbf5e3eaf,
0x3f20bf3c,0xbf003f54,0x3ebbbf67,0xbe643f75,0xbf48bf2e,0x3f5e3f10,0xbf6ebede,0x3f7a3e97,
0x3e8b3f61,0xbf78bf00,0xbe64bf5a,0x3f7b3f0b,0x3f763ef5,0x3e7d3f5e,0xbf7abf05,0xbe4bbf57,
0x3f20be64,0x3f203f80,0xbf67be64,0xbe64bf67,0xbf48bf7a,0x3f480000,0x3ede3f7a,0xbf7abede,
0xbf80bf15,0x3eeabf00,0x3f203f7f,0xbf78bea3,0xbd4c3f50,0xbf64bf5e,0x3f48bdcc,0x3e7d3f73,
0x3f0b3f7f,0xbf80bf00,0x3f20bed3,0x3e8b3f7b,0x3f57bdcc,0xbd4c3f5e,0xbf48bf69,0x3f763e4b,
0x3ebbbf3c,0x3ebb3f80,0xbf67bf3c,0x3f753d99,0xbf6ebf2e,0x3f6e0000,0xbede3f2e,0xbe97bf7f,
0xbf78bccc,0x3f33bf00,0xbe643f61,0xbea3bf80,0x3e7d3f80,0xbf37bf5e,0x3f7a3ef5,0xbf733d4c,
0x3f443f44,0xbf71bf00,0x3f803e32,0xbf713e32,0x3f25bf25,0xbeaf3f5e,0xbf7c,0x3eaf3f7c,
0x3d99bf7d,0x3d993f80,0xbe64bf7d,0x3ebb3f75,0xbf7fbe19,0x3f7f0000,0xbf7a3e19,0x3f6ebe97,
0x3e323f54,0xbf7dbf15,0xbdffbf4c,0x3f7f3f20,0x3f7c3f10,0x3e193f50,0xbf7ebf1b,0xbdccbf48,
0x3f803f20,0xbe643f20,0xbf67bf67,0x3f20be64,0xbf48,0x3f7a3f48,0xbede3ede,0xbf48bf7a,
0x3e32bf3c,0x3f543f6c,0xbf5abccc,0xbdffbf67,0xbf7cbf2e,0x3f10bec7,0x3f053f80,0xbf7ebede,
0xbf71bf3c,0x3f54bdff,0xbccc3f61,0xbf4cbf67,0xbeaf3f2e,0xbf10bf7e,0x3f803ef5,0xbf1b3ede,
0xbf003f20,0xbe64bf7d,0x3f543f54,0xbf7dbe64,0x3f5e3f48,0xbf7abe19,0x3f10bf10,0x3e193f7a,
0x3f443f54,0xbf7dbed3,0x3f6cbdff,0xbf153f20,0x3f25bf10,0xbe193f69,0xbec7bf7e,0x3f503f48,
0x3f44bf00,0xbf003f44,0x3e32bf71,0x3e323f80,0xbf25bf5e,0x3f5e3f25,0xbf7cbeaf,0x3f7c0000,
0xbf00bf67,0x3f203f54,0xbf3cbf3c,0x3f543f20,0xbf5e3ede,0x3f48bf10,0xbf2e3f2e,0x3f10bf48,
0x3d993f44,0xbf80bf29,0xbcccbf3c,0x3f33,0x3f7f3f25,0x3d4c3f40,0xbf80bf2e,0xbf37,
0x3f203f80,0xbf67be64,0xbe64bf67,0x3f20,0x3f480000,0x3ede3f7a,0xbf7abede,0xbf48,
0x3f753f44,0xbf293e8b,0xbed3bf7d,0x3f0b,0x3e97bf25,0x3f403f76,0xbf69be19,0xbf57,
0x3f753e32,0xbea33f33,0xbf15bf7d,0x3eea,0xbe97bf7c,0x3f733f37,0xbf503e19,0xbf64,
0x3f20bf00,0x3d993f75,0xbf3cbf67,0x3ebb,0xbf48bf5e,0x3f7f3e97,0xbf2e3ede,0xbf6e,
0x3d99bf71,0x3eea3f7b,0xbf5abf3c,0x3e8b,0xbf7fbeaf,0x3f64be4b,0xbf053f2e,0xbf76,
0xbf00bf71,0x3f443f44,0xbf71bf00,0x3e32,0xbf5e3eaf,0x3f25bf25,0xbeaf3f5e,0xbf7c,
0xbf67bf00,0x3f753ebb,0xbf7dbe64,0x3d99,0xbede3f5e,0x3e97bf6e,0xbe193f7a,0xbf7f,
0xbccc3f80,0x3d99bf80,0xbdff3f7f,0x3e32bf7d,0x3f800000,0xbf7fbd4c,0x3f7e3dcc,0xbf7cbe19,
0xbdff3f80,0x3ebbbf78,0xbf153f61,0x3f44bf3c,0x3f7e0000,0xbf6ebe7d,0x3f503ef5,0xbf25bf2e,
0xbe643f80,0x3f20bf67,0xbf673f20,0x3f80be64,0x3f7a0000,0xbf48bede,0x3ede3f48,0xbf7a,
0xbea33f80,0x3f54bf4c,0xbf803e8b,0x3f443ebb,0x3f730000,0xbf10bf1b,0xbd4c3f76,0x3f25bf6e,
0xbed33f80,0x3f75bf29,0xbf5abdff,0x3e323f54,0x3f690000,0xbe97bf40,0xbf053f7e,0x3f7cbf10,
0xbf003f80,0x3f80bf00,0xbf00bf00,0xbf003f80,0x3f5e0000,0xbf5e,0xbf5e3f5e,0x3f5e0000,
0xbf153f80,0x3f75bea3,0xbcccbf4c,0xbf713f54,0x3f500000,0x3e97bf73,0xbf803f1b,0x3eaf3f10,
0xbf293f80,0x3f54bdff,0x3eeabf78,0xbf713ebb,0x3f400000,0x3f10bf7e,0xbf643e7d,0xbeaf3f6e,
0xbe643f7b,0x3e8bbf78,0xbea33f75,0x3ebbbf71,0x3f7a3e4b,0xbf76be7d,0x3f733e97,0xbf6ebeaf,
0xbf673f0b,0x3f7bbea3,0xbf803d99,0x3f753e32,0x3ede3f57,0xbe4bbf73,0xbd4c3f7f,0x3e97bf7c,
0xbf67be64,0x3f203f20,0xbe64bf67,0xbe643f80,0xbede3f7a,0x3f48bf48,0xbf7a3ede,0x3f7a0000,
0xbe64bf5a,0xbed33f7f,0x3f61bf3c,0xbf7d3e32,0xbf7a3f05,0x3f693dcc,0xbef5bf2e,0xbe193f7c,
0x3f20bf78,0xbf803eea,0x3f333ebb,0x3d99bf71,0xbf48be7d,0x3d4c3f64,0x3f37bf6e,0xbf7f3eaf,
0x3f80bf00,0xbf00bf00,0xbf003f80,0x3f80bf00,0xbf5e,0xbf5e3f5e,0x3f5e0000,0xbf5e,
0x3f203e8b,0x3f0bbf80,0xbf783ebb,0x3d993f44,0x3f48bf76,0xbf573d4c,0xbe7d3f6e,0x3f7fbf25,
0xbe643f61,0x3f7fbf15,0xbcccbf3c,0xbf7d3f44,0x3f7abef5,0x3dccbf50,0xbf803f2e,0x3e193f25,
0xbed33f6c,0x3eeabf67,0xbf003f61,0x3f0bbf5a,0x3f693ec7,0xbf64bede,0x3f5e3ef5,0xbf57bf05,
0xbf5abed3,0x3f333f20,0xbf00bf4c,0x3e8b3f6c,0xbf053f69,0x3f37bf48,0xbf5e3f1b,0x3f76bec7,
0x3f20bf67,0xbf673f20,0x3f80be64,0xbf67be64,0xbf48bede,0x3ede3f48,0xbf7a,0xbede3f7a,
0x3f333eea,0xbdffbf67,0xbf003f7b,0x3f6cbf29,0x3f37bf64,0xbf7e3ede,0x3f5e3e4b,0xbec7bf40,
0xbf4c3f61,0x3f7bbe64,0xbf00bf15,0xbea33f7f,0x3f1b3ef5,0x3e4bbf7a,0xbf5e3f50,0x3f73bdcc,
0xbf00bf00,0xbf003f80,0x3f80bf00,0xbf00bf00,0xbf5e3f5e,0x3f5e0000,0xbf5e,0xbf5e3f5e,
0x3f6cbf5a,0xbf29be64,0xbf003f7f,0x3f7bbed3,0xbec7bf05,0xbf403f7a,0x3f5ebdcc,0x3e4bbf69,
0x3e8b3f0b,0x3f6cbf67,0xbf00bea3,0xbf4c3f7b,0x3f76bf57,0xbec7bede,0xbf5e3f73,0x3f1b3e4b,
0xbf153f54,0x3f20bf4c,0xbf293f44,0x3f33bf3c,0x3f503f10,0xbf48bf1b,0x3f403f25,0xbf37bf2e,
0xbcccbf7d,0xbe643f7f,0x3eeabf71,0xbf293f54,0xbf803e19,0x3f7a3dcc,0xbf64beaf,0x3f403f10,
0x3f203f20,0xbe64bf67,0xbe643f80,0x3f20bf67,0x3f48bf48,0xbf7a3ede,0x3f7a0000,0xbf48bede,
0xbf783d99,0x3f203f0b,0xbcccbf71,0xbf153f75,0xbe7d3f7f,0x3f48bf57,0xbf803eaf,0x3f503e97,
0x3f6cbf3c,0xbf67bccc,0x3e8b3f44,0x3f0bbf7d,0xbec7bf2e,0xbede3f80,0x3f76bf25,0xbf57be19,
0xbf003f80,0x3f80bf00,0xbf00bf00,0xbf003f80,0x3f5e0000,0xbf5e,0xbf5e3f5e,0x3f5e0000,
0xbdffbf3c,0xbf673f61,0x3f333e32,0x3eeabf7d,0xbf7e3f2e,0x3ede3ef5,0x3f37bf7c,0xbf643e19,
0x3f333d99,0x3f20bf80,0xbf5a3e32,0xbed33f75,0x3f37bf7f,0xbf483d4c,0xbf053f7c,0x3f69be97,
0xbf3c3f33,0x3f44bf29,0xbf4c3f20,0x3f54bf15,0x3f2e3f37,0xbf25bf40,0x3f1b3f48,0xbf10bf50,
0x3f54bf29,0xbf713eea,0x3f7fbe64,0xbf7dbccc,0xbf10bf40,0x3eaf3f64,0xbdccbf7a,0xbe193f80,
0xbf673f20,0x3f80be64,0xbf67be64,0x3f203f20,0x3ede3f48,0xbf7a,0xbede3f7a,0x3f48bf48,
0x3f75bf15,0xbf71bccc,0x3f0b3f20,0x3d99bf78,0xbe97bf50,0xbeaf3f80,0x3f57bf48,0xbf7f3e7d,
0xbf7d3f0b,0x3f443e8b,0xbcccbf67,0xbf3c3f6c,0x3e193f57,0x3f25bf76,0xbf803ede,0x3f2e3ec7,
0x3f80bf00,0xbf00bf00,0xbf003f80,0x3f80bf00,0xbf5e,0xbf5e3f5e,0x3f5e0000,0xbf5e,
0xbf7d3eea,0x3e323f33,0x3f61bf67,0xbf3cbdff,0xbe193f64,0x3f7cbf37,0xbef5bede,0xbf2e3f7e,
0x3f75bed3,0x3e32bf5a,0xbf803f20,0x3d993f33,0x3e97bf69,0xbf7c3f05,0xbd4c3f48,0x3f7fbf37,
0xbf5a3f0b,0x3f61bf00,0xbf673eea,0x3f6cbed3,0x3f053f57,0xbef5bf5e,0x3ede3f64,0xbec7bf69,
0x3f6c3e8b,0xbf4cbf00,0x3f203f33,0xbed3bf5a,0x3ec7bf76,0xbf1b3f5e,0x3f48bf37,0xbf693f05,
0xbe64bf67,0xbe643f80,0x3f20bf67,0xbf673f20,0xbf7a3ede,0x3f7a0000,0xbf48bede,0x3ede3f48,
0xbf293f6c,0x3f7bbf00,0xbf67bdff,0x3eea3f33,0x3f403ec7,0xbe4bbf5e,0xbede3f7e,0x3f64bf37,
0x3f7fbea3,0xbf15bf00,0xbe643f7b,0x3f61bf4c,0x3dccbf73,0xbf503f5e,0x3f7abe4b,0xbef5bf1b,
0xbf00bf00,0xbf003f80,0x3f80bf00,0xbf00bf00,0xbf5e3f5e,0x3f5e0000,0xbf5e,0xbf5e3f5e,
0xbed33f7b,0x3f7fbf00,0xbe64bf29,0xbf5a3f6c,0x3f69be4b,0x3dccbf5e,0xbf7a3f40,0x3f053ec7,
0x3f7bbf4c,0xbea3bf00,0xbf673f6c,0x3f0b3e8b,0xbe4bbf1b,0xbf733f5e,0x3ede3ec7,0x3f57bf76,
0xbf713ebb,0x3f75bea3,0xbf783e8b,0x3f7bbe64,0x3eaf3f6e,0xbe97bf73,0x3e7d3f76,0xbe4bbf7a,
0x3e323f75,0x3d99bf80,0xbea33f7b,0x3f0bbf67,0x3f7cbe97,0xbf7f3d4c,0x3f733e4b,0xbf57bede,
0x3f80be64,0xbf67be64,0x3f203f20,0xbe64bf67,0xbf7a,0xbede3f7a,0x3f48bf48,0xbf7a3ede,
0x3e32bf7d,0xbf3c3f61,0x3f7fbed3,0xbf5abe64,0xbf7c3e19,0x3f2e3ef5,0xbdccbf69,0xbf053f7a,
0xbf713d99,0x3ebb3f33,0x3eeabf80,0xbf783f20,0xbeaf3f7f,0x3f6ebf37,0xbf64bd4c,0x3e7d3f48,
0xbf003f80,0x3f80bf00,0xbf00bf00,0xbf003f80,0x3f5e0000,0xbf5e,0xbf5e3f5e,0x3f5e0000,
0x3f443d99,0x3ebbbf78,0xbf803f0b,0x3e8b3f20,0x3f25bf7f,0xbf6e3e7d,0xbd4c3f57,0x3f76bf48,
0x3f44bf7d,0xbf3cbccc,0xbf153f7f,0x3f61be64,0xbf25be19,0xbf2e3f80,0x3f50bdcc,0x3ef5bf7a,
0xbf7d3e32,0x3f7fbdff,0xbf803d99,0xbccc,0x3e193f7c,0xbdccbf7e,0x3d4c3f7f,0xbf80,
0xbf3c3f44,0x3f61bf15,0xbf783ebb,0xbdff,0x3f2e3f25,0xbef5bf50,0x3e7d3f6e,0xbf7e,
0xbe643f80,0x3f20bf67,0xbf673f20,0xbe64,0x3f7a0000,0xbf48bede,0x3ede3f48,0xbf7a,
0x3ebb3f44,0x3e8bbf80,0xbf4c3f54,0xbea3,0x3f6ebf25,0xbf763d4c,0x3f1b3f10,0xbf73,
0x3f543e32,0xbdffbf5a,0xbf293f75,0xbed3,0x3f10bf7c,0xbf7e3f05,0x3f403e97,0xbf69,
0x3f80bf00,0xbf00bf00,0xbf003f80,0xbf00,0xbf5e,0xbf5e3f5e,0x3f5e0000,0xbf5e,
0x3f54bf71,0xbf4cbccc,0xbea33f75,0xbf15,0xbf10beaf,0xbf1b3f80,0x3f73be97,0xbf50,
0x3ebbbf71,0xbf783eea,0xbdff3f54,0xbf29,0xbf6e3eaf,0xbe7d3f64,0x3f7ebf10,0xbf40,
0xbf3c3f80,0x3f203d99,0x3f54bf7d,0xbf00be64,0x3f2e0000,0x3f48bf7f,0xbf10be19,0xbf5e3f7a,
0xbf4c3f80,0x3ebb3e8b,0x3f7fbf5a,0x3e32bf3c,0x3f1b0000,0x3f6ebf76,0xbdccbf05,0xbf7c3f2e,
0xbf5a3f80,0x3d993eea,0x3f6cbf15,0x3f44bf7d,0x3f050000,0x3f7fbf64,0x3ec7bf50,0xbf253e19,
0xbf673f80,0xbe643f20,0x3f20be64,0x3f80bf67,0x3ede0000,0x3f7abf48,0x3f48bf7a,0xbede,
0xbf713f80,0xbf003f44,0x3e323e32,0x3f44bf00,0x3eaf0000,0x3f5ebf25,0x3f7cbf7c,0x3f25bf5e,
0xbf783f80,0xbf3c3f61,0xbea33f0b,0x3e323d99,0x3e7d0000,0x3f2ebef5,0x3f73bf57,0x3f7cbf7f,
0xbf7d3f80,0xbf673f75,0xbf3c3f54,0xbf003f20,0x3e190000,0x3edebe97,0x3f2ebf10,0x3f5ebf48,
0xbf803f80,0xbf7d3f7f,0xbf783f7b,0xbf713f75,0x3d4c0000,0x3e19bdcc,0x3e7dbe4b,0x3eafbe97,
0xbf673f75,0x3ebb3ebb,0x3f75bf67,0xbe64bf00,0x3ede3e97,0x3f6ebf6e,0xbe97bede,0xbf7a3f5e,
0xbf673eea,0xbf293f7b,0x3f0b3d99,0x3f75bf71,0xbede3f64,0x3f40be4b,0x3f57bf7f,0xbe97beaf,
0xbe64bea3,0xbf783f33,0xbf293f75,0x3ebb3e32,0xbf7a3f73,0xbe7d3f37,0x3f40be97,0x3f6ebf7c,
0x3f20bf67,0xbe64be64,0xbf673f20,0xbf673f80,0xbf483ede,0xbf7a3f7a,0xbede3f48,0x3ede0000,
0x3f80bf71,0x3f44bf71,0x3e32bf00,0xbf003e32,0xbeaf,0xbf253eaf,0xbf7c3f5e,0xbf5e3f7c,
0x3f20bed3,0x3f6cbf4c,0x3f7fbf7d,0x3f54bf71,0x3f48bf69,0x3ec7bf1b,0xbdccbe19,0xbf103eaf,
0xbe643ebb,0x3d993d99,0x3ebbbe64,0x3f20bf00,0x3f7abf6e,0x3f7fbf7f,0x3f6ebf7a,0x3f48bf5e,
0xbf673f6c,0xbf5a3f61,0xbf4c3f54,0xbf3c3f44,0x3edebec7,0x3f05bef5,0x3f1bbf10,0x3f2ebf25,
0xbf7d3f54,0x3d993f20,0x3f80bf3c,0x3d99bf3c,0x3e193f10,0x3f7fbf48,0xbf2e,0xbf7f3f2e,
0xbcccbf15,0xbf783f20,0xbf003f6c,0x3f33bdff,0xbf803f50,0xbe7d3f48,0x3f5ebec7,0x3f37bf7e,
0x3f7fbf4c,0x3f0bbf67,0xbf00bccc,0xbf803f61,0x3dccbf1b,0xbf573ede,0xbf5e3f80,0x3d4c3ef5,
0xbe643f20,0x3f20be64,0x3f80bf67,0x3f20bf67,0x3f7abf48,0x3f48bf7a,0xbede,0xbf483ede,
0xbf713f44,0xbf713f80,0xbf003f44,0x3e323e32,0xbeaf3f25,0x3eaf0000,0x3f5ebf25,0x3f7cbf7c,
0x3eeabf29,0xbcccbe64,0xbf003e8b,0xbf5a3f33,0xbf643f40,0xbf803f7a,0xbf5e3f76,0xbf053f37,
0x3f54bf3c,0x3f75bf67,0x3f80bf7d,0x3f75bf7d,0x3f10bf2e,0x3e97bede,0xbe19,0xbe973e19,
0xbf293f33,0xbf153f20,0xbf003f0b,0xbed33eea,0x3f40bf37,0x3f50bf48,0x3f5ebf57,0x3f69bf64,
0xbf7d3f20,0xbe643f54,0x3f75bf00,0x3ebbbf67,0xbe193f48,0x3f7abf10,0x3e97bf5e,0xbf6e3ede,
0x3f61bf7d,0xbe64bed3,0xbf803f44,0xbea33f54,0xbef5be19,0xbf7a3f69,0xbd4c3f25,0x3f73bf10,
0xbed33f54,0x3f20bdff,0x3f7bbf71,0x3e8bbf3c,0x3f69bf10,0x3f48bf7e,0xbe4bbeaf,0xbf763f2e,
0xbe64be64,0xbf673f20,0xbf673f80,0xbe643f20,0xbf7a3f7a,0xbede3f48,0x3ede0000,0x3f7abf48,
0x3f44bf00,0x3f80bf71,0x3f44bf71,0x3e32bf00,0x3f25bf5e,0xbeaf,0xbf253eaf,0xbf7c3f5e,
0xbf803f75,0xbf673f7b,0xbf153f44,0xbdff3ebb,0xbd4c3e97,0x3edebe4b,0x3f50bf25,0x3f7ebf6e,
0x3f54bf67,0x3f20bf3c,0x3ebbbf00,0x3d99be64,0xbf103ede,0xbf483f2e,0xbf6e3f5e,0xbf7f3f7a,
0xbea33ebb,0xbe643e8b,0xbdff3e32,0xbccc3d99,0x3f73bf6e,0x3f7abf76,0x3f7ebf7c,0x3f80bf7f,
0xbf673ebb,0xbf003f75,0x3f54be64,0x3f20bf7d,0xbede3f6e,0x3f5ebe97,0x3f10bf7a,0xbf483e19,
0x3f54bea3,0x3f44bf80,0xbed3be64,0xbf7d3f61,0x3f10bf73,0xbf253d4c,0xbf693f7a,0x3e193ef5,
0xbf3c3e8b,0xbf713f7b,0xbdff3f20,0x3f54bed3,0xbf2e3f76,0x3eaf3e4b,0x3f7ebf48,0x3f10bf69,
0x3f20be64,0x3f80bf67,0x3f20bf67,0xbe64be64,0x3f48bf7a,0xbede,0xbf483ede,0xbf7a3f7a,
0xbf003e32,0xbf713f44,0xbf713f80,0xbf003f44,0xbf5e3f7c,0xbeaf3f25,0x3eaf0000,0x3f5ebf25,
0x3ebbbdff,0x3f44bf15,0x3f7bbf67,0x3f75bf80,0x3f6ebf7e,0x3f25bf50,0x3e4bbede,0xbe973d4c,
0xbe643d99,0xbf003ebb,0xbf3c3f20,0xbf673f54,0xbf7a3f7f,0xbf5e3f6e,0xbf2e3f48,0xbede3f10,
0x3d99bccc,0x3e32bdff,0x3e8bbe64,0x3ebbbea3,0x3f7fbf80,0x3f7cbf7e,0x3f76bf7a,0x3f6ebf73,
0xbf3c3d99,0xbf3c3f80,0x3f203d99,0x3f54bf7d,0xbf2e3f7f,0x3f2e0000,0x3f48bf7f,0xbf10be19,
0xbdff3f33,0x3f6cbf00,0x3f20bf78,0xbf15bccc,0x3f7ebf37,0x3ec7bf5e,0xbf483e7d,0xbf503f80,
0x3f61bf80,0xbcccbf00,0xbf673f0b,0xbf4c3f7f,0xbef5bd4c,0xbf803f5e,0xbede3f57,0x3f1bbdcc,
0xbf673f20,0xbf673f80,0xbe643f20,0x3f20be64,0xbede3f48,0x3ede0000,0x3f7abf48,0x3f48bf7a,
0x3e323e32,0x3f44bf00,0x3f80bf71,0x3f44bf71,0x3f7cbf7c,0x3f25bf5e,0xbeaf,0xbf253eaf,
0x3f33bf5a,0x3e8bbf00,0xbe64bccc,0xbf293eea,0xbf373f05,0xbf763f5e,0xbf7a3f80,0xbf403f64,
0xbf7d3f75,0xbf7d3f80,0xbf673f75,0xbf3c3f54,0xbe193e97,0x3e190000,0x3edebe97,0x3f2ebf10,
0x3eeabed3,0x3f0bbf00,0x3f20bf15,0x3f33bf29,0x3f64bf69,0x3f57bf5e,0x3f48bf50,0x3f37bf40,
0xbf00be64,0xbf673f75,0x3ebb3ebb,0x3f75bf67,0xbf5e3f7a,0x3ede3e97,0x3f6ebf6e,0xbe97bede,
0xbf713f75,0x3d993f0b,0x3f7bbf29,0x3eeabf67,0x3eaf3e97,0x3f7fbf57,0x3e4bbf40,0xbf643ede,
0x3e323ebb,0x3f75bf29,0x3f33bf78,0xbea3be64,0x3f7cbf6e,0x3e97bf40,0xbf373e7d,0xbf733f7a,
0x3f80bf67,0x3f20bf67,0xbe64be64,0xbf673f20,0xbede,0xbf483ede,0xbf7a3f7a,0xbede3f48,
0x3e32bf00,0xbf003e32,0xbf713f44,0xbf713f80,0xbf7c3f5e,0xbf5e3f7c,0xbeaf3f25,0x3eaf0000,
0xbf713f54,0xbf7d3f7f,0xbf4c3f6c,0xbed33f20,0xbeaf3f10,0x3e193dcc,0x3f1bbec7,0x3f69bf48,
0xbf003f20,0xbe643ebb,0x3d993d99,0x3ebbbe64,0x3f5ebf48,0x3f7abf6e,0x3f7fbf7f,0x3f6ebf7a,
0x3f44bf3c,0x3f54bf4c,0x3f61bf5a,0x3f6cbf67,0x3f25bf2e,0x3f10bf1b,0x3ef5bf05,0x3ec7bede,
0xbe64bf00,0xbf7d3f54,0x3d993f20,0xbf3c,0xbf7a3f5e,0x3e193f10,0x3f7fbf48,0xbf2e,
0xbf3c3e32,0xbf5a3f7f,0x3e8b3ebb,0xbf4c,0xbf2e3f7c,0x3f053dcc,0x3f76bf6e,0xbf1b,
0xbf7d3f44,0xbf153f6c,0x3eea3d99,0xbf5a,0xbe193f25,0x3f50bec7,0x3f64bf7f,0xbf05,
0xbf673f80,0xbe643f20,0x3f20be64,0xbf67,0x3ede0000,0x3f7abf48,0x3f48bf7a,0xbede,
0xbf003f44,0x3e323e32,0x3f44bf00,0xbf71,0x3f5ebf25,0x3f7cbf7c,0x3f25bf5e,0xbeaf,
0x3d993e32,0x3f0bbea3,0x3f61bf3c,0xbf78,0x3f7fbf7c,0x3f57bf73,0x3ef5bf2e,0xbe7d,
0x3f20bf00,0x3f54bf3c,0x3f75bf67,0xbf7d,0x3f48bf5e,0x3f10bf2e,0x3e97bede,0xbe19,
0x3f75bf71,0x3f7bbf78,0x3f7fbf7d,0xbf80,0x3e97beaf,0x3e4bbe7d,0x3dccbe19,0xbd4c,
0x3bf4,0x3c74,0x3c74,0x3c74,0x3bf4,0x3ac23c73,0x3b413c6f,0x3b903c69,
0x3bf4,0x3b413c6f,0x3bbd3c61,0x3c093c49,0x3bf4,0x3b903c69,0x3c093c49,0x3c3f3c18,
0x3bf4,0x3bbd3c61,0x3c2f3c2a,0x3c633bb2,0x3bf4,0x3be93c56,0x3c4d3c04,0x3c733a92,
0x3bf4,0x3c093c49,0x3c633bb2,0x3c6ebb59,0x3bf4,0x3c1d3c3b,0x3c703b29,0x3c53bbf4,
0x3bf4,0x3c2f3c2a,0x3c74b9c2,0x3c26bc33,0x3bf4,0x3c3f3c18,0x3c6ebb59,0x3bd4bc5c,
0x3bf4,0x3c4d3c04,0x3c5ebbc9,0x3b11bc71,0x3bf4,0x3c593bde,0x3c46bc0e,0xbb11bc71,
0x3bf4,0x3c633bb2,0x3c26bc33,0xbbd4bc5c,0x3bf4,0x3c6b3b84,0x3bfebc50,0xbc26bc33,
0x3bf4,0x3c703b29,0x3ba7bc65,0xbc53bbf4,0x3bf4,0x3c733a92,0x3b11bc71,0xbc6ebb59,
0x3c74,0x3c74,0x3c74,0x3c74,0x3bbd3c61,0x3be93c56,0x3c093c49,0x3c1d3c3b,
0x3c2f3c2a,0x3c4d3c04,0x3c633bb2,0x3c703b29,0x3c633bb2,0x3c733a92,0x3c6ebb59,0x3c53bbf4,
0x3c74b9c2,0x3c5ebbc9,0x3c26bc33,0x3ba7bc65,0x3c5ebbc9,0x3c13bc42,0x3b11bc71,0xbba7bc65,
0x3c26bc33,0x3b11bc71,0xbbd4bc5c,0xbc53bbf4,0x3ba7bc65,0xbba7bc65,0xbc53bbf4,0xbc703b29,
0xba42bc73,0xbc37bc21,0xbc733a92,0xbc1d3c3b,0xbbd4bc5c,0xbc6ebb59,0xbc3f3c18,0x3c74,
0xbc37bc21,0xbc6b3b84,0xbb903c69,0x3c1d3c3b,0xbc67bb9b,0xbc2f3c2a,0x3b903c69,0x3c703b29,
0xbc733a92,0xbb903c69,0x3c3f3c18,0x3c53bbf4,0xbc593bde,0x3b413c6f,0x3c733a92,0x3ba7bc65,
0xbc1d3c3b,0x3c1d3c3b,0x3c53bbf4,0xbba7bc65,0xbb903c69,0x3c633bb2,0x3bd4bc5c,0xbc53bbf4,
0x3c74,0x3c74,0x3c74,0x3c74,0x3c2f3c2a,0x3c3f3c18,0x3c4d3c04,0x3c593bde,
0x3c74b9c2,0x3c6ebb59,0x3c5ebbc9,0x3c46bc0e,0x3c26bc33,0x3bd4bc5c,0x3b11bc71,0xbb11bc71,
0xba42bc73,0xbbd4bc5c,0xbc37bc21,0xbc67bb9b,0xbc37bc21,0xbc6ebb59,0xbc6b3b84,0xbc2f3c2a,
0xbc733a92,0xbc3f3c18,0xbb903c69,0x3b903c69,0xbc1d3c3b,0x3c74,0x3c1d3c3b,0x3c703b29,
0x3ac23c73,0x3c3f3c18,0x3c72baf2,0x3c13bc42,0x3c3f3c18,0x3c6ebb59,0x3bd4bc5c,0xbbd4bc5c,
0x3c72baf2,0x3bd4bc5c,0xbbfebc50,0xbc74b9c2,0x3c13bc42,0xbbd4bc5c,0xbc74b9c2,0xbbe93c56,
0xbb11bc71,0xbc6ebb59,0xbc093c49,0x3c093c49,0xbc46bc0e,0xbc3f3c18,0x3bbd3c61,0x3c72baf2,
0xbc703b29,0x3c74,0x3c703b29,0x3ba7bc65,0xbc093c49,0x3c3f3c18,0x3c26bc33,0xbc26bc33,
0x3c74,0x3c74,0x3c74,0x3c74,0x3c633bb2,0x3c6b3b84,0x3c703b29,0x3c733a92,
0x3c26bc33,0x3bfebc50,0x3ba7bc65,0x3b11bc71,0xbbd4bc5c,0xbc26bc33,0xbc53bbf4,0xbc6ebb59,
0xbc733a92,0xbc593bde,0xbc1d3c3b,0xbb903c69,0xbb903c69,0x3b413c6f,0x3c1d3c3b,0x3c633bb2,
0x3c3f3c18,0x3c733a92,0x3c53bbf4,0x3bd4bc5c,0x3c53bbf4,0x3ba7bc65,0xbba7bc65,0xbc53bbf4,
0xbb11bc71,0xbc46bc0e,0xbc703b29,0xbc093c49,0xbc6ebb59,0xbc3f3c18,0x3c74,0x3c3f3c18,
0xbc093c49,0x3bbd3c61,0x3c703b29,0x3c26bc33,0x3c093c49,0x3c72baf2,0x3ba7bc65,0xbc26bc33,
0x3c6ebb59,0x3b11bc71,0xbc53bbf4,0xbc3f3c18,0x3b11bc71,0xbc5ebbc9,0xbc1d3c3b,0x3c093c49,
0xbc53bbf4,0xbc1d3c3b,0x3c1d3c3b,0x3c53bbf4,0xbc3f3c18,0x3c093c49,0x3c53bbf4,0xbbd4bc5c,
0x3c74,0x3c74,0x3c74,0x3c74,0x3c74b9c2,0x3c72baf2,0x3c6ebb59,0x3c67bb9b,
0xba42bc73,0xbb71bc6c,0xbbd4bc5c,0xbc13bc42,0xbc733a92,0xbc633bb2,0xbc3f3c18,0xbc093c49,
0x3ac23c73,0x3be93c56,0x3c3f3c18,0x3c6b3b84,0x3c72baf2,0x3c46bc0e,0x3bd4bc5c,0xba42bc73,
0xbb11bc71,0xbc26bc33,0xbc6ebb59,0xbc633bb2,0xbc703b29,0xbc1d3c3b,0x3c74,0x3c1d3c3b,
0x3b413c6f,0x3c4d3c04,0x3c6ebb59,0x3bfebc50,0x3c6ebb59,0x3bd4bc5c,0xbbd4bc5c,0xbc6ebb59,
0xbb71bc6c,0xbc67bb9b,0xbc3f3c18,0x3ac23c73,0xbc6b3b84,0xbb413c6f,0x3c3f3c18,0x3c5ebbc9,
0x3b903c69,0x3c733a92,0x3bd4bc5c,0xbc26bc33,0x3c67bb9b,0xba42bc73,0xbc6ebb59,0xbbe93c56,
0xbba7bc65,0xbc703b29,0x3c74,0x3c703b29,0xbc633bb2,0x3b903c69,0x3c6ebb59,0xbb11bc71,
0x3c74,0x3c74,0x3c74,0x3c74,0x3c5ebbc9,0x3c53bbf4,0x3c46bc0e,0x3c37bc21,
0xbc37bc21,0xbc53bbf4,0xbc67bb9b,0xbc72baf2,0xbb903c69,0x3c74,0x3b903c69,0x3c093c49,
0x3c72baf2,0x3c53bbf4,0x3c13bc42,0x3b71bc6c,0xbbfebc50,0xbc53bbf4,0xbc74b9c2,0xbc593bde,
0xbc093c49,0x3c74,0x3c093c49,0x3c633bb2,0x3c703b29,0x3c53bbf4,0x3ba7bc65,0xbba7bc65,
0xbb71bc6c,0xbc53bbf4,0xbc6b3b84,0xbbe93c56,0xbc3f3c18,0x3c74,0x3c3f3c18,0x3c6ebb59,
0x3c593bde,0x3c53bbf4,0x3a42bc73,0xbc46bc0e,0x3a42bc73,0xbc53bbf4,0xbc4d3c04,0x3ac23c73,
0xbc633bb2,0x3c74,0x3c633bb2,0x3c26bc33,0x3c2f3c2a,0x3c53bbf4,0xbb71bc6c,0xbc74b9c2,
0x3ba7bc65,0xbc53bbf4,0xbc1d3c3b,0x3c1d3c3b,0xbc733a92,0x3c74,0x3c733a92,0x3b11bc71,
0x3c74,0x3c74,0x3c74,0x3c74,0x3c26bc33,0x3c13bc42,0x3bfebc50,0x3bd4bc5c,
0xbc733a92,0xbc6b3b84,0xbc593bde,0xbc3f3c18,0x3c3f3c18,0x3c633bb2,0x3c733a92,0x3c6ebb59,
0xbb11bc71,0xbbfebc50,0xbc46bc0e,0xbc6ebb59,0xbc093c49,0xbac23c73,0x3bbd3c61,0x3c3f3c18,
0x3c6ebb59,0x3c26bc33,0x3b11bc71,0xbbd4bc5c,0xbc53bbf4,0xbc703b29,0xbc1d3c3b,0x3c74,
0x3b903c69,0x3c593bde,0x3c67bb9b,0x3bd4bc5c,0x3bd4bc5c,0xbbd4bc5c,0xbc6ebb59,0xbc3f3c18,
0xbc633bb2,0xbb413c6f,0x3c2f3c2a,0x3c6ebb59,0x3c633bb2,0x3c37bc21,0xbb71bc6c,0xbc6ebb59,
0xbbd4bc5c,0xbc733a92,0xbb903c69,0x3c3f3c18,0xbb903c69,0x3c4d3c04,0x3c37bc21,0xbbd4bc5c,
0x3c53bbf4,0xbba7bc65,0xbc703b29,0x3c74,0xbc6ebb59,0xbb903c69,0x3c633bb2,0x3bd4bc5c,
0x3c74,0x3c74,0x3c74,0x3c74,0x3ba7bc65,0x3b71bc6c,0x3b11bc71,0x3a42bc73,
0xbc1d3c3b,0xbbe93c56,0xbb903c69,0xbac23c73,0x3c53bbf4,0x3c26bc33,0x3bd4bc5c,0x3b11bc71,
0xbc703b29,0xbc4d3c04,0xbc093c49,0xbb413c6f,0x3c703b29,0x3c67bb9b,0x3c26bc33,0x3b71bc6c,
0xbc53bbf4,0xbc733a92,0xbc3f3c18,0xbb903c69,0x3c1d3c3b,0x3c703b29,0x3c53bbf4,0x3ba7bc65,
0xbba7bc65,0xbc5ebbc9,0xbc633bb2,0xbbbd3c61,0x3c74,0x3c3f3c18,0x3c6ebb59,0x3bd4bc5c,
0x3ba7bc65,0xbc13bc42,0xbc733a92,0xbbe93c56,0xbc1d3c3b,0x3bbd3c61,0x3c733a92,0x3bfebc50,
0x3c53bbf4,0xbb11bc71,0xbc6ebb59,0xbc093c49,0xbc703b29,0xbac23c73,0x3c633bb2,0x3c13bc42,
0x3c703b29,0x3ba7bc65,0xbc53bbf4,0xbc1d3c3b,0xbc53bbf4,0xbc093c49,0x3c3f3c18,0x3c26bc33,
0x3bf4,0x3c74b9c2,0xba42bc73,0xbc733a92,0x3bf4,0x3c72baf2,0xbb71bc6c,0xbc633bb2,
0x3bf4,0x3c6ebb59,0xbbd4bc5c,0xbc3f3c18,0x3bf4,0x3c67bb9b,0xbc13bc42,0xbc093c49,
0x3bf4,0x3c5ebbc9,0xbc37bc21,0xbb903c69,0x3bf4,0x3c53bbf4,0xbc53bbf4,0x3c74,
0x3bf4,0x3c46bc0e,0xbc67bb9b,0x3b903c69,0x3bf4,0x3c37bc21,0xbc72baf2,0x3c093c49,
0x3bf4,0x3c26bc33,0xbc733a92,0x3c3f3c18,0x3bf4,0x3c13bc42,0xbc6b3b84,0x3c633bb2,
0x3bf4,0x3bfebc50,0xbc593bde,0x3c733a92,0x3bf4,0x3bd4bc5c,0xbc3f3c18,0x3c6ebb59,
0x3bf4,0x3ba7bc65,0xbc1d3c3b,0x3c53bbf4,0x3bf4,0x3b71bc6c,0xbbe93c56,0x3c26bc33,
0x3bf4,0x3b11bc71,0xbb903c69,0x3bd4bc5c,0x3bf4,0x3a42bc73,0xbac23c73,0x3b11bc71,
0x3ac23c73,0x3c72baf2,0xbb11bc71,0xbc703b29,0x3be93c56,0x3c46bc0e,0xbc26bc33,0xbc1d3c3b,
0x3c3f3c18,0x3bd4bc5c,0xbc6ebb59,0x3c74,0x3c6b3b84,0xba42bc73,0xbc633bb2,0x3c1d3c3b,
0x3c72baf2,0xbbfebc50,0xbc093c49,0x3c703b29,0x3c53bbf4,0xbc53bbf4,0x3c74,0x3c53bbf4,
0x3c13bc42,0xbc74b9c2,0x3c093c49,0x3ba7bc65,0x3b71bc6c,0xbc593bde,0x3c633bb2,0xbba7bc65,
0xbb11bc71,0xbc093c49,0x3c6ebb59,0xbc53bbf4,0xbbfebc50,0xbac23c73,0x3c26bc33,0xbc703b29,
0xbc46bc0e,0x3bbd3c61,0x3b11bc71,0xbc1d3c3b,0xbc6ebb59,0x3c3f3c18,0xbbd4bc5c,0x3c74,
0xbc703b29,0x3c703b29,0xbc53bbf4,0x3c1d3c3b,0xbc4d3c04,0x3c67bb9b,0xbc733a92,0x3c703b29,
0xbc093c49,0x3c26bc33,0xbc3f3c18,0x3c53bbf4,0xbb413c6f,0x3b71bc6c,0xbb903c69,0x3ba7bc65,
0x3b413c6f,0x3c6ebb59,0xbb71bc6c,0xbc6b3b84,0x3c4d3c04,0x3bd4bc5c,0xbc67bb9b,0xbb413c6f,
0x3c6ebb59,0xbbd4bc5c,0xbc3f3c18,0x3c3f3c18,0x3bfebc50,0xbc6ebb59,0x3ac23c73,0x3c5ebbc9,
0xbb71bc6c,0xbc3f3c18,0x3c593bde,0x3a42bc73,0xbc53bbf4,0x3c74,0x3c53bbf4,0xbc53bbf4,
0xbc6b3b84,0x3c3f3c18,0x3a42bc73,0xbc4d3c04,0xbbe93c56,0x3c6ebb59,0xbc46bc0e,0x3ac23c73,
0x3b903c69,0x3bd4bc5c,0xbc633bb2,0x3c633bb2,0x3c593bde,0xbbd4bc5c,0xbb413c6f,0x3c37bc21,
0x3c67bb9b,0xbc6ebb59,0x3c2f3c2a,0xbb71bc6c,0x3bd4bc5c,0xbc3f3c18,0x3c6ebb59,0xbc6ebb59,
0xbba7bc65,0x3c74,0x3ba7bc65,0xbc1d3c3b,0xbc5ebbc9,0x3c3f3c18,0xbc13bc42,0x3bbd3c61,
0xbc633bb2,0x3c6ebb59,0xbc733a92,0x3c733a92,0xbbbd3c61,0x3bd4bc5c,0xbbe93c56,0x3bfebc50,
0x3b903c69,0x3c67bb9b,0xbba7bc65,0xbc633bb2,0x3c733a92,0xba42bc73,0xbc703b29,0x3b903c69,
0x3bd4bc5c,0xbc6ebb59,0x3c74,0x3c6ebb59,0xbc26bc33,0xbbe93c56,0x3c703b29,0xbb11bc71,
0xbc633bb2,0x3c2f3c2a,0x3ba7bc65,0xbc733a92,0x3c74,0x3c53bbf4,0xbc53bbf4,0x3c74,
0x3c633bb2,0xbb71bc6c,0xbc1d3c3b,0x3c733a92,0x3c26bc33,0xbc74b9c2,0x3c1d3c3b,0x3b11bc71,
0xbbd4bc5c,0xbb903c69,0x3c53bbf4,0xbc6ebb59,0xbc733a92,0x3c4d3c04,0xbba7bc65,0xbb903c69,
0xbb903c69,0x3c37bc21,0xbc703b29,0x3c633bb2,0x3c3f3c18,0xbbd4bc5c,0x3c74,0x3bd4bc5c,
0x3c53bbf4,0xbc703b29,0x3c703b29,0xbc53bbf4,0xbb11bc71,0xbac23c73,0x3ba7bc65,0xbc093c49,
0xbc6ebb59,0x3c633bb2,0xbc53bbf4,0x3c3f3c18,0xbc093c49,0x3c13bc42,0xbc1d3c3b,0x3c26bc33,
0x3bbd3c61,0x3c5ebbc9,0xbbd4bc5c,0xbc593bde,0x3c5ebbc9,0xbbfebc50,0xbc3f3c18,0x3c2f3c2a,
0xbbd4bc5c,0xbc3f3c18,0x3c3f3c18,0x3bd4bc5c,0xbc593bde,0x3c2f3c2a,0x3bd4bc5c,0xbc72baf2,
0x3be93c56,0x3c13bc42,0xbc6ebb59,0x3b413c6f,0x3c53bbf4,0xbc53bbf4,0x3c74,0x3c53bbf4,
0xbbfebc50,0xbbbd3c61,0x3c6ebb59,0xbc37bc21,0xbc4d3c04,0x3c6b3b84,0xbbd4bc5c,0xbbbd3c61,
0x3c093c49,0x3b11bc71,0xbc3f3c18,0x3c733a92,0x3c46bc0e,0xbc74b9c2,0x3c3f3c18,0xbb71bc6c,
0xbc13bc42,0x3ac23c73,0x3bd4bc5c,0xbc4d3c04,0xbc3f3c18,0x3c6ebb59,0xbc6ebb59,0x3c3f3c18,
0x3c1d3c3b,0xbba7bc65,0x3c74,0x3ba7bc65,0x3c37bc21,0xbc593bde,0x3c6ebb59,0xbc74b9c2,
0xbc26bc33,0x3c093c49,0xbbd4bc5c,0x3b903c69,0xbc2f3c2a,0x3c37bc21,0xbc3f3c18,0x3c46bc0e,
0x3be93c56,0x3c53bbf4,0xbbfebc50,0xbc4d3c04,0x3c13bc42,0xbc53bbf4,0xbbbd3c61,0x3c6b3b84,
0xbc6ebb59,0x3c74,0x3c6ebb59,0xbbd4bc5c,0x3b413c6f,0x3c53bbf4,0xbc37bc21,0xbbbd3c61,
0x3c46bc0e,0xbc53bbf4,0xbac23c73,0x3c67bb9b,0xbc53bbf4,0x3c74,0x3c53bbf4,0xbc53bbf4,
0xbac23c73,0x3c53bbf4,0xbc5ebbc9,0x3b413c6f,0x3c67bb9b,0xbc53bbf4,0x3b413c6f,0x3c13bc42,
0xbc26bc33,0x3c74,0x3c26bc33,0xbc733a92,0xbbbd3c61,0x3c53bbf4,0xbc72baf2,0x3c2f3c2a,
0x3c74b9c2,0xbc53bbf4,0x3be93c56,0x3a42bc73,0xbbd4bc5c,0x3c74,0x3bd4bc5c,0xbc3f3c18,
0xbc1d3c3b,0x3c53bbf4,0xbc703b29,0x3c703b29,0x3c6b3b84,0xbc53bbf4,0x3c2f3c2a,0xbbfebc50,
0xbb11bc71,0x3c74,0x3b11bc71,0xbb903c69,0xbc4d3c04,0x3c53bbf4,0xbc593bde,0x3c5ebbc9,
0x3c093c49,0x3c46bc0e,0xbc13bc42,0xbc3f3c18,0x3b11bc71,0xbc74b9c2,0x3ac23c73,0x3c6ebb59,
0xbc3f3c18,0x3c3f3c18,0x3bd4bc5c,0xbc6ebb59,0x3c733a92,0xbb71bc6c,0xbc4d3c04,0x3c3f3c18,
0xbc26bc33,0xbbbd3c61,0x3c74b9c2,0xbbd4bc5c,0x3c74,0x3c53bbf4,0xbc53bbf4,0x3c74,
0x3c26bc33,0xbc72baf2,0x3be93c56,0x3bd4bc5c,0xbc733a92,0x3c2f3c2a,0x3a42bc73,0xbc3f3c18,
0x3c3f3c18,0xbb11bc71,0xbc093c49,0x3c6ebb59,0xbb11bc71,0xbbe93c56,0x3c5ebbc9,0xbc6ebb59,
0xbc093c49,0x3c5ebbc9,0xbc72baf2,0x3c3f3c18,0x3c6ebb59,0xbc6ebb59,0x3c3f3c18,0xbbd4bc5c,
0xbc53bbf4,0x3c1d3c3b,0xbba7bc65,0x3c74,0x3b903c69,0xba42bc73,0xbb413c6f,0x3bd4bc5c,
0x3bd4bc5c,0xbc093c49,0x3c26bc33,0xbc3f3c18,0xbc633bb2,0x3c67bb9b,0xbc6b3b84,0x3c6ebb59,
0x3c1d3c3b,0x3c37bc21,0xbc26bc33,0xbc2f3c2a,0xbba7bc65,0xbc593bde,0x3c093c49,0x3c37bc21,
0x3c74,0x3c6ebb59,0xbbd4bc5c,0xbc3f3c18,0x3ba7bc65,0xbc74b9c2,0x3b903c69,0x3c46bc0e,
0xbc1d3c3b,0x3c6b3b84,0xbb11bc71,0xbc4d3c04,0x3c53bbf4,0xbc53bbf4,0x3c74,0x3c53bbf4,
0xbc703b29,0x3c2f3c2a,0x3b11bc71,0xbc593bde,0x3c703b29,0xbbfebc50,0xbb903c69,0x3c5ebbc9,
0xbc53bbf4,0x3b903c69,0x3bd4bc5c,0xbc633bb2,0x3c1d3c3b,0xba42bc73,0xbc093c49,0x3c67bb9b,
0xbba7bc65,0xbb413c6f,0x3c26bc33,0xbc6b3b84,0x3c74,0x3bd4bc5c,0xbc3f3c18,0x3c6ebb59,
0x3ba7bc65,0xbc1d3c3b,0x3c53bbf4,0xbc703b29,0xbc1d3c3b,0x3c46bc0e,0xbc633bb2,0x3c72baf2,
0x3c53bbf4,0xbc633bb2,0x3c6ebb59,0xbc733a92,0xbc703b29,0x3c72baf2,0xbc733a92,0x3c74b9c2,
0x3bf4,0xba42bc73,0x3ac23c73,0xbb11bc71,0x3bf4,0xbb11bc71,0x3b903c69,0xbbd4bc5c,
0x3bf4,0xbb71bc6c,0x3be93c56,0xbc26bc33,0x3bf4,0xbba7bc65,0x3c1d3c3b,0xbc53bbf4,
0x3bf4,0xbbd4bc5c,0x3c3f3c18,0xbc6ebb59,0x3bf4,0xbbfebc50,0x3c593bde,0xbc733a92,
0x3bf4,0xbc13bc42,0x3c6b3b84,0xbc633bb2,0x3bf4,0xbc26bc33,0x3c733a92,0xbc3f3c18,
0x3bf4,0xbc37bc21,0x3c72baf2,0xbc093c49,0x3bf4,0xbc46bc0e,0x3c67bb9b,0xbb903c69,
0x3bf4,0xbc53bbf4,0x3c53bbf4,0x3c74,0x3bf4,0xbc5ebbc9,0x3c37bc21,0x3b903c69,
0x3bf4,0xbc67bb9b,0x3c13bc42,0x3c093c49,0x3bf4,0xbc6ebb59,0x3bd4bc5c,0x3c3f3c18,
0x3bf4,0xbc72baf2,0x3b71bc6c,0x3c633bb2,0x3bf4,0xbc74b9c2,0x3a42bc73,0x3c733a92,
0x3b413c6f,0xbb71bc6c,0x3b903c69,0xbba7bc65,0x3c093c49,0xbc26bc33,0x3c3f3c18,0xbc53bbf4,
0x3c4d3c04,0xbc67bb9b,0x3c733a92,0xbc703b29,0x3c703b29,0xbc703b29,0x3c53bbf4,0xbc1d3c3b,
0x3c6ebb59,0xbc3f3c18,0x3bd4bc5c,0x3c74,0x3c46bc0e,0xbbbd3c61,0xbb11bc71,0x3c1d3c3b,
0x3bfebc50,0x3ac23c73,0xbc26bc33,0x3c703b29,0x3b11bc71,0x3c093c49,0xbc6ebb59,0x3c53bbf4,
0xbb71bc6c,0x3c593bde,0xbc633bb2,0x3ba7bc65,0xbc13bc42,0x3c74b9c2,0xbc093c49,0xbba7bc65,
0xbc53bbf4,0x3c53bbf4,0x3c74,0xbc53bbf4,0xbc72baf2,0x3bfebc50,0x3c093c49,0xbc703b29,
0xbc6b3b84,0x3a42bc73,0x3c633bb2,0xbc1d3c3b,0xbc3f3c18,0xbbd4bc5c,0x3c6ebb59,0x3c74,
0xbbe93c56,0xbc46bc0e,0x3c26bc33,0x3c1d3c3b,0xbac23c73,0xbc72baf2,0x3b11bc71,0x3c703b29,
0x3bbd3c61,0xbbd4bc5c,0x3be93c56,0xbbfebc50,0x3c633bb2,0xbc6ebb59,0x3c733a92,0xbc733a92,
0x3c5ebbc9,0xbc3f3c18,0x3c13bc42,0xbbbd3c61,0x3ba7bc65,0x3c74,0xbba7bc65,0x3c1d3c3b,
0xbbd4bc5c,0x3c3f3c18,0xbc6ebb59,0x3c6ebb59,0xbc67bb9b,0x3c6ebb59,0xbc2f3c2a,0x3b71bc6c,
0xbc593bde,0x3bd4bc5c,0x3b413c6f,0xbc37bc21,0xbb903c69,0xbbd4bc5c,0x3c633bb2,0xbc633bb2,
0x3be93c56,0xbc6ebb59,0x3c46bc0e,0xbac23c73,0x3c6b3b84,0xbc3f3c18,0xba42bc73,0x3c4d3c04,
0x3c53bbf4,0x3c74,0xbc53bbf4,0x3c53bbf4,0x3b71bc6c,0x3c3f3c18,0xbc593bde,0xba42bc73,
0xbbfebc50,0x3c6ebb59,0xbac23c73,0xbc5ebbc9,0xbc6ebb59,0x3bd4bc5c,0x3c3f3c18,0xbc3f3c18,
0xbc4d3c04,0xbbd4bc5c,0x3c67bb9b,0x3b413c6f,0xbb413c6f,0xbc6ebb59,0x3b71bc6c,0x3c6b3b84,
0x3c093c49,0xbc13bc42,0x3c1d3c3b,0xbc26bc33,0x3c6ebb59,0xbc633bb2,0x3c53bbf4,0xbc3f3c18,
0x3b11bc71,0x3ac23c73,0xbba7bc65,0x3c093c49,0xbc53bbf4,0x3c703b29,0xbc703b29,0x3c53bbf4,
0xbc3f3c18,0x3bd4bc5c,0x3c74,0xbbd4bc5c,0x3b903c69,0xbc37bc21,0x3c703b29,0xbc633bb2,
0x3c733a92,0xbc4d3c04,0x3ba7bc65,0x3b903c69,0x3bd4bc5c,0x3b903c69,0xbc53bbf4,0x3c6ebb59,
0xbc26bc33,0x3c74b9c2,0xbc1d3c3b,0xbb11bc71,0xbc633bb2,0x3b71bc6c,0x3c1d3c3b,0xbc733a92,
0x3c74,0xbc53bbf4,0x3c53bbf4,0x3c74,0x3c633bb2,0xbc2f3c2a,0xbba7bc65,0x3c733a92,
0x3c26bc33,0x3be93c56,0xbc703b29,0x3b11bc71,0xbbd4bc5c,0x3c6ebb59,0x3c74,0xbc6ebb59,
0xbc733a92,0x3a42bc73,0x3c703b29,0xbb903c69,0xbb903c69,0xbc67bb9b,0x3ba7bc65,0x3c633bb2,
0x3c2f3c2a,0xbc37bc21,0x3c3f3c18,0xbc46bc0e,0x3c26bc33,0xbc093c49,0x3bd4bc5c,0xbb903c69,
0xbc37bc21,0x3c593bde,0xbc6ebb59,0x3c74b9c2,0xbc1d3c3b,0x3ba7bc65,0x3c74,0xbba7bc65,
0x3c3f3c18,0xbc6ebb59,0x3c6ebb59,0xbc3f3c18,0x3c13bc42,0xbac23c73,0xbbd4bc5c,0x3c4d3c04,
0xbc46bc0e,0x3c74b9c2,0xbc3f3c18,0x3b71bc6c,0xbc093c49,0xbb11bc71,0x3c3f3c18,0xbc733a92,
0x3c4d3c04,0xbc6b3b84,0x3bd4bc5c,0x3bbd3c61,0x3bfebc50,0x3bbd3c61,0xbc6ebb59,0x3c37bc21,
0xbc53bbf4,0x3c53bbf4,0x3c74,0xbc53bbf4,0xbbe93c56,0xbc13bc42,0x3c6ebb59,0xbb413c6f,
0x3c593bde,0xbc2f3c2a,0xbbd4bc5c,0x3c72baf2,0x3bd4bc5c,0x3c3f3c18,0xbc3f3c18,0xbbd4bc5c,
0xbc5ebbc9,0x3bfebc50,0x3c3f3c18,0xbc2f3c2a,0xbbbd3c61,0xbc5ebbc9,0x3bd4bc5c,0x3c593bde,
0x3c4d3c04,0xbc53bbf4,0x3c593bde,0xbc5ebbc9,0x3b11bc71,0x3c74,0xbb11bc71,0x3b903c69,
0xbc6b3b84,0x3c53bbf4,0xbc2f3c2a,0x3bfebc50,0x3c1d3c3b,0xbc53bbf4,0x3c703b29,0xbc703b29,
0x3bd4bc5c,0x3c74,0xbbd4bc5c,0x3c3f3c18,0xbc74b9c2,0x3c53bbf4,0xbbe93c56,0xba42bc73,
0x3bbd3c61,0xbc53bbf4,0x3c72baf2,0xbc2f3c2a,0x3c26bc33,0x3c74,0xbc26bc33,0x3c733a92,
0xbc67bb9b,0x3c53bbf4,0xbb413c6f,0xbc13bc42,0x3ac23c73,0xbc53bbf4,0x3c5ebbc9,0xbb413c6f,
0x3c53bbf4,0x3c74,0xbc53bbf4,0x3c53bbf4,0xbc46bc0e,0x3c53bbf4,0x3ac23c73,0xbc67bb9b,
0xbb413c6f,0xbc53bbf4,0x3c37bc21,0x3bbd3c61,0x3c6ebb59,0x3c74,0xbc6ebb59,0x3bd4bc5c,
0xbc13bc42,0x3c53bbf4,0x3bbd3c61,0xbc6b3b84,0xbbe93c56,0xbc53bbf4,0x3bfebc50,0x3c4d3c04,
0x3c633bb2,0xbc67bb9b,0x3c6b3b84,0xbc6ebb59,0xbbd4bc5c,0x3c093c49,0xbc26bc33,0x3c3f3c18,
0xbb903c69,0x3a42bc73,0x3b413c6f,0xbbd4bc5c,0x3c53bbf4,0xbc1d3c3b,0x3ba7bc65,0x3c74,
0xbc6ebb59,0x3c6ebb59,0xbc3f3c18,0x3bd4bc5c,0x3c093c49,0xbc5ebbc9,0x3c72baf2,0xbc3f3c18,
0x3b11bc71,0x3be93c56,0xbc5ebbc9,0x3c6ebb59,0xbc3f3c18,0x3b11bc71,0x3c093c49,0xbc6ebb59,
0x3c733a92,0xbc2f3c2a,0xba42bc73,0x3c3f3c18,0xbc26bc33,0x3c72baf2,0xbbe93c56,0xbbd4bc5c,
0x3c74,0xbc53bbf4,0x3c53bbf4,0x3c74,0x3c26bc33,0x3bbd3c61,0xbc74b9c2,0x3bd4bc5c,
0xbc733a92,0x3b71bc6c,0x3c4d3c04,0xbc3f3c18,0x3c3f3c18,0xbc3f3c18,0xbbd4bc5c,0x3c6ebb59,
0xbb11bc71,0x3c74b9c2,0xbac23c73,0xbc6ebb59,0xbc093c49,0xbc46bc0e,0x3c13bc42,0x3c3f3c18,
0x3c703b29,0xbc72baf2,0x3c733a92,0xbc74b9c2,0xbc53bbf4,0x3c633bb2,0xbc6ebb59,0x3c733a92,
0x3c1d3c3b,0xbc46bc0e,0x3c633bb2,0xbc72baf2,0xbba7bc65,0x3c1d3c3b,0xbc53bbf4,0x3c703b29,
0x3c74,0xbbd4bc5c,0x3c3f3c18,0xbc6ebb59,0x3ba7bc65,0x3b413c6f,0xbc26bc33,0x3c6b3b84,
0xbc1d3c3b,0x3a42bc73,0x3c093c49,0xbc67bb9b,0x3c53bbf4,0xbb903c69,0xbbd4bc5c,0x3c633bb2,
0xbc703b29,0x3bfebc50,0x3b903c69,0xbc5ebbc9,0x3c703b29,0xbc2f3c2a,0xbb11bc71,0x3c593bde,
0xbc53bbf4,0x3c53bbf4,0x3c74,0xbc53bbf4,0x3c1d3c3b,0xbc6b3b84,0x3b11bc71,0x3c4d3c04,
0xbba7bc65,0x3c74b9c2,0xbb903c69,0xbc46bc0e,0x3c74,0xbc6ebb59,0x3bd4bc5c,0x3c3f3c18,
0x3ba7bc65,0x3c593bde,0xbc093c49,0xbc37bc21,0xbc1d3c3b,0xbc37bc21,0x3c26bc33,0x3c2f3c2a,
0x3f803f80,0x3f803f80,0x3f803f80,0x3f803f80,0x3f7f3f80,0x3f753f7b,0x3f613f6c,0x3f443f54,
0x3f7b3f80,0x3f543f6c,0x3f0b3f33,0x3e323ebb,0x3f753f80,0x3f203f54,0x3d993ebb,0xbf00be64,
0x3f6c3f80,0x3ebb3f33,0xbed3bccc,0xbf71bf3c,0x3f613f80,0x3d993f0b,0xbf4cbed3,0xbf71bf7d,
0x3f543f80,0xbe643ebb,0xbf7dbf3c,0xbf00bf67,0x3f443f80,0xbf003e32,0xbf71bf71,0x3e32bf00,
0x3f333f80,0xbf3cbccc,0xbf29bf80,0x3f443d99,0x3f203f80,0xbf67be64,0xbe64bf67,0x3f803f20,
0x3f0b3f80,0xbf7dbed3,0x3e8bbf29,0x3f443f75,0x3eea3f80,0xbf7dbf15,0x3f33bea3,0x3e323f75,
0x3ebb3f80,0xbf67bf3c,0x3f753d99,0xbf003f20,0x3e8b3f80,0xbf3cbf5a,0x3f7b3eea,0xbf713d99,
0x3e323f80,0xbf00bf71,0x3f443f44,0xbf71bf00,0x3d993f80,0xbe64bf7d,0x3ebb3f75,0xbf00bf67,
0x3f803f80,0x3f803f80,0x3f803f80,0x3f803f80,0x3f203f33,0x3eea3f0b,0x3e8b3ebb,0x3d993e32,
0xbe64bccc,0xbf15bed3,0xbf5abf3c,0xbf7dbf71,0xbf67bf3c,0xbf7dbf7d,0xbf3cbf67,0xbe64bf00,
0xbf67bf80,0xbea3bf29,0x3eea3d99,0x3f753f44,0xbe64bf29,0x3f333e8b,0x3f7b3f75,0x3ebb3f44,
0x3f203d99,0x3f753f75,0x3d993f20,0xbf67bf00,0x3f803f44,0x3e323f44,0xbf71bf00,0xbf00bf71,
0x3f203f7f,0xbf4cbdff,0xbf15bf7d,0x3f543e32,0xbe643f20,0xbf67bf67,0x3f20be64,0x3f203f80,
0xbf67bdff,0xbcccbf5a,0x3f6c3f54,0xbf3c3e32,0xbf67bf4c,0x3f61bccc,0xbdff3f54,0xbf3cbf71,
0xbe64bf7d,0x3f543f54,0xbf7dbe64,0x3f20bf00,0x3f20bf15,0xbdff3f6c,0xbed3bf7d,0x3f543f44,
0x3f803e32,0xbf713e32,0x3f44bf00,0xbf003f44,0x3f203f54,0xbf3cbf3c,0x3f543f20,0xbf67bf00,
0x0,0x0,0x0,0x0,0xba8b0000,0xbabfb99b,0xba33b9d1,0x39dcbaf7,
0xb99b0000,0xbaf7b9d1,0xb9deba81,0xb93d38e0,0xbabf0000,0xbac6baf7,0x37c138e0,0xa580390e,
0xb9d10000,0x38e0ba81,0x3a58b7ee,0x3ae13aad,0xba330000,0x37c1b9de,0xb9873a58,0x3ae1ba10,
0xbaf70000,0x390e38e0,0xba103aad,0x25e03ab4,0x39dc0000,0xa580b93d,0x3ae13ae1,0xb93d25e0,
0xba810000,0x3aadb7ee,0xbac83aa3,0x39dc37c1,0xbac60000,0x3ab4390e,0x390e3ab4,0xbac6,
0xb9de0000,0xba103a58,0xba29bac8,0x39dcbabf,0xba570000,0xba10bae0,0xba81b905,0xb93dbabf,
0x38e00000,0x3ab43aad,0xbabf37c1,0xa580bac6,0xba290000,0x3aadbae3,0xb99bba57,0x3ae137c1,
0xb93d0000,0x25e03ae1,0x39dc39dc,0x3ae1a580,0x37c10000,0x390eba10,0x38e0babf,0x25e03ab4,
0x0,0x0,0x0,0x0,0xbac6ba81,0xba57b9de,0xba2938e0,0x37c1b93d,
0x390eb7ee,0xbae03a58,0xbae33aad,0xba103ae1,0x3ab43aad,0xba10ba10,0x3aad3ab4,0x390e25e0,
0x3ab43aa3,0xb905bac8,0xba5737c1,0xbabf39dc,0x390ebac8,0xba81ba29,0xb99bbabf,0x38e039dc,
0xbac637c1,0xbabfbabf,0x37c1bac6,0x3ab4a580,0x39dc,0xb93d39dc,0x3ae1a580,0x25e03ae1,
0xbac6ba8b,0xb9873930,0xbae0ba10,0xbaf7b93d,0x390ebac6,0x3ab43ab4,0xbac6390e,0xbac60000,
0x3ab43930,0xb7eebae3,0xb9d1baf7,0x3aadb93d,0x3ab4b987,0xba33b7ee,0x3930baf7,0x3aad3ae1,
0x390eba10,0xbaf7baf7,0xba10390e,0xbac625e0,0xbac6bae0,0x3930b9d1,0x3a58ba10,0xbaf739dc,
0xb93d,0x3ae1b93d,0x39dc25e0,0xa58039dc,0xbac6baf7,0x3aad3aad,0xbaf7bac6,0x3ab4a580,
0xbccc3f80,0x3d99bf80,0xbdff3f7f,0x3e32bf7d,0xbdff3f80,0x3ebbbf78,0xbf153f61,0x3f44bf3c,
0xbe643f80,0x3f20bf67,0xbf673f20,0x3f80be64,0xbea33f80,0x3f54bf4c,0xbf803e8b,0x3f443ebb,
0xbed33f80,0x3f75bf29,0xbf5abdff,0x3e323f54,0xbf003f80,0x3f80bf00,0xbf00bf00,0xbf003f80,
0xbf153f80,0x3f75bea3,0xbcccbf4c,0xbf713f54,0xbf293f80,0x3f54bdff,0x3eeabf78,0xbf713ebb,
0xbf3c3f80,0x3f203d99,0x3f54bf7d,0xbf00be64,0xbf4c3f80,0x3ebb3e8b,0x3f7fbf5a,0x3e32bf3c,
0xbf5a3f80,0x3d993eea,0x3f6cbf15,0x3f44bf7d,0xbf673f80,0xbe643f20,0x3f20be64,0x3f80bf67,
0xbf713f80,0xbf003f44,0x3e323e32,0x3f44bf00,0xbf783f80,0xbf3c3f61,0xbea33f0b,0x3e323d99,
0xbf7d3f80,0xbf673f75,0xbf3c3f54,0xbf003f20,0xbf803f80,0xbf7d3f7f,0xbf783f7b,0xbf713f75,
0xbe643f7b,0x3e8bbf78,0xbea33f75,0x3ebbbf71,0xbf673f0b,0x3f7bbea3,0xbf803d99,0x3f753e32,
0xbf67be64,0x3f203f20,0xbe64bf67,0xbe643f80,0xbe64bf5a,0xbed33f7f,0x3f61bf3c,0xbf7d3e32,
0x3f20bf78,0xbf803eea,0x3f333ebb,0x3d99bf71,0x3f80bf00,0xbf00bf00,0xbf003f80,0x3f80bf00,
0x3f203e8b,0x3f0bbf80,0xbf783ebb,0x3d993f44,0xbe643f61,0x3f7fbf15,0xbcccbf3c,0xbf7d3f44,
0xbf673f75,0x3ebb3ebb,0x3f75bf67,0xbe64bf00,0xbf673eea,0xbf293f7b,0x3f0b3d99,0x3f75bf71,
0xbe64bea3,0xbf783f33,0xbf293f75,0x3ebb3e32,0x3f20bf67,0xbe64be64,0xbf673f20,0xbf673f80,
0x3f80bf71,0x3f44bf71,0x3e32bf00,0xbf003e32,0x3f20bed3,0x3f6cbf4c,0x3f7fbf7d,0x3f54bf71,
0xbe643ebb,0x3d993d99,0x3ebbbe64,0x3f20bf00,0xbf673f6c,0xbf5a3f61,0xbf4c3f54,0xbf3c3f44,
0xb7ee0000,0x37c13aa3,0x3930ba8b,0xb93dba10,0x39300000,0x38e0b9ac,0xbae0ba33,0x39dc3aad,
0x390e0000,0xbac63ab4,0x3ab4bac6,0x390e,0xb9050000,0xbaf7b987,0x3aa3ba29,0x39dc38e0,
0x3a580000,0xbabfbac8,0xbae33930,0xb93dbaf7,0xa5800000,0x25e0,0x25e0a580,0xa5800000,
0xbae00000,0xbabfb905,0xb7eeb987,0x3ae1baf7,0xbac80000,0xbaf73930,0xba57b9ac,0x3ae138e0,
0x3aad0000,0xbac637c1,0xbaf7ba10,0x25e0390e,0xb9870000,0x38e0ba29,0xba8bbae3,0xb93d3aad,
0xbae30000,0x37c1ba57,0xb9d1bae0,0x39dcba10,0x3ab40000,0x390ebac6,0xbac6390e,0x3ab4,
0x3ae10000,0xa58039dc,0xb93db93d,0x39dc25e0,0xb9ac0000,0x3aadba33,0xb905b9de,0xb93d37c1,
0xba100000,0x3ab4babf,0x3aadbaf7,0xa580bac6,0x3aa30000,0xba10ba8b,0xb9acb99b,0x3ae1babf,
0x390eb99b,0xba29b9ac,0xb905babf,0x38e03ae1,0x3ab4b9de,0xb99bb905,0x3aa337c1,0xbabfb93d,
0x3ab4390e,0xbac6bac6,0x390e3ab4,0x390e0000,0x390ebae3,0x3a58ba8b,0xba333aad,0xba10b93d,
0xbac6b9ac,0x3aa3ba57,0xba8138e0,0x37c13ae1,0x25e0,0x25e0a580,0xa5800000,0x25e0,
0xbac6ba29,0xb9de3aa3,0xb9ac38e0,0x37c139dc,0x390eba33,0xba8bbae0,0xb7ee3aad,0xba1039dc,
0x3ab4babf,0x38e038e0,0xbabf3ab4,0x390ea580,0x3ab4ba57,0xbac8b99b,0xb9de37c1,0xbabf3ae1,
0x390eb905,0xb9acba81,0xbac8babf,0x38e0b93d,0xbac63ab4,0x390e390e,0x3ab4bac6,0x3ab40000,
0x3ae1,0x39dc3ae1,0xb93da580,0x25e0b93d,0xbac63a58,0xb9d1b987,0xba8bba10,0xbaf73ae1,
0x390e38e0,0x37c137c1,0x38e0390e,0xbac625e0,0x3ab4b9d1,0xbae3ba33,0xb987baf7,0x3aad39dc,
0x0,0x0,0x0,0x0,0x3dcc0000,0x3e973e4b,0x3ef53ec7,0x3f253f10,
0x3e4b0000,0x3f103ec7,0x3f573f37,0x3f7c3f6e,0x3e970000,0x3f483f10,0x3f7f3f6e,0x3f5e3f7a,
0x3ec70000,0x3f6e3f37,0x3f693f80,0x3eaf3f2e,0x3ef50000,0x3f7f3f57,0x3f1b3f69,0xbeaf3e19,
0x3f100000,0x3f7a3f6e,0x3e193f2e,0xbf5ebede,0x3f250000,0x3f5e3f7c,0xbeaf3eaf,0xbf7cbf5e,
0x3f370000,0x3f2e3f80,0xbf40bd4c,0xbf25bf7f,0x3f480000,0x3ede3f7a,0xbf7abede,0xbf48,
0x3f570000,0x3e193f69,0xbf76bf40,0x3f25be97,0x3f640000,0xbe193f50,0xbf37bf73,0x3f7c3e97,
0x3f6e0000,0xbede3f2e,0xbe97bf7f,0x3f5e3f48,0x3f760000,0xbf2e3f05,0x3e4bbf64,0x3eaf3f7f,
0x3f7c0000,0xbf5e3eaf,0x3f25bf25,0xbeaf3f5e,0x3f7f0000,0xbf7a3e19,0x3f6ebe97,0xbf5e3ede,
0x0,0x0,0x0,0x0,0x3f483f37,0x3f643f57,0x3f763f6e,0x3f7f3f7c,
0x3f7a3f80,0x3f503f69,0x3f053f2e,0x3e193eaf,0x3ede3f2e,0xbe193e19,0xbf2ebede,0xbf7abf5e,
0xbedebd4c,0xbf73bf40,0xbf64bf7f,0xbe97bf25,0xbf7abf40,0xbf37bf76,0x3e4bbe97,0x3f6e3f25,
0xbf48bf7f,0x3e97be97,0x3f7f3f48,0x3ede3f5e,0xbf25,0x3f7c3f25,0x3eaf3f5e,0xbf5ebeaf,
0x3f483dcc,0x3f1b3f7e,0xbf50be19,0xbf10bf7c,0x3f7a3f48,0xbede3ede,0xbf48bf7a,0x3f480000,
0x3ede3f7e,0xbf80bf05,0x3ec7bf10,0x3f2e3f7c,0xbede3f1b,0xbef5bf80,0x3f7e3f10,0xbf2e3eaf,
0xbf7abe19,0x3f10bf10,0x3e193f7a,0xbf48bf5e,0xbf48bf50,0x3f7e3ec7,0xbf693e19,0x3f10bf25,
0xbf7c,0x3eaf3f7c,0xbf25bf5e,0x3f5e3f25,0x3f48bf10,0xbf2e3f2e,0x3f10bf48,0xbede3f5e,
0x0,0x0,0x0,0x0,0xb82e0000,0xb92fb8c9,0xb989b979,0xbae53a57,
0xb8c90000,0x3a57b979,0x393f3a86,0x39e33a9b,0xb92f0000,0x3a183a57,0x3a913a9b,0xba98bad6,
0xb9790000,0x3a9b3a86,0x3ab1b9a3,0x396a39fe,0xb9890000,0x3a91393f,0xbada3ab1,0xb96ab9c3,
0x3a570000,0xbad63a9b,0xb9c339fe,0x3a98b998,0xbae50000,0xba9839e3,0xb96a396a,0xb9e33a98,
0x3a860000,0x39feb9a3,0x3965b82d,0x3ae5ba91,0x3a180000,0x3998bad6,0x3ad6b998,0xba18,
0x393f0000,0xb9c33ab1,0xbadd3965,0xbae5392f,0xba450000,0x39c3ba13,0xba863aa9,0x39e3b92f,
0x3a9b0000,0xb99839fe,0x392fba91,0xba983a18,0x3add0000,0xb9fe3afa,0xb8c93a45,0x396a3a91,
0x39e30000,0x3a98396a,0xbae53ae5,0xb96aba98,0x3a910000,0x3ad6b9c3,0x3a9b392f,0x3a983998,
0x0,0x0,0x0,0x0,0x3a183a86,0xba45393f,0x3add3a9b,0x3a9139e3,
0xbad6b9a3,0xba133ab1,0x3afa39fe,0xb9c3396a,0x399839fe,0x39c3b9c3,0xb9feb998,0x3ad63a98,
0xb998b82d,0x3aa93965,0x3a45ba91,0x392f3ae5,0x3ad63965,0xba86badd,0xb8c9392f,0x3a9bbae5,
0xba18ba91,0xb92f392f,0x3a913a18,0x3998ba98,0x3ae5,0x39e3bae5,0x396aba98,0x3a98b96a,
0x3a18b82e,0xbada3859,0x3a1339c3,0xba57b9e3,0xbad63a18,0xb9983998,0xba183ad6,0x3a180000,
0x39983859,0x39a3bafa,0xb979ba57,0x39fe39e3,0xb998bada,0x398939a3,0x38593a57,0xb9fe396a,
0x3ad639c3,0x3a57ba57,0xb9c3bad6,0xba183a98,0xba183a13,0x3859b979,0xbab1b9c3,0x3a573ae5,
0xb9e3,0x396a39e3,0x3ae53a98,0xba98bae5,0x3a18ba57,0xb9fe39fe,0x3a57ba18,0xb998ba98,
0x3f800000,0xbf7fbd4c,0x3f7e3dcc,0xbf7cbe19,0x3f7e0000,0xbf6ebe7d,0x3f503ef5,0xbf25bf2e,
0x3f7a0000,0xbf48bede,0x3ede3f48,0xbf7a,0x3f730000,0xbf10bf1b,0xbd4c3f76,0x3f25bf6e,
0x3f690000,0xbe97bf40,0xbf053f7e,0x3f7cbf10,0x3f5e0000,0xbf5e,0xbf5e3f5e,0x3f5e0000,
0x3f500000,0x3e97bf73,0xbf803f1b,0x3eaf3f10,0x3f400000,0x3f10bf7e,0xbf643e7d,0xbeaf3f6e,
0x3f2e0000,0x3f48bf7f,0xbf10be19,0xbf5e3f7a,0x3f1b0000,0x3f6ebf76,0xbdccbf05,0xbf7c3f2e,
0x3f050000,0x3f7fbf64,0x3ec7bf50,0xbf253e19,0x3ede0000,0x3f7abf48,0x3f48bf7a,0xbede,
0x3eaf0000,0x3f5ebf25,0x3f7cbf7c,0x3f25bf5e,0x3e7d0000,0x3f2ebef5,0x3f73bf57,0x3f7cbf7f,
0x3e190000,0x3edebe97,0x3f2ebf10,0x3f5ebf48,0x3d4c0000,0x3e19bdcc,0x3e7dbe4b,0x3eafbe97,
0x3f7a3e4b,0xbf76be7d,0x3f733e97,0xbf6ebeaf,0x3ede3f57,0xbe4bbf73,0xbd4c3f7f,0x3e97bf7c,
0xbede3f7a,0x3f48bf48,0xbf7a3ede,0x3f7a0000,0xbf7a3f05,0x3f693dcc,0xbef5bf2e,0xbe193f7c,
0xbf48be7d,0x3d4c3f64,0x3f37bf6e,0xbf7f3eaf,0xbf5e,0xbf5e3f5e,0x3f5e0000,0xbf5e,
0x3f48bf76,0xbf573d4c,0xbe7d3f6e,0x3f7fbf25,0x3f7abef5,0x3dccbf50,0xbf803f2e,0x3e193f25,
0x3ede3e97,0x3f6ebf6e,0xbe97bede,0xbf7a3f5e,0xbede3f64,0x3f40be4b,0x3f57bf7f,0xbe97beaf,
0xbf7a3f73,0xbe7d3f37,0x3f40be97,0x3f6ebf7c,0xbf483ede,0xbf7a3f7a,0xbede3f48,0x3ede0000,
0xbeaf,0xbf253eaf,0xbf7c3f5e,0xbf5e3f7c,0x3f48bf69,0x3ec7bf1b,0xbdccbe19,0xbf103eaf,
0x3f7abf6e,0x3f7fbf7f,0x3f6ebf7a,0x3f48bf5e,0x3edebec7,0x3f05bef5,0x3f1bbf10,0x3f2ebf25,
0xb9a30000,0xba91b82d,0x3859b82e,0xb9e339c3,0x38590000,0xba9b39a4,0xba13b989,0x3ae5b9fe,
0xbad60000,0xba18b998,0x39983a18,0x3ad6,0xbaa90000,0xba573ada,0xb82d3add,0xbae5ba9b,
0x3ab10000,0x392f3965,0xbafa3859,0x39e3ba57,0xba980000,0x3a98,0x3a98ba98,0xba980000,
0xba130000,0xb92f3aa9,0x39a3bada,0x396a3a57,0xb9650000,0x3a57b859,0x3a45b9a4,0xb96a3a9b,
0x39fe0000,0x3a18ba91,0xba5739c3,0x3a98bad6,0xbada0000,0x3a9bbadd,0x382ebafa,0xb9e339fe,
0x3afa0000,0x3a913a45,0xb9793a13,0x3ae5b9c3,0x39980000,0xbad6ba18,0x3a183ad6,0xb998,
0x396a0000,0xba983ae5,0x39e3b9e3,0xbae53a98,0xb9a40000,0x39fe3989,0xbaa9b93f,0x39e3ba91,
0xb9c30000,0x3998392f,0x39feba57,0xba98ba18,0x382d0000,0xb9c3382e,0xb9a438c9,0x396a392f,
0xbad6b8c9,0xbadd39a4,0xbaa9b92f,0xba9bb96a,0x3998393f,0x38c93aa9,0xb82d3a91,0xb92fb9e3,
0xb998bad6,0x3a18ba18,0x3ad63998,0xbad60000,0x3ad63afa,0x3ab1b82e,0x3989b9fe,0x39c339e3,
0xba1839a4,0x382dba45,0x3a86ba9b,0xba91396a,0x3a98,0x3a98ba98,0xba980000,0x3a98,
0x3a18badd,0xb93f382d,0x39a43a9b,0x3a913ae5,0xbad63989,0xb82e3a13,0x39a339fe,0xb9c3bae5,
0x3998b92f,0x3a9bba9b,0x392fb998,0x3ad6ba98,0xb998ba45,0xb96538c9,0x393fba91,0x392fb96a,
0x3ad6baa9,0x39a43a86,0xb965392f,0x3a9bb9e3,0xba183998,0x3ad6bad6,0xb9983a18,0x39980000,
0xb96a,0x3ae5396a,0xb9e3ba98,0x3a9839e3,0x3a18bab1,0xb9793ada,0x382e39c3,0xba57396a,
0xbad6ba9b,0x3a91ba91,0x3a9b3ad6,0x3a183a98,0x39983979,0x3afa3989,0xbadaba57,0x39fe3ae5,
};

typedef unsigned short u16;
typedef unsigned long long u64;
typedef __attribute__((ext_vector_type(8))) short bf16x8;
typedef __attribute__((ext_vector_type(4))) short s16x4;
typedef __attribute__((ext_vector_type(4))) float f32x4;
typedef __attribute__((ext_vector_type(2))) float f32x2;
typedef __attribute__((ext_vector_type(4))) unsigned u32x4;
typedef __attribute__((ext_vector_type(2))) __bf16 bf16x2_t;
typedef __attribute__((address_space(3))) s16x4* lds_s16x4_p;
typedef __attribute__((address_space(3))) void* lds_void_p;
#define DEVINL __device__ __forceinline__

#define STAMP(i)
#define STAMP_FLUSH()
#define STAMP2(a, b) do { if (s == 0) { STAMP(a); } else { STAMP(b); } } while (0)
typedef unsigned stv_t;

#define MFMA16(a, b, c) __builtin_amdgcn_mfma_f32_16x16x32_bf16((a), (b), (c), 0, 0, 0)
#define MFMAU(a, b, c) MFMA16(__builtin_bit_cast(bf16x8, a), __builtin_bit_cast(bf16x8, b), c)
#define SB0() __builtin_amdgcn_sched_barrier(0)
constexpr unsigned OOB = 0x7ffffff0u;
constexpr int SC1 = 16;

template <int N> struct IC { static constexpr int v = N; };
template <int... I, class F> DEVINL void static_for_impl(std::integer_sequence<int, I...>, F&& f) { (f(IC<I>{}), ...); }
template <int N, class F> DEVINL void static_for(F&& f) { static_for_impl(std::make_integer_sequence<int, N>{}, f); }
DEVINL void bar() { asm volatile("" ::: "memory"); __builtin_amdgcn_s_barrier(); asm volatile("" ::: "memory"); }
template <int N> DEVINL void wait_vm() { asm volatile("s_waitcnt vmcnt(%0)" ::"n"(N) : "memory"); }
DEVINL void wait_lds() { __builtin_amdgcn_s_waitcnt(0xc07f); }

constexpr int T0 = 16000, T2 = 4000;
constexpr int NT1 = 167;
constexpr int XST = 84 * 128;
constexpr size_t X1_TB = 16 * XST, X1_G = 2 * X1_TB, X1_S = 32 * X1_G;
constexpr size_t X2_TB = 8 * XST, X2_G = 2 * X2_TB, X2_S = 32 * X2_G;
constexpr size_t WF_BYTES = (size_t)32 * 524288;
constexpr size_t Y1_S = (size_t)84 * 65536, Y2_S = (size_t)42 * 65536;
constexpr size_t OFF_FLAG = 0;
constexpr int F_X = 0, F_W1 = 768, F_W2 = 1024, F_G1 = 1280, F_M = 1792, F_G2 = 2304, F_S = 2816, F_EPOCH = 2840, F_H = 512, F_END = 2848;
constexpr size_t OFF_WFT = OFF_FLAG + F_END * 128;
constexpr size_t OFF_WOT = OFF_WFT + 128 * 1024;
constexpr size_t OFF_GE = OFF_WOT + 128 * 1024;
constexpr size_t OFF_X1F = OFF_GE + 32 * 1024;
constexpr size_t OFF_W1F = OFF_X1F + 2 * X1_S;
constexpr size_t OFF_Y1F = OFF_W1F + WF_BYTES;
constexpr size_t OFF_X2F = OFF_Y1F + 2 * Y1_S;
constexpr size_t OFF_W2F = OFF_X2F + 2 * X2_S;
constexpr size_t OFF_Y2F = OFF_W2F + WF_BYTES;
constexpr size_t WS_USED = OFF_Y2F + 2 * Y2_S;
constexpr int TAB_F = 0, TAB_G = 8, TAB_T = 14;
constexpr int LDS_BYTES = 81920;

DEVINL unsigned pack_bf16(float a, float b) {
  f32x2 v = {a, b};
  bf16x2_t r = __builtin_convertvector(v, bf16x2_t);
  return __builtin_bit_cast(unsigned, r);
}
DEVINL u32x4 pack8(const f32x4& a, const f32x4& b) {
  return (u32x4){pack_bf16(a[0], a[1]), pack_bf16(a[2], a[3]), pack_bf16(b[0], b[1]), pack_bf16(b[2], b[3])};
}
DEVINL s16x4 tr_read(const char* p) { return __builtin_amdgcn_ds_read_tr16_b64_v4i16((lds_s16x4_p)(p)); }
DEVINL bf16x8 cat8(s16x4 a, s16x4 b) { return __builtin_shufflevector(a, b, 0, 1, 2, 3, 4, 5, 6, 7); }
DEVINL bf16x8 tr_frag(const char* p, int second) { return cat8(tr_read(p), tr_read(p + second)); }
template <int AUX> DEVINL void dma16(__amdgpu_buffer_rsrc_t rs, char* lds_dst, unsigned voff, unsigned soff) {
  const unsigned m = (unsigned)(size_t)(lds_void_p)lds_dst;
  if constexpr (AUX == 0) asm volatile("s_mov_b32 m0, %0\n\ts_nop 0\n\tbuffer_load_dwordx4 %1, %2, %3 offen lds" :: "s"(m), "v"(voff), "s"(rs), "s"(soff) : "memory");
  else asm volatile("s_mov_b32 m0, %0\n\ts_nop 0\n\tbuffer_load_dwordx4 %1, %2, %3 offen sc1 lds" :: "s"(m), "v"(voff), "s"(rs), "s"(soff) : "memory");
}
DEVINL __amdgpu_buffer_rsrc_t mkrsrc(const void* p, unsigned bytes) { return __builtin_amdgcn_make_buffer_rsrc((void*)p, 0, bytes, 0x00020000); }
DEVINL void st16(void* p, u32x4 v) { asm volatile("global_store_dwordx4 %0, %1, off sc1\n\ts_nop 1" ::"v"(p), "v"(v) : "memory"); }
template <int I, bool DEV> DEVINL void ldfrag(u32x4& d, const char* base, unsigned lane16) {
  const char* b = base + (I >> 3) * 8192 + 4096;
  if constexpr (DEV) asm volatile("global_load_dwordx4 %0, %1, %2 offset:%3 sc1" : "=v"(d) : "v"(lane16), "s"(b), "n"((I & 7) * 1024 - 4096) : "memory");
  else asm volatile("global_load_dwordx4 %0, %1, %2 offset:%3" : "=v"(d) : "v"(lane16), "s"(b), "n"((I & 7) * 1024 - 4096) : "memory");
}
template <int I> DEVINL void ldfrag_rot(u32x4& d, const char* base, int rot, unsigned lane16) {
  ldfrag<I & 7, true>(d, base + (((I >> 3) + rot) & 3) * 8192, lane16);
}
DEVINL void tie(u32x4& a) { asm volatile("" : "+v"(a)); }
template <int N> DEVINL void tie(u32x4 (&a)[N]) {
#pragma unroll
  for (int i = 0; i < N; ++i) tie(a[i]);
}
DEVINL int lane_id() { int l; asm volatile("v_mbcnt_lo_u32_b32 %0, -1, 0\n\tv_mbcnt_hi_u32_b32 %0, -1, %0" : "=v"(l)); return l; }
DEVINL void sgpr_settle() { asm volatile("s_nop 4" ::: "memory"); }

DEVINL void set_flag(char* ws, int flag, u64 tag, int w) {
  u64 d;
  unsigned o;
  asm volatile("s_cmp_lg_u32 %2, 0\n\ts_cbranch_scc1 1f\n\ts_mov_b64 exec, 1\n\ts_nop 1\n\tv_mov_b32 %1, %3\n\tv_mov_b64 %0, %5\n\t"
               "global_store_dwordx2 %1, %0, %4 sc1\n\ts_nop 1\n\ts_mov_b64 exec, -1\n1:"
               : "=&v"(d), "=&v"(o) : "s"(w), "s"(flag * 128), "s"(ws + OFF_FLAG), "s"(tag) : "scc", "memory");
}
DEVINL void publish(char* ws, int flag, u64 tag, int w) { wait_vm<0>(); bar(); set_flag(ws, flag, tag, w); }
#define POLL_MODE 2
#define POLL_SLEEP 1
DEVINL void poll(const char* ws, int flag, u64 tag) {
  const unsigned off = flag * 128;
  u64 v;
  unsigned cnt;
  asm volatile("s_nop 4\n\ts_mov_b32 %1, 40000\n"
               "1:\n\tglobal_load_dwordx2 %0, %2, %3 sc1\n\ts_waitcnt vmcnt(0)\n\tv_cmp_ne_u64 vcc, %4, %0\n\ts_nop 1\n\ts_cbranch_vccz 2f\n\t"
               "s_cmp_eq_u64 vcc, -1\n\ts_cbranch_scc1 3f\n\t"
               "s_mov_b64 exec, vcc\n\ts_sub_u32 %1, %1, 1\n\ts_cbranch_scc1 2f\n\ts_sleep %5\n\ts_branch 1b\n"
               "3:\n\ts_mov_b64 exec, 1\n"
               "4:\n\ts_sleep %5\n\tglobal_load_dwordx2 %0, %2, %3 sc1\n\ts_waitcnt vmcnt(0)\n\tv_cmp_ne_u64 vcc, %4, %0\n\ts_sub_u32 %1, %1, 1\n\ts_cbranch_scc1 2f\n\ts_cbranch_vccnz 4b\n\t"
               "s_mov_b64 exec, -2\n\ts_branch 1b\n"
               "2:\n\ts_mov_b64 exec, -1"
               : "=&v"(v), "=&s"(cnt) : "v"(off), "s"(ws + OFF_FLAG), "s"(tag), "n"(POLL_SLEEP) : "vcc", "scc", "memory");
}

DEVINL void flag_peek(u64& v, const char* ws, unsigned off) {
  asm volatile("global_load_dwordx2 %0, %1, %2 sc1" : "=v"(v) : "v"(off), "s"(ws + OFF_FLAG) : "memory");
}
DEVINL void flag_check(u64& v, const char* ws, unsigned off, u64 tag) {
  unsigned cnt;
  asm volatile("s_mov_b32 %1, 40000\n"
               "1:\n\tv_cmp_ne_u64 vcc, %4, %0\n\ts_nop 1\n\ts_cbranch_vccz 2f\n\t"
               "s_mov_b64 exec, vcc\n\ts_sub_u32 %1, %1, 1\n\ts_cbranch_scc1 2f\n\tglobal_load_dwordx2 %0, %2, %3 sc1\n\ts_waitcnt vmcnt(0)\n\ts_branch 1b\n"
               "2:\n\ts_mov_b64 exec, -1"
               : "+v"(v), "=&s"(cnt) : "v"(off), "s"(ws + OFF_FLAG), "s"(tag) : "vcc", "scc", "memory");
}

constexpr float GC = 0.468668693f, GK = 4.91222954f;
DEVINL u32x4 gelu_pack8(const f32x4& a, const f32x4& b) {
  float y[8] = {a[0], a[1], a[2], a[3], b[0], b[1], b[2], b[3]}, t[8];
#pragma unroll
  for (int i = 0; i < 8; ++i) { t[i] = __builtin_fmaf(y[i], y[i], GK); asm("" : "+v"(t[i])); }
  SB0();
#pragma unroll
  for (int i = 0; i < 8; ++i) { t[i] *= y[i]; asm("" : "+v"(t[i])); }
  SB0();
#pragma unroll
  for (int i = 0; i < 8; ++i) t[i] = __builtin_amdgcn_exp2f(-t[i]);
  SB0();
#pragma unroll
  for (int i = 0; i < 8; ++i) { t[i] = __builtin_fmaf(t[i], GC, GC); asm("" : "+v"(t[i])); }
  SB0();
#pragma unroll
  for (int i = 0; i < 8; ++i) t[i] = __builtin_amdgcn_rcpf(t[i]);
  SB0();
#pragma unroll
  for (int i = 0; i < 8; ++i) { t[i] *= y[i]; asm("" : "+v"(t[i])); }
  SB0();
  return (u32x4){pack_bf16(t[0], t[1]), pack_bf16(t[2], t[3]), pack_bf16(t[4], t[5]), pack_bf16(t[6], t[7])};
}

template <int RS, size_t XG>
DEVINL void fwd_unit(const char* la, const u32x4 (&F)[8], char* gd) {
  bf16x8 A[2][2];
#pragma unroll
  for (int i = 0; i < 2; ++i)
#pragma unroll
    for (int ks = 0; ks < 2; ++ks) A[i][ks] = tr_frag(la + 8 * i + 32 * RS * ks, 4 * RS);
#pragma unroll
  for (int nt = 0; nt < 4; ++nt) {
    f32x4 c0 = {0, 0, 0, 0}, c1 = {0, 0, 0, 0};
#pragma unroll
    for (int ks = 0; ks < 2; ++ks) { c0 = MFMAU(A[0][ks], F[2 * nt + ks], c0); c1 = MFMAU(A[1][ks], F[2 * nt + ks], c1); }
    st16(gd + nt * 8 * XG, pack8(c0, c1));
  }
}
template <size_t XG> DEVINL unsigned x_lane_off(int lane, int tl) {
  return (unsigned)(((lane & 15) >> 1) * XG + tl * 128 + ((((lane & 1) * 4 + (lane >> 4)) ^ ((tl >> 1) & 7)) * 16));
}

DEVINL void w_issue(const u16* __restrict__ wsrc, char* reg, int q, int w, int lane, bool live) {
  const int co = q >> 2, cb = q & 3, par = co >> 5, c0 = (co & 31) * 8;
  __amdgpu_buffer_rsrc_t rs = mkrsrc(wsrc, 31 * 65536 * 2);
#pragma unroll
  for (int i = 0; i < 4; ++i) {
    const int cl = 2 * w + (i >> 1), tap = 8 * (i & 1) + (lane >> 3);
    const unsigned off = ((2 * tap + par) * 256 + c0 + cl) * 512 + cb * 128 + (((lane & 7) ^ (2 * (((tap >> 1) & 1) + 2 * (i & 1)))) * 16);
    dma16<0>(rs, reg + (w * 4 + i) * 1024, live ? off : OOB, 0);
  }
}
template <int PACE>
DEVINL void w_compute(const u32x4 (&Tm)[4], char* __restrict__ wf, const char* reg, int q, int w, int lane) {
  const int co = q >> 2, cb = q & 3;
  const int G = lane >> 4, rq = (lane & 15) >> 2, p = lane & 3;
  char* dst = wf + (size_t)((cb * 4 + w) * 32 + (co >> 2) * 2) * 1024 + (co & 3) * 256 + (unsigned)((lane & 15) * 16 + 4 * G * 524288);
  const char* la = reg + (8 * (G & 1) + rq) * 128 + (((2 * w + (p >> 1)) ^ (2 * ((rq >> 1) + 2 * (G & 1)))) * 16) + 8 * (p & 1);
  bf16x8 B[8];
#pragma unroll
  for (int cl = 0; cl < 8; ++cl) B[cl] = tr_frag(la + cl * 2048, 4 * 128);
#pragma unroll
  for (int mt = 0; mt < 4; ++mt) {
    f32x4 d[8];
#pragma unroll
    for (int cl = 0; cl < 8; ++cl) d[cl] = MFMAU(Tm[mt], B[cl], ((f32x4){0, 0, 0, 0}));
#pragma unroll
    for (int r = 0; r < 4; ++r) {
      u32x4 o = {pack_bf16(d[0][r], d[1][r]), pack_bf16(d[2][r], d[3][r]), pack_bf16(d[4][r], d[5][r]), pack_bf16(d[6][r], d[7][r])};
      if constexpr (PACE < 16) wait_vm<PACE>();
      st16(dst + (size_t)(16 * (mt & 1) + r) * 524288 + (mt >> 1) * 1024, o);
    }
  }
}

constexpr float LOG2E2 = 2.0f * 1.4426950408889634f;
DEVINL void s_transpose(const u16* __restrict__ src, char* __restrict__ dst, char* lds, int ks, int half, int lane) {
  __amdgpu_buffer_rsrc_t rs = mkrsrc(src, 131072);
#pragma unroll
  for (int i = 0; i < 8; ++i) {
    const int row = 4 * i + (lane >> 4);
    dma16<0>(rs, lds + i * 1024, (ks * 32 + row) * 512 + half * 256 + (((lane & 15) ^ (4 * (row & 3))) * 16), 0);
  }
  wait_vm<0>();
  const int G = lane >> 4, rq = (lane & 15) >> 2, p = lane & 3;
#pragma unroll
  for (int ml = 0; ml < 8; ++ml) {
    const char* la = lds + (8 * G + rq) * 256 + (((4 * (ml >> 1) + p) ^ (4 * (rq & 3))) * 16) + 8 * (ml & 1);
    st16(dst + ((size_t)((8 * half + ml) * 8 + ks) * 64 + lane) * 16, __builtin_bit_cast(u32x4, tr_frag(la, 4 * 256)));
  }
}
DEVINL void s_ge(const u16* __restrict__ g, const u16* __restrict__ wg, float* __restrict__ ge, char* lds, int eb, int lane) {
  __amdgpu_buffer_rsrc_t rs = mkrsrc(wg, 131072);
#pragma unroll
  for (int i = 0; i < 8; ++i) dma16<0>(rs, lds + i * 1024, (i * 32 + (lane >> 1)) * 512 + eb * 32 + (lane & 1) * 16, 0);
  bf16x8 gb[8];
#pragma unroll
  for (int ks = 0; ks < 8; ++ks) gb[ks] = *(const bf16x8*)(g + (lane & 15) * 256 + ks * 32 + (lane >> 4) * 8);
  wait_vm<0>();
  const int G = lane >> 4, rq = (lane & 15) >> 2, p = lane & 3;
  f32x4 acc = {0, 0, 0, 0};
#pragma unroll
  for (int ks = 0; ks < 8; ++ks) acc = MFMA16(tr_frag(lds + (ks * 32 + 8 * G + rq) * 32 + 8 * p, 4 * 32), gb[ks], acc);
  acc *= LOG2E2;
  f32x4 ex;
#pragma unroll
  for (int r = 0; r < 4; ++r) ex[r] = __builtin_amdgcn_exp2f(acc[r] > 64.f ? 64.f : (acc[r] < -64.f ? -64.f : acc[r]));
  const int b = (lane >> 3) & 1, u = lane & 7;
  float* d = ge + ((b * 4 + (eb >> 2)) * 8 + u) * 64 + (eb & 3) * 16 + 4 * G;
  st16(d, __builtin_bit_cast(u32x4, acc));
  st16(d + 4096, __builtin_bit_cast(u32x4, ex));
}

#define G1_K 13
#define G1_P 19
#define G1_K2 23
#define GORD 0
#define GZ 0
#define GX 0
#define SPREAD 1
#define G1_WR 4
constexpr int RING = 7, SLOT = 11264, RING_DUMMY = RING * SLOT;
template <int PH> struct GPlan {
  static constexpr int NST = PH == 1 ? 32 : 8;
  int first[NST + 1];
  int wfirst[NST + 1];
  int before[NST];
  int pos[NST];
  int posF, posF2, posY;
  constexpr GPlan() : first{}, wfirst{}, before{}, pos{}, posF(0), posF2(0), posY(0) {
    int n = 0, u = 0, k = 0;
    for (; u < RING; ++u) pos[u] = (n += 3);
    for (int t = 0; t < NST; ++t) {
      before[t] = n;
      first[t] = u;
      wfirst[t] = k;
      if (PH == 1 && t == G1_K - 2) posF = ++n;
      if (PH == 1 && t == G1_K2 - 2) posF2 = ++n;
      if (PH == 1 && t == 16) n += 6;
      for (; t >= 1 && u <= t + RING - 1 && u < NST && (PH != 1 || u < 16 || t >= G1_K); ++u) pos[u] = (n += 3);
      if (PH == 1 && t == 16) posY = n;
      for (int i = 0; i < G1_WR && PH == 1 && t >= G1_K2 && k < 2 * (t - 16); ++i) { ++k; ++n; }
      for (; PH == 3 && k < 4 * t; ++k) ++n;
    }
    first[NST] = u;
    wfirst[NST] = k;
  }
  constexpr int nreq(int t) const { return 3 * (first[t + 1] - first[t]) + wfirst[t + 1] - wfirst[t]; }
  constexpr int slot(int t, int j) const {
    const int n = nreq(t);
    if (!SPREAD) return -1;
    if (n <= 6) return 4 * (j * 6 / n) + 1;
    if (n <= 12) return 2 * (j * 12 / n);
    return j * 24 / n;
  }
  constexpr int wait(int t) const {
    int need = pos[t + 1];
    if (PH == 1 && t == G1_K && posF > need) need = posF;
    if (PH == 1 && t == G1_K2 && posF2 > need) need = posF2;
    if (PH == 1 && t == G1_P && posY > need) need = posY;
    return before[t] - need;
  }
};
template <int PH>
DEVINL void g_phase(char* lds, char* ws, const char* xsrc, const char* wsrc, u32x4 (&W)[32], char* ydst, unsigned yoff,
                    int fbase, int fbase2, int yflag, u64 tag, int rot, int w, stv_t& stv) {
  const int lane = lane_id();
  constexpr GPlan<PH> PL;
  constexpr int NST = PL.NST, NACC = PH == 1 ? 6 : 3, ROWS = PH == 1 ? 84 : 42;
  __amdgpu_buffer_rsrc_t xrs = mkrsrc(xsrc, PH == 1 ? X1_S + 16 * XST : 8 * XST);
  __amdgpu_buffer_rsrc_t yrs = mkrsrc(ydst, PH == 1 ? Y1_S + 42 * 65536 : 21 * 65536);
  unsigned xo[3];
  int xd[3];
#pragma unroll
  for (int i = 0; i < 3; ++i) {
    const int piece = i * 4 + w, row = piece * 8 + (lane >> 3);
    xo[i] = row < 84 ? piece * 1024 + lane * 16 : OOB;
    xd[i] = piece * 1024;
  }
  const unsigned lane16 = lane * 16;
  const bool dummy = (w == 3);
  unsigned xq[4];
#pragma unroll
  for (int i = 0; i < 4; ++i) xq[i] = ((i + rot) & 3) * ((PH == 1 ? 4 : 2) * XST);
  auto piece = [&](auto jc) {
    constexpr int u = decltype(jc)::v / 3, i = decltype(jc)::v % 3, slot = u % RING;
    constexpr int Q4 = PH == 1 ? 4 : 2;
    const unsigned so = xq[(u / Q4) & 3] + (u >= 16 ? X1_S : 0) + (u % Q4) * XST;
    dma16<SC1>(xrs, lds + (i == 2 && dummy ? RING_DUMMY : slot * SLOT + xd[i]), xo[i], so);
  };
  sgpr_settle();
  static_for<3 * RING>(piece);
  const int xa = (lane & 15) * 128 + (((lane >> 4) ^ ((lane & 15) >> 1)) * 16);
  int xb[4] = {xa, xa ^ 64, xa + 4 * SLOT, (xa ^ 64) + 4 * SLOT};
  asm volatile("" : "+v"(xb[2]), "+v"(xb[3]));
  const int za = (lane & 8) ? (xa & 255) + 512 + ((lane & 2) << 7) : xa;
  int xz[4] = {za, za ^ 64, za + 4 * SLOT, (za ^ 64) + 4 * SLOT};
  if (GZ) asm volatile("" : "+v"(xz[2]), "+v"(xz[3]));
  u32x4 xc[6], xs[6];
  auto ldx = [&](int pi, int slot) {
    const int ro = (PH == 1 ? pi * 16 : (pi / 3) * 42 + (pi % 3) * 16) * 128 + (slot & 3) * SLOT;
    if (GZ && PH == 1 && pi == 5) {
      xc[pi] = *(const u32x4*)(lds + xz[(slot >> 2) * 2] + ro);
      xs[pi] = *(const u32x4*)(lds + xz[(slot >> 2) * 2 + 1] + ro);
    } else {
      xc[pi] = *(const u32x4*)(lds + xb[(slot >> 2) * 2] + ro);
      xs[pi] = *(const u32x4*)(lds + xb[(slot >> 2) * 2 + 1] + ro);
    }
  };
  f32x4 R[NACC], Q[NACC];
  auto sty = [&](int n, unsigned base) {
    const int l = lane_id();
    const int tl = n * 16 + (l & 15);
    const unsigned off = (tl >> 1) * 65536 + base + (tl & 1) * 64 + (l >> 4) * 16;
    __builtin_amdgcn_raw_buffer_store_b128(pack8(R[n], Q[n]), yrs, tl < ROWS ? off : OOB, 0, SC1);
  };
  wait_vm<PL.before[0] - PL.pos[0]>();
  tie(W);
  bar();
  STAMP(PH == 1 ? 5 : (PH == 2 ? 14 : 18));
#pragma unroll
  for (int pi = 0; pi < 6; ++pi) ldx(pi, 0);
  u64 fv;
  unsigned foff;
  static_for<NST>([&](auto ic) {
    constexpr int st = decltype(ic)::v;
    constexpr bool MORE = st + 1 < NST;
    if constexpr (MORE) {
      wait_vm<PL.wait(st)>();
      if constexpr (PH == 1 && st == G1_K) { STAMP(8); flag_check(fv, ws, foff, tag); }
      if constexpr (PH == 1 && st == G1_K2) { STAMP(21); flag_check(fv, ws, foff, tag); STAMP(22); }
      if constexpr (!(GX & 1) && !((GX & 8) && (st & 1))) bar();
      if constexpr (PH == 1 && st == G1_K) STAMP(9);
      if constexpr (PH == 1 && st == 16) STAMP(6);
      if constexpr (PH == 1 && st == G1_P) { set_flag(ws, yflag, tag, w); STAMP(7); }
      SB0();
      if constexpr (PH == 1 && st == G1_K - 2) { foff = (fbase + (lane_id() & 31)) * 128; flag_peek(fv, ws, foff); }
      if constexpr (PH == 1 && st == G1_K2 - 2) { foff = (fbase2 + lane_id()) * 128; flag_peek(fv, ws, foff); }
    }
    auto req = [&](auto mc) {
      static_for<PL.nreq(st)>([&](auto jc) {
        constexpr int j = decltype(jc)::v, nx = 3 * (PL.first[st + 1] - PL.first[st]);
        if constexpr (PL.slot(st, j) == decltype(mc)::v && !(GX & 2)) {
          SB0();
          if constexpr (j < nx) piece(IC<3 * PL.first[st] + j>{});
          else ldfrag_rot<PL.wfirst[st] + j - nx>(W[PL.wfirst[st] + j - nx], wsrc, PH == 1 ? rot : 0, lane16);
          SB0();
        }
      });
    };
    req(IC<-1>{});
    u32x4 nw;
    constexpr f32x4 Z = {0, 0, 0, 0};
#define GM(a, b, c) MFMAU(a, b, c)
    if constexpr (GORD == 1 && PH == 1) {
      constexpr int ks = st & 15;
      static_for<24>([&](auto mc) {
        constexpr int m = decltype(mc)::v, n = m < 12 ? m >> 1 : (m - 12) % 6;
        if constexpr (st == 16 && m < 12 && !(m & 1)) sty(n, yoff);
        if constexpr (m < 12 && !(m & 1)) R[n] = GM(W[2 * ks], xc[n], ks == 0 ? Z : R[n]);
        else if constexpr (m < 12) Q[n] = GM(W[2 * ks], xs[n], ks == 0 ? Z : Q[n]);
        else if constexpr (m < 18) R[n] = GM(W[2 * ks + 1], xs[n], R[n]);
        else Q[n] = GM(nw, xc[n], Q[n]);
        if constexpr (m == 0) { tie(W[2 * ks + 1]); nw = W[2 * ks + 1] ^ 0x80008000u; }
        req(mc);
        if constexpr (MORE && m >= 12) {
          constexpr int slot = (st + 1) % RING, ro = n * 2048 + (slot & 3) * SLOT;
          if constexpr (m < 18) xs[n] = *(const u32x4*)(lds + xb[(slot >> 2) * 2 + 1] + ro);
          else xc[n] = *(const u32x4*)(lds + xb[(slot >> 2) * 2] + ro);
        }
        SB0();
      });
    } else
    static_for<6>([&](auto pc) {
      constexpr int pi = decltype(pc)::v, ks = PH == 1 ? st & 15 : 2 * st + pi / 3, n = PH == 1 ? pi : pi % 3;
      if constexpr (PH == 1 && st == 16) sty(n, yoff);
      if constexpr (GORD == 2) {
        if constexpr (n == 0) { tie(W[2 * ks + 1]); }
        R[n] = GM(W[2 * ks + 1], xs[pi], ks == 0 ? Z : R[n]);
        if constexpr (n == 0) { nw = W[2 * ks + 1] ^ 0x80008000u; }
        req(IC<4 * pi>{});
        Q[n] = GM(W[2 * ks], xs[pi], ks == 0 ? Z : Q[n]);
        req(IC<4 * pi + 1>{});
        R[n] = GM(W[2 * ks], xc[pi], R[n]);
        req(IC<4 * pi + 2>{});
        Q[n] = GM(nw, xc[pi], Q[n]);
        req(IC<4 * pi + 3>{});
      } else {
      R[n] = GM(W[2 * ks], xc[pi], ks == 0 ? Z : R[n]);
      if constexpr (n == 0) { tie(W[2 * ks + 1]); nw = W[2 * ks + 1] ^ 0x80008000u; }
      req(IC<4 * pi>{});
      Q[n] = GM(W[2 * ks], xs[pi], ks == 0 ? Z : Q[n]);
      req(IC<4 * pi + 1>{});
      R[n] = GM(W[2 * ks + 1], xs[pi], R[n]);
      req(IC<4 * pi + 2>{});
      Q[n] = GM(nw, xc[pi], Q[n]);
      req(IC<4 * pi + 3>{});
      }
      if constexpr (MORE && !(GX & 4)) ldx(pi, (st + 1) % RING);
      SB0();
    });
  });
  STAMP(PH == 1 ? 10 : (PH == 2 ? 15 : 19));
#pragma unroll
  for (int n = 0; n < NACC; ++n) sty(n, PH == 1 ? yoff + Y1_S : yoff);
  if constexpr (PH != 2) static_for<32 - PL.wfirst[NST]>([&](auto jc) { constexpr int i = PL.wfirst[NST] + decltype(jc)::v; ldfrag_rot<i>(W[i], wsrc, PH == 1 ? rot : 0, lane16); });
}

DEVINL float jval(float r) {
  float t = __builtin_fmaf(-2.0f, r, 1.0f);
  asm("" : "+v"(t));
  return t;
}
template <bool FAST> DEVINL float jden(float f, float g) {
  float t = FAST ? __builtin_fmaf(f, g, 1.0f) : 1.0f + __builtin_amdgcn_exp2f(f + g);
  asm("" : "+v"(t));
  return t;
}
#define OUT_POL "sc1"
DEVINL void st_out(void* p, u32x4 v) { asm volatile("global_store_dwordx4 %0, %1, off " OUT_POL "\n\ts_nop 1" ::"v"(p), "v"(v) : "memory"); }
DEVINL void out_rows(char* orow, const u32x4& P0, const u32x4& P1) {
  u32x4 s1, s2;
#pragma unroll
  for (int i = 0; i < 4; ++i) {
    s1[i] = __builtin_amdgcn_update_dpp(P0[i], P1[i], 0x128, 0xf, 0xc, false);
    s2[i] = __builtin_amdgcn_update_dpp(P1[i], P0[i], 0x128, 0xf, 0x3, false);
  }
  st_out(orow, s1);
  st_out(orow + 8 * 4096, s2);
}
constexpr int J_Y = 32768, J_F = 65536, J_G = J_F + 8192;
constexpr int J_AH = 2;
template <bool FAST> DEVINL void j_make(const f32x4 (&fe)[4], char* lds, int u, int w, int lane) {
  const float* gl = (const float*)(lds + J_G + w * 2048) + 8 * (lane >> 4) + u * 64;
#pragma unroll
  for (int h = 0; h < 2; ++h) {
    const f32x4 g0 = *(const f32x4*)(gl + h * 32), g1 = *(const f32x4*)(gl + h * 32 + 4);
    f32x4 t0, t1;
#pragma unroll
    for (int r = 0; r < 4; ++r) {
      t0[r] = jval(__builtin_amdgcn_rcpf(jden<FAST>(fe[2 * h][r], g0[r])));
      t1[r] = jval(__builtin_amdgcn_rcpf(jden<FAST>(fe[2 * h + 1][r], g1[r])));
    }
    *(u32x4*)(lds + u * 8192 + (2 * w + h) * 1024 + lane * 16) = pack8(t0, t1);
  }
}
DEVINL void j_loop(u32x4 (&woa)[32], const f32x4 (&fe)[4], char* lds, int jw, int jstep, char* orow, int w, int lane, stv_t& stv) {
  constexpr int AH = J_AH;
  constexpr bool FAST = true;
  const float* gl = (const float*)(lds + J_G + w * 2048) + 8 * (lane >> 4);
  const char* jr = lds + lane * 16;
  f32x4 g[4];
  g[0] = *(const f32x4*)(gl + AH * 64); g[1] = *(const f32x4*)(gl + AH * 64 + 4);
  wait_lds();
  bar();
  STAMP(28);
  u32x4 B[8];
#pragma unroll
  for (int ks = 0; ks < 8; ++ks) B[ks] = *(const u32x4*)(jr + ks * 1024);
  f32x4 acc[4];
  u32x4 P0;
  static_for<8>([&](auto ic) {
    constexpr int u = decltype(ic)::v;
    constexpr bool FIRST = (u == 0), RD = (u + 1 < 8), J = (u + AH < 8);
    SB0();
    float d[16];
    unsigned jo[8];
    static_for<32>([&](auto qc) {
      constexpr int q = decltype(qc)::v, hf = q >> 4, ks = (q >> 1) & 7, m = 2 * hf + (q & 1);
      if constexpr (FIRST && q == 0) { wait_vm<16>(); static_for<16>([&](auto c) { tie(woa[decltype(c)::v]); }); }
      if constexpr (FIRST && q == 16) { wait_vm<0>(); static_for<16>([&](auto c) { tie(woa[16 + decltype(c)::v]); }); }
      if constexpr (RD && u >= 1 && q == 14) bar();
      if constexpr (ks == 0) acc[m] = MFMAU(woa[m * 8 + ks], B[ks], ((f32x4){0, 0, 0, 0}));
      else acc[m] = MFMAU(woa[m * 8 + ks], B[ks], acc[m]);
      if constexpr (RD && q >= 18 && !(q & 1)) B[ks - 1] = *(const u32x4*)(jr + (u + 1) * 8192 + (ks - 1) * 1024);
      if constexpr (!FIRST && q == 3) out_rows(orow + (u - 1) * 512, P0, pack8(acc[2], acc[3]));
      if constexpr (q == 19) P0 = pack8(acc[0], acc[1]);
      if constexpr (J) {
        if constexpr (q == 2) { g[2] = *(const f32x4*)(gl + (u + AH) * 64 + 32); g[3] = *(const f32x4*)(gl + (u + AH) * 64 + 36); }
        if constexpr (q >= 5 && q < 21) { constexpr int e = q - 5; d[e] = jden<FAST>(fe[e >> 2][e & 3], g[e >> 2][e & 3]); }
        if constexpr (q >= 7 && q < 23) { constexpr int e = q - 7; d[e] = __builtin_amdgcn_rcpf(d[e]); }
        if constexpr (q >= 9 && q < 25) { constexpr int e = q - 9; d[e] = jval(d[e]); }
        if constexpr (q >= 12 && q < 28 && !(q & 1)) { constexpr int e = q - 11; jo[e >> 1] = pack_bf16(d[e - 1], d[e]); }
        if constexpr (q == 19) *(u32x4*)(lds + jw) = (u32x4){jo[0], jo[1], jo[2], jo[3]};
        if constexpr (q == 27) { *(u32x4*)(lds + jw + 1024) = (u32x4){jo[4], jo[5], jo[6], jo[7]}; jw += jstep; asm("" : "+v"(jw)); }
        if constexpr (u + AH + 1 < 8 && q == 29) { g[0] = *(const f32x4*)(gl + (u + AH + 1) * 64); g[1] = *(const f32x4*)(gl + (u + AH + 1) * 64 + 4); }
      }
      SB0();
    });
    if constexpr (RD) B[7] = *(const u32x4*)(jr + (u + 1) * 8192 + 7 * 1024);
    SB0();
  });
  out_rows(orow + 7 * 512, P0, pack8(acc[2], acc[3]));
}

#define HINT 0
#define ROT 1
#define W2_PACE 16
#define PRIO(x) do { if ((x) >= 0) __builtin_amdgcn_s_setprio((x) < 0 ? 0 : (x)); } while (0)
#define PRIO_G0 -1
#define PRIO_G1 -1
#define PRIO_G2A -1
#define PRIO_G2B -1
#define PRIO_JB -1
#define PRIO_T0 -1
#define PRIO_MA -1
#define PRIO_MB 1
#define PRIO_JA 0
constexpr int M_HS = 96;

DEVINL void x_issue(const u16* __restrict__ x, char* reg, int s, int tp, int par, int ch, bool live, int lane) {
  __amdgpu_buffer_rsrc_t rs = mkrsrc(x + (size_t)s * T0 * 256, T0 * 512);
  const int t0 = 96 * tp - 111 + par + 2 * (lane >> 3);
  const int base = t0 * 512 + ch * 128 + (((lane & 7) ^ (4 * ((lane >> 4) & 1))) * 16);
  sgpr_settle();
  if (live) {
#pragma unroll
    for (int i = 0; i < 8; ++i) dma16<0>(rs, reg + i * 1024, (tp == 0 || (i == 0 && t0 < 0)) ? OOB : (unsigned)(base + i * 8192), 0);
  }
}
DEVINL u64 load_tag(const char* ws) {
  u64 tag;
  asm volatile("s_load_dwordx2 %0, %1, 0x0\n\ts_waitcnt lgkmcnt(0)" : "=s"(tag) : "s"(ws + OFF_FLAG + F_EPOCH * 128) : "memory");
  return tag * 6364136223846793005ull + 1442695040888963407ull;
}

__global__ __launch_bounds__(256, 2) void k_all(const u16* __restrict__ x, const u16* __restrict__ w1, const u16* __restrict__ w2, const u16* __restrict__ g_,
                                                const u16* __restrict__ wf, const u16* __restrict__ wg, const u16* __restrict__ wo,
                                                char* __restrict__ ws, char* __restrict__ out) {
  __shared__ __attribute__((aligned(16))) char lds[LDS_BYTES];
  int lane = threadIdx.x & 63;
  const int w = __builtin_amdgcn_readfirstlane(threadIdx.x >> 6);
  const int role = blockIdx.x >> 8, xl = blockIdx.x & 7, idx = (blockIdx.x >> 3) & 31;
  const int q = xl * 32 + idx;
  stv_t stv = {};
  STAMP(0);
  u64 tag;

  u32x4 W[32];
  if (role == 0) {
    const unsigned lane16 = lane * 16;
    PRIO(PRIO_G0);
    {
      u32x4 Tm[4];
      static_for<4>([&](auto ic) { constexpr int i = decltype(ic)::v; ldfrag<TAB_T + i, false>(Tm[i], (const char*)TAB, lane16); });
      w_issue(w1, lds, q, w, lane, true);
      tag = load_tag(ws);
      wait_vm<0>();
      tie(Tm);
      bar();
      STAMP(1);
      w_compute<16>(Tm, ws + OFF_W1F, lds, q, w, lane);
      publish(ws, F_W1 + (q & 3) * 64 + (q >> 2), tag, w);
      STAMP(2);
    }
    const int grp = xl * 4 + (idx >> 3), TB = (idx >> 2) & 1, cb = idx & 3;
    const int gflag = (TB * 4 + cb) * 32 + grp, rot = ROT ? cb + 2 * TB : 0;
    const unsigned yoff = (grp * 16 + cb * 4 + w) * 128;
    const size_t woff = (size_t)(grp * 16 + cb * 4 + w) * 32768;
    {
      poll(ws, F_W1 + cb * 64 + lane, tag);
      STAMP(3);
      static_for<32>([&](auto ic) { constexpr int i = decltype(ic)::v; ldfrag_rot<i>(W[i], ws + OFF_W1F + woff, rot, lane16); });
      poll(ws, F_X + TB * 128 + w * 32 + (lane & 31), tag);
      bar();
      if (HINT) set_flag(ws, F_H + q, tag, w);
      STAMP(4);
      PRIO(PRIO_G1);
      g_phase<1>(lds, ws, ws + OFF_X1F + grp * X1_G + TB * X1_TB, ws + OFF_W2F + woff, W, ws + OFF_Y1F + (size_t)TB * 42 * 65536, yoff,
                 F_X + 256 + TB * 128 + w * 32, F_W2 + cb * 64, F_G1 + gflag, tag, rot, w, stv);
      publish(ws, F_G1 + 256 + gflag, tag, w);
      STAMP(11);
      PRIO(PRIO_G2A);
    }
    lane = lane_id();
    poll(ws, F_M + TB * 112 + w * 28 + (lane < 28 ? lane : 0), tag);
    bar();
    STAMP(13);
    g_phase<2>(lds, ws, ws + OFF_X2F + grp * X2_G + TB * X2_TB, nullptr, W, ws + OFF_Y2F + (size_t)TB * 21 * 65536, yoff, 0, 0, 0, tag, rot, w, stv);
    publish(ws, F_G2 + gflag, tag, w);
    STAMP(16);
    lane = lane_id();
    poll(ws, lane >= 48 ? F_S + (lane < 60 ? lane - 48 : 0) : F_M + 256 + TB * 112 + w * 28 + (lane < 28 ? lane : 0), tag);
    bar();
    STAMP(17);
    PRIO(PRIO_G2B);
    g_phase<3>(lds, ws, ws + OFF_X2F + X2_S + grp * X2_G + TB * X2_TB, ws + OFF_WFT + w * 32768, W, ws + OFF_Y2F + Y2_S + (size_t)TB * 21 * 65536, yoff, 0, 0, 0,
               tag, rot, w, stv);
    publish(ws, F_G2 + 256 + gflag, tag, w);
    STAMP(20);
    PRIO(PRIO_JB);
  } else {
    const int G = lane >> 4, rq = (lane & 15) >> 2, p = lane & 3;
    const unsigned lane16 = lane * 16;
    PRIO(PRIO_T0);
    {
      u32x4 F[8], Tm[4];
      const int eu = w == (idx & 3) ? idx : ((idx & 3) == 0 && w == 2 ? 32 + (idx >> 2) : -1);
      const int tpv[2] = {xl * 21 + (idx >> 1), xl * 21 + 16 + (eu >> 3)}, parv[2] = {idx & 1, (eu >> 2) & 1}, chv[2] = {w, eu & 3};
      static_for<8>([&](auto ic) { constexpr int i = decltype(ic)::v; ldfrag<TAB_F + i, false>(F[i], (const char*)TAB, lane16); });
      static_for<4>([&](auto ic) { constexpr int i = decltype(ic)::v; ldfrag<TAB_T + i, false>(Tm[i], (const char*)TAB, lane16); });
      x_issue(x, lds + w * 8192, 0, tpv[0], parv[0], chv[0], true, lane);
      x_issue(x, lds + 65536 + (eu >> 5) * 8192, 0, tpv[1], parv[1], chv[1], eu >= 0, lane);
      tag = load_tag(ws);
      wait_vm<8>();
      if (eu < 0) wait_vm<0>();
      tie(F); tie(Tm);
      STAMP(1);
#pragma unroll 1
      for (int s = 0; s < 2; ++s) {
#pragma unroll 1
        for (int r = 0; r < (eu >= 0 ? 2 : 1); ++r) {
          if (r) wait_vm<8>();
          const int tl = (r ? tpv[1] : tpv[0]) - 84 * (xl >> 2);
          char* gd = ws + OFF_X1F + s * X1_S + (xl >> 2) * X1_TB + (size_t)(r ? parv[1] * 8 + chv[1] * 2 : parv[0] * 8 + chv[0] * 2) * XST + x_lane_off<X1_G>(lane, tl);
          const char* reg = lds + (r == s ? w * 8192 : (s ? 32768 + w * 8192 : 65536 + (eu >> 5) * 8192));
#pragma unroll
          for (int h = 0; h < 2; ++h) {
            const char* la = reg + (8 * G + rq) * 128 + (((4 * h + p) ^ (4 * ((rq >> 1) & 1))) * 16);
            fwd_unit<128, X1_G>(la, F, gd + h * XST);
          }
        }
        publish(ws, F_X + s * 256 + q, tag, w);
        STAMP2(2, 4);
        if (s == 0) {
          x_issue(x, lds + 32768 + w * 8192, 1, tpv[0], parv[0], chv[0], true, lane);
          x_issue(x, lds + w * 8192, 1, tpv[1], parv[1], chv[1], eu >= 0, lane);
          if (HINT) poll(ws, F_H + q, tag); else wait_vm<0>();
          STAMP(3);
          w_issue(w2, lds + 65536, q, w, lane, true);
        }
      }
      w_compute<W2_PACE>(Tm, ws + OFF_W2F, lds + 65536, q, w, lane);
      publish(ws, F_W2 + (q & 3) * 64 + (q >> 2), tag, w);
      STAMP(5);
      if (idx >= 28 && (idx - 28) * 8 + xl < 12) {
        const int sid = (idx - 28) * 8 + xl, t = sid * 4 + w;
        char* reg = lds + w * 8192;
        if (t < 16) s_transpose(wf, ws + OFF_WFT, reg, t >> 1, t & 1, lane);
        else if (t < 32) s_transpose(wo, ws + OFF_WOT, reg, (t - 16) >> 1, t & 1, lane);
        else s_ge(g_, wg, (float*)(ws + OFF_GE), reg, t - 32, lane);
        publish(ws, F_S + sid, tag, w);
      }
    }
    if (idx < 28) {
      u32x4 Gt[6], F[2];
      char* reg = lds + w * 8192;
      char* him = lds + 32768;
      const int pr = 3 * idx + w;
      static_for<6>([&](auto ic) { constexpr int i = decltype(ic)::v; ldfrag<TAB_G + i, false>(Gt[i], (const char*)TAB, lane16); });
      static_for<2>([&](auto ic) { constexpr int i = decltype(ic)::v; ldfrag<i, false>(F[i], (const char*)TAB + (TAB_F + 2 * w) * 1024, lane16); });
#pragma unroll 1
      for (int s = 0; s < 2; ++s) {
        poll(ws, F_G1 + s * 256 + ((pr >= 42 ? 4 : 0) + (xl >> 1)) * 32 + (lane & 31), tag);
        if (s == 0) { PRIO(PRIO_MA); } else { PRIO(PRIO_MB); }
        tie(Gt); tie(F);
        __amdgpu_buffer_rsrc_t rs = mkrsrc(ws + OFF_Y1F + s * Y1_S, Y1_S);
        sgpr_settle();
#pragma unroll
        for (int i = 0; i < 8; ++i) {
          const unsigned off = pr * 65536 + (i * 4 + G) * 2048 + xl * 256 + (((lane & 15) ^ ((G & 1) << 2) ^ (i & 1)) * 16);
          dma16<SC1>(rs, reg + i * 1024, off, 0);
        }
        STAMP2(6, 10);
        wait_vm<0>();
        STAMP2(7, 11);
        bf16x8 A[2][2][2];
#pragma unroll
        for (int tl = 0; tl < 2; ++tl)
#pragma unroll
          for (int i = 0; i < 2; ++i)
#pragma unroll
            for (int ks = 0; ks < 2; ++ks) {
              const int ch = (((p >> 1) * 8 + tl * 4 + 2 * (p & 1) + i) ^ ((rq >> 1) << 2) ^ (G & 1));
              A[tl][i][ks] = tr_frag(reg + (16 * ks + 4 * G + (rq >> 1)) * 256 + ch * 16 + (rq & 1) * 8, 512);
            }
        f32x4 c[2][2];
        auto idft = [&](int b) {
          constexpr f32x4 Z = {0, 0, 0, 0};
#pragma unroll
          for (int i = 0; i < 2; ++i) c[b & 1][i] = MFMAU(A[b / 3][i][0], Gt[2 * (b % 3)], Z);
#pragma unroll
          for (int i = 0; i < 2; ++i) c[b & 1][i] = MFMAU(A[b / 3][i][1], Gt[2 * (b % 3) + 1], c[b & 1][i]);
        };
        idft(0);
        const unsigned keep = pr == 83 ? 0u : ~0u;
#pragma unroll
        for (int b = 0; b < 6; ++b) {
          if (b + 1 < 6) idft(b + 1);
          SB0();
          u32x4 o = gelu_pack8(c[b & 1][0], c[b & 1][1]);
          if (b == 5) o &= keep;
          *(u32x4*)(him + (w * 96 + b * 16 + (lane & 15)) * M_HS + G * 16) = o;
          SB0();
        }
        wait_lds();
        bar();
        STAMP2(8, 12);
        const int TB2 = idx >= 14;
        char* gd = ws + OFF_X2F + s * X2_S + TB2 * X2_TB + (size_t)xl * (XST / 2) + (size_t)w * 8 * X2_G;
        const char* la = him + (33 + 2 * (8 * G + rq)) * M_HS + p * 16;
        bf16x8 B[6][2][2];
#pragma unroll
        for (int u = 0; u < 6; ++u)
#pragma unroll
          for (int i = 0; i < 2; ++i)
#pragma unroll
            for (int ks = 0; ks < 2; ++ks) B[u][i][ks] = tr_frag(la + ((u >> 1) * 96 + (u & 1) + 64 * ks) * M_HS + 8 * i, 8 * M_HS);
#pragma unroll
        for (int u = 0; u < 6; ++u) {
          f32x4 c0 = {0, 0, 0, 0}, c1 = {0, 0, 0, 0};
#pragma unroll
          for (int ks = 0; ks < 2; ++ks) { c0 = MFMAU(B[u][0][ks], F[ks], c0); c1 = MFMAU(B[u][1][ks], F[ks], c1); }
          st16(gd + x_lane_off<X2_G>(lane, 3 * idx - 42 * TB2 + (u >> 1)) + (u & 1) * 8 * (XST / 2), pack8(c0, c1));
        }
        publish(ws, F_M + s * 256 + idx * 8 + xl, tag, w);
        STAMP2(9, 13);
      }
    }
    PRIO(PRIO_JA);
    if (w == 0) poll(ws, F_S + (lane < 12 ? lane : 0), tag);
    bar();
    sgpr_settle();
    static_for<32>([&](auto ic) { constexpr int i = decltype(ic)::v; ldfrag<i, true>(W[i], ws + OFF_WFT + w * 32768, lane16); });
    wait_vm<0>();
  }
  tie(W);
  const int s = role ^ 1;
  lane = lane_id();
  const int G = lane >> 4, rq = (lane & 15) >> 2, p = lane & 3;
  const unsigned lane16 = lane * 16;

  const int jb = q;
  if (jb >= 250) {
    if (blockIdx.x == 255 && w == 0) {
#pragma unroll 1
      for (int i = 4; i < 8; ++i) poll(ws, F_G2 + i * 64 + lane, tag);
      set_flag(ws, F_EPOCH, tag, w);
    }
    STAMP_FLUSH();
    return;
  }
  {
    const int tile = jb / 3, vt = jb - 3 * tile;
    u32x4 woa[32], Gt[2];
    sgpr_settle();
    static_for<2>([&](auto ic) { constexpr int i = decltype(ic)::v; ldfrag<i, false>(Gt[i], (const char*)TAB + (TAB_G + 2 * vt) * 1024, lane16); });
    __amdgpu_buffer_rsrc_t grs = mkrsrc(ws + OFF_GE, 32768);
#pragma unroll
    for (int i = 0; i < 2; ++i) dma16<SC1>(grs, lds + J_G + w * 2048 + i * 1024, 16384 + (s * 4 + w) * 2048 + i * 1024 + lane16, 0);
    static_for<16>([&](auto ic) { constexpr int i = decltype(ic)::v; ldfrag<i, true>(woa[i], ws + OFF_WOT + w * 32768, lane16); });
    poll(ws, F_G2 + s * 256 + ((tile >= 42 ? 4 : 0) + w) * 32 + (lane & 31), tag);
    tie(Gt);
    STAMP(24);
    char* reg = lds + J_Y + w * 8192;
    {
      __amdgpu_buffer_rsrc_t rs = mkrsrc(ws + OFF_Y2F + s * Y2_S + (size_t)(tile >> 1) * 65536, 65536);
#pragma unroll
      for (int i = 0; i < 8; ++i) {
        const int gg = i * 4 + G, c = (lane & 15) ^ ((G & 1) << 3) ^ (i & 1);
        dma16<SC1>(rs, reg + i * 1024, gg * 2048 + (4 * w + (c >> 2)) * 128 + (tile & 1) * 64 + (c & 3) * 16, 0);
      }
    }
    wait_vm<0>();
    STAMP(25);
#pragma unroll
    for (int h = 0; h < 2; ++h) {
      f32x4 c[2];
#pragma unroll
      for (int i = 0; i < 2; ++i) {
        const int ch = (8 * h + 2 * p + i) ^ ((rq >> 1) << 3) ^ (G & 1);
        const char* la = reg + (4 * G + (rq >> 1)) * 256 + ch * 16 + (rq & 1) * 8;
        c[i] = MFMAU(tr_frag(la, 512), Gt[0], ((f32x4){0, 0, 0, 0}));
        c[i] = MFMAU(tr_frag(la + 4096, 512), Gt[1], c[i]);
      }
      *(u32x4*)(lds + J_F + (2 * w + h) * 1024 + lane16) = gelu_pack8(c[0], c[1]);
    }
    wait_lds();
    bar();
    STAMP(26);
    f32x4 fe[4];
#pragma unroll
    for (int m = 0; m < 4; ++m) fe[m] = (f32x4){0, 0, 0, 0};
#pragma unroll
    for (int ks = 0; ks < 8; ++ks) {
      const u32x4 bfr = *(const u32x4*)(lds + J_F + ks * 1024 + lane16);
#pragma unroll
      for (int m = 0; m < 4; ++m) fe[m] = MFMAU(W[m * 8 + ks], bfr, fe[m]);
    }
    SB0();
    static_for<16>([&](auto ic) { constexpr int i = 16 + decltype(ic)::v; ldfrag<i, true>(woa[i], ws + OFF_WOT + w * 32768, lane16); });
    SB0();
    bool small = true;
#pragma unroll
    for (int m = 0; m < 4; ++m) {
      fe[m] *= LOG2E2;
#pragma unroll
      for (int r = 0; r < 4; ++r) small = small && (__builtin_fabsf(fe[m][r]) <= 32.0f);
    }
    STAMP(27);
    char* orow = out + ((size_t)(s * T2 + jb * 16 + (lane & 7)) * 8) * 512 + w * 128 + ((lane >> 3) & 1) * 64 + G * 16;
    const bool fast = __all(small);
    if (fast) {
#pragma unroll
      for (int m = 0; m < 4; ++m)
#pragma unroll
        for (int r = 0; r < 4; ++r) fe[m][r] = __builtin_amdgcn_exp2f(fe[m][r]);
#pragma unroll
      for (int u = 0; u < J_AH; ++u) j_make<true>(fe, lds, u, w, lane);
    } else {
#pragma unroll
      for (int i = 0; i < 2; ++i) dma16<SC1>(grs, lds + J_G + w * 2048 + i * 1024, (s * 4 + w) * 2048 + i * 1024 + lane16, 0);
      wait_vm<0>();
#pragma unroll 1
      for (int u = 0; u < 8; ++u) j_make<false>(fe, lds, u, w, lane);
    }
    j_loop(woa, fe, lds, (fast ? J_AH * 8192 : J_F) + 2 * w * 1024 + lane16, fast ? 8192 : 0, orow, w, lane, stv);
  }
  STAMP(29);
}

extern "C" void kernel_launch(void* const* d_in, const int* in_sizes, int n_in, void* d_out, int out_size, void* d_ws,
                              size_t ws_size, hipStream_t stream) {
  k_all<<<512, 256, 0, stream>>>((const u16*)d_in[0], (const u16*)d_in[1], (const u16*)d_in[2], (const u16*)d_in[3], (const u16*)d_in[4],
                                 (const u16*)d_in[5], (const u16*)d_in[6], (char*)d_ws, (char*)d_out);
}
```

```cpp
#include <hip/hip_runtime.h>
#include <hip/hip_bf16.h>
#include <utility>
__device__ const unsigned TAB[4608] __attribute__((aligned(128))) = {
0x3f803f80,0x3f803f80,0x3f803f80,0x3f803f80,0x0,0x0,0x0,0x0,
0x3f7f3f80,0x3f753f7b,0x3f613f6c,0x3f443f54,0x3dcc0000,0x3e973e4b,0x3ef53ec7,0x3f253f10,
0x3f7b3f80,0x3f543f6c,0x3f0b3f33,0x3e323ebb,0x3e4b0000,0x3f103ec7,0x3f573f37,0x3f7c3f6e,
0x3f753f80,0x3f203f54,0x3d993ebb,0xbf00be64,0x3e970000,0x3f483f10,0x3f7f3f6e,0x3f5e3f7a,
0x3f6c3f80,0x3ebb3f33,0xbed3bccc,0xbf71bf3c,0x3ec70000,0x3f6e3f37,0x3f693f80,0x3eaf3f2e,
0x3f613f80,0x3d993f0b,0xbf4cbed3,0xbf71bf7d,0x3ef50000,0x3f7f3f57,0x3f1b3f69,0xbeaf3e19,
0x3f543f80,0xbe643ebb,0xbf7dbf3c,0xbf00bf67,0x3f100000,0x3f7a3f6e,0x3e193f2e,0xbf5ebede,
0x3f443f80,0xbf003e32,0xbf71bf71,0x3e32bf00,0x3f250000,0x3f5e3f7c,0xbeaf3eaf,0xbf7cbf5e,
0x3f803f80,0x3f803f80,0x3f803f80,0x3f803f80,0x0,0x0,0x0,0x0,
0x3f203f33,0x3eea3f0b,0x3e8b3ebb,0x3d993e32,0x3f483f37,0x3f643f57,0x3f763f6e,0x3f7f3f7c,
0xbe64bccc,0xbf15bed3,0xbf5abf3c,0xbf7dbf71,0x3f7a3f80,0x3f503f69,0x3f053f2e,0x3e193eaf,
0xbf67bf3c,0xbf7dbf7d,0xbf3cbf67,0xbe64bf00,0x3ede3f2e,0xbe193e19,0xbf2ebede,0xbf7abf5e,
0xbf67bf80,0xbea3bf29,0x3eea3d99,0x3f753f44,0xbedebd4c,0xbf73bf40,0xbf64bf7f,0xbe97bf25,
0xbe64bf29,0x3f333e8b,0x3f7b3f75,0x3ebb3f44,0xbf7abf40,0xbf37bf76,0x3e4bbe97,0x3f6e3f25,
0x3f203d99,0x3f753f75,0x3d993f20,0xbf67bf00,0xbf48bf7f,0x3e97be97,0x3f7f3f48,0x3ede3f5e,
0x3f803f44,0x3e323f44,0xbf71bf00,0xbf00bf71,0xbf25,0x3f7c3f25,0x3eaf3f5e,0xbf5ebeaf,
0x3f803f80,0x3f803f80,0x3f803f80,0x3f803f80,0x0,0x0,0x0,0x0,
0xbdffbccc,0xbea3be64,0xbf00bed3,0xbf29bf15,0x3f7e3f80,0x3f733f7a,0x3f5e3f69,0x3f403f50,
0xbf78bf80,0xbf4cbf67,0xbf00bf29,0xbdffbea3,0xbe7dbd4c,0xbf1bbede,0xbf5ebf40,0xbf7ebf73,
0x3ebb3d99,0x3f543f20,0x3f803f75,0x3f543f75,0xbf6ebf7f,0xbf10bf48,0xbe97,0x3f103e97,
0x3f613f7f,0x3e8b3f20,0xbf00bdff,0xbf78bf4c,0x3ef53dcc,0x3f763f48,0x3f5e3f7e,0x3e7d3f1b,
0xbf15bdff,0xbf80bf67,0xbf00bf5a,0x3eeabccc,0x3f503f7e,0xbd4c3ede,0xbf5ebf05,0xbf64bf80,
0xbf3cbf7d,0x3ebbbe64,0x3f803f54,0x3ebb3f54,0xbf2ebe19,0xbf6ebf7a,0xbf10,0x3f6e3f10,
0x3f443e32,0x3f443f80,0xbf003e32,0xbf71bf71,0xbf25bf7c,0x3f250000,0x3f5e3f7c,0xbeaf3eaf,
0x3f803f80,0x3f803f80,0x3f803f80,0x3f803f80,0x0,0x0,0x0,0x0,
0xbf4cbf3c,0xbf67bf5a,0xbf78bf71,0xbf80bf7d,0x3f1b3f2e,0x3ede3f05,0x3e7d3eaf,0x3d4c3e19,
0x3e8b3d99,0x3f203eea,0x3f613f44,0x3f7f3f75,0xbf76bf7f,0xbf48bf64,0xbef5bf25,0xbdccbe97,
0x3ebb3f20,0xbe643d99,0xbf3cbf00,0xbf7dbf67,0x3f6e3f48,0x3f7a3f7f,0x3f2e3f5e,0x3e193ede,
0xbf5abf7d,0xbe64bf15,0x3f0b3e32,0x3f7b3f54,0xbf05be19,0xbf7abf50,0xbf57bf7c,0xbe4bbf10,
0x3f7f3f54,0x3f203f6c,0xbea33e32,0xbf78bf3c,0xbdccbf10,0x3f483ec7,0x3f733f7c,0x3e7d3f2e,
0xbf3cbe64,0xbf67bf7d,0x3d99bf00,0x3f753f20,0x3f2e3f7a,0xbede3e19,0xbf7fbf5e,0xbe97bf48,
0x3e32bf00,0x3f803f44,0x3e323f44,0xbf71bf00,0xbf7cbf5e,0xbf25,0x3f7c3f25,0x3eaf3f5e,
0x3f803f80,0x3f803f80,0x3f803f80,0x3f803f80,0x0,0x0,0x0,0x0,
0xbf7dbf80,0xbf71bf78,0xbf5abf67,0xbf3cbf4c,0xbe19bd4c,0xbeafbe7d,0xbf05bede,0xbf2ebf1b,
0x3f753f7f,0x3f443f61,0x3eea3f20,0x3d993e8b,0x3e973dcc,0x3f253ef5,0x3f643f48,0x3f7f3f76,
0xbf67bf7d,0xbf00bf3c,0x3d99be64,0x3f203ebb,0xbedebe19,0xbf5ebf2e,0xbf7fbf7a,0xbf48bf6e,
0x3f543f7b,0x3e323f0b,0xbf15be64,0xbf7dbf5a,0x3f103e4b,0x3f7c3f57,0x3f503f7a,0x3e193f05,
0xbf3cbf78,0x3e32bea3,0x3f6c3f20,0x3f543f7f,0xbf2ebe7d,0xbf7cbf73,0xbec7bf48,0x3f103dcc,
0x3f203f75,0xbf003d99,0xbf7dbf67,0xbe64bf3c,0x3f483e97,0x3f5e3f7f,0xbe193ede,0xbf7abf2e,
0xbf00bf71,0x3f443e32,0x3f443f80,0xbf003e32,0xbf5ebeaf,0xbf25bf7c,0x3f250000,0x3f5e3f7c,
0x3f803f80,0x3f803f80,0x3f803f80,0x3f803f80,0x0,0x0,0x0,0x0,
0xbf15bf29,0xbed3bf00,0xbe64bea3,0xbcccbdff,0xbf50bf40,0xbf69bf5e,0xbf7abf73,0xbf80bf7e,
0xbea3bdff,0xbf29bf00,0xbf67bf4c,0xbf80bf78,0x3f733f7e,0x3f403f5e,0x3ede3f1b,0x3d4c3e7d,
0x3f753f54,0x3f753f80,0x3f203f54,0x3d993ebb,0xbe97bf10,0x3e970000,0x3f483f10,0x3f7f3f6e,
0xbf4cbf78,0xbdffbf00,0x3f203e8b,0x3f7f3f61,0xbf1bbe7d,0xbf7ebf5e,0xbf48bf76,0xbdccbef5,
0xbccc3eea,0xbf5abf00,0xbf67bf80,0xbdffbf15,0x3f803f64,0x3f053f5e,0xbede3d4c,0xbf7ebf50,
0x3f543ebb,0x3f543f80,0xbe643ebb,0xbf7dbf3c,0xbf10bf6e,0x3f100000,0x3f7a3f6e,0x3e193f2e,
0xbf71bf71,0x3e32bf00,0x3f803f44,0x3e323f44,0xbeaf3eaf,0xbf7cbf5e,0xbf25,0x3f7c3f25,
0x3f803f80,0x3f803f80,0x3f803f80,0x3f803f80,0x0,0x0,0x0,0x0,
0x3e323d99,0x3ebb3e8b,0x3f0b3eea,0x3f333f20,0xbf7cbf7f,0xbf6ebf76,0xbf57bf64,0xbf37bf48,
0xbf71bf7d,0xbf3cbf5a,0xbed3bf15,0xbcccbe64,0xbeafbe19,0xbf2ebf05,0xbf69bf50,0xbf80bf7a,
0xbf00be64,0xbf67bf3c,0xbf7dbf7d,0xbf3cbf67,0x3f5e3f7a,0x3ede3f2e,0xbe193e19,0xbf2ebede,
0x3f443f75,0x3d993eea,0xbf29bea3,0xbf80bf67,0x3f253e97,0x3f7f3f64,0x3f403f73,0x3d4c3ede,
0x3f443ebb,0x3f753f7b,0x3e8b3f33,0xbf29be64,0xbf25bf6e,0x3e97be4b,0x3f763f37,0x3f403f7a,
0xbf00bf67,0x3f203d99,0x3f753f75,0x3d993f20,0xbf5ebede,0xbf48bf7f,0x3e97be97,0x3f7f3f48,
0xbf71bf00,0xbf00bf71,0x3f443e32,0x3f443f80,0x3eaf3f5e,0xbf5ebeaf,0xbf25bf7c,0x3f250000,
0x3f803f80,0x3f803f80,0x3f803f80,0x3f80,0x0,0x0,0x0,0x0,
0x3f543f44,0x3f6c3f61,0x3f7b3f75,0x3f7f,0xbf10bf25,0xbec7bef5,0xbe4bbe97,0xbdcc,
0x3ebb3e32,0x3f333f0b,0x3f6c3f54,0x3f7b,0xbf6ebf7c,0xbf37bf57,0xbec7bf10,0xbe4b,
0xbe64bf00,0x3ebb3d99,0x3f543f20,0x3f75,0xbf7abf5e,0xbf6ebf7f,0xbf10bf48,0xbe97,
0xbf3cbf71,0xbcccbed3,0x3f333ebb,0x3f6c,0xbf2ebeaf,0xbf80bf69,0xbf37bf6e,0xbec7,
0xbf7dbf71,0xbed3bf4c,0x3f0b3d99,0x3f61,0xbe193eaf,0xbf69bf1b,0xbf57bf7f,0xbef5,
0xbf67bf00,0xbf3cbf7d,0x3ebbbe64,0x3f54,0x3ede3f5e,0xbf2ebe19,0xbf6ebf7a,0xbf10,
0xbf003e32,0xbf71bf71,0x3e32bf00,0x3f44,0x3f5e3f7c,0xbeaf3eaf,0xbf7cbf5e,0xbf25,
0x3f333f80,0xbf3cbccc,0xbf29bf80,0x3f443d99,0x3f370000,0x3f2e3f80,0xbf40bd4c,0xbf25bf7f,
0x3f203f80,0xbf67be64,0xbe64bf67,0x3f803f20,0x3f480000,0x3ede3f7a,0xbf7abede,0xbf48,
0x3f0b3f80,0xbf7dbed3,0x3e8bbf29,0x3f443f75,0x3f570000,0x3e193f69,0xbf76bf40,0x3f25be97,
0x3eea3f80,0xbf7dbf15,0x3f33bea3,0x3e323f75,0x3f640000,0xbe193f50,0xbf37bf73,0x3f7c3e97,
0x3ebb3f80,0xbf67bf3c,0x3f753d99,0xbf003f20,0x3f6e0000,0xbede3f2e,0xbe97bf7f,0x3f5e3f48,
0x3e8b3f80,0xbf3cbf5a,0x3f7b3eea,0xbf713d99,0x3f760000,0xbf2e3f05,0x3e4bbf64,0x3eaf3f7f,
0x3e323f80,0xbf00bf71,0x3f443f44,0xbf71bf00,0x3f7c0000,0xbf5e3eaf,0x3f25bf25,0xbeaf3f5e,
0x3d993f80,0xbe64bf7d,0x3ebb3f75,0xbf00bf67,0x3f7f0000,0xbf7a3e19,0x3f6ebe97,0xbf5e3ede,
0x3f203f7f,0xbf4cbdff,0xbf15bf7d,0x3f543e32,0x3f483dcc,0x3f1b3f7e,0xbf50be19,0xbf10bf7c,
0xbe643f20,0xbf67bf67,0x3f20be64,0x3f203f80,0x3f7a3f48,0xbede3ede,0xbf48bf7a,0x3f480000,
0xbf67bdff,0xbcccbf5a,0x3f6c3f54,0xbf3c3e32,0x3ede3f7e,0xbf80bf05,0x3ec7bf10,0x3f2e3f7c,
0xbf67bf4c,0x3f61bccc,0xbdff3f54,0xbf3cbf71,0xbede3f1b,0xbef5bf80,0x3f7e3f10,0xbf2e3eaf,
0xbe64bf7d,0x3f543f54,0xbf7dbe64,0x3f20bf00,0xbf7abe19,0x3f10bf10,0x3e193f7a,0xbf48bf5e,
0x3f20bf15,0xbdff3f6c,0xbed3bf7d,0x3f543f44,0xbf48bf50,0x3f7e3ec7,0xbf693e19,0x3f10bf25,
0x3f803e32,0xbf713e32,0x3f44bf00,0xbf003f44,0xbf7c,0x3eaf3f7c,0xbf25bf5e,0x3f5e3f25,
0x3f203f54,0xbf3cbf3c,0x3f543f20,0xbf67bf00,0x3f48bf10,0xbf2e3f2e,0x3f10bf48,0xbede3f5e,
0x3f0b3f7b,0xbf5abe64,0xbf00bf78,0x3f613e8b,0x3f573e4b,0x3f053f7a,0xbf5ebe7d,0xbef5bf76,
0xbf67be64,0xbe64bf67,0x3f803f20,0xbe643f20,0x3ede3f7a,0xbf7abede,0xbf48,0x3f7a3f48,
0xbea3bf78,0x3f7f3f20,0xbf003eea,0xbf15bf80,0xbf73be7d,0x3dccbf48,0x3f5e3f64,0xbf503d4c,
0x3f7b3e8b,0xbed33f20,0xbf00bf80,0x3f7f3f0b,0xbe4bbf76,0x3f693f48,0xbf5e3d4c,0x3dccbf57,
0x3d993f75,0xbf3cbf67,0x3f803ebb,0xbf3c3ebb,0x3f7f3e97,0xbf2e3ede,0xbf6e,0x3f2e3f6e,
0xbf80bea3,0x3f61be64,0xbf003f33,0xbcccbf78,0xbd4c3f73,0xbef5bf7a,0x3f5e3f37,0xbf80be7d,
0x3e32bf71,0x3e323f80,0xbf00bf71,0x3f443f44,0xbf7cbeaf,0x3f7c0000,0xbf5e3eaf,0x3f25bf25,
0x3f753ebb,0xbf7dbe64,0x3f803d99,0xbf7d3d99,0x3e97bf6e,0xbe193f7a,0xbf7f,0x3e193f7f,
0x3eea3f75,0xbf67bea3,0xbed3bf71,0x3f6c3ebb,0x3f643e97,0x3ede3f73,0xbf69beaf,0xbec7bf6e,
0xbf67bf67,0x3f20be64,0x3f203f80,0xbf67be64,0xbede3ede,0xbf48bf7a,0x3f480000,0x3ede3f7a,
0x3f7b3ebb,0xbe643f33,0xbf4cbf71,0x3f613d99,0xbe4bbf6e,0x3f7a3f37,0xbf1b3eaf,0xbef5bf7f,
0xbf293ebb,0xbe64bf78,0x3f6c3f44,0xbf5a3d99,0x3f403f6e,0xbf7abe7d,0x3ec7bf25,0x3f053f7f,
0x3d99bf67,0x3f203f75,0xbf7dbf00,0x3f54be64,0xbf7fbede,0x3f48be97,0xbe193f5e,0xbf10bf7a,
0x3f0b3f75,0xbf67bf29,0x3f7f3e32,0xbf4c3ebb,0x3f57be97,0xbede3f40,0xbdccbf7c,0x3f1b3f6e,
0xbf71bf00,0x3f803e32,0xbf713e32,0x3f44bf00,0xbeaf3f5e,0xbf7c,0x3eaf3f7c,0xbf25bf5e,
0x3f75be64,0xbf673ebb,0x3f54bf00,0xbf3c3f20,0xbe97bf7a,0x3ede3f6e,0xbf10bf5e,0x3f2e3f48,
0x3ebb3f6c,0xbf71bed3,0xbea3bf67,0x3f753eea,0x3f6e3ec7,0x3eaf3f69,0xbf73bede,0xbe97bf64,
0xbe64bf67,0x3f803f20,0xbe643f20,0xbf67bf67,0xbf7abede,0xbf48,0x3f7a3f48,0xbede3ede,
0x3d993f61,0xbf71bf4c,0x3f33be64,0x3ebb3f7b,0x3f7f3ef5,0xbeaf3f1b,0xbf37bf7a,0x3f6e3e4b,
0x3d99bf5a,0x3f443f6c,0xbf78be64,0x3ebbbf29,0xbf7fbf05,0x3f25bec7,0x3e7d3f7a,0xbf6ebf40,
0xbe643f54,0xbf00bf7d,0x3f753f20,0xbf673d99,0x3f7a3f10,0xbf5e3e19,0x3e97bf48,0x3ede3f7f,
0x3ebbbf4c,0x3e323f7f,0xbf29bf67,0x3f753f0b,0xbf6ebf1b,0x3f7c3dcc,0xbf403ede,0x3e97bf57,
0xbf003f44,0x3e32bf71,0x3e323f80,0xbf00bf71,0x3f5e3f25,0xbf7cbeaf,0x3f7c0000,0xbf5e3eaf,
0x3f20bf3c,0xbf003f54,0x3ebbbf67,0xbe643f75,0xbf48bf2e,0x3f5e3f10,0xbf6ebede,0x3f7a3e97,
0x3e8b3f61,0xbf78bf00,0xbe64bf5a,0x3f7b3f0b,0x3f763ef5,0x3e7d3f5e,0xbf7abf05,0xbe4bbf57,
0x3f20be64,0x3f203f80,0xbf67be64,0xbe64bf67,0xbf48bf7a,0x3f480000,0x3ede3f7a,0xbf7abede,
0xbf80bf15,0x3eeabf00,0x3f203f7f,0xbf78bea3,0xbd4c3f50,0xbf64bf5e,0x3f48bdcc,0x3e7d3f73,
0x3f0b3f7f,0xbf80bf00,0x3f20bed3,0x3e8b3f7b,0x3f57bdcc,0xbd4c3f5e,0xbf48bf69,0x3f763e4b,
0x3ebbbf3c,0x3ebb3f80,0xbf67bf3c,0x3f753d99,0xbf6ebf2e,0x3f6e0000,0xbede3f2e,0xbe97bf7f,
0xbf78bccc,0x3f33bf00,0xbe643f61,0xbea3bf80,0x3e7d3f80,0xbf37bf5e,0x3f7a3ef5,0xbf733d4c,
0x3f443f44,0xbf71bf00,0x3f803e32,0xbf713e32,0x3f25bf25,0xbeaf3f5e,0xbf7c,0x3eaf3f7c,
0x3d99bf7d,0x3d993f80,0xbe64bf7d,0x3ebb3f75,0xbf7fbe19,0x3f7f0000,0xbf7a3e19,0x3f6ebe97,
0x3e323f54,0xbf7dbf15,0xbdffbf4c,0x3f7f3f20,0x3f7c3f10,0x3e193f50,0xbf7ebf1b,0xbdccbf48,
0x3f803f20,0xbe643f20,0xbf67bf67,0x3f20be64,0xbf48,0x3f7a3f48,0xbede3ede,0xbf48bf7a,
0x3e32bf3c,0x3f543f6c,0xbf5abccc,0xbdffbf67,0xbf7cbf2e,0x3f10bec7,0x3f053f80,0xbf7ebede,
0xbf71bf3c,0x3f54bdff,0xbccc3f61,0xbf4cbf67,0xbeaf3f2e,0xbf10bf7e,0x3f803ef5,0xbf1b3ede,
0xbf003f20,0xbe64bf7d,0x3f543f54,0xbf7dbe64,0x3f5e3f48,0xbf7abe19,0x3f10bf10,0x3e193f7a,
0x3f443f54,0xbf7dbed3,0x3f6cbdff,0xbf153f20,0x3f25bf10,0xbe193f69,0xbec7bf7e,0x3f503f48,
0x3f44bf00,0xbf003f44,0x3e32bf71,0x3e323f80,0xbf25bf5e,0x3f5e3f25,0xbf7cbeaf,0x3f7c0000,
0xbf00bf67,0x3f203f54,0xbf3cbf3c,0x3f543f20,0xbf5e3ede,0x3f48bf10,0xbf2e3f2e,0x3f10bf48,
0x3d993f44,0xbf80bf29,0xbcccbf3c,0x3f33,0x3f7f3f25,0x3d4c3f40,0xbf80bf2e,0xbf37,
0x3f203f80,0xbf67be64,0xbe64bf67,0x3f20,0x3f480000,0x3ede3f7a,0xbf7abede,0xbf48,
0x3f753f44,0xbf293e8b,0xbed3bf7d,0x3f0b,0x3e97bf25,0x3f403f76,0xbf69be19,0xbf57,
0x3f753e32,0xbea33f33,0xbf15bf7d,0x3eea,0xbe97bf7c,0x3f733f37,0xbf503e19,0xbf64,
0x3f20bf00,0x3d993f75,0xbf3cbf67,0x3ebb,0xbf48bf5e,0x3f7f3e97,0xbf2e3ede,0xbf6e,
0x3d99bf71,0x3eea3f7b,0xbf5abf3c,0x3e8b,0xbf7fbeaf,0x3f64be4b,0xbf053f2e,0xbf76,
0xbf00bf71,0x3f443f44,0xbf71bf00,0x3e32,0xbf5e3eaf,0x3f25bf25,0xbeaf3f5e,0xbf7c,
0xbf67bf00,0x3f753ebb,0xbf7dbe64,0x3d99,0xbede3f5e,0x3e97bf6e,0xbe193f7a,0xbf7f,
0xbccc3f80,0x3d99bf80,0xbdff3f7f,0x3e32bf7d,0x3f800000,0xbf7fbd4c,0x3f7e3dcc,0xbf7cbe19,
0xbdff3f80,0x3ebbbf78,0xbf153f61,0x3f44bf3c,0x3f7e0000,0xbf6ebe7d,0x3f503ef5,0xbf25bf2e,
0xbe643f80,0x3f20bf67,0xbf673f20,0x3f80be64,0x3f7a0000,0xbf48bede,0x3ede3f48,0xbf7a,
0xbea33f80,0x3f54bf4c,0xbf803e8b,0x3f443ebb,0x3f730000,0xbf10bf1b,0xbd4c3f76,0x3f25bf6e,
0xbed33f80,0x3f75bf29,0xbf5abdff,0x3e323f54,0x3f690000,0xbe97bf40,0xbf053f7e,0x3f7cbf10,
0xbf003f80,0x3f80bf00,0xbf00bf00,0xbf003f80,0x3f5e0000,0xbf5e,0xbf5e3f5e,0x3f5e0000,
0xbf153f80,0x3f75bea3,0xbcccbf4c,0xbf713f54,0x3f500000,0x3e97bf73,0xbf803f1b,0x3eaf3f10,
0xbf293f80,0x3f54bdff,0x3eeabf78,0xbf713ebb,0x3f400000,0x3f10bf7e,0xbf643e7d,0xbeaf3f6e,
0xbe643f7b,0x3e8bbf78,0xbea33f75,0x3ebbbf71,0x3f7a3e4b,0xbf76be7d,0x3f733e97,0xbf6ebeaf,
0xbf673f0b,0x3f7bbea3,0xbf803d99,0x3f753e32,0x3ede3f57,0xbe4bbf73,0xbd4c3f7f,0x3e97bf7c,
0xbf67be64,0x3f203f20,0xbe64bf67,0xbe643f80,0xbede3f7a,0x3f48bf48,0xbf7a3ede,0x3f7a0000,
0xbe64bf5a,0xbed33f7f,0x3f61bf3c,0xbf7d3e32,0xbf7a3f05,0x3f693dcc,0xbef5bf2e,0xbe193f7c,
0x3f20bf78,0xbf803eea,0x3f333ebb,0x3d99bf71,0xbf48be7d,0x3d4c3f64,0x3f37bf6e,0xbf7f3eaf,
0x3f80bf00,0xbf00bf00,0xbf003f80,0x3f80bf00,0xbf5e,0xbf5e3f5e,0x3f5e0000,0xbf5e,
0x3f203e8b,0x3f0bbf80,0xbf783ebb,0x3d993f44,0x3f48bf76,0xbf573d4c,0xbe7d3f6e,0x3f7fbf25,
0xbe643f61,0x3f7fbf15,0xbcccbf3c,0xbf7d3f44,0x3f7abef5,0x3dccbf50,0xbf803f2e,0x3e193f25,
0xbed33f6c,0x3eeabf67,0xbf003f61,0x3f0bbf5a,0x3f693ec7,0xbf64bede,0x3f5e3ef5,0xbf57bf05,
0xbf5abed3,0x3f333f20,0xbf00bf4c,0x3e8b3f6c,0xbf053f69,0x3f37bf48,0xbf5e3f1b,0x3f76bec7,
0x3f20bf67,0xbf673f20,0x3f80be64,0xbf67be64,0xbf48bede,0x3ede3f48,0xbf7a,0xbede3f7a,
0x3f333eea,0xbdffbf67,0xbf003f7b,0x3f6cbf29,0x3f37bf64,0xbf7e3ede,0x3f5e3e4b,0xbec7bf40,
0xbf4c3f61,0x3f7bbe64,0xbf00bf15,0xbea33f7f,0x3f1b3ef5,0x3e4bbf7a,0xbf5e3f50,0x3f73bdcc,
0xbf00bf00,0xbf003f80,0x3f80bf00,0xbf00bf00,0xbf5e3f5e,0x3f5e0000,0xbf5e,0xbf5e3f5e,
0x3f6cbf5a,0xbf29be64,0xbf003f7f,0x3f7bbed3,0xbec7bf05,0xbf403f7a,0x3f5ebdcc,0x3e4bbf69,
0x3e8b3f0b,0x3f6cbf67,0xbf00bea3,0xbf4c3f7b,0x3f76bf57,0xbec7bede,0xbf5e3f73,0x3f1b3e4b,
0xbf153f54,0x3f20bf4c,0xbf293f44,0x3f33bf3c,0x3f503f10,0xbf48bf1b,0x3f403f25,0xbf37bf2e,
0xbcccbf7d,0xbe643f7f,0x3eeabf71,0xbf293f54,0xbf803e19,0x3f7a3dcc,0xbf64beaf,0x3f403f10,
0x3f203f20,0xbe64bf67,0xbe643f80,0x3f20bf67,0x3f48bf48,0xbf7a3ede,0x3f7a0000,0xbf48bede,
0xbf783d99,0x3f203f0b,0xbcccbf71,0xbf153f75,0xbe7d3f7f,0x3f48bf57,0xbf803eaf,0x3f503e97,
0x3f6cbf3c,0xbf67bccc,0x3e8b3f44,0x3f0bbf7d,0xbec7bf2e,0xbede3f80,0x3f76bf25,0xbf57be19,
0xbf003f80,0x3f80bf00,0xbf00bf00,0xbf003f80,0x3f5e0000,0xbf5e,0xbf5e3f5e,0x3f5e0000,
0xbdffbf3c,0xbf673f61,0x3f333e32,0x3eeabf7d,0xbf7e3f2e,0x3ede3ef5,0x3f37bf7c,0xbf643e19,
0x3f333d99,0x3f20bf80,0xbf5a3e32,0xbed33f75,0x3f37bf7f,0xbf483d4c,0xbf053f7c,0x3f69be97,
0xbf3c3f33,0x3f44bf29,0xbf4c3f20,0x3f54bf15,0x3f2e3f37,0xbf25bf40,0x3f1b3f48,0xbf10bf50,
0x3f54bf29,0xbf713eea,0x3f7fbe64,0xbf7dbccc,0xbf10bf40,0x3eaf3f64,0xbdccbf7a,0xbe193f80,
0xbf673f20,0x3f80be64,0xbf67be64,0x3f203f20,0x3ede3f48,0xbf7a,0xbede3f7a,0x3f48bf48,
0x3f75bf15,0xbf71bccc,0x3f0b3f20,0x3d99bf78,0xbe97bf50,0xbeaf3f80,0x3f57bf48,0xbf7f3e7d,
0xbf7d3f0b,0x3f443e8b,0xbcccbf67,0xbf3c3f6c,0x3e193f57,0x3f25bf76,0xbf803ede,0x3f2e3ec7,
0x3f80bf00,0xbf00bf00,0xbf003f80,0x3f80bf00,0xbf5e,0xbf5e3f5e,0x3f5e0000,0xbf5e,
0xbf7d3eea,0x3e323f33,0x3f61bf67,0xbf3cbdff,0xbe193f64,0x3f7cbf37,0xbef5bede,0xbf2e3f7e,
0x3f75bed3,0x3e32bf5a,0xbf803f20,0x3d993f33,0x3e97bf69,0xbf7c3f05,0xbd4c3f48,0x3f7fbf37,
0xbf5a3f0b,0x3f61bf00,0xbf673eea,0x3f6cbed3,0x3f053f57,0xbef5bf5e,0x3ede3f64,0xbec7bf69,
0x3f6c3e8b,0xbf4cbf00,0x3f203f33,0xbed3bf5a,0x3ec7bf76,0xbf1b3f5e,0x3f48bf37,0xbf693f05,
0xbe64bf67,0xbe643f80,0x3f20bf67,0xbf673f20,0xbf7a3ede,0x3f7a0000,0xbf48bede,0x3ede3f48,
0xbf293f6c,0x3f7bbf00,0xbf67bdff,0x3eea3f33,0x3f403ec7,0xbe4bbf5e,0xbede3f7e,0x3f64bf37,
0x3f7fbea3,0xbf15bf00,0xbe643f7b,0x3f61bf4c,0x3dccbf73,0xbf503f5e,0x3f7abe4b,0xbef5bf1b,
0xbf00bf00,0xbf003f80,0x3f80bf00,0xbf00bf00,0xbf5e3f5e,0x3f5e0000,0xbf5e,0xbf5e3f5e,
0xbed33f7b,0x3f7fbf00,0xbe64bf29,0xbf5a3f6c,0x3f69be4b,0x3dccbf5e,0xbf7a3f40,0x3f053ec7,
0x3f7bbf4c,0xbea3bf00,0xbf673f6c,0x3f0b3e8b,0xbe4bbf1b,0xbf733f5e,0x3ede3ec7,0x3f57bf76,
0xbf713ebb,0x3f75bea3,0xbf783e8b,0x3f7bbe64,0x3eaf3f6e,0xbe97bf73,0x3e7d3f76,0xbe4bbf7a,
0x3e323f75,0x3d99bf80,0xbea33f7b,0x3f0bbf67,0x3f7cbe97,0xbf7f3d4c,0x3f733e4b,0xbf57bede,
0x3f80be64,0xbf67be64,0x3f203f20,0xbe64bf67,0xbf7a,0xbede3f7a,0x3f48bf48,0xbf7a3ede,
0x3e32bf7d,0xbf3c3f61,0x3f7fbed3,0xbf5abe64,0xbf7c3e19,0x3f2e3ef5,0xbdccbf69,0xbf053f7a,
0xbf713d99,0x3ebb3f33,0x3eeabf80,0xbf783f20,0xbeaf3f7f,0x3f6ebf37,0xbf64bd4c,0x3e7d3f48,
0xbf003f80,0x3f80bf00,0xbf00bf00,0xbf003f80,0x3f5e0000,0xbf5e,0xbf5e3f5e,0x3f5e0000,
0x3f443d99,0x3ebbbf78,0xbf803f0b,0x3e8b3f20,0x3f25bf7f,0xbf6e3e7d,0xbd4c3f57,0x3f76bf48,
0x3f44bf7d,0xbf3cbccc,0xbf153f7f,0x3f61be64,0xbf25be19,0xbf2e3f80,0x3f50bdcc,0x3ef5bf7a,
0xbf7d3e32,0x3f7fbdff,0xbf803d99,0xbccc,0x3e193f7c,0xbdccbf7e,0x3d4c3f7f,0xbf80,
0xbf3c3f44,0x3f61bf15,0xbf783ebb,0xbdff,0x3f2e3f25,0xbef5bf50,0x3e7d3f6e,0xbf7e,
0xbe643f80,0x3f20bf67,0xbf673f20,0xbe64,0x3f7a0000,0xbf48bede,0x3ede3f48,0xbf7a,
0x3ebb3f44,0x3e8bbf80,0xbf4c3f54,0xbea3,0x3f6ebf25,0xbf763d4c,0x3f1b3f10,0xbf73,
0x3f543e32,0xbdffbf5a,0xbf293f75,0xbed3,0x3f10bf7c,0xbf7e3f05,0x3f403e97,0xbf69,
0x3f80bf00,0xbf00bf00,0xbf003f80,0xbf00,0xbf5e,0xbf5e3f5e,0x3f5e0000,0xbf5e,
0x3f54bf71,0xbf4cbccc,0xbea33f75,0xbf15,0xbf10beaf,0xbf1b3f80,0x3f73be97,0xbf50,
0x3ebbbf71,0xbf783eea,0xbdff3f54,0xbf29,0xbf6e3eaf,0xbe7d3f64,0x3f7ebf10,0xbf40,
0xbf3c3f80,0x3f203d99,0x3f54bf7d,0xbf00be64,0x3f2e0000,0x3f48bf7f,0xbf10be19,0xbf5e3f7a,
0xbf4c3f80,0x3ebb3e8b,0x3f7fbf5a,0x3e32bf3c,0x3f1b0000,0x3f6ebf76,0xbdccbf05,0xbf7c3f2e,
0xbf5a3f80,0x3d993eea,0x3f6cbf15,0x3f44bf7d,0x3f050000,0x3f7fbf64,0x3ec7bf50,0xbf253e19,
0xbf673f80,0xbe643f20,0x3f20be64,0x3f80bf67,0x3ede0000,0x3f7abf48,0x3f48bf7a,0xbede,
0xbf713f80,0xbf003f44,0x3e323e32,0x3f44bf00,0x3eaf0000,0x3f5ebf25,0x3f7cbf7c,0x3f25bf5e,
0xbf783f80,0xbf3c3f61,0xbea33f0b,0x3e323d99,0x3e7d0000,0x3f2ebef5,0x3f73bf57,0x3f7cbf7f,
0xbf7d3f80,0xbf673f75,0xbf3c3f54,0xbf003f20,0x3e190000,0x3edebe97,0x3f2ebf10,0x3f5ebf48,
0xbf803f80,0xbf7d3f7f,0xbf783f7b,0xbf713f75,0x3d4c0000,0x3e19bdcc,0x3e7dbe4b,0x3eafbe97,
0xbf673f75,0x3ebb3ebb,0x3f75bf67,0xbe64bf00,0x3ede3e97,0x3f6ebf6e,0xbe97bede,0xbf7a3f5e,
0xbf673eea,0xbf293f7b,0x3f0b3d99,0x3f75bf71,0xbede3f64,0x3f40be4b,0x3f57bf7f,0xbe97beaf,
0xbe64bea3,0xbf783f33,0xbf293f75,0x3ebb3e32,0xbf7a3f73,0xbe7d3f37,0x3f40be97,0x3f6ebf7c,
0x3f20bf67,0xbe64be64,0xbf673f20,0xbf673f80,0xbf483ede,0xbf7a3f7a,0xbede3f48,0x3ede0000,
0x3f80bf71,0x3f44bf71,0x3e32bf00,0xbf003e32,0xbeaf,0xbf253eaf,0xbf7c3f5e,0xbf5e3f7c,
0x3f20bed3,0x3f6cbf4c,0x3f7fbf7d,0x3f54bf71,0x3f48bf69,0x3ec7bf1b,0xbdccbe19,0xbf103eaf,
0xbe643ebb,0x3d993d99,0x3ebbbe64,0x3f20bf00,0x3f7abf6e,0x3f7fbf7f,0x3f6ebf7a,0x3f48bf5e,
0xbf673f6c,0xbf5a3f61,0xbf4c3f54,0xbf3c3f44,0x3edebec7,0x3f05bef5,0x3f1bbf10,0x3f2ebf25,
0xbf7d3f54,0x3d993f20,0x3f80bf3c,0x3d99bf3c,0x3e193f10,0x3f7fbf48,0xbf2e,0xbf7f3f2e,
0xbcccbf15,0xbf783f20,0xbf003f6c,0x3f33bdff,0xbf803f50,0xbe7d3f48,0x3f5ebec7,0x3f37bf7e,
0x3f7fbf4c,0x3f0bbf67,0xbf00bccc,0xbf803f61,0x3dccbf1b,0xbf573ede,0xbf5e3f80,0x3d4c3ef5,
0xbe643f20,0x3f20be64,0x3f80bf67,0x3f20bf67,0x3f7abf48,0x3f48bf7a,0xbede,0xbf483ede,
0xbf713f44,0xbf713f80,0xbf003f44,0x3e323e32,0xbeaf3f25,0x3eaf0000,0x3f5ebf25,0x3f7cbf7c,
0x3eeabf29,0xbcccbe64,0xbf003e8b,0xbf5a3f33,0xbf643f40,0xbf803f7a,0xbf5e3f76,0xbf053f37,
0x3f54bf3c,0x3f75bf67,0x3f80bf7d,0x3f75bf7d,0x3f10bf2e,0x3e97bede,0xbe19,0xbe973e19,
0xbf293f33,0xbf153f20,0xbf003f0b,0xbed33eea,0x3f40bf37,0x3f50bf48,0x3f5ebf57,0x3f69bf64,
0xbf7d3f20,0xbe643f54,0x3f75bf00,0x3ebbbf67,0xbe193f48,0x3f7abf10,0x3e97bf5e,0xbf6e3ede,
0x3f61bf7d,0xbe64bed3,0xbf803f44,0xbea33f54,0xbef5be19,0xbf7a3f69,0xbd4c3f25,0x3f73bf10,
0xbed33f54,0x3f20bdff,0x3f7bbf71,0x3e8bbf3c,0x3f69bf10,0x3f48bf7e,0xbe4bbeaf,0xbf763f2e,
0xbe64be64,0xbf673f20,0xbf673f80,0xbe643f20,0xbf7a3f7a,0xbede3f48,0x3ede0000,0x3f7abf48,
0x3f44bf00,0x3f80bf71,0x3f44bf71,0x3e32bf00,0x3f25bf5e,0xbeaf,0xbf253eaf,0xbf7c3f5e,
0xbf803f75,0xbf673f7b,0xbf153f44,0xbdff3ebb,0xbd4c3e97,0x3edebe4b,0x3f50bf25,0x3f7ebf6e,
0x3f54bf67,0x3f20bf3c,0x3ebbbf00,0x3d99be64,0xbf103ede,0xbf483f2e,0xbf6e3f5e,0xbf7f3f7a,
0xbea33ebb,0xbe643e8b,0xbdff3e32,0xbccc3d99,0x3f73bf6e,0x3f7abf76,0x3f7ebf7c,0x3f80bf7f,
0xbf673ebb,0xbf003f75,0x3f54be64,0x3f20bf7d,0xbede3f6e,0x3f5ebe97,0x3f10bf7a,0xbf483e19,
0x3f54bea3,0x3f44bf80,0xbed3be64,0xbf7d3f61,0x3f10bf73,0xbf253d4c,0xbf693f7a,0x3e193ef5,
0xbf3c3e8b,0xbf713f7b,0xbdff3f20,0x3f54bed3,0xbf2e3f76,0x3eaf3e4b,0x3f7ebf48,0x3f10bf69,
0x3f20be64,0x3f80bf67,0x3f20bf67,0xbe64be64,0x3f48bf7a,0xbede,0xbf483ede,0xbf7a3f7a,
0xbf003e32,0xbf713f44,0xbf713f80,0xbf003f44,0xbf5e3f7c,0xbeaf3f25,0x3eaf0000,0x3f5ebf25,
0x3ebbbdff,0x3f44bf15,0x3f7bbf67,0x3f75bf80,0x3f6ebf7e,0x3f25bf50,0x3e4bbede,0xbe973d4c,
0xbe643d99,0xbf003ebb,0xbf3c3f20,0xbf673f54,0xbf7a3f7f,0xbf5e3f6e,0xbf2e3f48,0xbede3f10,
0x3d99bccc,0x3e32bdff,0x3e8bbe64,0x3ebbbea3,0x3f7fbf80,0x3f7cbf7e,0x3f76bf7a,0x3f6ebf73,
0xbf3c3d99,0xbf3c3f80,0x3f203d99,0x3f54bf7d,0xbf2e3f7f,0x3f2e0000,0x3f48bf7f,0xbf10be19,
0xbdff3f33,0x3f6cbf00,0x3f20bf78,0xbf15bccc,0x3f7ebf37,0x3ec7bf5e,0xbf483e7d,0xbf503f80,
0x3f61bf80,0xbcccbf00,0xbf673f0b,0xbf4c3f7f,0xbef5bd4c,0xbf803f5e,0xbede3f57,0x3f1bbdcc,
0xbf673f20,0xbf673f80,0xbe643f20,0x3f20be64,0xbede3f48,0x3ede0000,0x3f7abf48,0x3f48bf7a,
0x3e323e32,0x3f44bf00,0x3f80bf71,0x3f44bf71,0x3f7cbf7c,0x3f25bf5e,0xbeaf,0xbf253eaf,
0x3f33bf5a,0x3e8bbf00,0xbe64bccc,0xbf293eea,0xbf373f05,0xbf763f5e,0xbf7a3f80,0xbf403f64,
0xbf7d3f75,0xbf7d3f80,0xbf673f75,0xbf3c3f54,0xbe193e97,0x3e190000,0x3edebe97,0x3f2ebf10,
0x3eeabed3,0x3f0bbf00,0x3f20bf15,0x3f33bf29,0x3f64bf69,0x3f57bf5e,0x3f48bf50,0x3f37bf40,
0xbf00be64,0xbf673f75,0x3ebb3ebb,0x3f75bf67,0xbf5e3f7a,0x3ede3e97,0x3f6ebf6e,0xbe97bede,
0xbf713f75,0x3d993f0b,0x3f7bbf29,0x3eeabf67,0x3eaf3e97,0x3f7fbf57,0x3e4bbf40,0xbf643ede,
0x3e323ebb,0x3f75bf29,0x3f33bf78,0xbea3be64,0x3f7cbf6e,0x3e97bf40,0xbf373e7d,0xbf733f7a,
0x3f80bf67,0x3f20bf67,0xbe64be64,0xbf673f20,0xbede,0xbf483ede,0xbf7a3f7a,0xbede3f48,
0x3e32bf00,0xbf003e32,0xbf713f44,0xbf713f80,0xbf7c3f5e,0xbf5e3f7c,0xbeaf3f25,0x3eaf0000,
0xbf713f54,0xbf7d3f7f,0xbf4c3f6c,0xbed33f20,0xbeaf3f10,0x3e193dcc,0x3f1bbec7,0x3f69bf48,
0xbf003f20,0xbe643ebb,0x3d993d99,0x3ebbbe64,0x3f5ebf48,0x3f7abf6e,0x3f7fbf7f,0x3f6ebf7a,
0x3f44bf3c,0x3f54bf4c,0x3f61bf5a,0x3f6cbf67,0x3f25bf2e,0x3f10bf1b,0x3ef5bf05,0x3ec7bede,
0xbe64bf00,0xbf7d3f54,0x3d993f20,0xbf3c,0xbf7a3f5e,0x3e193f10,0x3f7fbf48,0xbf2e,
0xbf3c3e32,0xbf5a3f7f,0x3e8b3ebb,0xbf4c,0xbf2e3f7c,0x3f053dcc,0x3f76bf6e,0xbf1b,
0xbf7d3f44,0xbf153f6c,0x3eea3d99,0xbf5a,0xbe193f25,0x3f50bec7,0x3f64bf7f,0xbf05,
0xbf673f80,0xbe643f20,0x3f20be64,0xbf67,0x3ede0000,0x3f7abf48,0x3f48bf7a,0xbede,
0xbf003f44,0x3e323e32,0x3f44bf00,0xbf71,0x3f5ebf25,0x3f7cbf7c,0x3f25bf5e,0xbeaf,
0x3d993e32,0x3f0bbea3,0x3f61bf3c,0xbf78,0x3f7fbf7c,0x3f57bf73,0x3ef5bf2e,0xbe7d,
0x3f20bf00,0x3f54bf3c,0x3f75bf67,0xbf7d,0x3f48bf5e,0x3f10bf2e,0x3e97bede,0xbe19,
0x3f75bf71,0x3f7bbf78,0x3f7fbf7d,0xbf80,0x3e97beaf,0x3e4bbe7d,0x3dccbe19,0xbd4c,
0x3bf4,0x3c74,0x3c74,0x3c74,0x3bf4,0x3ac23c73,0x3b413c6f,0x3b903c69,
0x3bf4,0x3b413c6f,0x3bbd3c61,0x3c093c49,0x3bf4,0x3b903c69,0x3c093c49,0x3c3f3c18,
0x3bf4,0x3bbd3c61,0x3c2f3c2a,0x3c633bb2,0x3bf4,0x3be93c56,0x3c4d3c04,0x3c733a92,
0x3bf4,0x3c093c49,0x3c633bb2,0x3c6ebb59,0x3bf4,0x3c1d3c3b,0x3c703b29,0x3c53bbf4,
0x3bf4,0x3c2f3c2a,0x3c74b9c2,0x3c26bc33,0x3bf4,0x3c3f3c18,0x3c6ebb59,0x3bd4bc5c,
0x3bf4,0x3c4d3c04,0x3c5ebbc9,0x3b11bc71,0x3bf4,0x3c593bde,0x3c46bc0e,0xbb11bc71,
0x3bf4,0x3c633bb2,0x3c26bc33,0xbbd4bc5c,0x3bf4,0x3c6b3b84,0x3bfebc50,0xbc26bc33,
0x3bf4,0x3c703b29,0x3ba7bc65,0xbc53bbf4,0x3bf4,0x3c733a92,0x3b11bc71,0xbc6ebb59,
0x3c74,0x3c74,0x3c74,0x3c74,0x3bbd3c61,0x3be93c56,0x3c093c49,0x3c1d3c3b,
0x3c2f3c2a,0x3c4d3c04,0x3c633bb2,0x3c703b29,0x3c633bb2,0x3c733a92,0x3c6ebb59,0x3c53bbf4,
0x3c74b9c2,0x3c5ebbc9,0x3c26bc33,0x3ba7bc65,0x3c5ebbc9,0x3c13bc42,0x3b11bc71,0xbba7bc65,
0x3c26bc33,0x3b11bc71,0xbbd4bc5c,0xbc53bbf4,0x3ba7bc65,0xbba7bc65,0xbc53bbf4,0xbc703b29,
0xba42bc73,0xbc37bc21,0xbc733a92,0xbc1d3c3b,0xbbd4bc5c,0xbc6ebb59,0xbc3f3c18,0x3c74,
0xbc37bc21,0xbc6b3b84,0xbb903c69,0x3c1d3c3b,0xbc67bb9b,0xbc2f3c2a,0x3b903c69,0x3c703b29,
0xbc733a92,0xbb903c69,0x3c3f3c18,0x3c53bbf4,0xbc593bde,0x3b413c6f,0x3c733a92,0x3ba7bc65,
0xbc1d3c3b,0x3c1d3c3b,0x3c53bbf4,0xbba7bc65,0xbb903c69,0x3c633bb2,0x3bd4bc5c,0xbc53bbf4,
0x3c74,0x3c74,0x3c74,0x3c74,0x3c2f3c2a,0x3c3f3c18,0x3c4d3c04,0x3c593bde,
0x3c74b9c2,0x3c6ebb59,0x3c5ebbc9,0x3c46bc0e,0x3c26bc33,0x3bd4bc5c,0x3b11bc71,0xbb11bc71,
0xba42bc73,0xbbd4bc5c,0xbc37bc21,0xbc67bb9b,0xbc37bc21,0xbc6ebb59,0xbc6b3b84,0xbc2f3c2a,
0xbc733a92,0xbc3f3c18,0xbb903c69,0x3b903c69,0xbc1d3c3b,0x3c74,0x3c1d3c3b,0x3c703b29,
0x3ac23c73,0x3c3f3c18,0x3c72baf2,0x3c13bc42,0x3c3f3c18,0x3c6ebb59,0x3bd4bc5c,0xbbd4bc5c,
0x3c72baf2,0x3bd4bc5c,0xbbfebc50,0xbc74b9c2,0x3c13bc42,0xbbd4bc5c,0xbc74b9c2,0xbbe93c56,
0xbb11bc71,0xbc6ebb59,0xbc093c49,0x3c093c49,0xbc46bc0e,0xbc3f3c18,0x3bbd3c61,0x3c72baf2,
0xbc703b29,0x3c74,0x3c703b29,0x3ba7bc65,0xbc093c49,0x3c3f3c18,0x3c26bc33,0xbc26bc33,
0x3c74,0x3c74,0x3c74,0x3c74,0x3c633bb2,0x3c6b3b84,0x3c703b29,0x3c733a92,
0x3c26bc33,0x3bfebc50,0x3ba7bc65,0x3b11bc71,0xbbd4bc5c,0xbc26bc33,0xbc53bbf4,0xbc6ebb59,
0xbc733a92,0xbc593bde,0xbc1d3c3b,0xbb903c69,0xbb903c69,0x3b413c6f,0x3c1d3c3b,0x3c633bb2,
0x3c3f3c18,0x3c733a92,0x3c53bbf4,0x3bd4bc5c,0x3c53bbf4,0x3ba7bc65,0xbba7bc65,0xbc53bbf4,
0xbb11bc71,0xbc46bc0e,0xbc703b29,0xbc093c49,0xbc6ebb59,0xbc3f3c18,0x3c74,0x3c3f3c18,
0xbc093c49,0x3bbd3c61,0x3c703b29,0x3c26bc33,0x3c093c49,0x3c72baf2,0x3ba7bc65,0xbc26bc33,
0x3c6ebb59,0x3b11bc71,0xbc53bbf4,0xbc3f3c18,0x3b11bc71,0xbc5ebbc9,0xbc1d3c3b,0x3c093c49,
0xbc53bbf4,0xbc1d3c3b,0x3c1d3c3b,0x3c53bbf4,0xbc3f3c18,0x3c093c49,0x3c53bbf4,0xbbd4bc5c,
0x3c74,0x3c74,0x3c74,0x3c74,0x3c74b9c2,0x3c72baf2,0x3c6ebb59,0x3c67bb9b,
0xba42bc73,0xbb71bc6c,0xbbd4bc5c,0xbc13bc42,0xbc733a92,0xbc633bb2,0xbc3f3c18,0xbc093c49,
0x3ac23c73,0x3be93c56,0x3c3f3c18,0x3c6b3b84,0x3c72baf2,0x3c46bc0e,0x3bd4bc5c,0xba42bc73,
0xbb11bc71,0xbc26bc33,0xbc6ebb59,0xbc633bb2,0xbc703b29,0xbc1d3c3b,0x3c74,0x3c1d3c3b,
0x3b413c6f,0x3c4d3c04,0x3c6ebb59,0x3bfebc50,0x3c6ebb59,0x3bd4bc5c,0xbbd4bc5c,0xbc6ebb59,
0xbb71bc6c,0xbc67bb9b,0xbc3f3c18,0x3ac23c73,0xbc6b3b84,0xbb413c6f,0x3c3f3c18,0x3c5ebbc9,
0x3b903c69,0x3c733a92,0x3bd4bc5c,0xbc26bc33,0x3c67bb9b,0xba42bc73,0xbc6ebb59,0xbbe93c56,
0xbba7bc65,0xbc703b29,0x3c74,0x3c703b29,0xbc633bb2,0x3b903c69,0x3c6ebb59,0xbb11bc71,
0x3c74,0x3c74,0x3c74,0x3c74,0x3c5ebbc9,0x3c53bbf4,0x3c46bc0e,0x3c37bc21,
0xbc37bc21,0xbc53bbf4,0xbc67bb9b,0xbc72baf2,0xbb903c69,0x3c74,0x3b903c69,0x3c093c49,
0x3c72baf2,0x3c53bbf4,0x3c13bc42,0x3b71bc6c,0xbbfebc50,0xbc53bbf4,0xbc74b9c2,0xbc593bde,
0xbc093c49,0x3c74,0x3c093c49,0x3c633bb2,0x3c703b29,0x3c53bbf4,0x3ba7bc65,0xbba7bc65,
0xbb71bc6c,0xbc53bbf4,0xbc6b3b84,0xbbe93c56,0xbc3f3c18,0x3c74,0x3c3f3c18,0x3c6ebb59,
0x3c593bde,0x3c53bbf4,0x3a42bc73,0xbc46bc0e,0x3a42bc73,0xbc53bbf4,0xbc4d3c04,0x3ac23c73,
0xbc633bb2,0x3c74,0x3c633bb2,0x3c26bc33,0x3c2f3c2a,0x3c53bbf4,0xbb71bc6c,0xbc74b9c2,
0x3ba7bc65,0xbc53bbf4,0xbc1d3c3b,0x3c1d3c3b,0xbc733a92,0x3c74,0x3c733a92,0x3b11bc71,
0x3c74,0x3c74,0x3c74,0x3c74,0x3c26bc33,0x3c13bc42,0x3bfebc50,0x3bd4bc5c,
0xbc733a92,0xbc6b3b84,0xbc593bde,0xbc3f3c18,0x3c3f3c18,0x3c633bb2,0x3c733a92,0x3c6ebb59,
0xbb11bc71,0xbbfebc50,0xbc46bc0e,0xbc6ebb59,0xbc093c49,0xbac23c73,0x3bbd3c61,0x3c3f3c18,
0x3c6ebb59,0x3c26bc33,0x3b11bc71,0xbbd4bc5c,0xbc53bbf4,0xbc703b29,0xbc1d3c3b,0x3c74,
0x3b903c69,0x3c593bde,0x3c67bb9b,0x3bd4bc5c,0x3bd4bc5c,0xbbd4bc5c,0xbc6ebb59,0xbc3f3c18,
0xbc633bb2,0xbb413c6f,0x3c2f3c2a,0x3c6ebb59,0x3c633bb2,0x3c37bc21,0xbb71bc6c,0xbc6ebb59,
0xbbd4bc5c,0xbc733a92,0xbb903c69,0x3c3f3c18,0xbb903c69,0x3c4d3c04,0x3c37bc21,0xbbd4bc5c,
0x3c53bbf4,0xbba7bc65,0xbc703b29,0x3c74,0xbc6ebb59,0xbb903c69,0x3c633bb2,0x3bd4bc5c,
0x3c74,0x3c74,0x3c74,0x3c74,0x3ba7bc65,0x3b71bc6c,0x3b11bc71,0x3a42bc73,
0xbc1d3c3b,0xbbe93c56,0xbb903c69,0xbac23c73,0x3c53bbf4,0x3c26bc33,0x3bd4bc5c,0x3b11bc71,
0xbc703b29,0xbc4d3c04,0xbc093c49,0xbb413c6f,0x3c703b29,0x3c67bb9b,0x3c26bc33,0x3b71bc6c,
0xbc53bbf4,0xbc733a92,0xbc3f3c18,0xbb903c69,0x3c1d3c3b,0x3c703b29,0x3c53bbf4,0x3ba7bc65,
0xbba7bc65,0xbc5ebbc9,0xbc633bb2,0xbbbd3c61,0x3c74,0x3c3f3c18,0x3c6ebb59,0x3bd4bc5c,
0x3ba7bc65,0xbc13bc42,0xbc733a92,0xbbe93c56,0xbc1d3c3b,0x3bbd3c61,0x3c733a92,0x3bfebc50,
0x3c53bbf4,0xbb11bc71,0xbc6ebb59,0xbc093c49,0xbc703b29,0xbac23c73,0x3c633bb2,0x3c13bc42,
0x3c703b29,0x3ba7bc65,0xbc53bbf4,0xbc1d3c3b,0xbc53bbf4,0xbc093c49,0x3c3f3c18,0x3c26bc33,
0x3bf4,0x3c74b9c2,0xba42bc73,0xbc733a92,0x3bf4,0x3c72baf2,0xbb71bc6c,0xbc633bb2,
0x3bf4,0x3c6ebb59,0xbbd4bc5c,0xbc3f3c18,0x3bf4,0x3c67bb9b,0xbc13bc42,0xbc093c49,
0x3bf4,0x3c5ebbc9,0xbc37bc21,0xbb903c69,0x3bf4,0x3c53bbf4,0xbc53bbf4,0x3c74,
0x3bf4,0x3c46bc0e,0xbc67bb9b,0x3b903c69,0x3bf4,0x3c37bc21,0xbc72baf2,0x3c093c49,
0x3bf4,0x3c26bc33,0xbc733a92,0x3c3f3c18,0x3bf4,0x3c13bc42,0xbc6b3b84,0x3c633bb2,
0x3bf4,0x3bfebc50,0xbc593bde,0x3c733a92,0x3bf4,0x3bd4bc5c,0xbc3f3c18,0x3c6ebb59,
0x3bf4,0x3ba7bc65,0xbc1d3c3b,0x3c53bbf4,0x3bf4,0x3b71bc6c,0xbbe93c56,0x3c26bc33,
0x3bf4,0x3b11bc71,0xbb903c69,0x3bd4bc5c,0x3bf4,0x3a42bc73,0xbac23c73,0x3b11bc71,
0x3ac23c73,0x3c72baf2,0xbb11bc71,0xbc703b29,0x3be93c56,0x3c46bc0e,0xbc26bc33,0xbc1d3c3b,
0x3c3f3c18,0x3bd4bc5c,0xbc6ebb59,0x3c74,0x3c6b3b84,0xba42bc73,0xbc633bb2,0x3c1d3c3b,
0x3c72baf2,0xbbfebc50,0xbc093c49,0x3c703b29,0x3c53bbf4,0xbc53bbf4,0x3c74,0x3c53bbf4,
0x3c13bc42,0xbc74b9c2,0x3c093c49,0x3ba7bc65,0x3b71bc6c,0xbc593bde,0x3c633bb2,0xbba7bc65,
0xbb11bc71,0xbc093c49,0x3c6ebb59,0xbc53bbf4,0xbbfebc50,0xbac23c73,0x3c26bc33,0xbc703b29,
0xbc46bc0e,0x3bbd3c61,0x3b11bc71,0xbc1d3c3b,0xbc6ebb59,0x3c3f3c18,0xbbd4bc5c,0x3c74,
0xbc703b29,0x3c703b29,0xbc53bbf4,0x3c1d3c3b,0xbc4d3c04,0x3c67bb9b,0xbc733a92,0x3c703b29,
0xbc093c49,0x3c26bc33,0xbc3f3c18,0x3c53bbf4,0xbb413c6f,0x3b71bc6c,0xbb903c69,0x3ba7bc65,
0x3b413c6f,0x3c6ebb59,0xbb71bc6c,0xbc6b3b84,0x3c4d3c04,0x3bd4bc5c,0xbc67bb9b,0xbb413c6f,
0x3c6ebb59,0xbbd4bc5c,0xbc3f3c18,0x3c3f3c18,0x3bfebc50,0xbc6ebb59,0x3ac23c73,0x3c5ebbc9,
0xbb71bc6c,0xbc3f3c18,0x3c593bde,0x3a42bc73,0xbc53bbf4,0x3c74,0x3c53bbf4,0xbc53bbf4,
0xbc6b3b84,0x3c3f3c18,0x3a42bc73,0xbc4d3c04,0xbbe93c56,0x3c6ebb59,0xbc46bc0e,0x3ac23c73,
0x3b903c69,0x3bd4bc5c,0xbc633bb2,0x3c633bb2,0x3c593bde,0xbbd4bc5c,0xbb413c6f,0x3c37bc21,
0x3c67bb9b,0xbc6ebb59,0x3c2f3c2a,0xbb71bc6c,0x3bd4bc5c,0xbc3f3c18,0x3c6ebb59,0xbc6ebb59,
0xbba7bc65,0x3c74,0x3ba7bc65,0xbc1d3c3b,0xbc5ebbc9,0x3c3f3c18,0xbc13bc42,0x3bbd3c61,
0xbc633bb2,0x3c6ebb59,0xbc733a92,0x3c733a92,0xbbbd3c61,0x3bd4bc5c,0xbbe93c56,0x3bfebc50,
0x3b903c69,0x3c67bb9b,0xbba7bc65,0xbc633bb2,0x3c733a92,0xba42bc73,0xbc703b29,0x3b903c69,
0x3bd4bc5c,0xbc6ebb59,0x3c74,0x3c6ebb59,0xbc26bc33,0xbbe93c56,0x3c703b29,0xbb11bc71,
0xbc633bb2,0x3c2f3c2a,0x3ba7bc65,0xbc733a92,0x3c74,0x3c53bbf4,0xbc53bbf4,0x3c74,
0x3c633bb2,0xbb71bc6c,0xbc1d3c3b,0x3c733a92,0x3c26bc33,0xbc74b9c2,0x3c1d3c3b,0x3b11bc71,
0xbbd4bc5c,0xbb903c69,0x3c53bbf4,0xbc6ebb59,0xbc733a92,0x3c4d3c04,0xbba7bc65,0xbb903c69,
0xbb903c69,0x3c37bc21,0xbc703b29,0x3c633bb2,0x3c3f3c18,0xbbd4bc5c,0x3c74,0x3bd4bc5c,
0x3c53bbf4,0xbc703b29,0x3c703b29,0xbc53bbf4,0xbb11bc71,0xbac23c73,0x3ba7bc65,0xbc093c49,
0xbc6ebb59,0x3c633bb2,0xbc53bbf4,0x3c3f3c18,0xbc093c49,0x3c13bc42,0xbc1d3c3b,0x3c26bc33,
0x3bbd3c61,0x3c5ebbc9,0xbbd4bc5c,0xbc593bde,0x3c5ebbc9,0xbbfebc50,0xbc3f3c18,0x3c2f3c2a,
0xbbd4bc5c,0xbc3f3c18,0x3c3f3c18,0x3bd4bc5c,0xbc593bde,0x3c2f3c2a,0x3bd4bc5c,0xbc72baf2,
0x3be93c56,0x3c13bc42,0xbc6ebb59,0x3b413c6f,0x3c53bbf4,0xbc53bbf4,0x3c74,0x3c53bbf4,
0xbbfebc50,0xbbbd3c61,0x3c6ebb59,0xbc37bc21,0xbc4d3c04,0x3c6b3b84,0xbbd4bc5c,0xbbbd3c61,
0x3c093c49,0x3b11bc71,0xbc3f3c18,0x3c733a92,0x3c46bc0e,0xbc74b9c2,0x3c3f3c18,0xbb71bc6c,
0xbc13bc42,0x3ac23c73,0x3bd4bc5c,0xbc4d3c04,0xbc3f3c18,0x3c6ebb59,0xbc6ebb59,0x3c3f3c18,
0x3c1d3c3b,0xbba7bc65,0x3c74,0x3ba7bc65,0x3c37bc21,0xbc593bde,0x3c6ebb59,0xbc74b9c2,
0xbc26bc33,0x3c093c49,0xbbd4bc5c,0x3b903c69,0xbc2f3c2a,0x3c37bc21,0xbc3f3c18,0x3c46bc0e,
0x3be93c56,0x3c53bbf4,0xbbfebc50,0xbc4d3c04,0x3c13bc42,0xbc53bbf4,0xbbbd3c61,0x3c6b3b84,
0xbc6ebb59,0x3c74,0x3c6ebb59,0xbbd4bc5c,0x3b413c6f,0x3c53bbf4,0xbc37bc21,0xbbbd3c61,
0x3c46bc0e,0xbc53bbf4,0xbac23c73,0x3c67bb9b,0xbc53bbf4,0x3c74,0x3c53bbf4,0xbc53bbf4,
0xbac23c73,0x3c53bbf4,0xbc5ebbc9,0x3b413c6f,0x3c67bb9b,0xbc53bbf4,0x3b413c6f,0x3c13bc42,
0xbc26bc33,0x3c74,0x3c26bc33,0xbc733a92,0xbbbd3c61,0x3c53bbf4,0xbc72baf2,0x3c2f3c2a,
0x3c74b9c2,0xbc53bbf4,0x3be93c56,0x3a42bc73,0xbbd4bc5c,0x3c74,0x3bd4bc5c,0xbc3f3c18,
0xbc1d3c3b,0x3c53bbf4,0xbc703b29,0x3c703b29,0x3c6b3b84,0xbc53bbf4,0x3c2f3c2a,0xbbfebc50,
0xbb11bc71,0x3c74,0x3b11bc71,0xbb903c69,0xbc4d3c04,0x3c53bbf4,0xbc593bde,0x3c5ebbc9,
0x3c093c49,0x3c46bc0e,0xbc13bc42,0xbc3f3c18,0x3b11bc71,0xbc74b9c2,0x3ac23c73,0x3c6ebb59,
0xbc3f3c18,0x3c3f3c18,0x3bd4bc5c,0xbc6ebb59,0x3c733a92,0xbb71bc6c,0xbc4d3c04,0x3c3f3c18,
0xbc26bc33,0xbbbd3c61,0x3c74b9c2,0xbbd4bc5c,0x3c74,0x3c53bbf4,0xbc53bbf4,0x3c74,
0x3c26bc33,0xbc72baf2,0x3be93c56,0x3bd4bc5c,0xbc733a92,0x3c2f3c2a,0x3a42bc73,0xbc3f3c18,
0x3c3f3c18,0xbb11bc71,0xbc093c49,0x3c6ebb59,0xbb11bc71,0xbbe93c56,0x3c5ebbc9,0xbc6ebb59,
0xbc093c49,0x3c5ebbc9,0xbc72baf2,0x3c3f3c18,0x3c6ebb59,0xbc6ebb59,0x3c3f3c18,0xbbd4bc5c,
0xbc53bbf4,0x3c1d3c3b,0xbba7bc65,0x3c74,0x3b903c69,0xba42bc73,0xbb413c6f,0x3bd4bc5c,
0x3bd4bc5c,0xbc093c49,0x3c26bc33,0xbc3f3c18,0xbc633bb2,0x3c67bb9b,0xbc6b3b84,0x3c6ebb59,
0x3c1d3c3b,0x3c37bc21,0xbc26bc33,0xbc2f3c2a,0xbba7bc65,0xbc593bde,0x3c093c49,0x3c37bc21,
0x3c74,0x3c6ebb59,0xbbd4bc5c,0xbc3f3c18,0x3ba7bc65,0xbc74b9c2,0x3b903c69,0x3c46bc0e,
0xbc1d3c3b,0x3c6b3b84,0xbb11bc71,0xbc4d3c04,0x3c53bbf4,0xbc53bbf4,0x3c74,0x3c53bbf4,
0xbc703b29,0x3c2f3c2a,0x3b11bc71,0xbc593bde,0x3c703b29,0xbbfebc50,0xbb903c69,0x3c5ebbc9,
0xbc53bbf4,0x3b903c69,0x3bd4bc5c,0xbc633bb2,0x3c1d3c3b,0xba42bc73,0xbc093c49,0x3c67bb9b,
0xbba7bc65,0xbb413c6f,0x3c26bc33,0xbc6b3b84,0x3c74,0x3bd4bc5c,0xbc3f3c18,0x3c6ebb59,
0x3ba7bc65,0xbc1d3c3b,0x3c53bbf4,0xbc703b29,0xbc1d3c3b,0x3c46bc0e,0xbc633bb2,0x3c72baf2,
0x3c53bbf4,0xbc633bb2,0x3c6ebb59,0xbc733a92,0xbc703b29,0x3c72baf2,0xbc733a92,0x3c74b9c2,
0x3bf4,0xba42bc73,0x3ac23c73,0xbb11bc71,0x3bf4,0xbb11bc71,0x3b903c69,0xbbd4bc5c,
0x3bf4,0xbb71bc6c,0x3be93c56,0xbc26bc33,0x3bf4,0xbba7bc65,0x3c1d3c3b,0xbc53bbf4,
0x3bf4,0xbbd4bc5c,0x3c3f3c18,0xbc6ebb59,0x3bf4,0xbbfebc50,0x3c593bde,0xbc733a92,
0x3bf4,0xbc13bc42,0x3c6b3b84,0xbc633bb2,0x3bf4,0xbc26bc33,0x3c733a92,0xbc3f3c18,
0x3bf4,0xbc37bc21,0x3c72baf2,0xbc093c49,0x3bf4,0xbc46bc0e,0x3c67bb9b,0xbb903c69,
0x3bf4,0xbc53bbf4,0x3c53bbf4,0x3c74,0x3bf4,0xbc5ebbc9,0x3c37bc21,0x3b903c69,
0x3bf4,0xbc67bb9b,0x3c13bc42,0x3c093c49,0x3bf4,0xbc6ebb59,0x3bd4bc5c,0x3c3f3c18,
0x3bf4,0xbc72baf2,0x3b71bc6c,0x3c633bb2,0x3bf4,0xbc74b9c2,0x3a42bc73,0x3c733a92,
0x3b413c6f,0xbb71bc6c,0x3b903c69,0xbba7bc65,0x3c093c49,0xbc26bc33,0x3c3f3c18,0xbc53bbf4,
0x3c4d3c04,0xbc67bb9b,0x3c733a92,0xbc703b29,0x3c703b29,0xbc703b29,0x3c53bbf4,0xbc1d3c3b,
0x3c6ebb59,0xbc3f3c18,0x3bd4bc5c,0x3c74,0x3c46bc0e,0xbbbd3c61,0xbb11bc71,0x3c1d3c3b,
0x3bfebc50,0x3ac23c73,0xbc26bc33,0x3c703b29,0x3b11bc71,0x3c093c49,0xbc6ebb59,0x3c53bbf4,
0xbb71bc6c,0x3c593bde,0xbc633bb2,0x3ba7bc65,0xbc13bc42,0x3c74b9c2,0xbc093c49,0xbba7bc65,
0xbc53bbf4,0x3c53bbf4,0x3c74,0xbc53bbf4,0xbc72baf2,0x3bfebc50,0x3c093c49,0xbc703b29,
0xbc6b3b84,0x3a42bc73,0x3c633bb2,0xbc1d3c3b,0xbc3f3c18,0xbbd4bc5c,0x3c6ebb59,0x3c74,
0xbbe93c56,0xbc46bc0e,0x3c26bc33,0x3c1d3c3b,0xbac23c73,0xbc72baf2,0x3b11bc71,0x3c703b29,
0x3bbd3c61,0xbbd4bc5c,0x3be93c56,0xbbfebc50,0x3c633bb2,0xbc6ebb59,0x3c733a92,0xbc733a92,
0x3c5ebbc9,0xbc3f3c18,0x3c13bc42,0xbbbd3c61,0x3ba7bc65,0x3c74,0xbba7bc65,0x3c1d3c3b,
0xbbd4bc5c,0x3c3f3c18,0xbc6ebb59,0x3c6ebb59,0xbc67bb9b,0x3c6ebb59,0xbc2f3c2a,0x3b71bc6c,
0xbc593bde,0x3bd4bc5c,0x3b413c6f,0xbc37bc21,0xbb903c69,0xbbd4bc5c,0x3c633bb2,0xbc633bb2,
0x3be93c56,0xbc6ebb59,0x3c46bc0e,0xbac23c73,0x3c6b3b84,0xbc3f3c18,0xba42bc73,0x3c4d3c04,
0x3c53bbf4,0x3c74,0xbc53bbf4,0x3c53bbf4,0x3b71bc6c,0x3c3f3c18,0xbc593bde,0xba42bc73,
0xbbfebc50,0x3c6ebb59,0xbac23c73,0xbc5ebbc9,0xbc6ebb59,0x3bd4bc5c,0x3c3f3c18,0xbc3f3c18,
0xbc4d3c04,0xbbd4bc5c,0x3c67bb9b,0x3b413c6f,0xbb413c6f,0xbc6ebb59,0x3b71bc6c,0x3c6b3b84,
0x3c093c49,0xbc13bc42,0x3c1d3c3b,0xbc26bc33,0x3c6ebb59,0xbc633bb2,0x3c53bbf4,0xbc3f3c18,
0x3b11bc71,0x3ac23c73,0xbba7bc65,0x3c093c49,0xbc53bbf4,0x3c703b29,0xbc703b29,0x3c53bbf4,
0xbc3f3c18,0x3bd4bc5c,0x3c74,0xbbd4bc5c,0x3b903c69,0xbc37bc21,0x3c703b29,0xbc633bb2,
0x3c733a92,0xbc4d3c04,0x3ba7bc65,0x3b903c69,0x3bd4bc5c,0x3b903c69,0xbc53bbf4,0x3c6ebb59,
0xbc26bc33,0x3c74b9c2,0xbc1d3c3b,0xbb11bc71,0xbc633bb2,0x3b71bc6c,0x3c1d3c3b,0xbc733a92,
0x3c74,0xbc53bbf4,0x3c53bbf4,0x3c74,0x3c633bb2,0xbc2f3c2a,0xbba7bc65,0x3c733a92,
0x3c26bc33,0x3be93c56,0xbc703b29,0x3b11bc71,0xbbd4bc5c,0x3c6ebb59,0x3c74,0xbc6ebb59,
0xbc733a92,0x3a42bc73,0x3c703b29,0xbb903c69,0xbb903c69,0xbc67bb9b,0x3ba7bc65,0x3c633bb2,
0x3c2f3c2a,0xbc37bc21,0x3c3f3c18,0xbc46bc0e,0x3c26bc33,0xbc093c49,0x3bd4bc5c,0xbb903c69,
0xbc37bc21,0x3c593bde,0xbc6ebb59,0x3c74b9c2,0xbc1d3c3b,0x3ba7bc65,0x3c74,0xbba7bc65,
0x3c3f3c18,0xbc6ebb59,0x3c6ebb59,0xbc3f3c18,0x3c13bc42,0xbac23c73,0xbbd4bc5c,0x3c4d3c04,
0xbc46bc0e,0x3c74b9c2,0xbc3f3c18,0x3b71bc6c,0xbc093c49,0xbb11bc71,0x3c3f3c18,0xbc733a92,
0x3c4d3c04,0xbc6b3b84,0x3bd4bc5c,0x3bbd3c61,0x3bfebc50,0x3bbd3c61,0xbc6ebb59,0x3c37bc21,
0xbc53bbf4,0x3c53bbf4,0x3c74,0xbc53bbf4,0xbbe93c56,0xbc13bc42,0x3c6ebb59,0xbb413c6f,
0x3c593bde,0xbc2f3c2a,0xbbd4bc5c,0x3c72baf2,0x3bd4bc5c,0x3c3f3c18,0xbc3f3c18,0xbbd4bc5c,
0xbc5ebbc9,0x3bfebc50,0x3c3f3c18,0xbc2f3c2a,0xbbbd3c61,0xbc5ebbc9,0x3bd4bc5c,0x3c593bde,
0x3c4d3c04,0xbc53bbf4,0x3c593bde,0xbc5ebbc9,0x3b11bc71,0x3c74,0xbb11bc71,0x3b903c69,
0xbc6b3b84,0x3c53bbf4,0xbc2f3c2a,0x3bfebc50,0x3c1d3c3b,0xbc53bbf4,0x3c703b29,0xbc703b29,
0x3bd4bc5c,0x3c74,0xbbd4bc5c,0x3c3f3c18,0xbc74b9c2,0x3c53bbf4,0xbbe93c56,0xba42bc73,
0x3bbd3c61,0xbc53bbf4,0x3c72baf2,0xbc2f3c2a,0x3c26bc33,0x3c74,0xbc26bc33,0x3c733a92,
0xbc67bb9b,0x3c53bbf4,0xbb413c6f,0xbc13bc42,0x3ac23c73,0xbc53bbf4,0x3c5ebbc9,0xbb413c6f,
0x3c53bbf4,0x3c74,0xbc53bbf4,0x3c53bbf4,0xbc46bc0e,0x3c53bbf4,0x3ac23c73,0xbc67bb9b,
0xbb413c6f,0xbc53bbf4,0x3c37bc21,0x3bbd3c61,0x3c6ebb59,0x3c74,0xbc6ebb59,0x3bd4bc5c,
0xbc13bc42,0x3c53bbf4,0x3bbd3c61,0xbc6b3b84,0xbbe93c56,0xbc53bbf4,0x3bfebc50,0x3c4d3c04,
0x3c633bb2,0xbc67bb9b,0x3c6b3b84,0xbc6ebb59,0xbbd4bc5c,0x3c093c49,0xbc26bc33,0x3c3f3c18,
0xbb903c69,0x3a42bc73,0x3b413c6f,0xbbd4bc5c,0x3c53bbf4,0xbc1d3c3b,0x3ba7bc65,0x3c74,
0xbc6ebb59,0x3c6ebb59,0xbc3f3c18,0x3bd4bc5c,0x3c093c49,0xbc5ebbc9,0x3c72baf2,0xbc3f3c18,
0x3b11bc71,0x3be93c56,0xbc5ebbc9,0x3c6ebb59,0xbc3f3c18,0x3b11bc71,0x3c093c49,0xbc6ebb59,
0x3c733a92,0xbc2f3c2a,0xba42bc73,0x3c3f3c18,0xbc26bc33,0x3c72baf2,0xbbe93c56,0xbbd4bc5c,
0x3c74,0xbc53bbf4,0x3c53bbf4,0x3c74,0x3c26bc33,0x3bbd3c61,0xbc74b9c2,0x3bd4bc5c,
0xbc733a92,0x3b71bc6c,0x3c4d3c04,0xbc3f3c18,0x3c3f3c18,0xbc3f3c18,0xbbd4bc5c,0x3c6ebb59,
0xbb11bc71,0x3c74b9c2,0xbac23c73,0xbc6ebb59,0xbc093c49,0xbc46bc0e,0x3c13bc42,0x3c3f3c18,
0x3c703b29,0xbc72baf2,0x3c733a92,0xbc74b9c2,0xbc53bbf4,0x3c633bb2,0xbc6ebb59,0x3c733a92,
0x3c1d3c3b,0xbc46bc0e,0x3c633bb2,0xbc72baf2,0xbba7bc65,0x3c1d3c3b,0xbc53bbf4,0x3c703b29,
0x3c74,0xbbd4bc5c,0x3c3f3c18,0xbc6ebb59,0x3ba7bc65,0x3b413c6f,0xbc26bc33,0x3c6b3b84,
0xbc1d3c3b,0x3a42bc73,0x3c093c49,0xbc67bb9b,0x3c53bbf4,0xbb903c69,0xbbd4bc5c,0x3c633bb2,
0xbc703b29,0x3bfebc50,0x3b903c69,0xbc5ebbc9,0x3c703b29,0xbc2f3c2a,0xbb11bc71,0x3c593bde,
0xbc53bbf4,0x3c53bbf4,0x3c74,0xbc53bbf4,0x3c1d3c3b,0xbc6b3b84,0x3b11bc71,0x3c4d3c04,
0xbba7bc65,0x3c74b9c2,0xbb903c69,0xbc46bc0e,0x3c74,0xbc6ebb59,0x3bd4bc5c,0x3c3f3c18,
0x3ba7bc65,0x3c593bde,0xbc093c49,0xbc37bc21,0xbc1d3c3b,0xbc37bc21,0x3c26bc33,0x3c2f3c2a,
0x3f803f80,0x3f803f80,0x3f803f80,0x3f803f80,0x3f7f3f80,0x3f753f7b,0x3f613f6c,0x3f443f54,
0x3f7b3f80,0x3f543f6c,0x3f0b3f33,0x3e323ebb,0x3f753f80,0x3f203f54,0x3d993ebb,0xbf00be64,
0x3f6c3f80,0x3ebb3f33,0xbed3bccc,0xbf71bf3c,0x3f613f80,0x3d993f0b,0xbf4cbed3,0xbf71bf7d,
0x3f543f80,0xbe643ebb,0xbf7dbf3c,0xbf00bf67,0x3f443f80,0xbf003e32,0xbf71bf71,0x3e32bf00,
0x3f333f80,0xbf3cbccc,0xbf29bf80,0x3f443d99,0x3f203f80,0xbf67be64,0xbe64bf67,0x3f803f20,
0x3f0b3f80,0xbf7dbed3,0x3e8bbf29,0x3f443f75,0x3eea3f80,0xbf7dbf15,0x3f33bea3,0x3e323f75,
0x3ebb3f80,0xbf67bf3c,0x3f753d99,0xbf003f20,0x3e8b3f80,0xbf3cbf5a,0x3f7b3eea,0xbf713d99,
0x3e323f80,0xbf00bf71,0x3f443f44,0xbf71bf00,0x3d993f80,0xbe64bf7d,0x3ebb3f75,0xbf00bf67,
0x3f803f80,0x3f803f80,0x3f803f80,0x3f803f80,0x3f203f33,0x3eea3f0b,0x3e8b3ebb,0x3d993e32,
0xbe64bccc,0xbf15bed3,0xbf5abf3c,0xbf7dbf71,0xbf67bf3c,0xbf7dbf7d,0xbf3cbf67,0xbe64bf00,
0xbf67bf80,0xbea3bf29,0x3eea3d99,0x3f753f44,0xbe64bf29,0x3f333e8b,0x3f7b3f75,0x3ebb3f44,
0x3f203d99,0x3f753f75,0x3d993f20,0xbf67bf00,0x3f803f44,0x3e323f44,0xbf71bf00,0xbf00bf71,
0x3f203f7f,0xbf4cbdff,0xbf15bf7d,0x3f543e32,0xbe643f20,0xbf67bf67,0x3f20be64,0x3f203f80,
0xbf67bdff,0xbcccbf5a,0x3f6c3f54,0xbf3c3e32,0xbf67bf4c,0x3f61bccc,0xbdff3f54,0xbf3cbf71,
0xbe64bf7d,0x3f543f54,0xbf7dbe64,0x3f20bf00,0x3f20bf15,0xbdff3f6c,0xbed3bf7d,0x3f543f44,
0x3f803e32,0xbf713e32,0x3f44bf00,0xbf003f44,0x3f203f54,0xbf3cbf3c,0x3f543f20,0xbf67bf00,
0x0,0x0,0x0,0x0,0xba8b0000,0xbabfb99b,0xba33b9d1,0x39dcbaf7,
0xb99b0000,0xbaf7b9d1,0xb9deba81,0xb93d38e0,0xbabf0000,0xbac6baf7,0x37c138e0,0xa580390e,
0xb9d10000,0x38e0ba81,0x3a58b7ee,0x3ae13aad,0xba330000,0x37c1b9de,0xb9873a58,0x3ae1ba10,
0xbaf70000,0x390e38e0,0xba103aad,0x25e03ab4,0x39dc0000,0xa580b93d,0x3ae13ae1,0xb93d25e0,
0xba810000,0x3aadb7ee,0xbac83aa3,0x39dc37c1,0xbac60000,0x3ab4390e,0x390e3ab4,0xbac6,
0xb9de0000,0xba103a58,0xba29bac8,0x39dcbabf,0xba570000,0xba10bae0,0xba81b905,0xb93dbabf,
0x38e00000,0x3ab43aad,0xbabf37c1,0xa580bac6,0xba290000,0x3aadbae3,0xb99bba57,0x3ae137c1,
0xb93d0000,0x25e03ae1,0x39dc39dc,0x3ae1a580,0x37c10000,0x390eba10,0x38e0babf,0x25e03ab4,
0x0,0x0,0x0,0x0,0xbac6ba81,0xba57b9de,0xba2938e0,0x37c1b93d,
0x390eb7ee,0xbae03a58,0xbae33aad,0xba103ae1,0x3ab43aad,0xba10ba10,0x3aad3ab4,0x390e25e0,
0x3ab43aa3,0xb905bac8,0xba5737c1,0xbabf39dc,0x390ebac8,0xba81ba29,0xb99bbabf,0x38e039dc,
0xbac637c1,0xbabfbabf,0x37c1bac6,0x3ab4a580,0x39dc,0xb93d39dc,0x3ae1a580,0x25e03ae1,
0xbac6ba8b,0xb9873930,0xbae0ba10,0xbaf7b93d,0x390ebac6,0x3ab43ab4,0xbac6390e,0xbac60000,
0x3ab43930,0xb7eebae3,0xb9d1baf7,0x3aadb93d,0x3ab4b987,0xba33b7ee,0x3930baf7,0x3aad3ae1,
0x390eba10,0xbaf7baf7,0xba10390e,0xbac625e0,0xbac6bae0,0x3930b9d1,0x3a58ba10,0xbaf739dc,
0xb93d,0x3ae1b93d,0x39dc25e0,0xa58039dc,0xbac6baf7,0x3aad3aad,0xbaf7bac6,0x3ab4a580,
0xbccc3f80,0x3d99bf80,0xbdff3f7f,0x3e32bf7d,0xbdff3f80,0x3ebbbf78,0xbf153f61,0x3f44bf3c,
0xbe643f80,0x3f20bf67,0xbf673f20,0x3f80be64,0xbea33f80,0x3f54bf4c,0xbf803e8b,0x3f443ebb,
0xbed33f80,0x3f75bf29,0xbf5abdff,0x3e323f54,0xbf003f80,0x3f80bf00,0xbf00bf00,0xbf003f80,
0xbf153f80,0x3f75bea3,0xbcccbf4c,0xbf713f54,0xbf293f80,0x3f54bdff,0x3eeabf78,0xbf713ebb,
0xbf3c3f80,0x3f203d99,0x3f54bf7d,0xbf00be64,0xbf4c3f80,0x3ebb3e8b,0x3f7fbf5a,0x3e32bf3c,
0xbf5a3f80,0x3d993eea,0x3f6cbf15,0x3f44bf7d,0xbf673f80,0xbe643f20,0x3f20be64,0x3f80bf67,
0xbf713f80,0xbf003f44,0x3e323e32,0x3f44bf00,0xbf783f80,0xbf3c3f61,0xbea33f0b,0x3e323d99,
0xbf7d3f80,0xbf673f75,0xbf3c3f54,0xbf003f20,0xbf803f80,0xbf7d3f7f,0xbf783f7b,0xbf713f75,
0xbe643f7b,0x3e8bbf78,0xbea33f75,0x3ebbbf71,0xbf673f0b,0x3f7bbea3,0xbf803d99,0x3f753e32,
0xbf67be64,0x3f203f20,0xbe64bf67,0xbe643f80,0xbe64bf5a,0xbed33f7f,0x3f61bf3c,0xbf7d3e32,
0x3f20bf78,0xbf803eea,0x3f333ebb,0x3d99bf71,0x3f80bf00,0xbf00bf00,0xbf003f80,0x3f80bf00,
0x3f203e8b,0x3f0bbf80,0xbf783ebb,0x3d993f44,0xbe643f61,0x3f7fbf15,0xbcccbf3c,0xbf7d3f44,
0xbf673f75,0x3ebb3ebb,0x3f75bf67,0xbe64bf00,0xbf673eea,0xbf293f7b,0x3f0b3d99,0x3f75bf71,
0xbe64bea3,0xbf783f33,0xbf293f75,0x3ebb3e32,0x3f20bf67,0xbe64be64,0xbf673f20,0xbf673f80,
0x3f80bf71,0x3f44bf71,0x3e32bf00,0xbf003e32,0x3f20bed3,0x3f6cbf4c,0x3f7fbf7d,0x3f54bf71,
0xbe643ebb,0x3d993d99,0x3ebbbe64,0x3f20bf00,0xbf673f6c,0xbf5a3f61,0xbf4c3f54,0xbf3c3f44,
0xb7ee0000,0x37c13aa3,0x3930ba8b,0xb93dba10,0x39300000,0x38e0b9ac,0xbae0ba33,0x39dc3aad,
0x390e0000,0xbac63ab4,0x3ab4bac6,0x390e,0xb9050000,0xbaf7b987,0x3aa3ba29,0x39dc38e0,
0x3a580000,0xbabfbac8,0xbae33930,0xb93dbaf7,0xa5800000,0x25e0,0x25e0a580,0xa5800000,
0xbae00000,0xbabfb905,0xb7eeb987,0x3ae1baf7,0xbac80000,0xbaf73930,0xba57b9ac,0x3ae138e0,
0x3aad0000,0xbac637c1,0xbaf7ba10,0x25e0390e,0xb9870000,0x38e0ba29,0xba8bbae3,0xb93d3aad,
0xbae30000,0x37c1ba57,0xb9d1bae0,0x39dcba10,0x3ab40000,0x390ebac6,0xbac6390e,0x3ab4,
0x3ae10000,0xa58039dc,0xb93db93d,0x39dc25e0,0xb9ac0000,0x3aadba33,0xb905b9de,0xb93d37c1,
0xba100000,0x3ab4babf,0x3aadbaf7,0xa580bac6,0x3aa30000,0xba10ba8b,0xb9acb99b,0x3ae1babf,
0x390eb99b,0xba29b9ac,0xb905babf,0x38e03ae1,0x3ab4b9de,0xb99bb905,0x3aa337c1,0xbabfb93d,
0x3ab4390e,0xbac6bac6,0x390e3ab4,0x390e0000,0x390ebae3,0x3a58ba8b,0xba333aad,0xba10b93d,
0xbac6b9ac,0x3aa3ba57,0xba8138e0,0x37c13ae1,0x25e0,0x25e0a580,0xa5800000,0x25e0,
0xbac6ba29,0xb9de3aa3,0xb9ac38e0,0x37c139dc,0x390eba33,0xba8bbae0,0xb7ee3aad,0xba1039dc,
0x3ab4babf,0x38e038e0,0xbabf3ab4,0x390ea580,0x3ab4ba57,0xbac8b99b,0xb9de37c1,0xbabf3ae1,
0x390eb905,0xb9acba81,0xbac8babf,0x38e0b93d,0xbac63ab4,0x390e390e,0x3ab4bac6,0x3ab40000,
0x3ae1,0x39dc3ae1,0xb93da580,0x25e0b93d,0xbac63a58,0xb9d1b987,0xba8bba10,0xbaf73ae1,
0x390e38e0,0x37c137c1,0x38e0390e,0xbac625e0,0x3ab4b9d1,0xbae3ba33,0xb987baf7,0x3aad39dc,
0x0,0x0,0x0,0x0,0x3dcc0000,0x3e973e4b,0x3ef53ec7,0x3f253f10,
0x3e4b0000,0x3f103ec7,0x3f573f37,0x3f7c3f6e,0x3e970000,0x3f483f10,0x3f7f3f6e,0x3f5e3f7a,
0x3ec70000,0x3f6e3f37,0x3f693f80,0x3eaf3f2e,0x3ef50000,0x3f7f3f57,0x3f1b3f69,0xbeaf3e19,
0x3f100000,0x3f7a3f6e,0x3e193f2e,0xbf5ebede,0x3f250000,0x3f5e3f7c,0xbeaf3eaf,0xbf7cbf5e,
0x3f370000,0x3f2e3f80,0xbf40bd4c,0xbf25bf7f,0x3f480000,0x3ede3f7a,0xbf7abede,0xbf48,
0x3f570000,0x3e193f69,0xbf76bf40,0x3f25be97,0x3f640000,0xbe193f50,0xbf37bf73,0x3f7c3e97,
0x3f6e0000,0xbede3f2e,0xbe97bf7f,0x3f5e3f48,0x3f760000,0xbf2e3f05,0x3e4bbf64,0x3eaf3f7f,
0x3f7c0000,0xbf5e3eaf,0x3f25bf25,0xbeaf3f5e,0x3f7f0000,0xbf7a3e19,0x3f6ebe97,0xbf5e3ede,
0x0,0x0,0x0,0x0,0x3f483f37,0x3f643f57,0x3f763f6e,0x3f7f3f7c,
0x3f7a3f80,0x3f503f69,0x3f053f2e,0x3e193eaf,0x3ede3f2e,0xbe193e19,0xbf2ebede,0xbf7abf5e,
0xbedebd4c,0xbf73bf40,0xbf64bf7f,0xbe97bf25,0xbf7abf40,0xbf37bf76,0x3e4bbe97,0x3f6e3f25,
0xbf48bf7f,0x3e97be97,0x3f7f3f48,0x3ede3f5e,0xbf25,0x3f7c3f25,0x3eaf3f5e,0xbf5ebeaf,
0x3f483dcc,0x3f1b3f7e,0xbf50be19,0xbf10bf7c,0x3f7a3f48,0xbede3ede,0xbf48bf7a,0x3f480000,
0x3ede3f7e,0xbf80bf05,0x3ec7bf10,0x3f2e3f7c,0xbede3f1b,0xbef5bf80,0x3f7e3f10,0xbf2e3eaf,
0xbf7abe19,0x3f10bf10,0x3e193f7a,0xbf48bf5e,0xbf48bf50,0x3f7e3ec7,0xbf693e19,0x3f10bf25,
0xbf7c,0x3eaf3f7c,0xbf25bf5e,0x3f5e3f25,0x3f48bf10,0xbf2e3f2e,0x3f10bf48,0xbede3f5e,
0x0,0x0,0x0,0x0,0xb82e0000,0xb92fb8c9,0xb989b979,0xbae53a57,
0xb8c90000,0x3a57b979,0x393f3a86,0x39e33a9b,0xb92f0000,0x3a183a57,0x3a913a9b,0xba98bad6,
0xb9790000,0x3a9b3a86,0x3ab1b9a3,0x396a39fe,0xb9890000,0x3a91393f,0xbada3ab1,0xb96ab9c3,
0x3a570000,0xbad63a9b,0xb9c339fe,0x3a98b998,0xbae50000,0xba9839e3,0xb96a396a,0xb9e33a98,
0x3a860000,0x39feb9a3,0x3965b82d,0x3ae5ba91,0x3a180000,0x3998bad6,0x3ad6b998,0xba18,
0x393f0000,0xb9c33ab1,0xbadd3965,0xbae5392f,0xba450000,0x39c3ba13,0xba863aa9,0x39e3b92f,
0x3a9b0000,0xb99839fe,0x392fba91,0xba983a18,0x3add0000,0xb9fe3afa,0xb8c93a45,0x396a3a91,
0x39e30000,0x3a98396a,0xbae53ae5,0xb96aba98,0x3a910000,0x3ad6b9c3,0x3a9b392f,0x3a983998,
0x0,0x0,0x0,0x0,0x3a183a86,0xba45393f,0x3add3a9b,0x3a9139e3,
0xbad6b9a3,0xba133ab1,0x3afa39fe,0xb9c3396a,0x399839fe,0x39c3b9c3,0xb9feb998,0x3ad63a98,
0xb998b82d,0x3aa93965,0x3a45ba91,0x392f3ae5,0x3ad63965,0xba86badd,0xb8c9392f,0x3a9bbae5,
0xba18ba91,0xb92f392f,0x3a913a18,0x3998ba98,0x3ae5,0x39e3bae5,0x396aba98,0x3a98b96a,
0x3a18b82e,0xbada3859,0x3a1339c3,0xba57b9e3,0xbad63a18,0xb9983998,0xba183ad6,0x3a180000,
0x39983859,0x39a3bafa,0xb979ba57,0x39fe39e3,0xb998bada,0x398939a3,0x38593a57,0xb9fe396a,
0x3ad639c3,0x3a57ba57,0xb9c3bad6,0xba183a98,0xba183a13,0x3859b979,0xbab1b9c3,0x3a573ae5,
0xb9e3,0x396a39e3,0x3ae53a98,0xba98bae5,0x3a18ba57,0xb9fe39fe,0x3a57ba18,0xb998ba98,
0x3f800000,0xbf7fbd4c,0x3f7e3dcc,0xbf7cbe19,0x3f7e0000,0xbf6ebe7d,0x3f503ef5,0xbf25bf2e,
0x3f7a0000,0xbf48bede,0x3ede3f48,0xbf7a,0x3f730000,0xbf10bf1b,0xbd4c3f76,0x3f25bf6e,
0x3f690000,0xbe97bf40,0xbf053f7e,0x3f7cbf10,0x3f5e0000,0xbf5e,0xbf5e3f5e,0x3f5e0000,
0x3f500000,0x3e97bf73,0xbf803f1b,0x3eaf3f10,0x3f400000,0x3f10bf7e,0xbf643e7d,0xbeaf3f6e,
0x3f2e0000,0x3f48bf7f,0xbf10be19,0xbf5e3f7a,0x3f1b0000,0x3f6ebf76,0xbdccbf05,0xbf7c3f2e,
0x3f050000,0x3f7fbf64,0x3ec7bf50,0xbf253e19,0x3ede0000,0x3f7abf48,0x3f48bf7a,0xbede,
0x3eaf0000,0x3f5ebf25,0x3f7cbf7c,0x3f25bf5e,0x3e7d0000,0x3f2ebef5,0x3f73bf57,0x3f7cbf7f,
0x3e190000,0x3edebe97,0x3f2ebf10,0x3f5ebf48,0x3d4c0000,0x3e19bdcc,0x3e7dbe4b,0x3eafbe97,
0x3f7a3e4b,0xbf76be7d,0x3f733e97,0xbf6ebeaf,0x3ede3f57,0xbe4bbf73,0xbd4c3f7f,0x3e97bf7c,
0xbede3f7a,0x3f48bf48,0xbf7a3ede,0x3f7a0000,0xbf7a3f05,0x3f693dcc,0xbef5bf2e,0xbe193f7c,
0xbf48be7d,0x3d4c3f64,0x3f37bf6e,0xbf7f3eaf,0xbf5e,0xbf5e3f5e,0x3f5e0000,0xbf5e,
0x3f48bf76,0xbf573d4c,0xbe7d3f6e,0x3f7fbf25,0x3f7abef5,0x3dccbf50,0xbf803f2e,0x3e193f25,
0x3ede3e97,0x3f6ebf6e,0xbe97bede,0xbf7a3f5e,0xbede3f64,0x3f40be4b,0x3f57bf7f,0xbe97beaf,
0xbf7a3f73,0xbe7d3f37,0x3f40be97,0x3f6ebf7c,0xbf483ede,0xbf7a3f7a,0xbede3f48,0x3ede0000,
0xbeaf,0xbf253eaf,0xbf7c3f5e,0xbf5e3f7c,0x3f48bf69,0x3ec7bf1b,0xbdccbe19,0xbf103eaf,
0x3f7abf6e,0x3f7fbf7f,0x3f6ebf7a,0x3f48bf5e,0x3edebec7,0x3f05bef5,0x3f1bbf10,0x3f2ebf25,
0xb9a30000,0xba91b82d,0x3859b82e,0xb9e339c3,0x38590000,0xba9b39a4,0xba13b989,0x3ae5b9fe,
0xbad60000,0xba18b998,0x39983a18,0x3ad6,0xbaa90000,0xba573ada,0xb82d3add,0xbae5ba9b,
0x3ab10000,0x392f3965,0xbafa3859,0x39e3ba57,0xba980000,0x3a98,0x3a98ba98,0xba980000,
0xba130000,0xb92f3aa9,0x39a3bada,0x396a3a57,0xb9650000,0x3a57b859,0x3a45b9a4,0xb96a3a9b,
0x39fe0000,0x3a18ba91,0xba5739c3,0x3a98bad6,0xbada0000,0x3a9bbadd,0x382ebafa,0xb9e339fe,
0x3afa0000,0x3a913a45,0xb9793a13,0x3ae5b9c3,0x39980000,0xbad6ba18,0x3a183ad6,0xb998,
0x396a0000,0xba983ae5,0x39e3b9e3,0xbae53a98,0xb9a40000,0x39fe3989,0xbaa9b93f,0x39e3ba91,
0xb9c30000,0x3998392f,0x39feba57,0xba98ba18,0x382d0000,0xb9c3382e,0xb9a438c9,0x396a392f,
0xbad6b8c9,0xbadd39a4,0xbaa9b92f,0xba9bb96a,0x3998393f,0x38c93aa9,0xb82d3a91,0xb92fb9e3,
0xb998bad6,0x3a18ba18,0x3ad63998,0xbad60000,0x3ad63afa,0x3ab1b82e,0x3989b9fe,0x39c339e3,
0xba1839a4,0x382dba45,0x3a86ba9b,0xba91396a,0x3a98,0x3a98ba98,0xba980000,0x3a98,
0x3a18badd,0xb93f382d,0x39a43a9b,0x3a913ae5,0xbad63989,0xb82e3a13,0x39a339fe,0xb9c3bae5,
0x3998b92f,0x3a9bba9b,0x392fb998,0x3ad6ba98,0xb998ba45,0xb96538c9,0x393fba91,0x392fb96a,
0x3ad6baa9,0x39a43a86,0xb965392f,0x3a9bb9e3,0xba183998,0x3ad6bad6,0xb9983a18,0x39980000,
0xb96a,0x3ae5396a,0xb9e3ba98,0x3a9839e3,0x3a18bab1,0xb9793ada,0x382e39c3,0xba57396a,
0xbad6ba9b,0x3a91ba91,0x3a9b3ad6,0x3a183a98,0x39983979,0x3afa3989,0xbadaba57,0x39fe3ae5,
};

typedef unsigned short u16;
typedef unsigned long long u64;
typedef __attribute__((ext_vector_type(8))) short bf16x8;
typedef __attribute__((ext_vector_type(4))) short s16x4;
typedef __attribute__((ext_vector_type(4))) float f32x4;
typedef __attribute__((ext_vector_type(2))) float f32x2;
typedef __attribute__((ext_vector_type(4))) unsigned u32x4;
typedef __attribute__((ext_vector_type(2))) __bf16 bf16x2_t;
typedef __attribute__((address_space(3))) s16x4* lds_s16x4_p;
typedef __attribute__((address_space(3))) void* lds_void_p;
#define DEVINL __device__ __forceinline__

#define STAMP(i)
#define STAMP_FLUSH()
#define STAMP2(a, b) do { if (s == 0) { STAMP(a); } else { STAMP(b); } } while (0)
typedef unsigned stv_t;

#define MFMA16(a, b, c) __builtin_amdgcn_mfma_f32_16x16x32_bf16((a), (b), (c), 0, 0, 0)
#define MFMAU(a, b, c) MFMA16(__builtin_bit_cast(bf16x8, a), __builtin_bit_cast(bf16x8, b), c)
#define SB0() __builtin_amdgcn_sched_barrier(0)
constexpr unsigned OOB = 0x7ffffff0u;
constexpr int SC1 = 16;

template <int N> struct IC { static constexpr int v = N; };
template <int... I, class F> DEVINL void static_for_impl(std::integer_sequence<int, I...>, F&& f) { (f(IC<I>{}), ...); }
template <int N, class F> DEVINL void static_for(F&& f) { static_for_impl(std::make_integer_sequence<int, N>{}, f); }
DEVINL void bar() { asm volatile("" ::: "memory"); __builtin_amdgcn_s_barrier(); asm volatile("" ::: "memory"); }
template <int N> DEVINL void wait_vm() { asm volatile("s_waitcnt vmcnt(%0)" ::"n"(N) : "memory"); }
DEVINL void wait_lds() { __builtin_amdgcn_s_waitcnt(0xc07f); }

constexpr int T0 = 16000, T2 = 4000;
constexpr int NT1 = 167;
constexpr int XST = 32768;
constexpr size_t X1_TB = 16 * XST, X1_G = 2 * X1_TB, X1_S = 16 * X1_G;
constexpr size_t X2_TB = 8 * XST, X2_G = 2 * X2_TB, X2_S = 16 * X2_G;
constexpr size_t WF_G = 1 << 20, WF_BYTES = 16 * WF_G;
constexpr size_t Y1_S = (size_t)128 * 65536, Y2_S = (size_t)64 * 65536;
constexpr size_t OFF_FLAG = 0;
constexpr int F_X = 0, F_W1 = 768, F_W2 = 1024, F_G1 = 1280, F_M = 1792, F_G2 = 2304, F_S = 2816, F_EPOCH = 2840, F_H = 512, F_END = 2848;
constexpr size_t OFF_WFT = OFF_FLAG + F_END * 128;
constexpr size_t OFF_WOT = OFF_WFT + 128 * 1024;
constexpr size_t OFF_GE = OFF_WOT + 128 * 1024;
constexpr size_t OFF_X1F = 4 << 20;
constexpr size_t OFF_W1F = OFF_X1F + 2 * X1_S;
constexpr size_t OFF_Y1F = OFF_W1F + WF_BYTES;
constexpr size_t OFF_X2F = OFF_Y1F + 2 * Y1_S;
constexpr size_t OFF_W2F = OFF_X2F + 2 * X2_S;
constexpr size_t OFF_Y2F = OFF_W2F + WF_BYTES;
constexpr size_t WS_USED = OFF_Y2F + 2 * Y2_S;
constexpr int TAB_F = 0, TAB_G = 8, TAB_T = 14;
constexpr int LDS_BYTES = 81920;

DEVINL unsigned pack_bf16(float a, float b) {
  f32x2 v = {a, b};
  bf16x2_t r = __builtin_convertvector(v, bf16x2_t);
  return __builtin_bit_cast(unsigned, r);
}
DEVINL u32x4 pack8(const f32x4& a, const f32x4& b) {
  return (u32x4){pack_bf16(a[0], a[1]), pack_bf16(a[2], a[3]), pack_bf16(b[0], b[1]), pack_bf16(b[2], b[3])};
}
DEVINL s16x4 tr_read(const char* p) { return __builtin_amdgcn_ds_read_tr16_b64_v4i16((lds_s16x4_p)(p)); }
DEVINL bf16x8 cat8(s16x4 a, s16x4 b) { return __builtin_shufflevector(a, b, 0, 1, 2, 3, 4, 5, 6, 7); }
DEVINL bf16x8 tr_frag(const char* p, int second) { return cat8(tr_read(p), tr_read(p + second)); }
template <int AUX> DEVINL void dma16(__amdgpu_buffer_rsrc_t rs, char* lds_dst, unsigned voff, unsigned soff) {
  const unsigned m = (unsigned)(size_t)(lds_void_p)lds_dst;
  if constexpr (AUX == 0) asm volatile("s_mov_b32 m0, %0\n\ts_nop 0\n\tbuffer_load_dwordx4 %1, %2, %3 offen lds" :: "s"(m), "v"(voff), "s"(rs), "s"(soff) : "memory");
  else asm volatile("s_mov_b32 m0, %0\n\ts_nop 0\n\tbuffer_load_dwordx4 %1, %2, %3 offen sc1 lds" :: "s"(m), "v"(voff), "s"(rs), "s"(soff) : "memory");
}
DEVINL __amdgpu_buffer_rsrc_t mkrsrc(const void* p, unsigned bytes) { return __builtin_amdgcn_make_buffer_rsrc((void*)p, 0, bytes, 0x00020000); }
DEVINL void st16(void* p, u32x4 v) { asm volatile("global_store_dwordx4 %0, %1, off sc1\n\ts_nop 1" ::"v"(p), "v"(v) : "memory"); }
template <int I, bool DEV> DEVINL void ldfrag(u32x4& d, const char* base, unsigned lane16) {
  const char* b = base + (I >> 3) * 8192 + 4096;
  if constexpr (DEV) asm volatile("global_load_dwordx4 %0, %1, %2 offset:%3 sc1" : "=v"(d) : "v"(lane16), "s"(b), "n"((I & 7) * 1024 - 4096) : "memory");
  else asm volatile("global_load_dwordx4 %0, %1, %2 offset:%3" : "=v"(d) : "v"(lane16), "s"(b), "n"((I & 7) * 1024 - 4096) : "memory");
}
template <int I, int BS = 16384> DEVINL void ldfrag_rot(u32x4& d, const char* base, int rot, unsigned lane16) {
  ldfrag<I & 7, true>(d, base + (((I >> 3) + rot) & 3) * BS, lane16);
}
DEVINL void tie(u32x4& a) { asm volatile("" : "+v"(a)); }
template <int N> DEVINL void tie(u32x4 (&a)[N]) {
#pragma unroll
  for (int i = 0; i < N; ++i) tie(a[i]);
}
DEVINL int lane_id() { int l; asm volatile("v_mbcnt_lo_u32_b32 %0, -1, 0\n\tv_mbcnt_hi_u32_b32 %0, -1, %0" : "=v"(l)); return l; }
DEVINL void sgpr_settle() { asm volatile("s_nop 4" ::: "memory"); }

#define CT_STEP 64
DEVINL void code_touch(unsigned& d, int slice) {
  u64 pc;
  unsigned len, off = slice * CT_STEP;
  asm volatile("s_getpc_b64 %0\n.Lpc%=:\n\ts_mov_b32 %1, .Lpc%=-k_all" : "=s"(pc), "=s"(len));
  pc -= len;
  asm volatile("s_mov_b32 %1, .Lk_end-k_all\n\ts_max_u32 %1, %1, .Lt_end-k_all\n"
               "1:\n\ts_load_dword %0, %3, %2\n\ts_add_u32 %2, %2, %4\n\ts_cmp_lt_u32 %2, %1\n\ts_cbranch_scc1 1b"
               : "=&s"(d), "=&s"(len), "+s"(off) : "s"(pc), "n"(128 * CT_STEP) : "scc", "memory");
}
DEVINL void tie(unsigned& a) { asm volatile("" : "+v"(a)); }

#define PRIO(x) __builtin_amdgcn_s_setprio(x)
#define PRIO_F 3
#define PRIO_G0 2
#define PRIO_GW 0
#define PRIO_G1 0
#define PRIO_G2A 0
#define PRIO_G2B 0
#define PRIO_JB 0
#define PRIO_T0 1
#define PRIO_T1 1
#define PRIO_T2 0
#define PRIO_MA 0
#define PRIO_MB 1
#define PRIO_JA 0
DEVINL void set_flag(char* ws, int flag, u64 tag, int w) {
  u64 d;
  unsigned o;
  asm volatile("s_cmp_lg_u32 %2, 0\n\ts_cbranch_scc1 1f\n\ts_mov_b64 exec, 1\n\ts_nop 1\n\tv_mov_b32 %1, %3\n\tv_mov_b64 %0, %5\n\t"
               "global_store_dwordx2 %1, %0, %4 sc1\n\ts_nop 1\n\ts_mov_b64 exec, -1\n1:"
               : "=&v"(d), "=&v"(o) : "s"(w), "s"(flag * 128), "s"(ws + OFF_FLAG), "s"(tag) : "scc", "memory");
}
template <int N = 0> DEVINL void publish(char* ws, int flag, u64 tag, int w) { wait_vm<N>(); PRIO(PRIO_F); bar(); set_flag(ws, flag, tag, w); }
#define POLL_SLEEP 1
DEVINL void poll(const char* ws, int flag, u64 mask, u64 tag) {
  const unsigned off = flag * 128;
  u64 v;
  unsigned cnt;
  asm volatile("s_mov_b64 exec, %6\n\ts_nop 4\n\ts_mov_b32 %1, 40000\n"
               "1:\n\tglobal_load_dwordx2 %0, %2, %3 sc1\n\ts_waitcnt vmcnt(0)\n\tv_cmp_ne_u64 vcc, %4, %0\n\ts_nop 1\n\ts_cbranch_vccz 2f\n\t"
               "s_cmp_eq_u64 vcc, %6\n\ts_cbranch_scc1 3f\n\t"
               "s_mov_b64 exec, vcc\n\ts_sub_u32 %1, %1, 1\n\ts_cbranch_scc1 2f\n\ts_sleep %5\n\ts_branch 1b\n"
               "3:\n\ts_mov_b64 exec, 1\n"
               "4:\n\ts_sleep %5\n\tglobal_load_dwordx2 %0, %2, %3 sc1\n\ts_waitcnt vmcnt(0)\n\tv_cmp_ne_u64 vcc, %4, %0\n\ts_sub_u32 %1, %1, 1\n\ts_cbranch_scc1 2f\n\ts_cbranch_vccnz 4b\n\t"
               "s_andn2_b64 exec, %6, 1\n\ts_branch 1b\n"
               "2:\n\ts_mov_b64 exec, -1"
               : "=&v"(v), "=&s"(cnt) : "v"(off), "s"(ws + OFF_FLAG), "s"(tag), "n"(POLL_SLEEP), "s"(mask) : "vcc", "scc", "memory");
}
constexpr u64 lanes(int n) { return (1ull << n) - 1; }
DEVINL int poll_vote(const char* ws, int flag, u64 tag) {
  const unsigned off = flag * 128;
  u64 v, t;
  unsigned cnt;
  asm volatile("s_mov_b64 exec, 0xffff\n\ts_nop 4\n\ts_mov_b32 %1, 40000\n"
               "1:\n\tglobal_load_dwordx2 %0, %3, %4 sc1\n\ts_waitcnt vmcnt(0)\n\tv_lshlrev_b64 %2, 1, %0\n\tv_cmp_ne_u64 vcc, %5, %2\n\ts_nop 1\n\ts_cbranch_vccz 2f\n\t"
               "s_mov_b64 exec, vcc\n\ts_sub_u32 %1, %1, 1\n\ts_cbranch_scc1 2f\n\ts_sleep %6\n\ts_branch 1b\n"
               "2:\n\ts_mov_b64 exec, 0xffff\n\ts_nop 1\n\tv_cmp_ne_u64 vcc, %7, %0\n\ts_mov_b64 exec, -1\n\ts_nop 1\n\ts_bcnt1_i32_b64 %1, vcc"
               : "=&v"(v), "=&s"(cnt), "=&v"(t) : "v"(off), "s"(ws + OFF_FLAG), "s"(tag << 1), "n"(POLL_SLEEP), "s"(tag) : "vcc", "scc", "memory");
  return cnt;
}
DEVINL int near_side(const char* p, int first) {
  u64 t0, t1, t2;
  unsigned d;
  asm volatile("s_memtime %0\n\ts_waitcnt lgkmcnt(0)\n\ts_load_dword %3, %4, 0x0\n\ts_waitcnt lgkmcnt(0)\n\t"
               "s_memtime %1\n\ts_waitcnt lgkmcnt(0)\n\ts_load_dword %3, %5, 0x0\n\ts_waitcnt lgkmcnt(0)\n\ts_memtime %2\n\ts_waitcnt lgkmcnt(0)"
               : "=&s"(t0), "=&s"(t1), "=&s"(t2), "=&s"(d) : "s"(p + first * 8192), "s"(p + (first ^ 1) * 8192) : "memory");
  return first ^ ((unsigned)t2 - (unsigned)t1 < (unsigned)t1 - (unsigned)t0);
}

template <int N> DEVINL void flag_peek(u64& v, const char* ws, unsigned off) {
  asm volatile("s_bfm_b64 exec, %3, 0\n\ts_nop 1\n\tglobal_load_dwordx2 %0, %1, %2 sc1\n\ts_mov_b64 exec, -1" : "=v"(v) : "v"(off), "s"(ws + OFF_FLAG), "n"(N) : "memory");
}
template <int N> DEVINL void flag_check(u64& v, const char* ws, unsigned off, u64 tag) {
  unsigned cnt;
  asm volatile("s_bfm_b64 exec, %5, 0\n\ts_mov_b32 %1, 40000\n"
               "1:\n\tv_cmp_ne_u64 vcc, %4, %0\n\ts_nop 1\n\ts_cbranch_vccz 2f\n\t"
               "s_mov_b64 exec, vcc\n\ts_sub_u32 %1, %1, 1\n\ts_cbranch_scc1 2f\n\tglobal_load_dwordx2 %0, %2, %3 sc1\n\ts_waitcnt vmcnt(0)\n\ts_branch 1b\n"
               "2:\n\ts_mov_b64 exec, -1"
               : "+v"(v), "=&s"(cnt) : "v"(off), "s"(ws + OFF_FLAG), "s"(tag), "n"(N) : "vcc", "scc", "memory");
}

constexpr float GC = 0.468668693f, GK = 4.91222954f;
DEVINL u32x4 gelu_pack8(const f32x4& a, const f32x4& b) {
  float y[8] = {a[0], a[1], a[2], a[3], b[0], b[1], b[2], b[3]}, t[8];
#pragma unroll
  for (int i = 0; i < 8; ++i) { t[i] = __builtin_fmaf(y[i], y[i], GK); asm("" : "+v"(t[i])); }
  SB0();
#pragma unroll
  for (int i = 0; i < 8; ++i) { t[i] *= y[i]; asm("" : "+v"(t[i])); }
  SB0();
#pragma unroll
  for (int i = 0; i < 8; ++i) t[i] = __builtin_amdgcn_exp2f(-t[i]);
  SB0();
#pragma unroll
  for (int i = 0; i < 8; ++i) { t[i] = __builtin_fmaf(t[i], GC, GC); asm("" : "+v"(t[i])); }
  SB0();
#pragma unroll
  for (int i = 0; i < 8; ++i) t[i] = __builtin_amdgcn_rcpf(t[i]);
  SB0();
#pragma unroll
  for (int i = 0; i < 8; ++i) { t[i] *= y[i]; asm("" : "+v"(t[i])); }
  SB0();
  return (u32x4){pack_bf16(t[0], t[1]), pack_bf16(t[2], t[3]), pack_bf16(t[4], t[5]), pack_bf16(t[6], t[7])};
}

#define X_PACE 16
#define WL_PACE 32
#define XB_SLEEP 0
#define XA_SLEEP 0
template <int RS, size_t XG>
DEVINL void fwd_unit(const char* la, const u32x4 (&F)[8], char* gd, unsigned lo) {
  bf16x8 A[2][2];
#pragma unroll
  for (int i = 0; i < 2; ++i)
#pragma unroll
    for (int ks = 0; ks < 2; ++ks) A[i][ks] = tr_frag(la + 8 * i + 32 * RS * ks, 4 * RS);
#pragma unroll
  for (int nt = 0; nt < 4; ++nt) {
    f32x4 c0 = {0, 0, 0, 0}, c1 = {0, 0, 0, 0};
#pragma unroll
    for (int ks = 0; ks < 2; ++ks) { c0 = MFMAU(A[0][ks], F[2 * nt + ks], c0); c1 = MFMAU(A[1][ks], F[2 * nt + ks], c1); }
    if constexpr (X_PACE < 16) wait_vm<X_PACE - 1>();
    st16(gd + (nt & 1) * 8 * XG + (lo ^ ((nt >> 1) << 13)), pack8(c0, c1));
  }
}
template <size_t XG> DEVINL unsigned x_lane_off(int lane, int tl, int row, unsigned pb) {
  const unsigned go = ((lane & 15) >> 1) * XG;
  return go + (((go >> 8) & 8192) ^ pb) + (row >> 6) * 16384 + (row & 63) * 128 + ((((lane & 1) * 4 + (lane >> 4)) ^ ((tl >> 1) & 7)) * 16);
}

DEVINL void w_issue(const u16* __restrict__ wsrc, char* reg, int q, int w, int lane, bool live) {
  const int co = q >> 2, cb = q & 3, par = co >> 5, c0 = (co & 31) * 8;
  __amdgpu_buffer_rsrc_t rs = mkrsrc(wsrc, 31 * 65536 * 2);
#pragma unroll
  for (int i = 0; i < 4; ++i) {
    const int cl = 2 * w + (i >> 1), tap = 8 * (i & 1) + (lane >> 3);
    const unsigned off = ((2 * tap + par) * 256 + c0 + cl) * 512 + cb * 128 + (((lane & 7) ^ (2 * (((tap >> 1) & 1) + 2 * (i & 1)))) * 16);
    dma16<0>(rs, reg + (w * 4 + i) * 1024, live ? off : OOB, 0);
  }
}
template <int PACE>
DEVINL void w_compute(const u32x4 (&Tm)[4], char* __restrict__ wf, const char* reg, int q, int w, int lane, unsigned pb) {
  const int co = q >> 2, cb = q & 3;
  const int G = lane >> 4, rq = (lane & 15) >> 2, p = lane & 3;
  char* dst = wf + (cb * 4 + w) * 65536 + (co >> 4) * 16384 + ((co >> 2) & 3) * 2048 + (co & 3) * 256 + (unsigned)((lane & 15) * 16 + 4 * G * WF_G);
  const char* la = reg + (8 * (G & 1) + rq) * 128 + (((2 * w + (p >> 1)) ^ (2 * ((rq >> 1) + 2 * (G & 1)))) * 16) + 8 * (p & 1);
  bf16x8 B[8];
#pragma unroll
  for (int cl = 0; cl < 8; ++cl) B[cl] = tr_frag(la + cl * 2048, 4 * 128);
#pragma unroll
  for (int mt = 0; mt < 4; ++mt) {
    f32x4 d[8];
#pragma unroll
    for (int cl = 0; cl < 8; ++cl) d[cl] = MFMAU(Tm[mt], B[cl], ((f32x4){0, 0, 0, 0}));
#pragma unroll
    for (int r = 0; r < 4; ++r) {
      u32x4 o = {pack_bf16(d[0][r], d[1][r]), pack_bf16(d[2][r], d[3][r]), pack_bf16(d[4][r], d[5][r]), pack_bf16(d[6][r], d[7][r])};
      if constexpr (PACE < 16) wait_vm<PACE>();
      st16(dst + (pb ^ (((mt & 1) ^ (r >> 1)) << 13)) + r * WF_G + (mt >> 1) * 1024, o);
    }
  }
}

constexpr float LOG2E2 = 2.0f * 1.4426950408889634f;
DEVINL void s_transpose(const u16* __restrict__ src, char* __restrict__ dst, char* lds, int ks, int half, int lane) {
  __amdgpu_buffer_rsrc_t rs = mkrsrc(src, 131072);
#pragma unroll
  for (int i = 0; i < 8; ++i) {
    const int row = 4 * i + (lane >> 4);
    dma16<0>(rs, lds + i * 1024, (ks * 32 + row) * 512 + half * 256 + (((lane & 15) ^ (4 * (row & 3))) * 16), 0);
  }
  wait_vm<0>();
  const int G = lane >> 4, rq = (lane & 15) >> 2, p = lane & 3;
#pragma unroll
  for (int ml = 0; ml < 8; ++ml) {
    const char* la = lds + (8 * G + rq) * 256 + (((4 * (ml >> 1) + p) ^ (4 * (rq & 3))) * 16) + 8 * (ml & 1);
    st16(dst + ((size_t)((8 * half + ml) * 8 + ks) * 64 + lane) * 16, __builtin_bit_cast(u32x4, tr_frag(la, 4 * 256)));
  }
}
DEVINL void s_ge(const u16* __restrict__ g, const u16* __restrict__ wg, float* __restrict__ ge, char* lds, int eb, int lane) {
  __amdgpu_buffer_rsrc_t rs = mkrsrc(wg, 131072);
#pragma unroll
  for (int i = 0; i < 8; ++i) dma16<0>(rs, lds + i * 1024, (i * 32 + (lane >> 1)) * 512 + eb * 32 + (lane & 1) * 16, 0);
  bf16x8 gb[8];
#pragma unroll
  for (int ks = 0; ks < 8; ++ks) gb[ks] = *(const bf16x8*)(g + (lane & 15) * 256 + ks * 32 + (lane >> 4) * 8);
  wait_vm<0>();
  const int G = lane >> 4, rq = (lane & 15) >> 2, p = lane & 3;
  f32x4 acc = {0, 0, 0, 0};
#pragma unroll
  for (int ks = 0; ks < 8; ++ks) acc = MFMA16(tr_frag(lds + (ks * 32 + 8 * G + rq) * 32 + 8 * p, 4 * 32), gb[ks], acc);
  acc *= LOG2E2;
  f32x4 ex;
#pragma unroll
  for (int r = 0; r < 4; ++r) ex[r] = __builtin_amdgcn_exp2f(acc[r] > 64.f ? 64.f : (acc[r] < -64.f ? -64.f : acc[r]));
  const int b = (lane >> 3) & 1, u = lane & 7;
  float* d = ge + ((b * 4 + (eb >> 2)) * 8 + u) * 64 + (eb & 3) * 16 + 4 * G;
  st16(d, __builtin_bit_cast(u32x4, acc));
  st16(d + 4096, __builtin_bit_cast(u32x4, ex));
}

#define G1_K 13
#define G1_P 19
#define G1_K2 23
#define GORD 0
#define GZ 0
#define GX 0
#define SPREAD 1
#define G1_WR 4
#define WPRE 16
#define PRO1 7
#define PRO2 7
#define RAMP 2
constexpr int RING = 7, SLOT = 11264, RING_DUMMY = RING * SLOT;
template <int PH> struct GPlan {
  static constexpr int NST = PH == 1 ? 32 : 8, PRO = PH == 1 ? PRO1 : PRO2;
  int first[NST + 1];
  int wfirst[NST + 1];
  int before[NST];
  int pos[NST];
  int posF, posF2, posY;
  constexpr GPlan() : first{}, wfirst{}, before{}, pos{}, posF(0), posF2(0), posY(0) {
    int n = 0, u = 0, k = 0;
    for (; u < PRO; ++u) { pos[u] = (n += 3); n += w1(u); }
    for (int t = 0; t < NST; ++t) {
      before[t] = n;
      first[t] = u;
      wfirst[t] = k;
      if (PH == 1 && t == G1_K - 2) posF = ++n;
      if (PH == 1 && t == G1_K2 - 2) posF2 = ++n;
      if (PH == 1 && t == 16) n += 6;
      for (; u <= t + RING - 1 && u < NST && (PH != 1 || u < 16 || t >= G1_K) && (u >= 16 || u < first[t] + RAMP); ++u) { pos[u] = (n += 3); n += w1(u); }
      if (PH == 1 && t == 16) posY = n;
      for (int i = 0; i < G1_WR && PH == 1 && t >= G1_K2 && k < 2 * (t - 16); ++i) { ++k; ++n; }
      for (; PH == 3 && k < 4 * t; ++k) ++n;
    }
    first[NST] = u;
    wfirst[NST] = k;
  }
  static constexpr int w1(int u) { return PH == 1 && u + WPRE < 16 ? 2 : 0; }
  constexpr int nx(int t) const { int n = 0; for (int u = first[t]; u < first[t + 1]; ++u) n += 3 + w1(u); return n; }
  constexpr int nreq(int t) const { return nx(t) + wfirst[t + 1] - wfirst[t]; }
  constexpr int xreq(int t, int j) const { int u = first[t]; while (j >= 3 + w1(u)) { j -= 3 + w1(u); ++u; } return 5 * u + j; }
  constexpr int slot(int t, int j) const {
    const int n = nreq(t);
    if (!SPREAD) return -1;
    if (n <= 6) return 4 * (j * 6 / n) + 1;
    if (n <= 12) return 2 * (j * 12 / n);
    return j * 24 / n;
  }
  constexpr int wait(int t) const {
    int need = pos[t + 1];
    if (PH == 1 && t == G1_K && posF > need) need = posF;
    if (PH == 1 && t == G1_K2 && posF2 > need) need = posF2;
    if (PH == 1 && t == G1_P && posY > need) need = posY;
    return before[t] - need;
  }
};
template <int PH>
DEVINL void g_phase(char* lds, char* ws, const char* xsrc, const char* wsrc1, const char* wsrc, u32x4 (&W)[32], char* ydst, unsigned yoff,
                    int fbase, int fbase2, int yflag, u64 tag, int rot, int w, stv_t& stv) {
  const int lane = lane_id();
  constexpr GPlan<PH> PL;
  constexpr int NST = PL.NST, NACC = PH == 1 ? 6 : 3, ROWS = PH == 1 ? 84 : 42;
  __amdgpu_buffer_rsrc_t xrs = mkrsrc(xsrc, PH == 1 ? X1_S + 16 * XST : 8 * XST);
  __amdgpu_buffer_rsrc_t yrs = mkrsrc(ydst, PH == 1 ? 2 * Y1_S : Y2_S);
  unsigned xo[3];
  int xd[3];
#pragma unroll
  for (int i = 0; i < 3; ++i) {
    const int piece = i * 4 + w, row = piece * 8 + (lane >> 3);
    xo[i] = row < 84 ? piece * 1024 + (i == 2 ? 8192 : 0) + lane * 16 : OOB;
    xd[i] = piece * 1024;
  }
  const unsigned lane16 = lane * 16;
  const bool dummy = (w == 3);
  unsigned xq[4];
#pragma unroll
  for (int i = 0; i < 4; ++i) xq[i] = ((i + rot) & 3) * ((PH == 1 ? 4 : 2) * XST);
  auto piece = [&](auto jc) {
    constexpr int u = decltype(jc)::v / 3, i = decltype(jc)::v % 3, slot = u % RING;
    constexpr int Q4 = PH == 1 ? 4 : 2;
    const unsigned so = xq[(u / Q4) & 3] + (u >= 16 ? X1_S : 0) + (u % Q4) * XST;
    dma16<SC1>(xrs, lds + (i == 2 && dummy ? RING_DUMMY : slot * SLOT + xd[i]), xo[i], so);
  };
  sgpr_settle();
  static_for<PL.PRO>([&](auto uc) {
    constexpr int u = decltype(uc)::v;
    static_for<3>([&](auto ic) { piece(IC<3 * u + decltype(ic)::v>{}); });
    static_for<PL.w1(u)>([&](auto ic) { constexpr int i = 2 * (u + WPRE) + decltype(ic)::v; ldfrag_rot<i>(W[i], wsrc1, rot, lane16); });
  });
  const int xa = (lane & 15) * 128 + (((lane >> 4) ^ ((lane & 15) >> 1)) * 16);
  int xb[4] = {xa, xa ^ 64, xa + 4 * SLOT, (xa ^ 64) + 4 * SLOT};
  asm volatile("" : "+v"(xb[2]), "+v"(xb[3]));
  const int za = (lane & 8) ? (xa & 255) + 512 + ((lane & 2) << 7) : xa;
  int xz[4] = {za, za ^ 64, za + 4 * SLOT, (za ^ 64) + 4 * SLOT};
  if (GZ) asm volatile("" : "+v"(xz[2]), "+v"(xz[3]));
  u32x4 xc[6], xs[6];
  auto ldx = [&](int pi, int slot) {
    const int ro = (PH == 1 ? pi * 16 : (pi / 3) * 42 + (pi % 3) * 16) * 128 + (slot & 3) * SLOT;
    if (GZ && PH == 1 && pi == 5) {
      xc[pi] = *(const u32x4*)(lds + xz[(slot >> 2) * 2] + ro);
      xs[pi] = *(const u32x4*)(lds + xz[(slot >> 2) * 2 + 1] + ro);
    } else {
      xc[pi] = *(const u32x4*)(lds + xb[(slot >> 2) * 2] + ro);
      xs[pi] = *(const u32x4*)(lds + xb[(slot >> 2) * 2 + 1] + ro);
    }
  };
  f32x4 R[NACC], Q[NACC];
  auto sty = [&](int n, unsigned base) {
    const int l = lane_id();
    const int tl = n * 16 + (l & 15);
    unsigned off = (tl >> 1) * 65536 + base + (tl & 1) * 64 + (l >> 4) * 16;
    off ^= (off >> 8) & 8192;
    __builtin_amdgcn_raw_buffer_store_b128(pack8(R[n], Q[n]), yrs, tl < ROWS ? off : OOB, 0, SC1);
  };
  wait_vm<PL.before[0] - PL.pos[0]>();
  static_for<PH == 1 ? 2 * WPRE : 32>([&](auto ic) { tie(W[decltype(ic)::v]); });
  bar();
  STAMP(PH == 1 ? 5 : (PH == 2 ? 14 : 18));
#pragma unroll
  for (int pi = 0; pi < 6; ++pi) ldx(pi, 0);
  u64 fv;
  unsigned foff;
  static_for<NST>([&](auto ic) {
    constexpr int st = decltype(ic)::v;
    constexpr bool MORE = st + 1 < NST;
    if constexpr (MORE) {
      wait_vm<PL.wait(st)>();
      if constexpr (PH == 1 && st + 1 >= WPRE && st + 1 < 16) { tie(W[2 * st + 2]); tie(W[2 * st + 3]); }
      if constexpr (PH == 1 && st == G1_K) { STAMP(8); flag_check<32>(fv, ws, foff, tag); }
      if constexpr (PH == 1 && st == G1_K2) { STAMP(21); flag_check<16>(fv, ws, foff, tag); STAMP(22); }
      if constexpr (!(GX & 1) && !((GX & 8) && (st & 1))) bar();
      if constexpr (PH == 1 && st == G1_K) STAMP(9);
      if constexpr (PH == 1 && st == 16) STAMP(6);
      if constexpr (PH == 1 && st == G1_P) { PRIO(PRIO_F); set_flag(ws, yflag, tag, w); PRIO(PRIO_G1); STAMP(7); }
      SB0();
      if constexpr (PH == 1 && st == G1_K - 2) { foff = (fbase + lane_id()) * 128; flag_peek<32>(fv, ws, foff); }
      if constexpr (PH == 1 && st == G1_K2 - 2) { foff = (fbase2 + lane_id()) * 128; flag_peek<16>(fv, ws, foff); }
    }
    auto req = [&](auto mc) {
      static_for<PL.nreq(st)>([&](auto jc) {
        constexpr int j = decltype(jc)::v, nx = PL.nx(st), xu = j < nx ? PL.xreq(st, j) / 5 : 0, xi = j < nx ? PL.xreq(st, j) % 5 : 0;
        if constexpr (PL.slot(st, j) == decltype(mc)::v && !(GX & 2)) {
          SB0();
          if constexpr (j < nx && xi < 3) piece(IC<3 * xu + xi>{});
          else if constexpr (j < nx) ldfrag_rot<2 * (xu + WPRE) + xi - 3>(W[2 * (xu + WPRE) + xi - 3], wsrc1, rot, lane16);
          else ldfrag_rot<PL.wfirst[st] + j - nx, PH == 1 ? 16384 : 8192>(W[PL.wfirst[st] + j - nx], wsrc, PH == 1 ? rot : 0, lane16);
          SB0();
        }
      });
    };
    req(IC<-1>{});
    u32x4 nw;
    constexpr f32x4 Z = {0, 0, 0, 0};
#define GM(a, b, c) MFMAU(a, b, c)
    if constexpr (GORD == 1 && PH == 1) {
      constexpr int ks = st & 15;
      static_for<24>([&](auto mc) {
        constexpr int m = decltype(mc)::v, n = m < 12 ? m >> 1 : (m - 12) % 6;
        if constexpr (st == 16 && m < 12 && !(m & 1)) sty(n, yoff);
        if constexpr (m < 12 && !(m & 1)) R[n] = GM(W[2 * ks], xc[n], ks == 0 ? Z : R[n]);
        else if constexpr (m < 12) Q[n] = GM(W[2 * ks], xs[n], ks == 0 ? Z : Q[n]);
        else if constexpr (m < 18) R[n] = GM(W[2 * ks + 1], xs[n], R[n]);
        else Q[n] = GM(nw, xc[n], Q[n]);
        if constexpr (m == 0) { tie(W[2 * ks + 1]); nw = W[2 * ks + 1] ^ 0x80008000u; }
        req(mc);
        if constexpr (MORE && m >= 12) {
          constexpr int slot = (st + 1) % RING, ro = n * 2048 + (slot & 3) * SLOT;
          if constexpr (m < 18) xs[n] = *(const u32x4*)(lds + xb[(slot >> 2) * 2 + 1] + ro);
          else xc[n] = *(const u32x4*)(lds + xb[(slot >> 2) * 2] + ro);
        }
        SB0();
      });
    } else
    static_for<6>([&](auto pc) {
      constexpr int pi = decltype(pc)::v, ks = PH == 1 ? st & 15 : 2 * st + pi / 3, n = PH == 1 ? pi : pi % 3;
      if constexpr (PH == 1 && st == 16) sty(n, yoff);
      if constexpr (GORD == 2) {
        if constexpr (n == 0) { tie(W[2 * ks + 1]); }
        R[n] = GM(W[2 * ks + 1], xs[pi], ks == 0 ? Z : R[n]);
        if constexpr (n == 0) { nw = W[2 * ks + 1] ^ 0x80008000u; }
        req(IC<4 * pi>{});
        Q[n] = GM(W[2 * ks], xs[pi], ks == 0 ? Z : Q[n]);
        req(IC<4 * pi + 1>{});
        R[n] = GM(W[2 * ks], xc[pi], R[n]);
        req(IC<4 * pi + 2>{});
        Q[n] = GM(nw, xc[pi], Q[n]);
        req(IC<4 * pi + 3>{});
      } else {
      R[n] = GM(W[2 * ks], xc[pi], ks == 0 ? Z : R[n]);
      if constexpr (n == 0) { tie(W[2 * ks + 1]); nw = W[2 * ks + 1] ^ 0x80008000u; }
      req(IC<4 * pi>{});
      Q[n] = GM(W[2 * ks], xs[pi], ks == 0 ? Z : Q[n]);
      req(IC<4 * pi + 1>{});
      R[n] = GM(W[2 * ks + 1], xs[pi], R[n]);
      req(IC<4 * pi + 2>{});
      Q[n] = GM(nw, xc[pi], Q[n]);
      req(IC<4 * pi + 3>{});
      }
      if constexpr (MORE && !(GX & 4)) ldx(pi, (st + 1) % RING);
      SB0();
    });
  });
  STAMP(PH == 1 ? 10 : (PH == 2 ? 15 : 19));
#pragma unroll
  for (int n = 0; n < NACC; ++n) sty(n, PH == 1 ? yoff + Y1_S : yoff);
  if constexpr (PH != 2) static_for<32 - PL.wfirst[NST]>([&](auto jc) { constexpr int i = PL.wfirst[NST] + decltype(jc)::v; ldfrag_rot<i, PH == 1 ? 16384 : 8192>(W[i], wsrc, PH == 1 ? rot : 0, lane16); });
}

DEVINL float jval(float r) {
  float t = __builtin_fmaf(-2.0f, r, 1.0f);
  asm("" : "+v"(t));
  return t;
}
template <bool FAST> DEVINL float jden(float f, float g) {
  float t = FAST ? __builtin_fmaf(f, g, 1.0f) : 1.0f + __builtin_amdgcn_exp2f(f + g);
  asm("" : "+v"(t));
  return t;
}
#define OUT_POL "sc1"
DEVINL void st_out(void* p, u32x4 v) { asm volatile("global_store_dwordx4 %0, %1, off " OUT_POL "\n\ts_nop 1" ::"v"(p), "v"(v) : "memory"); }
DEVINL void out_rows(char* orow, const u32x4& P0, const u32x4& P1) {
  u32x4 s1, s2;
#pragma unroll
  for (int i = 0; i < 4; ++i) {
    s1[i] = __builtin_amdgcn_update_dpp(P0[i], P1[i], 0x128, 0xf, 0xc, false);
    s2[i] = __builtin_amdgcn_update_dpp(P1[i], P0[i], 0x128, 0xf, 0x3, false);
  }
  st_out(orow, s1);
  st_out(orow + 8 * 4096, s2);
}
constexpr int J_Y = 32768, J_F = 65536, J_G = J_F + 8192;
constexpr int J_AH = 2;
template <bool FAST> DEVINL void j_make(const f32x4 (&fe)[4], char* lds, int u, int w, int lane) {
  const float* gl = (const float*)(lds + J_G + w * 2048) + 8 * (lane >> 4) + u * 64;
#pragma unroll
  for (int h = 0; h < 2; ++h) {
    const f32x4 g0 = *(const f32x4*)(gl + h * 32), g1 = *(const f32x4*)(gl + h * 32 + 4);
    f32x4 t0, t1;
#pragma unroll
    for (int r = 0; r < 4; ++r) {
      t0[r] = jval(__builtin_amdgcn_rcpf(jden<FAST>(fe[2 * h][r], g0[r])));
      t1[r] = jval(__builtin_amdgcn_rcpf(jden<FAST>(fe[2 * h + 1][r], g1[r])));
    }
    *(u32x4*)(lds + u * 8192 + (2 * w + h) * 1024 + lane * 16) = pack8(t0, t1);
  }
}
DEVINL void j_loop(u32x4 (&woa)[32], const f32x4 (&fe)[4], char* lds, int jw, int jstep, char* orow, int w, int lane, stv_t& stv) {
  constexpr int AH = J_AH;
  constexpr bool FAST = true;
  const float* gl = (const float*)(lds + J_G + w * 2048) + 8 * (lane >> 4);
  const char* jr = lds + lane * 16;
  f32x4 g[4];
  g[0] = *(const f32x4*)(gl + AH * 64); g[1] = *(const f32x4*)(gl + AH * 64 + 4);
  wait_lds();
  bar();
  STAMP(28);
  u32x4 B[8];
#pragma unroll
  for (int ks = 0; ks < 8; ++ks) B[ks] = *(const u32x4*)(jr + ks * 1024);
  f32x4 acc[4];
  u32x4 P0;
  static_for<8>([&](auto ic) {
    constexpr int u = decltype(ic)::v;
    constexpr bool FIRST = (u == 0), RD = (u + 1 < 8), J = (u + AH < 8);
    SB0();
    float d[16];
    unsigned jo[8];
    static_for<32>([&](auto qc) {
      constexpr int q = decltype(qc)::v, hf = q >> 4, ks = (q >> 1) & 7, m = 2 * hf + (q & 1);
      if constexpr (FIRST && q == 0) { wait_vm<16>(); static_for<16>([&](auto c) { tie(woa[decltype(c)::v]); }); }
      if constexpr (FIRST && q == 16) { wait_vm<0>(); static_for<16>([&](auto c) { tie(woa[16 + decltype(c)::v]); }); }
      if constexpr (RD && u >= 1 && q == 14) bar();
      if constexpr (ks == 0) acc[m] = MFMAU(woa[m * 8 + ks], B[ks], ((f32x4){0, 0, 0, 0}));
      else acc[m] = MFMAU(woa[m * 8 + ks], B[ks], acc[m]);
      if constexpr (RD && q >= 18 && !(q & 1)) B[ks - 1] = *(const u32x4*)(jr + (u + 1) * 8192 + (ks - 1) * 1024);
      if constexpr (!FIRST && q == 3) out_rows(orow + (u - 1) * 512, P0, pack8(acc[2], acc[3]));
      if constexpr (q == 19) P0 = pack8(acc[0], acc[1]);
      if constexpr (J) {
        if constexpr (q == 2) { g[2] = *(const f32x4*)(gl + (u + AH) * 64 + 32); g[3] = *(const f32x4*)(gl + (u + AH) * 64 + 36); }
        if constexpr (q >= 5 && q < 21) { constexpr int e = q - 5; d[e] = jden<FAST>(fe[e >> 2][e & 3], g[e >> 2][e & 3]); }
        if constexpr (q >= 7 && q < 23) { constexpr int e = q - 7; d[e] = __builtin_amdgcn_rcpf(d[e]); }
        if constexpr (q >= 9 && q < 25) { constexpr int e = q - 9; d[e] = jval(d[e]); }
        if constexpr (q >= 12 && q < 28 && !(q & 1)) { constexpr int e = q - 11; jo[e >> 1] = pack_bf16(d[e - 1], d[e]); }
        if constexpr (q == 19) *(u32x4*)(lds + jw) = (u32x4){jo[0], jo[1], jo[2], jo[3]};
        if constexpr (q == 27) { *(u32x4*)(lds + jw + 1024) = (u32x4){jo[4], jo[5], jo[6], jo[7]}; jw += jstep; asm("" : "+v"(jw)); }
        if constexpr (u + AH + 1 < 8 && q == 29) { g[0] = *(const f32x4*)(gl + (u + AH + 1) * 64); g[1] = *(const f32x4*)(gl + (u + AH + 1) * 64 + 4); }
      }
      SB0();
    });
    if constexpr (RD) B[7] = *(const u32x4*)(jr + (u + 1) * 8192 + 7 * 1024);
    SB0();
  });
  out_rows(orow + 7 * 512, P0, pack8(acc[2], acc[3]));
}

#define HINT 0
#define TAGLATE 0
#define XLOOP 1
#define ETOUCH 4096
#define ROT 1
#define W2_PACE 1
constexpr int M_HS = 96;

DEVINL void x_issue(const u16* __restrict__ x, char* reg, int s, int tp, int par, int ch, bool live, int lane) {
  __amdgpu_buffer_rsrc_t rs = mkrsrc(x + (size_t)s * T0 * 256, T0 * 512);
  const int t0 = 96 * tp - 111 + par + 2 * (lane >> 3);
  const int base = t0 * 512 + ch * 128 + (((lane & 7) ^ (4 * ((lane >> 4) & 1))) * 16);
  unsigned o0 = (tp == 0 || t0 < 0) ? OOB : (unsigned)base, o1 = tp == 0 ? OOB : (unsigned)(base + 8192), so;
  if (live) asm volatile("s_mov_b32 m0, %4\n\ts_mov_b32 %0, 6\n\ts_nop 2\n\tbuffer_load_dwordx4 %2, %3, 0 offen lds\n"
               "1:\n\ts_add_u32 m0, m0, 1024\n\ts_sub_u32 %0, %0, 1\n\tbuffer_load_dwordx4 %1, %3, 0 offen lds\n\tv_add_u32 %1, 0x2000, %1\n\ts_cbranch_scc0 1b ;@L7"
               : "=&s"(so), "+v"(o1) : "v"(o0), "s"(rs), "s"((unsigned)(size_t)(lds_void_p)reg) : "scc", "memory");
}
DEVINL u64 load_tag(const char* ws) {
  u64 tag;
  asm volatile("s_load_dwordx2 %0, %1, 0x0\n\ts_waitcnt lgkmcnt(0)" : "=s"(tag) : "s"(ws + OFF_FLAG + F_EPOCH * 128) : "memory");
  return tag * 6364136223846793005ull + 1442695040888963407ull;
}

extern "C" __global__ __launch_bounds__(256, 2) void k_all(const u16* __restrict__ x, const u16* __restrict__ w1, const u16* __restrict__ w2, const u16* __restrict__ g_,
                                                const u16* __restrict__ wf, const u16* __restrict__ wg, const u16* __restrict__ wo,
                                                char* __restrict__ ws_, char* __restrict__ out) {
  constexpr unsigned pb = 0;
  char* const ws = ws_ + (-(size_t)ws_ & ((2 << 20) - 1));
  __shared__ __attribute__((aligned(16))) char lds[LDS_BYTES];
  int lane = threadIdx.x & 63;
  const int w = __builtin_amdgcn_readfirstlane(threadIdx.x >> 6);
  const int role = blockIdx.x >> 8, xl = blockIdx.x & 7, idx = (blockIdx.x >> 3) & 31;
  const int q = xl * 32 + idx;
  stv_t stv = {};
  STAMP(0);
  u64 tag;

  const int eu = w == (idx & 3) ? idx : ((idx & 3) == 0 && w == 2 ? 32 + (idx >> 2) : -1);
  const int tpv[2] = {xl * 21 + (idx >> 1), xl * 21 + 16 + (eu >> 3)}, parv[2] = {idx & 1, (eu >> 2) & 1}, chv[2] = {w, eu & 3};
  u32x4 F[8];
  static_for<4>([&](auto ic) { constexpr int i = decltype(ic)::v; ldfrag<i, false>(F[i], (const char*)TAB + (role ? TAB_F : TAB_T) * 1024, lane * 16); });
  asm volatile("s_cmp_eq_u32 %6, 0\n\ts_cbranch_scc1 1f\n\t;@f\n\tglobal_load_dwordx4 %0, %4, %5\n\tglobal_load_dwordx4 %1, %4, %5 offset:1024\n\t"
               "global_load_dwordx4 %2, %4, %5 offset:2048\n\tglobal_load_dwordx4 %3, %4, %5 offset:3072\n1:"
               : "=&v"(F[4]), "=&v"(F[5]), "=&v"(F[6]), "=&v"(F[7]) : "v"(lane * 16), "s"((const char*)TAB + (TAB_F + 4) * 1024), "s"(role) : "scc", "memory");
  asm volatile("" :: "s"(x), "s"(w1), "s"(ws_));
  int role2 = role;
  if (role2 == 0) {
    asm volatile(";@g");
    PRIO(PRIO_G0);
    w_issue(w1, lds, q, w, lane, true);
  }
  asm volatile("" : "+s"(role2));
  if (role2 != 0) {
    asm volatile(";@t");
    PRIO(PRIO_T0);
    x_issue(x, lds + w * 8192, 0, tpv[0], parv[0], chv[0], true, lane);
    x_issue(x, lds + 65536 + (eu >> 5) * 8192, 0, tpv[1], parv[1], chv[1], eu >= 0, lane);
  }
  unsigned per;
  {
    u64 pc;
    unsigned a, b;
    asm volatile("s_getpc_b64 %0\n.Lpe:\n\ts_mov_b32 %1, .Lg_beg-.Lpe\n\ts_mov_b32 %2, .Lt_beg-.Lpe" : "=s"(pc), "=s"(a), "=s"(b));
    const unsigned o = (idx * 4 + w) * 64;
    asm volatile("s_load_dword %0, %1, %2" : "=s"(per) : "s"(pc), "s"(((role ? b : a) + (o < ETOUCH ? o : 0)) & ~3u) : "memory");
  }
  asm volatile("" : "+s"(role2));

  u32x4 W[32];
  if (__builtin_expect(role2 == 0, 1)) {
    asm volatile(";@Ng\n;@r\n.Lg_beg:");
    const unsigned lane16 = lane * 16;
    {
      const int px = (xl + 1) & 7;
      const char* probe = ws + OFF_W2F + (px & 3) * 4 * WF_G + (idx & 15) * 65536 + (px >> 1) * 16384 + (px & 1) * 4096 + (idx >> 4) * 1024;
      if (!TAGLATE) { unsigned d; asm volatile("s_load_dword %0, %1, 0x100" : "=s"(d) : "s"(probe) : "memory"); tag = load_tag(ws); asm volatile("" :: "s"(d)); }
      wait_vm<0>();
      wait_lds();
      asm volatile("" :: "s"(per));
      static_for<4>([&](auto ic) { tie(F[decltype(ic)::v]); });
      const u32x4 Tm[4] = {F[0], F[1], F[2], F[3]};
      bar();
      STAMP(1);
      w_compute<16>(Tm, ws + OFF_W1F, lds, q, w, lane, pb);
      if (TAGLATE) { unsigned d; asm volatile("s_load_dword %0, %1, 0x100" : "=s"(d) : "s"(probe) : "memory"); tag = load_tag(ws); asm volatile("" :: "s"(d)); }
      int vote = 0;
      if (w == 0) vote = (xl >> 2) ^ near_side(probe, (idx >> 2) & 1);
      publish(ws, F_W1 + (q & 3) * 64 + (q >> 2), tag ^ ((u64)vote << 63), w);
      STAMP(2);
    }
    const int TB = (idx >> 2) & 1, cb = idx & 3;
    ((int*)lds)[w] = poll_vote(ws, F_W1 + cb * 64 + w * 16 + lane, tag);
    wait_lds();
    bar();
    const u32x4 votes = *(const u32x4*)lds;
    const int grp = (xl * 4 + (idx >> 3)) ^ (__builtin_amdgcn_readfirstlane(votes[0] + votes[1] + votes[2] + votes[3]) > 32 ? 16 : 0);
    const int gflag = (TB * 4 + cb) * 32 + grp, rot = ROT ? cb + 2 * TB : 0;
    const int gl = grp & 15;
    const unsigned sb = pb ^ ((grp >> 4) << 13);
    const unsigned yoff = (gl >> 2) * 16384 + sb + (gl & 3) * 2048 + (cb * 4 + w) * 128;
    const size_t woff = gl * WF_G + (cb * 4 + w) * 65536 + (sb ^ ((gl & 2) << 12));
    const size_t x1off = gl * X1_G + TB * X1_TB + (sb ^ ((gl & 2) << 12)), x2off = gl * X2_G + TB * X2_TB + (sb ^ ((gl & 4) << 11));
    {
      STAMP(3);
      PRIO(PRIO_GW);
      static_for<2 * WPRE>([&](auto ic) {
        constexpr int i = decltype(ic)::v;
        if constexpr (i >= WL_PACE) wait_vm<WL_PACE - 1>();
        ldfrag_rot<i>(W[i], ws + OFF_W1F + woff, rot, lane16);
      });
      PRIO(PRIO_F);
      poll(ws, F_X + TB * 128 + w * 32 + lane, lanes(32), tag);
      bar();
      if (HINT) set_flag(ws, F_H + q, tag, w);
      STAMP(4);
      PRIO(PRIO_G1);
      g_phase<1>(lds, ws, ws + OFF_X1F + x1off, ws + OFF_W1F + woff, ws + OFF_W2F + woff, W, ws + OFF_Y1F, yoff + TB * 42 * 65536,
                 F_X + 256 + TB * 128 + w * 32, F_W2 + cb * 64 + w * 16, F_G1 + gflag, tag, rot, w, stv);
      publish(ws, F_G1 + 256 + gflag, tag, w);
      STAMP(11);
    }
    lane = lane_id();
    poll(ws, F_M + TB * 112 + w * 28 + lane, lanes(28), tag);
    bar();
    STAMP(13);
    PRIO(PRIO_G2A);
    g_phase<2>(lds, ws, ws + OFF_X2F + x2off, nullptr, nullptr, W, ws + OFF_Y2F, yoff + TB * 21 * 65536, 0, 0, 0, tag, rot, w, stv);
    publish(ws, F_G2 + gflag, tag, w);
    STAMP(16);
    lane = lane_id();
    poll(ws, lane >= 28 ? F_S + w * 3 + lane - 28 : F_M + 256 + TB * 112 + w * 28 + lane, lanes(31), tag);
    bar();
    STAMP(17);
    PRIO(PRIO_G2B);
    g_phase<3>(lds, ws, ws + OFF_X2F + X2_S + x2off, nullptr, ws + OFF_WFT + w * 32768, W, ws + OFF_Y2F + Y2_S, yoff + TB * 21 * 65536, 0, 0, 0,
               tag, rot, w, stv);
    publish(ws, F_G2 + 256 + gflag, tag, w);
    STAMP(20);
    PRIO(PRIO_JB);
  } else {
    asm volatile(";@Nft\n;@r\n.Lt_beg:");
    const int G = lane >> 4, rq = (lane & 15) >> 2, p = lane & 3;
    const unsigned lane16 = lane * 16;
    {
      u32x4 Tm[4];
      tag = load_tag(ws);
      asm volatile("" :: "s"(per));
      wait_vm<8>();
      if (eu < 0) wait_vm<0>();
      tie(F);
      STAMP(1);
      if (XA_SLEEP) __builtin_amdgcn_s_sleep(XA_SLEEP);
#pragma unroll 1
      for (int s = 0; s < 2; ++s) {
#pragma unroll 1
        for (int r = 0; r < (eu >= 0 ? 2 : 1); ++r) {
          if (r) wait_vm<8>();
          const int tl = (r ? tpv[1] : tpv[0]) - 84 * (xl >> 2);
          char* gd = ws + OFF_X1F + s * X1_S + (xl >> 2) * X1_TB + (size_t)(r ? parv[1] * 8 + chv[1] * 2 : parv[0] * 8 + chv[0] * 2) * XST;
          const unsigned lo = x_lane_off<X1_G>(lane, tl, tl, pb);
          const char* reg = lds + (r == s ? w * 8192 : (s ? 32768 + w * 8192 : 65536 + (eu >> 5) * 8192));
#pragma unroll
          for (int h = 0; h < 2; ++h) {
            const char* la = reg + (8 * G + rq) * 128 + (((4 * h + p) ^ (4 * ((rq >> 1) & 1))) * 16);
            fwd_unit<128, X1_G>(la, F, gd + h * XST, lo);
          }
        }
        publish(ws, F_X + s * 256 + q, tag, w);
        STAMP2(2, 4);
        if (s == 0) {
          PRIO(PRIO_T1);
          unsigned ct;
          code_touch(ct, idx * 4 + w);
          if (XB_SLEEP) __builtin_amdgcn_s_sleep(XB_SLEEP);
          x_issue(x, lds + 32768 + w * 8192, 1, tpv[0], parv[0], chv[0], true, lane);
          x_issue(x, lds + w * 8192, 1, tpv[1], parv[1], chv[1], eu >= 0, lane);
          if (HINT) poll(ws, F_H + q, lanes(2), tag); else wait_vm<0>();
          wait_lds();
          asm volatile("" :: "s"(ct));
          STAMP(3);
          w_issue(w2, lds + 65536, q, w, lane, true);
        }
      }
      PRIO(PRIO_T2);
      static_for<4>([&](auto ic) { constexpr int i = decltype(ic)::v; ldfrag<TAB_T + i, false>(Tm[i], (const char*)TAB, lane16); });
      wait_vm<0>();
      tie(Tm);
      w_compute<W2_PACE>(Tm, ws + OFF_W2F, lds + 65536, q, w, lane, pb);
      publish(ws, F_W2 + (q & 3) * 64 + (q >> 2), tag, w);
      STAMP(5);
      if (idx >= 28 && (idx - 28) * 8 + xl < 12) {
        PRIO(PRIO_T2);
        const int sid = (idx - 28) * 8 + xl, t = sid * 4 + w;
        char* reg = lds + w * 8192;
        if (t < 16) s_transpose(wf, ws + OFF_WFT, reg, t >> 1, t & 1, lane);
        else if (t < 32) s_transpose(wo, ws + OFF_WOT, reg, (t - 16) >> 1, t & 1, lane);
        else s_ge(g_, wg, (float*)(ws + OFF_GE), reg, t - 32, lane);
        publish(ws, F_S + sid, tag, w);
      }
    }
    if (idx < 28) {
      u32x4 Gt[6], F[2];
      char* reg = lds + w * 8192;
      char* him = lds + 32768;
      const int pr = 3 * idx + w;
      static_for<6>([&](auto ic) { constexpr int i = decltype(ic)::v; ldfrag<TAB_G + i, false>(Gt[i], (const char*)TAB, lane16); });
      static_for<2>([&](auto ic) { constexpr int i = decltype(ic)::v; ldfrag<i, false>(F[i], (const char*)TAB + (TAB_F + 2 * w) * 1024, lane16); });
#pragma unroll 1
      for (int s = 0; s < 2; ++s) {
        poll(ws, F_G1 + s * 256 + ((idx >= 14 || lane >= 8 ? 4 : 0) + (xl >> 1)) * 32 + w * 8 + (lane & 7), idx == 13 ? lanes(16) : lanes(8), tag);
        bar();
        if (s == 0) { PRIO(PRIO_MA); } else { PRIO(PRIO_MB); }
        tie(Gt); tie(F);
        __amdgpu_buffer_rsrc_t rs = mkrsrc(ws + OFF_Y1F + s * Y1_S, 84 * 65536);
        sgpr_settle();
#pragma unroll
        for (int i = 0; i < 8; ++i) {
          const unsigned off = pr * 65536 + (i & 3) * 16384 + (((i >> 2) << 13) ^ ((pr & 32) << 8) ^ pb) + G * 2048 + xl * 256 + (((lane & 15) ^ ((G & 1) << 2) ^ (i & 1)) * 16);
          dma16<SC1>(rs, reg + i * 1024, off, 0);
        }
        STAMP2(6, 10);
        wait_vm<0>();
        STAMP2(7, 11);
        bf16x8 A[2][2][2];
#pragma unroll
        for (int tl = 0; tl < 2; ++tl)
#pragma unroll
          for (int i = 0; i < 2; ++i)
#pragma unroll
            for (int ks = 0; ks < 2; ++ks) {
              const int ch = (((p >> 1) * 8 + tl * 4 + 2 * (p & 1) + i) ^ ((rq >> 1) << 2) ^ (G & 1));
              A[tl][i][ks] = tr_frag(reg + (16 * ks + 4 * G + (rq >> 1)) * 256 + ch * 16 + (rq & 1) * 8, 512);
            }
        f32x4 c[2][2];
        auto idft = [&](int b) {
          constexpr f32x4 Z = {0, 0, 0, 0};
#pragma unroll
          for (int i = 0; i < 2; ++i) c[b & 1][i] = MFMAU(A[b / 3][i][0], Gt[2 * (b % 3)], Z);
#pragma unroll
          for (int i = 0; i < 2; ++i) c[b & 1][i] = MFMAU(A[b / 3][i][1], Gt[2 * (b % 3) + 1], c[b & 1][i]);
        };
        idft(0);
        const unsigned keep = pr == 83 ? 0u : ~0u;
#pragma unroll
        for (int b = 0; b < 6; ++b) {
          if (b + 1 < 6) idft(b + 1);
          SB0();
          u32x4 o = gelu_pack8(c[b & 1][0], c[b & 1][1]);
          if (b == 5) o &= keep;
          *(u32x4*)(him + (w * 96 + b * 16 + (lane & 15)) * M_HS + G * 16) = o;
          SB0();
        }
        wait_lds();
        bar();
        STAMP2(8, 12);
        const int TB2 = idx >= 14;
        char* gd = ws + OFF_X2F + s * X2_S + TB2 * X2_TB + (size_t)(xl >> 1) * XST + (size_t)(w & 1) * 8 * X2_G;
        const char* la = him + (33 + 2 * (8 * G + rq)) * M_HS + p * 16;
        bf16x8 B[6][2][2];
#pragma unroll
        for (int u = 0; u < 6; ++u)
#pragma unroll
          for (int i = 0; i < 2; ++i)
#pragma unroll
            for (int ks = 0; ks < 2; ++ks) B[u][i][ks] = tr_frag(la + ((u >> 1) * 96 + (u & 1) + 64 * ks) * M_HS + 8 * i, 8 * M_HS);
#pragma unroll
        for (int u = 0; u < 6; ++u) {
          f32x4 c0 = {0, 0, 0, 0}, c1 = {0, 0, 0, 0};
#pragma unroll
          for (int ks = 0; ks < 2; ++ks) { c0 = MFMAU(B[u][0][ks], F[ks], c0); c1 = MFMAU(B[u][1][ks], F[ks], c1); }
          const int tl = 3 * idx - 42 * TB2 + (u >> 1);
          st16(gd + x_lane_off<X2_G>(lane, tl, tl + 42 * (xl & 1), pb ^ ((w >> 1) << 13)) + (u & 1) * 4 * XST, pack8(c0, c1));
        }
        publish(ws, F_M + s * 256 + idx * 8 + xl, tag, w);
        STAMP2(9, 13);
      }
    }
    poll(ws, F_S + w * 3 + lane, lanes(3), tag);
    bar();
    PRIO(PRIO_JA);
    sgpr_settle();
    static_for<32>([&](auto ic) { constexpr int i = decltype(ic)::v; ldfrag<i, true>(W[i], ws + OFF_WFT + w * 32768, lane16); });
    wait_vm<0>();
    asm volatile(".Lt_end:");
  }
  asm volatile(";@Nr");
  tie(W);
  const int s = role ^ 1;
  lane = lane_id();
  const int G = lane >> 4, rq = (lane & 15) >> 2, p = lane & 3;
  const unsigned lane16 = lane * 16;

  const int jb = q;
  if (jb >= 250) {
    if (blockIdx.x == 255 && w == 0) {
#pragma unroll 1
      for (int i = 4; i < 8; ++i) poll(ws, F_G2 + i * 64 + lane, ~0ull, tag);
      set_flag(ws, F_EPOCH, tag, w);
    }
    STAMP_FLUSH();
    return;
  }
  {
    const int tile = jb / 3, vt = jb - 3 * tile;
    u32x4 woa[32], Gt[2];
    sgpr_settle();
    static_for<2>([&](auto ic) { constexpr int i = decltype(ic)::v; ldfrag<i, false>(Gt[i], (const char*)TAB + (TAB_G + 2 * vt) * 1024, lane16); });
    __amdgpu_buffer_rsrc_t grs = mkrsrc(ws + OFF_GE, 32768);
#pragma unroll
    for (int i = 0; i < 2; ++i) dma16<SC1>(grs, lds + J_G + w * 2048 + i * 1024, 16384 + (s * 4 + w) * 2048 + i * 1024 + lane16, 0);
    static_for<16>([&](auto ic) { constexpr int i = decltype(ic)::v; ldfrag<i, true>(woa[i], ws + OFF_WOT + w * 32768, lane16); });
    PRIO(PRIO_F);
    poll(ws, F_G2 + s * 256 + ((tile >= 42 ? 4 : 0) + w) * 32 + lane, lanes(32), tag);
    if (role) { PRIO(PRIO_JA); } else { PRIO(PRIO_JB); }
    tie(Gt);
    STAMP(24);
    char* reg = lds + J_Y + w * 8192;
    {
      __amdgpu_buffer_rsrc_t rs = mkrsrc(ws + OFF_Y2F + s * Y2_S + (size_t)(tile >> 1) * 65536, 65536);
#pragma unroll
      for (int i = 0; i < 8; ++i) {
        const int c = (lane & 15) ^ ((G & 1) << 3) ^ (i & 1);
        dma16<SC1>(rs, reg + i * 1024, (i & 3) * 16384 + (((i >> 2) << 13) ^ ((tile & 64) << 7) ^ pb) + G * 2048 + (4 * w + (c >> 2)) * 128 + (tile & 1) * 64 + (c & 3) * 16, 0);
      }
    }
    wait_vm<0>();
    STAMP(25);
#pragma unroll
    for (int h = 0; h < 2; ++h) {
      f32x4 c[2];
#pragma unroll
      for (int i = 0; i < 2; ++i) {
        const int ch = (8 * h + 2 * p + i) ^ ((rq >> 1) << 3) ^ (G & 1);
        const char* la = reg + (4 * G + (rq >> 1)) * 256 + ch * 16 + (rq & 1) * 8;
        c[i] = MFMAU(tr_frag(la, 512), Gt[0], ((f32x4){0, 0, 0, 0}));
        c[i] = MFMAU(tr_frag(la + 4096, 512), Gt[1], c[i]);
      }
      *(u32x4*)(lds + J_F + (2 * w + h) * 1024 + lane16) = gelu_pack8(c[0], c[1]);
    }
    wait_lds();
    bar();
    STAMP(26);
    f32x4 fe[4];
#pragma unroll
    for (int m = 0; m < 4; ++m) fe[m] = (f32x4){0, 0, 0, 0};
#pragma unroll
    for (int ks = 0; ks < 8; ++ks) {
      const u32x4 bfr = *(const u32x4*)(lds + J_F + ks * 1024 + lane16);
#pragma unroll
      for (int m = 0; m < 4; ++m) fe[m] = MFMAU(W[m * 8 + ks], bfr, fe[m]);
    }
    SB0();
    static_for<16>([&](auto ic) { constexpr int i = 16 + decltype(ic)::v; ldfrag<i, true>(woa[i], ws + OFF_WOT + w * 32768, lane16); });
    SB0();
    bool small = true;
#pragma unroll
    for (int m = 0; m < 4; ++m) {
      fe[m] *= LOG2E2;
#pragma unroll
      for (int r = 0; r < 4; ++r) small = small && (__builtin_fabsf(fe[m][r]) <= 32.0f);
    }
    STAMP(27);
    char* orow = out + ((size_t)(s * T2 + jb * 16 + (lane & 7)) * 8) * 512 + w * 128 + ((lane >> 3) & 1) * 64 + G * 16;
    const bool fast = __all(small);
    if (fast) {
#pragma unroll
      for (int m = 0; m < 4; ++m)
#pragma unroll
        for (int r = 0; r < 4; ++r) fe[m][r] = __builtin_amdgcn_exp2f(fe[m][r]);
#pragma unroll
      for (int u = 0; u < J_AH; ++u) j_make<true>(fe, lds, u, w, lane);
    } else {
#pragma unroll
      for (int i = 0; i < 2; ++i) dma16<SC1>(grs, lds + J_G + w * 2048 + i * 1024, (s * 4 + w) * 2048 + i * 1024 + lane16, 0);
      wait_vm<0>();
#pragma unroll 1
      for (int u = 0; u < 8; ++u) j_make<false>(fe, lds, u, w, lane);
    }
    j_loop(woa, fe, lds, (fast ? J_AH * 8192 : J_F) + 2 * w * 1024 + lane16, fast ? 8192 : 0, orow, w, lane, stv);
  }
  asm volatile(".Lk_end:");
  STAMP(29);
}

extern "C" void kernel_launch(void* const* d_in, const int* in_sizes, int n_in, void* d_out, int out_size, void* d_ws,
                              size_t ws_size, hipStream_t stream) {
  k_all<<<512, 256, 0, stream>>>((const u16*)d_in[0], (const u16*)d_in[1], (const u16*)d_in[2], (const u16*)d_in[3], (const u16*)d_in[4],
                                 (const u16*)d_in[5], (const u16*)d_in[6], (char*)d_ws, (char*)d_out);
}
```

```cpp
#include <hip/hip_runtime.h>
#include <hip/hip_bf16.h>
#include <utility>
__device__ const unsigned TAB[4608] __attribute__((aligned(128))) = {
0x3f803f80,0x3f803f80,0x3f803f80,0x3f803f80,0x0,0x0,0x0,0x0,
0x3f7f3f80,0x3f753f7b,0x3f613f6c,0x3f443f54,0x3dcc0000,0x3e973e4b,0x3ef53ec7,0x3f253f10,
0x3f7b3f80,0x3f543f6c,0x3f0b3f33,0x3e323ebb,0x3e4b0000,0x3f103ec7,0x3f573f37,0x3f7c3f6e,
0x3f753f80,0x3f203f54,0x3d993ebb,0xbf00be64,0x3e970000,0x3f483f10,0x3f7f3f6e,0x3f5e3f7a,
0x3f6c3f80,0x3ebb3f33,0xbed3bccc,0xbf71bf3c,0x3ec70000,0x3f6e3f37,0x3f693f80,0x3eaf3f2e,
0x3f613f80,0x3d993f0b,0xbf4cbed3,0xbf71bf7d,0x3ef50000,0x3f7f3f57,0x3f1b3f69,0xbeaf3e19,
0x3f543f80,0xbe643ebb,0xbf7dbf3c,0xbf00bf67,0x3f100000,0x3f7a3f6e,0x3e193f2e,0xbf5ebede,
0x3f443f80,0xbf003e32,0xbf71bf71,0x3e32bf00,0x3f250000,0x3f5e3f7c,0xbeaf3eaf,0xbf7cbf5e,
0x3f803f80,0x3f803f80,0x3f803f80,0x3f803f80,0x0,0x0,0x0,0x0,
0x3f203f33,0x3eea3f0b,0x3e8b3ebb,0x3d993e32,0x3f483f37,0x3f643f57,0x3f763f6e,0x3f7f3f7c,
0xbe64bccc,0xbf15bed3,0xbf5abf3c,0xbf7dbf71,0x3f7a3f80,0x3f503f69,0x3f053f2e,0x3e193eaf,
0xbf67bf3c,0xbf7dbf7d,0xbf3cbf67,0xbe64bf00,0x3ede3f2e,0xbe193e19,0xbf2ebede,0xbf7abf5e,
0xbf67bf80,0xbea3bf29,0x3eea3d99,0x3f753f44,0xbedebd4c,0xbf73bf40,0xbf64bf7f,0xbe97bf25,
0xbe64bf29,0x3f333e8b,0x3f7b3f75,0x3ebb3f44,0xbf7abf40,0xbf37bf76,0x3e4bbe97,0x3f6e3f25,
0x3f203d99,0x3f753f75,0x3d993f20,0xbf67bf00,0xbf48bf7f,0x3e97be97,0x3f7f3f48,0x3ede3f5e,
0x3f803f44,0x3e323f44,0xbf71bf00,0xbf00bf71,0xbf25,0x3f7c3f25,0x3eaf3f5e,0xbf5ebeaf,
0x3f803f80,0x3f803f80,0x3f803f80,0x3f803f80,0x0,0x0,0x0,0x0,
0xbdffbccc,0xbea3be64,0xbf00bed3,0xbf29bf15,0x3f7e3f80,0x3f733f7a,0x3f5e3f69,0x3f403f50,
0xbf78bf80,0xbf4cbf67,0xbf00bf29,0xbdffbea3,0xbe7dbd4c,0xbf1bbede,0xbf5ebf40,0xbf7ebf73,
0x3ebb3d99,0x3f543f20,0x3f803f75,0x3f543f75,0xbf6ebf7f,0xbf10bf48,0xbe97,0x3f103e97,
0x3f613f7f,0x3e8b3f20,0xbf00bdff,0xbf78bf4c,0x3ef53dcc,0x3f763f48,0x3f5e3f7e,0x3e7d3f1b,
0xbf15bdff,0xbf80bf67,0xbf00bf5a,0x3eeabccc,0x3f503f7e,0xbd4c3ede,0xbf5ebf05,0xbf64bf80,
0xbf3cbf7d,0x3ebbbe64,0x3f803f54,0x3ebb3f54,0xbf2ebe19,0xbf6ebf7a,0xbf10,0x3f6e3f10,
0x3f443e32,0x3f443f80,0xbf003e32,0xbf71bf71,0xbf25bf7c,0x3f250000,0x3f5e3f7c,0xbeaf3eaf,
0x3f803f80,0x3f803f80,0x3f803f80,0x3f803f80,0x0,0x0,0x0,0x0,
0xbf4cbf3c,0xbf67bf5a,0xbf78bf71,0xbf80bf7d,0x3f1b3f2e,0x3ede3f05,0x3e7d3eaf,0x3d4c3e19,
0x3e8b3d99,0x3f203eea,0x3f613f44,0x3f7f3f75,0xbf76bf7f,0xbf48bf64,0xbef5bf25,0xbdccbe97,
0x3ebb3f20,0xbe643d99,0xbf3cbf00,0xbf7dbf67,0x3f6e3f48,0x3f7a3f7f,0x3f2e3f5e,0x3e193ede,
0xbf5abf7d,0xbe64bf15,0x3f0b3e32,0x3f7b3f54,0xbf05be19,0xbf7abf50,0xbf57bf7c,0xbe4bbf10,
0x3f7f3f54,0x3f203f6c,0xbea33e32,0xbf78bf3c,0xbdccbf10,0x3f483ec7,0x3f733f7c,0x3e7d3f2e,
0xbf3cbe64,0xbf67bf7d,0x3d99bf00,0x3f753f20,0x3f2e3f7a,0xbede3e19,0xbf7fbf5e,0xbe97bf48,
0x3e32bf00,0x3f803f44,0x3e323f44,0xbf71bf00,0xbf7cbf5e,0xbf25,0x3f7c3f25,0x3eaf3f5e,
0x3f803f80,0x3f803f80,0x3f803f80,0x3f803f80,0x0,0x0,0x0,0x0,
0xbf7dbf80,0xbf71bf78,0xbf5abf67,0xbf3cbf4c,0xbe19bd4c,0xbeafbe7d,0xbf05bede,0xbf2ebf1b,
0x3f753f7f,0x3f443f61,0x3eea3f20,0x3d993e8b,0x3e973dcc,0x3f253ef5,0x3f643f48,0x3f7f3f76,
0xbf67bf7d,0xbf00bf3c,0x3d99be64,0x3f203ebb,0xbedebe19,0xbf5ebf2e,0xbf7fbf7a,0xbf48bf6e,
0x3f543f7b,0x3e323f0b,0xbf15be64,0xbf7dbf5a,0x3f103e4b,0x3f7c3f57,0x3f503f7a,0x3e193f05,
0xbf3cbf78,0x3e32bea3,0x3f6c3f20,0x3f543f7f,0xbf2ebe7d,0xbf7cbf73,0xbec7bf48,0x3f103dcc,
0x3f203f75,0xbf003d99,0xbf7dbf67,0xbe64bf3c,0x3f483e97,0x3f5e3f7f,0xbe193ede,0xbf7abf2e,
0xbf00bf71,0x3f443e32,0x3f443f80,0xbf003e32,0xbf5ebeaf,0xbf25bf7c,0x3f250000,0x3f5e3f7c,
0x3f803f80,0x3f803f80,0x3f803f80,0x3f803f80,0x0,0x0,0x0,0x0,
0xbf15bf29,0xbed3bf00,0xbe64bea3,0xbcccbdff,0xbf50bf40,0xbf69bf5e,0xbf7abf73,0xbf80bf7e,
0xbea3bdff,0xbf29bf00,0xbf67bf4c,0xbf80bf78,0x3f733f7e,0x3f403f5e,0x3ede3f1b,0x3d4c3e7d,
0x3f753f54,0x3f753f80,0x3f203f54,0x3d993ebb,0xbe97bf10,0x3e970000,0x3f483f10,0x3f7f3f6e,
0xbf4cbf78,0xbdffbf00,0x3f203e8b,0x3f7f3f61,0xbf1bbe7d,0xbf7ebf5e,0xbf48bf76,0xbdccbef5,
0xbccc3eea,0xbf5abf00,0xbf67bf80,0xbdffbf15,0x3f803f64,0x3f053f5e,0xbede3d4c,0xbf7ebf50,
0x3f543ebb,0x3f543f80,0xbe643ebb,0xbf7dbf3c,0xbf10bf6e,0x3f100000,0x3f7a3f6e,0x3e193f2e,
0xbf71bf71,0x3e32bf00,0x3f803f44,0x3e323f44,0xbeaf3eaf,0xbf7cbf5e,0xbf25,0x3f7c3f25,
0x3f803f80,0x3f803f80,0x3f803f80,0x3f803f80,0x0,0x0,0x0,0x0,
0x3e323d99,0x3ebb3e8b,0x3f0b3eea,0x3f333f20,0xbf7cbf7f,0xbf6ebf76,0xbf57bf64,0xbf37bf48,
0xbf71bf7d,0xbf3cbf5a,0xbed3bf15,0xbcccbe64,0xbeafbe19,0xbf2ebf05,0xbf69bf50,0xbf80bf7a,
0xbf00be64,0xbf67bf3c,0xbf7dbf7d,0xbf3cbf67,0x3f5e3f7a,0x3ede3f2e,0xbe193e19,0xbf2ebede,
0x3f443f75,0x3d993eea,0xbf29bea3,0xbf80bf67,0x3f253e97,0x3f7f3f64,0x3f403f73,0x3d4c3ede,
0x3f443ebb,0x3f753f7b,0x3e8b3f33,0xbf29be64,0xbf25bf6e,0x3e97be4b,0x3f763f37,0x3f403f7a,
0xbf00bf67,0x3f203d99,0x3f753f75,0x3d993f20,0xbf5ebede,0xbf48bf7f,0x3e97be97,0x3f7f3f48,
0xbf71bf00,0xbf00bf71,0x3f443e32,0x3f443f80,0x3eaf3f5e,0xbf5ebeaf,0xbf25bf7c,0x3f250000,
0x3f803f80,0x3f803f80,0x3f803f80,0x3f80,0x0,0x0,0x0,0x0,
0x3f543f44,0x3f6c3f61,0x3f7b3f75,0x3f7f,0xbf10bf25,0xbec7bef5,0xbe4bbe97,0xbdcc,
0x3ebb3e32,0x3f333f0b,0x3f6c3f54,0x3f7b,0xbf6ebf7c,0xbf37bf57,0xbec7bf10,0xbe4b,
0xbe64bf00,0x3ebb3d99,0x3f543f20,0x3f75,0xbf7abf5e,0xbf6ebf7f,0xbf10bf48,0xbe97,
0xbf3cbf71,0xbcccbed3,0x3f333ebb,0x3f6c,0xbf2ebeaf,0xbf80bf69,0xbf37bf6e,0xbec7,
0xbf7dbf71,0xbed3bf4c,0x3f0b3d99,0x3f61,0xbe193eaf,0xbf69bf1b,0xbf57bf7f,0xbef5,
0xbf67bf00,0xbf3cbf7d,0x3ebbbe64,0x3f54,0x3ede3f5e,0xbf2ebe19,0xbf6ebf7a,0xbf10,
0xbf003e32,0xbf71bf71,0x3e32bf00,0x3f44,0x3f5e3f7c,0xbeaf3eaf,0xbf7cbf5e,0xbf25,
0x3f333f80,0xbf3cbccc,0xbf29bf80,0x3f443d99,0x3f370000,0x3f2e3f80,0xbf40bd4c,0xbf25bf7f,
0x3f203f80,0xbf67be64,0xbe64bf67,0x3f803f20,0x3f480000,0x3ede3f7a,0xbf7abede,0xbf48,
0x3f0b3f80,0xbf7dbed3,0x3e8bbf29,0x3f443f75,0x3f570000,0x3e193f69,0xbf76bf40,0x3f25be97,
0x3eea3f80,0xbf7dbf15,0x3f33bea3,0x3e323f75,0x3f640000,0xbe193f50,0xbf37bf73,0x3f7c3e97,
0x3ebb3f80,0xbf67bf3c,0x3f753d99,0xbf003f20,0x3f6e0000,0xbede3f2e,0xbe97bf7f,0x3f5e3f48,
0x3e8b3f80,0xbf3cbf5a,0x3f7b3eea,0xbf713d99,0x3f760000,0xbf2e3f05,0x3e4bbf64,0x3eaf3f7f,
0x3e323f80,0xbf00bf71,0x3f443f44,0xbf71bf00,0x3f7c0000,0xbf5e3eaf,0x3f25bf25,0xbeaf3f5e,
0x3d993f80,0xbe64bf7d,0x3ebb3f75,0xbf00bf67,0x3f7f0000,0xbf7a3e19,0x3f6ebe97,0xbf5e3ede,
0x3f203f7f,0xbf4cbdff,0xbf15bf7d,0x3f543e32,0x3f483dcc,0x3f1b3f7e,0xbf50be19,0xbf10bf7c,
0xbe643f20,0xbf67bf67,0x3f20be64,0x3f203f80,0x3f7a3f48,0xbede3ede,0xbf48bf7a,0x3f480000,
0xbf67bdff,0xbcccbf5a,0x3f6c3f54,0xbf3c3e32,0x3ede3f7e,0xbf80bf05,0x3ec7bf10,0x3f2e3f7c,
0xbf67bf4c,0x3f61bccc,0xbdff3f54,0xbf3cbf71,0xbede3f1b,0xbef5bf80,0x3f7e3f10,0xbf2e3eaf,
0xbe64bf7d,0x3f543f54,0xbf7dbe64,0x3f20bf00,0xbf7abe19,0x3f10bf10,0x3e193f7a,0xbf48bf5e,
0x3f20bf15,0xbdff3f6c,0xbed3bf7d,0x3f543f44,0xbf48bf50,0x3f7e3ec7,0xbf693e19,0x3f10bf25,
0x3f803e32,0xbf713e32,0x3f44bf00,0xbf003f44,0xbf7c,0x3eaf3f7c,0xbf25bf5e,0x3f5e3f25,
0x3f203f54,0xbf3cbf3c,0x3f543f20,0xbf67bf00,0x3f48bf10,0xbf2e3f2e,0x3f10bf48,0xbede3f5e,
0x3f0b3f7b,0xbf5abe64,0xbf00bf78,0x3f613e8b,0x3f573e4b,0x3f053f7a,0xbf5ebe7d,0xbef5bf76,
0xbf67be64,0xbe64bf67,0x3f803f20,0xbe643f20,0x3ede3f7a,0xbf7abede,0xbf48,0x3f7a3f48,
0xbea3bf78,0x3f7f3f20,0xbf003eea,0xbf15bf80,0xbf73be7d,0x3dccbf48,0x3f5e3f64,0xbf503d4c,
0x3f7b3e8b,0xbed33f20,0xbf00bf80,0x3f7f3f0b,0xbe4bbf76,0x3f693f48,0xbf5e3d4c,0x3dccbf57,
0x3d993f75,0xbf3cbf67,0x3f803ebb,0xbf3c3ebb,0x3f7f3e97,0xbf2e3ede,0xbf6e,0x3f2e3f6e,
0xbf80bea3,0x3f61be64,0xbf003f33,0xbcccbf78,0xbd4c3f73,0xbef5bf7a,0x3f5e3f37,0xbf80be7d,
0x3e32bf71,0x3e323f80,0xbf00bf71,0x3f443f44,0xbf7cbeaf,0x3f7c0000,0xbf5e3eaf,0x3f25bf25,
0x3f753ebb,0xbf7dbe64,0x3f803d99,0xbf7d3d99,0x3e97bf6e,0xbe193f7a,0xbf7f,0x3e193f7f,
0x3eea3f75,0xbf67bea3,0xbed3bf71,0x3f6c3ebb,0x3f643e97,0x3ede3f73,0xbf69beaf,0xbec7bf6e,
0xbf67bf67,0x3f20be64,0x3f203f80,0xbf67be64,0xbede3ede,0xbf48bf7a,0x3f480000,0x3ede3f7a,
0x3f7b3ebb,0xbe643f33,0xbf4cbf71,0x3f613d99,0xbe4bbf6e,0x3f7a3f37,0xbf1b3eaf,0xbef5bf7f,
0xbf293ebb,0xbe64bf78,0x3f6c3f44,0xbf5a3d99,0x3f403f6e,0xbf7abe7d,0x3ec7bf25,0x3f053f7f,
0x3d99bf67,0x3f203f75,0xbf7dbf00,0x3f54be64,0xbf7fbede,0x3f48be97,0xbe193f5e,0xbf10bf7a,
0x3f0b3f75,0xbf67bf29,0x3f7f3e32,0xbf4c3ebb,0x3f57be97,0xbede3f40,0xbdccbf7c,0x3f1b3f6e,
0xbf71bf00,0x3f803e32,0xbf713e32,0x3f44bf00,0xbeaf3f5e,0xbf7c,0x3eaf3f7c,0xbf25bf5e,
0x3f75be64,0xbf673ebb,0x3f54bf00,0xbf3c3f20,0xbe97bf7a,0x3ede3f6e,0xbf10bf5e,0x3f2e3f48,
0x3ebb3f6c,0xbf71bed3,0xbea3bf67,0x3f753eea,0x3f6e3ec7,0x3eaf3f69,0xbf73bede,0xbe97bf64,
0xbe64bf67,0x3f803f20,0xbe643f20,0xbf67bf67,0xbf7abede,0xbf48,0x3f7a3f48,0xbede3ede,
0x3d993f61,0xbf71bf4c,0x3f33be64,0x3ebb3f7b,0x3f7f3ef5,0xbeaf3f1b,0xbf37bf7a,0x3f6e3e4b,
0x3d99bf5a,0x3f443f6c,0xbf78be64,0x3ebbbf29,0xbf7fbf05,0x3f25bec7,0x3e7d3f7a,0xbf6ebf40,
0xbe643f54,0xbf00bf7d,0x3f753f20,0xbf673d99,0x3f7a3f10,0xbf5e3e19,0x3e97bf48,0x3ede3f7f,
0x3ebbbf4c,0x3e323f7f,0xbf29bf67,0x3f753f0b,0xbf6ebf1b,0x3f7c3dcc,0xbf403ede,0x3e97bf57,
0xbf003f44,0x3e32bf71,0x3e323f80,0xbf00bf71,0x3f5e3f25,0xbf7cbeaf,0x3f7c0000,0xbf5e3eaf,
0x3f20bf3c,0xbf003f54,0x3ebbbf67,0xbe643f75,0xbf48bf2e,0x3f5e3f10,0xbf6ebede,0x3f7a3e97,
0x3e8b3f61,0xbf78bf00,0xbe64bf5a,0x3f7b3f0b,0x3f763ef5,0x3e7d3f5e,0xbf7abf05,0xbe4bbf57,
0x3f20be64,0x3f203f80,0xbf67be64,0xbe64bf67,0xbf48bf7a,0x3f480000,0x3ede3f7a,0xbf7abede,
0xbf80bf15,0x3eeabf00,0x3f203f7f,0xbf78bea3,0xbd4c3f50,0xbf64bf5e,0x3f48bdcc,0x3e7d3f73,
0x3f0b3f7f,0xbf80bf00,0x3f20bed3,0x3e8b3f7b,0x3f57bdcc,0xbd4c3f5e,0xbf48bf69,0x3f763e4b,
0x3ebbbf3c,0x3ebb3f80,0xbf67bf3c,0x3f753d99,0xbf6ebf2e,0x3f6e0000,0xbede3f2e,0xbe97bf7f,
0xbf78bccc,0x3f33bf00,0xbe643f61,0xbea3bf80,0x3e7d3f80,0xbf37bf5e,0x3f7a3ef5,0xbf733d4c,
0x3f443f44,0xbf71bf00,0x3f803e32,0xbf713e32,0x3f25bf25,0xbeaf3f5e,0xbf7c,0x3eaf3f7c,
0x3d99bf7d,0x3d993f80,0xbe64bf7d,0x3ebb3f75,0xbf7fbe19,0x3f7f0000,0xbf7a3e19,0x3f6ebe97,
0x3e323f54,0xbf7dbf15,0xbdffbf4c,0x3f7f3f20,0x3f7c3f10,0x3e193f50,0xbf7ebf1b,0xbdccbf48,
0x3f803f20,0xbe643f20,0xbf67bf67,0x3f20be64,0xbf48,0x3f7a3f48,0xbede3ede,0xbf48bf7a,
0x3e32bf3c,0x3f543f6c,0xbf5abccc,0xbdffbf67,0xbf7cbf2e,0x3f10bec7,0x3f053f80,0xbf7ebede,
0xbf71bf3c,0x3f54bdff,0xbccc3f61,0xbf4cbf67,0xbeaf3f2e,0xbf10bf7e,0x3f803ef5,0xbf1b3ede,
0xbf003f20,0xbe64bf7d,0x3f543f54,0xbf7dbe64,0x3f5e3f48,0xbf7abe19,0x3f10bf10,0x3e193f7a,
0x3f443f54,0xbf7dbed3,0x3f6cbdff,0xbf153f20,0x3f25bf10,0xbe193f69,0xbec7bf7e,0x3f503f48,
0x3f44bf00,0xbf003f44,0x3e32bf71,0x3e323f80,0xbf25bf5e,0x3f5e3f25,0xbf7cbeaf,0x3f7c0000,
0xbf00bf67,0x3f203f54,0xbf3cbf3c,0x3f543f20,0xbf5e3ede,0x3f48bf10,0xbf2e3f2e,0x3f10bf48,
0x3d993f44,0xbf80bf29,0xbcccbf3c,0x3f33,0x3f7f3f25,0x3d4c3f40,0xbf80bf2e,0xbf37,
0x3f203f80,0xbf67be64,0xbe64bf67,0x3f20,0x3f480000,0x3ede3f7a,0xbf7abede,0xbf48,
0x3f753f44,0xbf293e8b,0xbed3bf7d,0x3f0b,0x3e97bf25,0x3f403f76,0xbf69be19,0xbf57,
0x3f753e32,0xbea33f33,0xbf15bf7d,0x3eea,0xbe97bf7c,0x3f733f37,0xbf503e19,0xbf64,
0x3f20bf00,0x3d993f75,0xbf3cbf67,0x3ebb,0xbf48bf5e,0x3f7f3e97,0xbf2e3ede,0xbf6e,
0x3d99bf71,0x3eea3f7b,0xbf5abf3c,0x3e8b,0xbf7fbeaf,0x3f64be4b,0xbf053f2e,0xbf76,
0xbf00bf71,0x3f443f44,0xbf71bf00,0x3e32,0xbf5e3eaf,0x3f25bf25,0xbeaf3f5e,0xbf7c,
0xbf67bf00,0x3f753ebb,0xbf7dbe64,0x3d99,0xbede3f5e,0x3e97bf6e,0xbe193f7a,0xbf7f,
0xbccc3f80,0x3d99bf80,0xbdff3f7f,0x3e32bf7d,0x3f800000,0xbf7fbd4c,0x3f7e3dcc,0xbf7cbe19,
0xbdff3f80,0x3ebbbf78,0xbf153f61,0x3f44bf3c,0x3f7e0000,0xbf6ebe7d,0x3f503ef5,0xbf25bf2e,
0xbe643f80,0x3f20bf67,0xbf673f20,0x3f80be64,0x3f7a0000,0xbf48bede,0x3ede3f48,0xbf7a,
0xbea33f80,0x3f54bf4c,0xbf803e8b,0x3f443ebb,0x3f730000,0xbf10bf1b,0xbd4c3f76,0x3f25bf6e,
0xbed33f80,0x3f75bf29,0xbf5abdff,0x3e323f54,0x3f690000,0xbe97bf40,0xbf053f7e,0x3f7cbf10,
0xbf003f80,0x3f80bf00,0xbf00bf00,0xbf003f80,0x3f5e0000,0xbf5e,0xbf5e3f5e,0x3f5e0000,
0xbf153f80,0x3f75bea3,0xbcccbf4c,0xbf713f54,0x3f500000,0x3e97bf73,0xbf803f1b,0x3eaf3f10,
0xbf293f80,0x3f54bdff,0x3eeabf78,0xbf713ebb,0x3f400000,0x3f10bf7e,0xbf643e7d,0xbeaf3f6e,
0xbe643f7b,0x3e8bbf78,0xbea33f75,0x3ebbbf71,0x3f7a3e4b,0xbf76be7d,0x3f733e97,0xbf6ebeaf,
0xbf673f0b,0x3f7bbea3,0xbf803d99,0x3f753e32,0x3ede3f57,0xbe4bbf73,0xbd4c3f7f,0x3e97bf7c,
0xbf67be64,0x3f203f20,0xbe64bf67,0xbe643f80,0xbede3f7a,0x3f48bf48,0xbf7a3ede,0x3f7a0000,
0xbe64bf5a,0xbed33f7f,0x3f61bf3c,0xbf7d3e32,0xbf7a3f05,0x3f693dcc,0xbef5bf2e,0xbe193f7c,
0x3f20bf78,0xbf803eea,0x3f333ebb,0x3d99bf71,0xbf48be7d,0x3d4c3f64,0x3f37bf6e,0xbf7f3eaf,
0x3f80bf00,0xbf00bf00,0xbf003f80,0x3f80bf00,0xbf5e,0xbf5e3f5e,0x3f5e0000,0xbf5e,
0x3f203e8b,0x3f0bbf80,0xbf783ebb,0x3d993f44,0x3f48bf76,0xbf573d4c,0xbe7d3f6e,0x3f7fbf25,
0xbe643f61,0x3f7fbf15,0xbcccbf3c,0xbf7d3f44,0x3f7abef5,0x3dccbf50,0xbf803f2e,0x3e193f25,
0xbed33f6c,0x3eeabf67,0xbf003f61,0x3f0bbf5a,0x3f693ec7,0xbf64bede,0x3f5e3ef5,0xbf57bf05,
0xbf5abed3,0x3f333f20,0xbf00bf4c,0x3e8b3f6c,0xbf053f69,0x3f37bf48,0xbf5e3f1b,0x3f76bec7,
0x3f20bf67,0xbf673f20,0x3f80be64,0xbf67be64,0xbf48bede,0x3ede3f48,0xbf7a,0xbede3f7a,
0x3f333eea,0xbdffbf67,0xbf003f7b,0x3f6cbf29,0x3f37bf64,0xbf7e3ede,0x3f5e3e4b,0xbec7bf40,
0xbf4c3f61,0x3f7bbe64,0xbf00bf15,0xbea33f7f,0x3f1b3ef5,0x3e4bbf7a,0xbf5e3f50,0x3f73bdcc,
0xbf00bf00,0xbf003f80,0x3f80bf00,0xbf00bf00,0xbf5e3f5e,0x3f5e0000,0xbf5e,0xbf5e3f5e,
0x3f6cbf5a,0xbf29be64,0xbf003f7f,0x3f7bbed3,0xbec7bf05,0xbf403f7a,0x3f5ebdcc,0x3e4bbf69,
0x3e8b3f0b,0x3f6cbf67,0xbf00bea3,0xbf4c3f7b,0x3f76bf57,0xbec7bede,0xbf5e3f73,0x3f1b3e4b,
0xbf153f54,0x3f20bf4c,0xbf293f44,0x3f33bf3c,0x3f503f10,0xbf48bf1b,0x3f403f25,0xbf37bf2e,
0xbcccbf7d,0xbe643f7f,0x3eeabf71,0xbf293f54,0xbf803e19,0x3f7a3dcc,0xbf64beaf,0x3f403f10,
0x3f203f20,0xbe64bf67,0xbe643f80,0x3f20bf67,0x3f48bf48,0xbf7a3ede,0x3f7a0000,0xbf48bede,
0xbf783d99,0x3f203f0b,0xbcccbf71,0xbf153f75,0xbe7d3f7f,0x3f48bf57,0xbf803eaf,0x3f503e97,
0x3f6cbf3c,0xbf67bccc,0x3e8b3f44,0x3f0bbf7d,0xbec7bf2e,0xbede3f80,0x3f76bf25,0xbf57be19,
0xbf003f80,0x3f80bf00,0xbf00bf00,0xbf003f80,0x3f5e0000,0xbf5e,0xbf5e3f5e,0x3f5e0000,
0xbdffbf3c,0xbf673f61,0x3f333e32,0x3eeabf7d,0xbf7e3f2e,0x3ede3ef5,0x3f37bf7c,0xbf643e19,
0x3f333d99,0x3f20bf80,0xbf5a3e32,0xbed33f75,0x3f37bf7f,0xbf483d4c,0xbf053f7c,0x3f69be97,
0xbf3c3f33,0x3f44bf29,0xbf4c3f20,0x3f54bf15,0x3f2e3f37,0xbf25bf40,0x3f1b3f48,0xbf10bf50,
0x3f54bf29,0xbf713eea,0x3f7fbe64,0xbf7dbccc,0xbf10bf40,0x3eaf3f64,0xbdccbf7a,0xbe193f80,
0xbf673f20,0x3f80be64,0xbf67be64,0x3f203f20,0x3ede3f48,0xbf7a,0xbede3f7a,0x3f48bf48,
0x3f75bf15,0xbf71bccc,0x3f0b3f20,0x3d99bf78,0xbe97bf50,0xbeaf3f80,0x3f57bf48,0xbf7f3e7d,
0xbf7d3f0b,0x3f443e8b,0xbcccbf67,0xbf3c3f6c,0x3e193f57,0x3f25bf76,0xbf803ede,0x3f2e3ec7,
0x3f80bf00,0xbf00bf00,0xbf003f80,0x3f80bf00,0xbf5e,0xbf5e3f5e,0x3f5e0000,0xbf5e,
0xbf7d3eea,0x3e323f33,0x3f61bf67,0xbf3cbdff,0xbe193f64,0x3f7cbf37,0xbef5bede,0xbf2e3f7e,
0x3f75bed3,0x3e32bf5a,0xbf803f20,0x3d993f33,0x3e97bf69,0xbf7c3f05,0xbd4c3f48,0x3f7fbf37,
0xbf5a3f0b,0x3f61bf00,0xbf673eea,0x3f6cbed3,0x3f053f57,0xbef5bf5e,0x3ede3f64,0xbec7bf69,
0x3f6c3e8b,0xbf4cbf00,0x3f203f33,0xbed3bf5a,0x3ec7bf76,0xbf1b3f5e,0x3f48bf37,0xbf693f05,
0xbe64bf67,0xbe643f80,0x3f20bf67,0xbf673f20,0xbf7a3ede,0x3f7a0000,0xbf48bede,0x3ede3f48,
0xbf293f6c,0x3f7bbf00,0xbf67bdff,0x3eea3f33,0x3f403ec7,0xbe4bbf5e,0xbede3f7e,0x3f64bf37,
0x3f7fbea3,0xbf15bf00,0xbe643f7b,0x3f61bf4c,0x3dccbf73,0xbf503f5e,0x3f7abe4b,0xbef5bf1b,
0xbf00bf00,0xbf003f80,0x3f80bf00,0xbf00bf00,0xbf5e3f5e,0x3f5e0000,0xbf5e,0xbf5e3f5e,
0xbed33f7b,0x3f7fbf00,0xbe64bf29,0xbf5a3f6c,0x3f69be4b,0x3dccbf5e,0xbf7a3f40,0x3f053ec7,
0x3f7bbf4c,0xbea3bf00,0xbf673f6c,0x3f0b3e8b,0xbe4bbf1b,0xbf733f5e,0x3ede3ec7,0x3f57bf76,
0xbf713ebb,0x3f75bea3,0xbf783e8b,0x3f7bbe64,0x3eaf3f6e,0xbe97bf73,0x3e7d3f76,0xbe4bbf7a,
0x3e323f75,0x3d99bf80,0xbea33f7b,0x3f0bbf67,0x3f7cbe97,0xbf7f3d4c,0x3f733e4b,0xbf57bede,
0x3f80be64,0xbf67be64,0x3f203f20,0xbe64bf67,0xbf7a,0xbede3f7a,0x3f48bf48,0xbf7a3ede,
0x3e32bf7d,0xbf3c3f61,0x3f7fbed3,0xbf5abe64,0xbf7c3e19,0x3f2e3ef5,0xbdccbf69,0xbf053f7a,
0xbf713d99,0x3ebb3f33,0x3eeabf80,0xbf783f20,0xbeaf3f7f,0x3f6ebf37,0xbf64bd4c,0x3e7d3f48,
0xbf003f80,0x3f80bf00,0xbf00bf00,0xbf003f80,0x3f5e0000,0xbf5e,0xbf5e3f5e,0x3f5e0000,
0x3f443d99,0x3ebbbf78,0xbf803f0b,0x3e8b3f20,0x3f25bf7f,0xbf6e3e7d,0xbd4c3f57,0x3f76bf48,
0x3f44bf7d,0xbf3cbccc,0xbf153f7f,0x3f61be64,0xbf25be19,0xbf2e3f80,0x3f50bdcc,0x3ef5bf7a,
0xbf7d3e32,0x3f7fbdff,0xbf803d99,0xbccc,0x3e193f7c,0xbdccbf7e,0x3d4c3f7f,0xbf80,
0xbf3c3f44,0x3f61bf15,0xbf783ebb,0xbdff,0x3f2e3f25,0xbef5bf50,0x3e7d3f6e,0xbf7e,
0xbe643f80,0x3f20bf67,0xbf673f20,0xbe64,0x3f7a0000,0xbf48bede,0x3ede3f48,0xbf7a,
0x3ebb3f44,0x3e8bbf80,0xbf4c3f54,0xbea3,0x3f6ebf25,0xbf763d4c,0x3f1b3f10,0xbf73,
0x3f543e32,0xbdffbf5a,0xbf293f75,0xbed3,0x3f10bf7c,0xbf7e3f05,0x3f403e97,0xbf69,
0x3f80bf00,0xbf00bf00,0xbf003f80,0xbf00,0xbf5e,0xbf5e3f5e,0x3f5e0000,0xbf5e,
0x3f54bf71,0xbf4cbccc,0xbea33f75,0xbf15,0xbf10beaf,0xbf1b3f80,0x3f73be97,0xbf50,
0x3ebbbf71,0xbf783eea,0xbdff3f54,0xbf29,0xbf6e3eaf,0xbe7d3f64,0x3f7ebf10,0xbf40,
0xbf3c3f80,0x3f203d99,0x3f54bf7d,0xbf00be64,0x3f2e0000,0x3f48bf7f,0xbf10be19,0xbf5e3f7a,
0xbf4c3f80,0x3ebb3e8b,0x3f7fbf5a,0x3e32bf3c,0x3f1b0000,0x3f6ebf76,0xbdccbf05,0xbf7c3f2e,
0xbf5a3f80,0x3d993eea,0x3f6cbf15,0x3f44bf7d,0x3f050000,0x3f7fbf64,0x3ec7bf50,0xbf253e19,
0xbf673f80,0xbe643f20,0x3f20be64,0x3f80bf67,0x3ede0000,0x3f7abf48,0x3f48bf7a,0xbede,
0xbf713f80,0xbf003f44,0x3e323e32,0x3f44bf00,0x3eaf0000,0x3f5ebf25,0x3f7cbf7c,0x3f25bf5e,
0xbf783f80,0xbf3c3f61,0xbea33f0b,0x3e323d99,0x3e7d0000,0x3f2ebef5,0x3f73bf57,0x3f7cbf7f,
0xbf7d3f80,0xbf673f75,0xbf3c3f54,0xbf003f20,0x3e190000,0x3edebe97,0x3f2ebf10,0x3f5ebf48,
0xbf803f80,0xbf7d3f7f,0xbf783f7b,0xbf713f75,0x3d4c0000,0x3e19bdcc,0x3e7dbe4b,0x3eafbe97,
0xbf673f75,0x3ebb3ebb,0x3f75bf67,0xbe64bf00,0x3ede3e97,0x3f6ebf6e,0xbe97bede,0xbf7a3f5e,
0xbf673eea,0xbf293f7b,0x3f0b3d99,0x3f75bf71,0xbede3f64,0x3f40be4b,0x3f57bf7f,0xbe97beaf,
0xbe64bea3,0xbf783f33,0xbf293f75,0x3ebb3e32,0xbf7a3f73,0xbe7d3f37,0x3f40be97,0x3f6ebf7c,
0x3f20bf67,0xbe64be64,0xbf673f20,0xbf673f80,0xbf483ede,0xbf7a3f7a,0xbede3f48,0x3ede0000,
0x3f80bf71,0x3f44bf71,0x3e32bf00,0xbf003e32,0xbeaf,0xbf253eaf,0xbf7c3f5e,0xbf5e3f7c,
0x3f20bed3,0x3f6cbf4c,0x3f7fbf7d,0x3f54bf71,0x3f48bf69,0x3ec7bf1b,0xbdccbe19,0xbf103eaf,
0xbe643ebb,0x3d993d99,0x3ebbbe64,0x3f20bf00,0x3f7abf6e,0x3f7fbf7f,0x3f6ebf7a,0x3f48bf5e,
0xbf673f6c,0xbf5a3f61,0xbf4c3f54,0xbf3c3f44,0x3edebec7,0x3f05bef5,0x3f1bbf10,0x3f2ebf25,
0xbf7d3f54,0x3d993f20,0x3f80bf3c,0x3d99bf3c,0x3e193f10,0x3f7fbf48,0xbf2e,0xbf7f3f2e,
0xbcccbf15,0xbf783f20,0xbf003f6c,0x3f33bdff,0xbf803f50,0xbe7d3f48,0x3f5ebec7,0x3f37bf7e,
0x3f7fbf4c,0x3f0bbf67,0xbf00bccc,0xbf803f61,0x3dccbf1b,0xbf573ede,0xbf5e3f80,0x3d4c3ef5,
0xbe643f20,0x3f20be64,0x3f80bf67,0x3f20bf67,0x3f7abf48,0x3f48bf7a,0xbede,0xbf483ede,
0xbf713f44,0xbf713f80,0xbf003f44,0x3e323e32,0xbeaf3f25,0x3eaf0000,0x3f5ebf25,0x3f7cbf7c,
0x3eeabf29,0xbcccbe64,0xbf003e8b,0xbf5a3f33,0xbf643f40,0xbf803f7a,0xbf5e3f76,0xbf053f37,
0x3f54bf3c,0x3f75bf67,0x3f80bf7d,0x3f75bf7d,0x3f10bf2e,0x3e97bede,0xbe19,0xbe973e19,
0xbf293f33,0xbf153f20,0xbf003f0b,0xbed33eea,0x3f40bf37,0x3f50bf48,0x3f5ebf57,0x3f69bf64,
0xbf7d3f20,0xbe643f54,0x3f75bf00,0x3ebbbf67,0xbe193f48,0x3f7abf10,0x3e97bf5e,0xbf6e3ede,
0x3f61bf7d,0xbe64bed3,0xbf803f44,0xbea33f54,0xbef5be19,0xbf7a3f69,0xbd4c3f25,0x3f73bf10,
0xbed33f54,0x3f20bdff,0x3f7bbf71,0x3e8bbf3c,0x3f69bf10,0x3f48bf7e,0xbe4bbeaf,0xbf763f2e,
0xbe64be64,0xbf673f20,0xbf673f80,0xbe643f20,0xbf7a3f7a,0xbede3f48,0x3ede0000,0x3f7abf48,
0x3f44bf00,0x3f80bf71,0x3f44bf71,0x3e32bf00,0x3f25bf5e,0xbeaf,0xbf253eaf,0xbf7c3f5e,
0xbf803f75,0xbf673f7b,0xbf153f44,0xbdff3ebb,0xbd4c3e97,0x3edebe4b,0x3f50bf25,0x3f7ebf6e,
0x3f54bf67,0x3f20bf3c,0x3ebbbf00,0x3d99be64,0xbf103ede,0xbf483f2e,0xbf6e3f5e,0xbf7f3f7a,
0xbea33ebb,0xbe643e8b,0xbdff3e32,0xbccc3d99,0x3f73bf6e,0x3f7abf76,0x3f7ebf7c,0x3f80bf7f,
0xbf673ebb,0xbf003f75,0x3f54be64,0x3f20bf7d,0xbede3f6e,0x3f5ebe97,0x3f10bf7a,0xbf483e19,
0x3f54bea3,0x3f44bf80,0xbed3be64,0xbf7d3f61,0x3f10bf73,0xbf253d4c,0xbf693f7a,0x3e193ef5,
0xbf3c3e8b,0xbf713f7b,0xbdff3f20,0x3f54bed3,0xbf2e3f76,0x3eaf3e4b,0x3f7ebf48,0x3f10bf69,
0x3f20be64,0x3f80bf67,0x3f20bf67,0xbe64be64,0x3f48bf7a,0xbede,0xbf483ede,0xbf7a3f7a,
0xbf003e32,0xbf713f44,0xbf713f80,0xbf003f44,0xbf5e3f7c,0xbeaf3f25,0x3eaf0000,0x3f5ebf25,
0x3ebbbdff,0x3f44bf15,0x3f7bbf67,0x3f75bf80,0x3f6ebf7e,0x3f25bf50,0x3e4bbede,0xbe973d4c,
0xbe643d99,0xbf003ebb,0xbf3c3f20,0xbf673f54,0xbf7a3f7f,0xbf5e3f6e,0xbf2e3f48,0xbede3f10,
0x3d99bccc,0x3e32bdff,0x3e8bbe64,0x3ebbbea3,0x3f7fbf80,0x3f7cbf7e,0x3f76bf7a,0x3f6ebf73,
0xbf3c3d99,0xbf3c3f80,0x3f203d99,0x3f54bf7d,0xbf2e3f7f,0x3f2e0000,0x3f48bf7f,0xbf10be19,
0xbdff3f33,0x3f6cbf00,0x3f20bf78,0xbf15bccc,0x3f7ebf37,0x3ec7bf5e,0xbf483e7d,0xbf503f80,
0x3f61bf80,0xbcccbf00,0xbf673f0b,0xbf4c3f7f,0xbef5bd4c,0xbf803f5e,0xbede3f57,0x3f1bbdcc,
0xbf673f20,0xbf673f80,0xbe643f20,0x3f20be64,0xbede3f48,0x3ede0000,0x3f7abf48,0x3f48bf7a,
0x3e323e32,0x3f44bf00,0x3f80bf71,0x3f44bf71,0x3f7cbf7c,0x3f25bf5e,0xbeaf,0xbf253eaf,
0x3f33bf5a,0x3e8bbf00,0xbe64bccc,0xbf293eea,0xbf373f05,0xbf763f5e,0xbf7a3f80,0xbf403f64,
0xbf7d3f75,0xbf7d3f80,0xbf673f75,0xbf3c3f54,0xbe193e97,0x3e190000,0x3edebe97,0x3f2ebf10,
0x3eeabed3,0x3f0bbf00,0x3f20bf15,0x3f33bf29,0x3f64bf69,0x3f57bf5e,0x3f48bf50,0x3f37bf40,
0xbf00be64,0xbf673f75,0x3ebb3ebb,0x3f75bf67,0xbf5e3f7a,0x3ede3e97,0x3f6ebf6e,0xbe97bede,
0xbf713f75,0x3d993f0b,0x3f7bbf29,0x3eeabf67,0x3eaf3e97,0x3f7fbf57,0x3e4bbf40,0xbf643ede,
0x3e323ebb,0x3f75bf29,0x3f33bf78,0xbea3be64,0x3f7cbf6e,0x3e97bf40,0xbf373e7d,0xbf733f7a,
0x3f80bf67,0x3f20bf67,0xbe64be64,0xbf673f20,0xbede,0xbf483ede,0xbf7a3f7a,0xbede3f48,
0x3e32bf00,0xbf003e32,0xbf713f44,0xbf713f80,0xbf7c3f5e,0xbf5e3f7c,0xbeaf3f25,0x3eaf0000,
0xbf713f54,0xbf7d3f7f,0xbf4c3f6c,0xbed33f20,0xbeaf3f10,0x3e193dcc,0x3f1bbec7,0x3f69bf48,
0xbf003f20,0xbe643ebb,0x3d993d99,0x3ebbbe64,0x3f5ebf48,0x3f7abf6e,0x3f7fbf7f,0x3f6ebf7a,
0x3f44bf3c,0x3f54bf4c,0x3f61bf5a,0x3f6cbf67,0x3f25bf2e,0x3f10bf1b,0x3ef5bf05,0x3ec7bede,
0xbe64bf00,0xbf7d3f54,0x3d993f20,0xbf3c,0xbf7a3f5e,0x3e193f10,0x3f7fbf48,0xbf2e,
0xbf3c3e32,0xbf5a3f7f,0x3e8b3ebb,0xbf4c,0xbf2e3f7c,0x3f053dcc,0x3f76bf6e,0xbf1b,
0xbf7d3f44,0xbf153f6c,0x3eea3d99,0xbf5a,0xbe193f25,0x3f50bec7,0x3f64bf7f,0xbf05,
0xbf673f80,0xbe643f20,0x3f20be64,0xbf67,0x3ede0000,0x3f7abf48,0x3f48bf7a,0xbede,
0xbf003f44,0x3e323e32,0x3f44bf00,0xbf71,0x3f5ebf25,0x3f7cbf7c,0x3f25bf5e,0xbeaf,
0x3d993e32,0x3f0bbea3,0x3f61bf3c,0xbf78,0x3f7fbf7c,0x3f57bf73,0x3ef5bf2e,0xbe7d,
0x3f20bf00,0x3f54bf3c,0x3f75bf67,0xbf7d,0x3f48bf5e,0x3f10bf2e,0x3e97bede,0xbe19,
0x3f75bf71,0x3f7bbf78,0x3f7fbf7d,0xbf80,0x3e97beaf,0x3e4bbe7d,0x3dccbe19,0xbd4c,
0x3bf4,0x3c74,0x3c74,0x3c74,0x3bf4,0x3ac23c73,0x3b413c6f,0x3b903c69,
0x3bf4,0x3b413c6f,0x3bbd3c61,0x3c093c49,0x3bf4,0x3b903c69,0x3c093c49,0x3c3f3c18,
0x3bf4,0x3bbd3c61,0x3c2f3c2a,0x3c633bb2,0x3bf4,0x3be93c56,0x3c4d3c04,0x3c733a92,
0x3bf4,0x3c093c49,0x3c633bb2,0x3c6ebb59,0x3bf4,0x3c1d3c3b,0x3c703b29,0x3c53bbf4,
0x3bf4,0x3c2f3c2a,0x3c74b9c2,0x3c26bc33,0x3bf4,0x3c3f3c18,0x3c6ebb59,0x3bd4bc5c,
0x3bf4,0x3c4d3c04,0x3c5ebbc9,0x3b11bc71,0x3bf4,0x3c593bde,0x3c46bc0e,0xbb11bc71,
0x3bf4,0x3c633bb2,0x3c26bc33,0xbbd4bc5c,0x3bf4,0x3c6b3b84,0x3bfebc50,0xbc26bc33,
0x3bf4,0x3c703b29,0x3ba7bc65,0xbc53bbf4,0x3bf4,0x3c733a92,0x3b11bc71,0xbc6ebb59,
0x3c74,0x3c74,0x3c74,0x3c74,0x3bbd3c61,0x3be93c56,0x3c093c49,0x3c1d3c3b,
0x3c2f3c2a,0x3c4d3c04,0x3c633bb2,0x3c703b29,0x3c633bb2,0x3c733a92,0x3c6ebb59,0x3c53bbf4,
0x3c74b9c2,0x3c5ebbc9,0x3c26bc33,0x3ba7bc65,0x3c5ebbc9,0x3c13bc42,0x3b11bc71,0xbba7bc65,
0x3c26bc33,0x3b11bc71,0xbbd4bc5c,0xbc53bbf4,0x3ba7bc65,0xbba7bc65,0xbc53bbf4,0xbc703b29,
0xba42bc73,0xbc37bc21,0xbc733a92,0xbc1d3c3b,0xbbd4bc5c,0xbc6ebb59,0xbc3f3c18,0x3c74,
0xbc37bc21,0xbc6b3b84,0xbb903c69,0x3c1d3c3b,0xbc67bb9b,0xbc2f3c2a,0x3b903c69,0x3c703b29,
0xbc733a92,0xbb903c69,0x3c3f3c18,0x3c53bbf4,0xbc593bde,0x3b413c6f,0x3c733a92,0x3ba7bc65,
0xbc1d3c3b,0x3c1d3c3b,0x3c53bbf4,0xbba7bc65,0xbb903c69,0x3c633bb2,0x3bd4bc5c,0xbc53bbf4,
0x3c74,0x3c74,0x3c74,0x3c74,0x3c2f3c2a,0x3c3f3c18,0x3c4d3c04,0x3c593bde,
0x3c74b9c2,0x3c6ebb59,0x3c5ebbc9,0x3c46bc0e,0x3c26bc33,0x3bd4bc5c,0x3b11bc71,0xbb11bc71,
0xba42bc73,0xbbd4bc5c,0xbc37bc21,0xbc67bb9b,0xbc37bc21,0xbc6ebb59,0xbc6b3b84,0xbc2f3c2a,
0xbc733a92,0xbc3f3c18,0xbb903c69,0x3b903c69,0xbc1d3c3b,0x3c74,0x3c1d3c3b,0x3c703b29,
0x3ac23c73,0x3c3f3c18,0x3c72baf2,0x3c13bc42,0x3c3f3c18,0x3c6ebb59,0x3bd4bc5c,0xbbd4bc5c,
0x3c72baf2,0x3bd4bc5c,0xbbfebc50,0xbc74b9c2,0x3c13bc42,0xbbd4bc5c,0xbc74b9c2,0xbbe93c56,
0xbb11bc71,0xbc6ebb59,0xbc093c49,0x3c093c49,0xbc46bc0e,0xbc3f3c18,0x3bbd3c61,0x3c72baf2,
0xbc703b29,0x3c74,0x3c703b29,0x3ba7bc65,0xbc093c49,0x3c3f3c18,0x3c26bc33,0xbc26bc33,
0x3c74,0x3c74,0x3c74,0x3c74,0x3c633bb2,0x3c6b3b84,0x3c703b29,0x3c733a92,
0x3c26bc33,0x3bfebc50,0x3ba7bc65,0x3b11bc71,0xbbd4bc5c,0xbc26bc33,0xbc53bbf4,0xbc6ebb59,
0xbc733a92,0xbc593bde,0xbc1d3c3b,0xbb903c69,0xbb903c69,0x3b413c6f,0x3c1d3c3b,0x3c633bb2,
0x3c3f3c18,0x3c733a92,0x3c53bbf4,0x3bd4bc5c,0x3c53bbf4,0x3ba7bc65,0xbba7bc65,0xbc53bbf4,
0xbb11bc71,0xbc46bc0e,0xbc703b29,0xbc093c49,0xbc6ebb59,0xbc3f3c18,0x3c74,0x3c3f3c18,
0xbc093c49,0x3bbd3c61,0x3c703b29,0x3c26bc33,0x3c093c49,0x3c72baf2,0x3ba7bc65,0xbc26bc33,
0x3c6ebb59,0x3b11bc71,0xbc53bbf4,0xbc3f3c18,0x3b11bc71,0xbc5ebbc9,0xbc1d3c3b,0x3c093c49,
0xbc53bbf4,0xbc1d3c3b,0x3c1d3c3b,0x3c53bbf4,0xbc3f3c18,0x3c093c49,0x3c53bbf4,0xbbd4bc5c,
0x3c74,0x3c74,0x3c74,0x3c74,0x3c74b9c2,0x3c72baf2,0x3c6ebb59,0x3c67bb9b,
0xba42bc73,0xbb71bc6c,0xbbd4bc5c,0xbc13bc42,0xbc733a92,0xbc633bb2,0xbc3f3c18,0xbc093c49,
0x3ac23c73,0x3be93c56,0x3c3f3c18,0x3c6b3b84,0x3c72baf2,0x3c46bc0e,0x3bd4bc5c,0xba42bc73,
0xbb11bc71,0xbc26bc33,0xbc6ebb59,0xbc633bb2,0xbc703b29,0xbc1d3c3b,0x3c74,0x3c1d3c3b,
0x3b413c6f,0x3c4d3c04,0x3c6ebb59,0x3bfebc50,0x3c6ebb59,0x3bd4bc5c,0xbbd4bc5c,0xbc6ebb59,
0xbb71bc6c,0xbc67bb9b,0xbc3f3c18,0x3ac23c73,0xbc6b3b84,0xbb413c6f,0x3c3f3c18,0x3c5ebbc9,
0x3b903c69,0x3c733a92,0x3bd4bc5c,0xbc26bc33,0x3c67bb9b,0xba42bc73,0xbc6ebb59,0xbbe93c56,
0xbba7bc65,0xbc703b29,0x3c74,0x3c703b29,0xbc633bb2,0x3b903c69,0x3c6ebb59,0xbb11bc71,
0x3c74,0x3c74,0x3c74,0x3c74,0x3c5ebbc9,0x3c53bbf4,0x3c46bc0e,0x3c37bc21,
0xbc37bc21,0xbc53bbf4,0xbc67bb9b,0xbc72baf2,0xbb903c69,0x3c74,0x3b903c69,0x3c093c49,
0x3c72baf2,0x3c53bbf4,0x3c13bc42,0x3b71bc6c,0xbbfebc50,0xbc53bbf4,0xbc74b9c2,0xbc593bde,
0xbc093c49,0x3c74,0x3c093c49,0x3c633bb2,0x3c703b29,0x3c53bbf4,0x3ba7bc65,0xbba7bc65,
0xbb71bc6c,0xbc53bbf4,0xbc6b3b84,0xbbe93c56,0xbc3f3c18,0x3c74,0x3c3f3c18,0x3c6ebb59,
0x3c593bde,0x3c53bbf4,0x3a42bc73,0xbc46bc0e,0x3a42bc73,0xbc53bbf4,0xbc4d3c04,0x3ac23c73,
0xbc633bb2,0x3c74,0x3c633bb2,0x3c26bc33,0x3c2f3c2a,0x3c53bbf4,0xbb71bc6c,0xbc74b9c2,
0x3ba7bc65,0xbc53bbf4,0xbc1d3c3b,0x3c1d3c3b,0xbc733a92,0x3c74,0x3c733a92,0x3b11bc71,
0x3c74,0x3c74,0x3c74,0x3c74,0x3c26bc33,0x3c13bc42,0x3bfebc50,0x3bd4bc5c,
0xbc733a92,0xbc6b3b84,0xbc593bde,0xbc3f3c18,0x3c3f3c18,0x3c633bb2,0x3c733a92,0x3c6ebb59,
0xbb11bc71,0xbbfebc50,0xbc46bc0e,0xbc6ebb59,0xbc093c49,0xbac23c73,0x3bbd3c61,0x3c3f3c18,
0x3c6ebb59,0x3c26bc33,0x3b11bc71,0xbbd4bc5c,0xbc53bbf4,0xbc703b29,0xbc1d3c3b,0x3c74,
0x3b903c69,0x3c593bde,0x3c67bb9b,0x3bd4bc5c,0x3bd4bc5c,0xbbd4bc5c,0xbc6ebb59,0xbc3f3c18,
0xbc633bb2,0xbb413c6f,0x3c2f3c2a,0x3c6ebb59,0x3c633bb2,0x3c37bc21,0xbb71bc6c,0xbc6ebb59,
0xbbd4bc5c,0xbc733a92,0xbb903c69,0x3c3f3c18,0xbb903c69,0x3c4d3c04,0x3c37bc21,0xbbd4bc5c,
0x3c53bbf4,0xbba7bc65,0xbc703b29,0x3c74,0xbc6ebb59,0xbb903c69,0x3c633bb2,0x3bd4bc5c,
0x3c74,0x3c74,0x3c74,0x3c74,0x3ba7bc65,0x3b71bc6c,0x3b11bc71,0x3a42bc73,
0xbc1d3c3b,0xbbe93c56,0xbb903c69,0xbac23c73,0x3c53bbf4,0x3c26bc33,0x3bd4bc5c,0x3b11bc71,
0xbc703b29,0xbc4d3c04,0xbc093c49,0xbb413c6f,0x3c703b29,0x3c67bb9b,0x3c26bc33,0x3b71bc6c,
0xbc53bbf4,0xbc733a92,0xbc3f3c18,0xbb903c69,0x3c1d3c3b,0x3c703b29,0x3c53bbf4,0x3ba7bc65,
0xbba7bc65,0xbc5ebbc9,0xbc633bb2,0xbbbd3c61,0x3c74,0x3c3f3c18,0x3c6ebb59,0x3bd4bc5c,
0x3ba7bc65,0xbc13bc42,0xbc733a92,0xbbe93c56,0xbc1d3c3b,0x3bbd3c61,0x3c733a92,0x3bfebc50,
0x3c53bbf4,0xbb11bc71,0xbc6ebb59,0xbc093c49,0xbc703b29,0xbac23c73,0x3c633bb2,0x3c13bc42,
0x3c703b29,0x3ba7bc65,0xbc53bbf4,0xbc1d3c3b,0xbc53bbf4,0xbc093c49,0x3c3f3c18,0x3c26bc33,
0x3bf4,0x3c74b9c2,0xba42bc73,0xbc733a92,0x3bf4,0x3c72baf2,0xbb71bc6c,0xbc633bb2,
0x3bf4,0x3c6ebb59,0xbbd4bc5c,0xbc3f3c18,0x3bf4,0x3c67bb9b,0xbc13bc42,0xbc093c49,
0x3bf4,0x3c5ebbc9,0xbc37bc21,0xbb903c69,0x3bf4,0x3c53bbf4,0xbc53bbf4,0x3c74,
0x3bf4,0x3c46bc0e,0xbc67bb9b,0x3b903c69,0x3bf4,0x3c37bc21,0xbc72baf2,0x3c093c49,
0x3bf4,0x3c26bc33,0xbc733a92,0x3c3f3c18,0x3bf4,0x3c13bc42,0xbc6b3b84,0x3c633bb2,
0x3bf4,0x3bfebc50,0xbc593bde,0x3c733a92,0x3bf4,0x3bd4bc5c,0xbc3f3c18,0x3c6ebb59,
0x3bf4,0x3ba7bc65,0xbc1d3c3b,0x3c53bbf4,0x3bf4,0x3b71bc6c,0xbbe93c56,0x3c26bc33,
0x3bf4,0x3b11bc71,0xbb903c69,0x3bd4bc5c,0x3bf4,0x3a42bc73,0xbac23c73,0x3b11bc71,
0x3ac23c73,0x3c72baf2,0xbb11bc71,0xbc703b29,0x3be93c56,0x3c46bc0e,0xbc26bc33,0xbc1d3c3b,
0x3c3f3c18,0x3bd4bc5c,0xbc6ebb59,0x3c74,0x3c6b3b84,0xba42bc73,0xbc633bb2,0x3c1d3c3b,
0x3c72baf2,0xbbfebc50,0xbc093c49,0x3c703b29,0x3c53bbf4,0xbc53bbf4,0x3c74,0x3c53bbf4,
0x3c13bc42,0xbc74b9c2,0x3c093c49,0x3ba7bc65,0x3b71bc6c,0xbc593bde,0x3c633bb2,0xbba7bc65,
0xbb11bc71,0xbc093c49,0x3c6ebb59,0xbc53bbf4,0xbbfebc50,0xbac23c73,0x3c26bc33,0xbc703b29,
0xbc46bc0e,0x3bbd3c61,0x3b11bc71,0xbc1d3c3b,0xbc6ebb59,0x3c3f3c18,0xbbd4bc5c,0x3c74,
0xbc703b29,0x3c703b29,0xbc53bbf4,0x3c1d3c3b,0xbc4d3c04,0x3c67bb9b,0xbc733a92,0x3c703b29,
0xbc093c49,0x3c26bc33,0xbc3f3c18,0x3c53bbf4,0xbb413c6f,0x3b71bc6c,0xbb903c69,0x3ba7bc65,
0x3b413c6f,0x3c6ebb59,0xbb71bc6c,0xbc6b3b84,0x3c4d3c04,0x3bd4bc5c,0xbc67bb9b,0xbb413c6f,
0x3c6ebb59,0xbbd4bc5c,0xbc3f3c18,0x3c3f3c18,0x3bfebc50,0xbc6ebb59,0x3ac23c73,0x3c5ebbc9,
0xbb71bc6c,0xbc3f3c18,0x3c593bde,0x3a42bc73,0xbc53bbf4,0x3c74,0x3c53bbf4,0xbc53bbf4,
0xbc6b3b84,0x3c3f3c18,0x3a42bc73,0xbc4d3c04,0xbbe93c56,0x3c6ebb59,0xbc46bc0e,0x3ac23c73,
0x3b903c69,0x3bd4bc5c,0xbc633bb2,0x3c633bb2,0x3c593bde,0xbbd4bc5c,0xbb413c6f,0x3c37bc21,
0x3c67bb9b,0xbc6ebb59,0x3c2f3c2a,0xbb71bc6c,0x3bd4bc5c,0xbc3f3c18,0x3c6ebb59,0xbc6ebb59,
0xbba7bc65,0x3c74,0x3ba7bc65,0xbc1d3c3b,0xbc5ebbc9,0x3c3f3c18,0xbc13bc42,0x3bbd3c61,
0xbc633bb2,0x3c6ebb59,0xbc733a92,0x3c733a92,0xbbbd3c61,0x3bd4bc5c,0xbbe93c56,0x3bfebc50,
0x3b903c69,0x3c67bb9b,0xbba7bc65,0xbc633bb2,0x3c733a92,0xba42bc73,0xbc703b29,0x3b903c69,
0x3bd4bc5c,0xbc6ebb59,0x3c74,0x3c6ebb59,0xbc26bc33,0xbbe93c56,0x3c703b29,0xbb11bc71,
0xbc633bb2,0x3c2f3c2a,0x3ba7bc65,0xbc733a92,0x3c74,0x3c53bbf4,0xbc53bbf4,0x3c74,
0x3c633bb2,0xbb71bc6c,0xbc1d3c3b,0x3c733a92,0x3c26bc33,0xbc74b9c2,0x3c1d3c3b,0x3b11bc71,
0xbbd4bc5c,0xbb903c69,0x3c53bbf4,0xbc6ebb59,0xbc733a92,0x3c4d3c04,0xbba7bc65,0xbb903c69,
0xbb903c69,0x3c37bc21,0xbc703b29,0x3c633bb2,0x3c3f3c18,0xbbd4bc5c,0x3c74,0x3bd4bc5c,
0x3c53bbf4,0xbc703b29,0x3c703b29,0xbc53bbf4,0xbb11bc71,0xbac23c73,0x3ba7bc65,0xbc093c49,
0xbc6ebb59,0x3c633bb2,0xbc53bbf4,0x3c3f3c18,0xbc093c49,0x3c13bc42,0xbc1d3c3b,0x3c26bc33,
0x3bbd3c61,0x3c5ebbc9,0xbbd4bc5c,0xbc593bde,0x3c5ebbc9,0xbbfebc50,0xbc3f3c18,0x3c2f3c2a,
0xbbd4bc5c,0xbc3f3c18,0x3c3f3c18,0x3bd4bc5c,0xbc593bde,0x3c2f3c2a,0x3bd4bc5c,0xbc72baf2,
0x3be93c56,0x3c13bc42,0xbc6ebb59,0x3b413c6f,0x3c53bbf4,0xbc53bbf4,0x3c74,0x3c53bbf4,
0xbbfebc50,0xbbbd3c61,0x3c6ebb59,0xbc37bc21,0xbc4d3c04,0x3c6b3b84,0xbbd4bc5c,0xbbbd3c61,
0x3c093c49,0x3b11bc71,0xbc3f3c18,0x3c733a92,0x3c46bc0e,0xbc74b9c2,0x3c3f3c18,0xbb71bc6c,
0xbc13bc42,0x3ac23c73,0x3bd4bc5c,0xbc4d3c04,0xbc3f3c18,0x3c6ebb59,0xbc6ebb59,0x3c3f3c18,
0x3c1d3c3b,0xbba7bc65,0x3c74,0x3ba7bc65,0x3c37bc21,0xbc593bde,0x3c6ebb59,0xbc74b9c2,
0xbc26bc33,0x3c093c49,0xbbd4bc5c,0x3b903c69,0xbc2f3c2a,0x3c37bc21,0xbc3f3c18,0x3c46bc0e,
0x3be93c56,0x3c53bbf4,0xbbfebc50,0xbc4d3c04,0x3c13bc42,0xbc53bbf4,0xbbbd3c61,0x3c6b3b84,
0xbc6ebb59,0x3c74,0x3c6ebb59,0xbbd4bc5c,0x3b413c6f,0x3c53bbf4,0xbc37bc21,0xbbbd3c61,
0x3c46bc0e,0xbc53bbf4,0xbac23c73,0x3c67bb9b,0xbc53bbf4,0x3c74,0x3c53bbf4,0xbc53bbf4,
0xbac23c73,0x3c53bbf4,0xbc5ebbc9,0x3b413c6f,0x3c67bb9b,0xbc53bbf4,0x3b413c6f,0x3c13bc42,
0xbc26bc33,0x3c74,0x3c26bc33,0xbc733a92,0xbbbd3c61,0x3c53bbf4,0xbc72baf2,0x3c2f3c2a,
0x3c74b9c2,0xbc53bbf4,0x3be93c56,0x3a42bc73,0xbbd4bc5c,0x3c74,0x3bd4bc5c,0xbc3f3c18,
0xbc1d3c3b,0x3c53bbf4,0xbc703b29,0x3c703b29,0x3c6b3b84,0xbc53bbf4,0x3c2f3c2a,0xbbfebc50,
0xbb11bc71,0x3c74,0x3b11bc71,0xbb903c69,0xbc4d3c04,0x3c53bbf4,0xbc593bde,0x3c5ebbc9,
0x3c093c49,0x3c46bc0e,0xbc13bc42,0xbc3f3c18,0x3b11bc71,0xbc74b9c2,0x3ac23c73,0x3c6ebb59,
0xbc3f3c18,0x3c3f3c18,0x3bd4bc5c,0xbc6ebb59,0x3c733a92,0xbb71bc6c,0xbc4d3c04,0x3c3f3c18,
0xbc26bc33,0xbbbd3c61,0x3c74b9c2,0xbbd4bc5c,0x3c74,0x3c53bbf4,0xbc53bbf4,0x3c74,
0x3c26bc33,0xbc72baf2,0x3be93c56,0x3bd4bc5c,0xbc733a92,0x3c2f3c2a,0x3a42bc73,0xbc3f3c18,
0x3c3f3c18,0xbb11bc71,0xbc093c49,0x3c6ebb59,0xbb11bc71,0xbbe93c56,0x3c5ebbc9,0xbc6ebb59,
0xbc093c49,0x3c5ebbc9,0xbc72baf2,0x3c3f3c18,0x3c6ebb59,0xbc6ebb59,0x3c3f3c18,0xbbd4bc5c,
0xbc53bbf4,0x3c1d3c3b,0xbba7bc65,0x3c74,0x3b903c69,0xba42bc73,0xbb413c6f,0x3bd4bc5c,
0x3bd4bc5c,0xbc093c49,0x3c26bc33,0xbc3f3c18,0xbc633bb2,0x3c67bb9b,0xbc6b3b84,0x3c6ebb59,
0x3c1d3c3b,0x3c37bc21,0xbc26bc33,0xbc2f3c2a,0xbba7bc65,0xbc593bde,0x3c093c49,0x3c37bc21,
0x3c74,0x3c6ebb59,0xbbd4bc5c,0xbc3f3c18,0x3ba7bc65,0xbc74b9c2,0x3b903c69,0x3c46bc0e,
0xbc1d3c3b,0x3c6b3b84,0xbb11bc71,0xbc4d3c04,0x3c53bbf4,0xbc53bbf4,0x3c74,0x3c53bbf4,
0xbc703b29,0x3c2f3c2a,0x3b11bc71,0xbc593bde,0x3c703b29,0xbbfebc50,0xbb903c69,0x3c5ebbc9,
0xbc53bbf4,0x3b903c69,0x3bd4bc5c,0xbc633bb2,0x3c1d3c3b,0xba42bc73,0xbc093c49,0x3c67bb9b,
0xbba7bc65,0xbb413c6f,0x3c26bc33,0xbc6b3b84,0x3c74,0x3bd4bc5c,0xbc3f3c18,0x3c6ebb59,
0x3ba7bc65,0xbc1d3c3b,0x3c53bbf4,0xbc703b29,0xbc1d3c3b,0x3c46bc0e,0xbc633bb2,0x3c72baf2,
0x3c53bbf4,0xbc633bb2,0x3c6ebb59,0xbc733a92,0xbc703b29,0x3c72baf2,0xbc733a92,0x3c74b9c2,
0x3bf4,0xba42bc73,0x3ac23c73,0xbb11bc71,0x3bf4,0xbb11bc71,0x3b903c69,0xbbd4bc5c,
0x3bf4,0xbb71bc6c,0x3be93c56,0xbc26bc33,0x3bf4,0xbba7bc65,0x3c1d3c3b,0xbc53bbf4,
0x3bf4,0xbbd4bc5c,0x3c3f3c18,0xbc6ebb59,0x3bf4,0xbbfebc50,0x3c593bde,0xbc733a92,
0x3bf4,0xbc13bc42,0x3c6b3b84,0xbc633bb2,0x3bf4,0xbc26bc33,0x3c733a92,0xbc3f3c18,
0x3bf4,0xbc37bc21,0x3c72baf2,0xbc093c49,0x3bf4,0xbc46bc0e,0x3c67bb9b,0xbb903c69,
0x3bf4,0xbc53bbf4,0x3c53bbf4,0x3c74,0x3bf4,0xbc5ebbc9,0x3c37bc21,0x3b903c69,
0x3bf4,0xbc67bb9b,0x3c13bc42,0x3c093c49,0x3bf4,0xbc6ebb59,0x3bd4bc5c,0x3c3f3c18,
0x3bf4,0xbc72baf2,0x3b71bc6c,0x3c633bb2,0x3bf4,0xbc74b9c2,0x3a42bc73,0x3c733a92,
0x3b413c6f,0xbb71bc6c,0x3b903c69,0xbba7bc65,0x3c093c49,0xbc26bc33,0x3c3f3c18,0xbc53bbf4,
0x3c4d3c04,0xbc67bb9b,0x3c733a92,0xbc703b29,0x3c703b29,0xbc703b29,0x3c53bbf4,0xbc1d3c3b,
0x3c6ebb59,0xbc3f3c18,0x3bd4bc5c,0x3c74,0x3c46bc0e,0xbbbd3c61,0xbb11bc71,0x3c1d3c3b,
0x3bfebc50,0x3ac23c73,0xbc26bc33,0x3c703b29,0x3b11bc71,0x3c093c49,0xbc6ebb59,0x3c53bbf4,
0xbb71bc6c,0x3c593bde,0xbc633bb2,0x3ba7bc65,0xbc13bc42,0x3c74b9c2,0xbc093c49,0xbba7bc65,
0xbc53bbf4,0x3c53bbf4,0x3c74,0xbc53bbf4,0xbc72baf2,0x3bfebc50,0x3c093c49,0xbc703b29,
0xbc6b3b84,0x3a42bc73,0x3c633bb2,0xbc1d3c3b,0xbc3f3c18,0xbbd4bc5c,0x3c6ebb59,0x3c74,
0xbbe93c56,0xbc46bc0e,0x3c26bc33,0x3c1d3c3b,0xbac23c73,0xbc72baf2,0x3b11bc71,0x3c703b29,
0x3bbd3c61,0xbbd4bc5c,0x3be93c56,0xbbfebc50,0x3c633bb2,0xbc6ebb59,0x3c733a92,0xbc733a92,
0x3c5ebbc9,0xbc3f3c18,0x3c13bc42,0xbbbd3c61,0x3ba7bc65,0x3c74,0xbba7bc65,0x3c1d3c3b,
0xbbd4bc5c,0x3c3f3c18,0xbc6ebb59,0x3c6ebb59,0xbc67bb9b,0x3c6ebb59,0xbc2f3c2a,0x3b71bc6c,
0xbc593bde,0x3bd4bc5c,0x3b413c6f,0xbc37bc21,0xbb903c69,0xbbd4bc5c,0x3c633bb2,0xbc633bb2,
0x3be93c56,0xbc6ebb59,0x3c46bc0e,0xbac23c73,0x3c6b3b84,0xbc3f3c18,0xba42bc73,0x3c4d3c04,
0x3c53bbf4,0x3c74,0xbc53bbf4,0x3c53bbf4,0x3b71bc6c,0x3c3f3c18,0xbc593bde,0xba42bc73,
0xbbfebc50,0x3c6ebb59,0xbac23c73,0xbc5ebbc9,0xbc6ebb59,0x3bd4bc5c,0x3c3f3c18,0xbc3f3c18,
0xbc4d3c04,0xbbd4bc5c,0x3c67bb9b,0x3b413c6f,0xbb413c6f,0xbc6ebb59,0x3b71bc6c,0x3c6b3b84,
0x3c093c49,0xbc13bc42,0x3c1d3c3b,0xbc26bc33,0x3c6ebb59,0xbc633bb2,0x3c53bbf4,0xbc3f3c18,
0x3b11bc71,0x3ac23c73,0xbba7bc65,0x3c093c49,0xbc53bbf4,0x3c703b29,0xbc703b29,0x3c53bbf4,
0xbc3f3c18,0x3bd4bc5c,0x3c74,0xbbd4bc5c,0x3b903c69,0xbc37bc21,0x3c703b29,0xbc633bb2,
0x3c733a92,0xbc4d3c04,0x3ba7bc65,0x3b903c69,0x3bd4bc5c,0x3b903c69,0xbc53bbf4,0x3c6ebb59,
0xbc26bc33,0x3c74b9c2,0xbc1d3c3b,0xbb11bc71,0xbc633bb2,0x3b71bc6c,0x3c1d3c3b,0xbc733a92,
0x3c74,0xbc53bbf4,0x3c53bbf4,0x3c74,0x3c633bb2,0xbc2f3c2a,0xbba7bc65,0x3c733a92,
0x3c26bc33,0x3be93c56,0xbc703b29,0x3b11bc71,0xbbd4bc5c,0x3c6ebb59,0x3c74,0xbc6ebb59,
0xbc733a92,0x3a42bc73,0x3c703b29,0xbb903c69,0xbb903c69,0xbc67bb9b,0x3ba7bc65,0x3c633bb2,
0x3c2f3c2a,0xbc37bc21,0x3c3f3c18,0xbc46bc0e,0x3c26bc33,0xbc093c49,0x3bd4bc5c,0xbb903c69,
0xbc37bc21,0x3c593bde,0xbc6ebb59,0x3c74b9c2,0xbc1d3c3b,0x3ba7bc65,0x3c74,0xbba7bc65,
0x3c3f3c18,0xbc6ebb59,0x3c6ebb59,0xbc3f3c18,0x3c13bc42,0xbac23c73,0xbbd4bc5c,0x3c4d3c04,
0xbc46bc0e,0x3c74b9c2,0xbc3f3c18,0x3b71bc6c,0xbc093c49,0xbb11bc71,0x3c3f3c18,0xbc733a92,
0x3c4d3c04,0xbc6b3b84,0x3bd4bc5c,0x3bbd3c61,0x3bfebc50,0x3bbd3c61,0xbc6ebb59,0x3c37bc21,
0xbc53bbf4,0x3c53bbf4,0x3c74,0xbc53bbf4,0xbbe93c56,0xbc13bc42,0x3c6ebb59,0xbb413c6f,
0x3c593bde,0xbc2f3c2a,0xbbd4bc5c,0x3c72baf2,0x3bd4bc5c,0x3c3f3c18,0xbc3f3c18,0xbbd4bc5c,
0xbc5ebbc9,0x3bfebc50,0x3c3f3c18,0xbc2f3c2a,0xbbbd3c61,0xbc5ebbc9,0x3bd4bc5c,0x3c593bde,
0x3c4d3c04,0xbc53bbf4,0x3c593bde,0xbc5ebbc9,0x3b11bc71,0x3c74,0xbb11bc71,0x3b903c69,
0xbc6b3b84,0x3c53bbf4,0xbc2f3c2a,0x3bfebc50,0x3c1d3c3b,0xbc53bbf4,0x3c703b29,0xbc703b29,
0x3bd4bc5c,0x3c74,0xbbd4bc5c,0x3c3f3c18,0xbc74b9c2,0x3c53bbf4,0xbbe93c56,0xba42bc73,
0x3bbd3c61,0xbc53bbf4,0x3c72baf2,0xbc2f3c2a,0x3c26bc33,0x3c74,0xbc26bc33,0x3c733a92,
0xbc67bb9b,0x3c53bbf4,0xbb413c6f,0xbc13bc42,0x3ac23c73,0xbc53bbf4,0x3c5ebbc9,0xbb413c6f,
0x3c53bbf4,0x3c74,0xbc53bbf4,0x3c53bbf4,0xbc46bc0e,0x3c53bbf4,0x3ac23c73,0xbc67bb9b,
0xbb413c6f,0xbc53bbf4,0x3c37bc21,0x3bbd3c61,0x3c6ebb59,0x3c74,0xbc6ebb59,0x3bd4bc5c,
0xbc13bc42,0x3c53bbf4,0x3bbd3c61,0xbc6b3b84,0xbbe93c56,0xbc53bbf4,0x3bfebc50,0x3c4d3c04,
0x3c633bb2,0xbc67bb9b,0x3c6b3b84,0xbc6ebb59,0xbbd4bc5c,0x3c093c49,0xbc26bc33,0x3c3f3c18,
0xbb903c69,0x3a42bc73,0x3b413c6f,0xbbd4bc5c,0x3c53bbf4,0xbc1d3c3b,0x3ba7bc65,0x3c74,
0xbc6ebb59,0x3c6ebb59,0xbc3f3c18,0x3bd4bc5c,0x3c093c49,0xbc5ebbc9,0x3c72baf2,0xbc3f3c18,
0x3b11bc71,0x3be93c56,0xbc5ebbc9,0x3c6ebb59,0xbc3f3c18,0x3b11bc71,0x3c093c49,0xbc6ebb59,
0x3c733a92,0xbc2f3c2a,0xba42bc73,0x3c3f3c18,0xbc26bc33,0x3c72baf2,0xbbe93c56,0xbbd4bc5c,
0x3c74,0xbc53bbf4,0x3c53bbf4,0x3c74,0x3c26bc33,0x3bbd3c61,0xbc74b9c2,0x3bd4bc5c,
0xbc733a92,0x3b71bc6c,0x3c4d3c04,0xbc3f3c18,0x3c3f3c18,0xbc3f3c18,0xbbd4bc5c,0x3c6ebb59,
0xbb11bc71,0x3c74b9c2,0xbac23c73,0xbc6ebb59,0xbc093c49,0xbc46bc0e,0x3c13bc42,0x3c3f3c18,
0x3c703b29,0xbc72baf2,0x3c733a92,0xbc74b9c2,0xbc53bbf4,0x3c633bb2,0xbc6ebb59,0x3c733a92,
0x3c1d3c3b,0xbc46bc0e,0x3c633bb2,0xbc72baf2,0xbba7bc65,0x3c1d3c3b,0xbc53bbf4,0x3c703b29,
0x3c74,0xbbd4bc5c,0x3c3f3c18,0xbc6ebb59,0x3ba7bc65,0x3b413c6f,0xbc26bc33,0x3c6b3b84,
0xbc1d3c3b,0x3a42bc73,0x3c093c49,0xbc67bb9b,0x3c53bbf4,0xbb903c69,0xbbd4bc5c,0x3c633bb2,
0xbc703b29,0x3bfebc50,0x3b903c69,0xbc5ebbc9,0x3c703b29,0xbc2f3c2a,0xbb11bc71,0x3c593bde,
0xbc53bbf4,0x3c53bbf4,0x3c74,0xbc53bbf4,0x3c1d3c3b,0xbc6b3b84,0x3b11bc71,0x3c4d3c04,
0xbba7bc65,0x3c74b9c2,0xbb903c69,0xbc46bc0e,0x3c74,0xbc6ebb59,0x3bd4bc5c,0x3c3f3c18,
0x3ba7bc65,0x3c593bde,0xbc093c49,0xbc37bc21,0xbc1d3c3b,0xbc37bc21,0x3c26bc33,0x3c2f3c2a,
0x3f803f80,0x3f803f80,0x3f803f80,0x3f803f80,0x3f7f3f80,0x3f753f7b,0x3f613f6c,0x3f443f54,
0x3f7b3f80,0x3f543f6c,0x3f0b3f33,0x3e323ebb,0x3f753f80,0x3f203f54,0x3d993ebb,0xbf00be64,
0x3f6c3f80,0x3ebb3f33,0xbed3bccc,0xbf71bf3c,0x3f613f80,0x3d993f0b,0xbf4cbed3,0xbf71bf7d,
0x3f543f80,0xbe643ebb,0xbf7dbf3c,0xbf00bf67,0x3f443f80,0xbf003e32,0xbf71bf71,0x3e32bf00,
0x3f333f80,0xbf3cbccc,0xbf29bf80,0x3f443d99,0x3f203f80,0xbf67be64,0xbe64bf67,0x3f803f20,
0x3f0b3f80,0xbf7dbed3,0x3e8bbf29,0x3f443f75,0x3eea3f80,0xbf7dbf15,0x3f33bea3,0x3e323f75,
0x3ebb3f80,0xbf67bf3c,0x3f753d99,0xbf003f20,0x3e8b3f80,0xbf3cbf5a,0x3f7b3eea,0xbf713d99,
0x3e323f80,0xbf00bf71,0x3f443f44,0xbf71bf00,0x3d993f80,0xbe64bf7d,0x3ebb3f75,0xbf00bf67,
0x3f803f80,0x3f803f80,0x3f803f80,0x3f803f80,0x3f203f33,0x3eea3f0b,0x3e8b3ebb,0x3d993e32,
0xbe64bccc,0xbf15bed3,0xbf5abf3c,0xbf7dbf71,0xbf67bf3c,0xbf7dbf7d,0xbf3cbf67,0xbe64bf00,
0xbf67bf80,0xbea3bf29,0x3eea3d99,0x3f753f44,0xbe64bf29,0x3f333e8b,0x3f7b3f75,0x3ebb3f44,
0x3f203d99,0x3f753f75,0x3d993f20,0xbf67bf00,0x3f803f44,0x3e323f44,0xbf71bf00,0xbf00bf71,
0x3f203f7f,0xbf4cbdff,0xbf15bf7d,0x3f543e32,0xbe643f20,0xbf67bf67,0x3f20be64,0x3f203f80,
0xbf67bdff,0xbcccbf5a,0x3f6c3f54,0xbf3c3e32,0xbf67bf4c,0x3f61bccc,0xbdff3f54,0xbf3cbf71,
0xbe64bf7d,0x3f543f54,0xbf7dbe64,0x3f20bf00,0x3f20bf15,0xbdff3f6c,0xbed3bf7d,0x3f543f44,
0x3f803e32,0xbf713e32,0x3f44bf00,0xbf003f44,0x3f203f54,0xbf3cbf3c,0x3f543f20,0xbf67bf00,
0x0,0x0,0x0,0x0,0xba8b0000,0xbabfb99b,0xba33b9d1,0x39dcbaf7,
0xb99b0000,0xbaf7b9d1,0xb9deba81,0xb93d38e0,0xbabf0000,0xbac6baf7,0x37c138e0,0xa580390e,
0xb9d10000,0x38e0ba81,0x3a58b7ee,0x3ae13aad,0xba330000,0x37c1b9de,0xb9873a58,0x3ae1ba10,
0xbaf70000,0x390e38e0,0xba103aad,0x25e03ab4,0x39dc0000,0xa580b93d,0x3ae13ae1,0xb93d25e0,
0xba810000,0x3aadb7ee,0xbac83aa3,0x39dc37c1,0xbac60000,0x3ab4390e,0x390e3ab4,0xbac6,
0xb9de0000,0xba103a58,0xba29bac8,0x39dcbabf,0xba570000,0xba10bae0,0xba81b905,0xb93dbabf,
0x38e00000,0x3ab43aad,0xbabf37c1,0xa580bac6,0xba290000,0x3aadbae3,0xb99bba57,0x3ae137c1,
0xb93d0000,0x25e03ae1,0x39dc39dc,0x3ae1a580,0x37c10000,0x390eba10,0x38e0babf,0x25e03ab4,
0x0,0x0,0x0,0x0,0xbac6ba81,0xba57b9de,0xba2938e0,0x37c1b93d,
0x390eb7ee,0xbae03a58,0xbae33aad,0xba103ae1,0x3ab43aad,0xba10ba10,0x3aad3ab4,0x390e25e0,
0x3ab43aa3,0xb905bac8,0xba5737c1,0xbabf39dc,0x390ebac8,0xba81ba29,0xb99bbabf,0x38e039dc,
0xbac637c1,0xbabfbabf,0x37c1bac6,0x3ab4a580,0x39dc,0xb93d39dc,0x3ae1a580,0x25e03ae1,
0xbac6ba8b,0xb9873930,0xbae0ba10,0xbaf7b93d,0x390ebac6,0x3ab43ab4,0xbac6390e,0xbac60000,
0x3ab43930,0xb7eebae3,0xb9d1baf7,0x3aadb93d,0x3ab4b987,0xba33b7ee,0x3930baf7,0x3aad3ae1,
0x390eba10,0xbaf7baf7,0xba10390e,0xbac625e0,0xbac6bae0,0x3930b9d1,0x3a58ba10,0xbaf739dc,
0xb93d,0x3ae1b93d,0x39dc25e0,0xa58039dc,0xbac6baf7,0x3aad3aad,0xbaf7bac6,0x3ab4a580,
0xbccc3f80,0x3d99bf80,0xbdff3f7f,0x3e32bf7d,0xbdff3f80,0x3ebbbf78,0xbf153f61,0x3f44bf3c,
0xbe643f80,0x3f20bf67,0xbf673f20,0x3f80be64,0xbea33f80,0x3f54bf4c,0xbf803e8b,0x3f443ebb,
0xbed33f80,0x3f75bf29,0xbf5abdff,0x3e323f54,0xbf003f80,0x3f80bf00,0xbf00bf00,0xbf003f80,
0xbf153f80,0x3f75bea3,0xbcccbf4c,0xbf713f54,0xbf293f80,0x3f54bdff,0x3eeabf78,0xbf713ebb,
0xbf3c3f80,0x3f203d99,0x3f54bf7d,0xbf00be64,0xbf4c3f80,0x3ebb3e8b,0x3f7fbf5a,0x3e32bf3c,
0xbf5a3f80,0x3d993eea,0x3f6cbf15,0x3f44bf7d,0xbf673f80,0xbe643f20,0x3f20be64,0x3f80bf67,
0xbf713f80,0xbf003f44,0x3e323e32,0x3f44bf00,0xbf783f80,0xbf3c3f61,0xbea33f0b,0x3e323d99,
0xbf7d3f80,0xbf673f75,0xbf3c3f54,0xbf003f20,0xbf803f80,0xbf7d3f7f,0xbf783f7b,0xbf713f75,
0xbe643f7b,0x3e8bbf78,0xbea33f75,0x3ebbbf71,0xbf673f0b,0x3f7bbea3,0xbf803d99,0x3f753e32,
0xbf67be64,0x3f203f20,0xbe64bf67,0xbe643f80,0xbe64bf5a,0xbed33f7f,0x3f61bf3c,0xbf7d3e32,
0x3f20bf78,0xbf803eea,0x3f333ebb,0x3d99bf71,0x3f80bf00,0xbf00bf00,0xbf003f80,0x3f80bf00,
0x3f203e8b,0x3f0bbf80,0xbf783ebb,0x3d993f44,0xbe643f61,0x3f7fbf15,0xbcccbf3c,0xbf7d3f44,
0xbf673f75,0x3ebb3ebb,0x3f75bf67,0xbe64bf00,0xbf673eea,0xbf293f7b,0x3f0b3d99,0x3f75bf71,
0xbe64bea3,0xbf783f33,0xbf293f75,0x3ebb3e32,0x3f20bf67,0xbe64be64,0xbf673f20,0xbf673f80,
0x3f80bf71,0x3f44bf71,0x3e32bf00,0xbf003e32,0x3f20bed3,0x3f6cbf4c,0x3f7fbf7d,0x3f54bf71,
0xbe643ebb,0x3d993d99,0x3ebbbe64,0x3f20bf00,0xbf673f6c,0xbf5a3f61,0xbf4c3f54,0xbf3c3f44,
0xb7ee0000,0x37c13aa3,0x3930ba8b,0xb93dba10,0x39300000,0x38e0b9ac,0xbae0ba33,0x39dc3aad,
0x390e0000,0xbac63ab4,0x3ab4bac6,0x390e,0xb9050000,0xbaf7b987,0x3aa3ba29,0x39dc38e0,
0x3a580000,0xbabfbac8,0xbae33930,0xb93dbaf7,0xa5800000,0x25e0,0x25e0a580,0xa5800000,
0xbae00000,0xbabfb905,0xb7eeb987,0x3ae1baf7,0xbac80000,0xbaf73930,0xba57b9ac,0x3ae138e0,
0x3aad0000,0xbac637c1,0xbaf7ba10,0x25e0390e,0xb9870000,0x38e0ba29,0xba8bbae3,0xb93d3aad,
0xbae30000,0x37c1ba57,0xb9d1bae0,0x39dcba10,0x3ab40000,0x390ebac6,0xbac6390e,0x3ab4,
0x3ae10000,0xa58039dc,0xb93db93d,0x39dc25e0,0xb9ac0000,0x3aadba33,0xb905b9de,0xb93d37c1,
0xba100000,0x3ab4babf,0x3aadbaf7,0xa580bac6,0x3aa30000,0xba10ba8b,0xb9acb99b,0x3ae1babf,
0x390eb99b,0xba29b9ac,0xb905babf,0x38e03ae1,0x3ab4b9de,0xb99bb905,0x3aa337c1,0xbabfb93d,
0x3ab4390e,0xbac6bac6,0x390e3ab4,0x390e0000,0x390ebae3,0x3a58ba8b,0xba333aad,0xba10b93d,
0xbac6b9ac,0x3aa3ba57,0xba8138e0,0x37c13ae1,0x25e0,0x25e0a580,0xa5800000,0x25e0,
0xbac6ba29,0xb9de3aa3,0xb9ac38e0,0x37c139dc,0x390eba33,0xba8bbae0,0xb7ee3aad,0xba1039dc,
0x3ab4babf,0x38e038e0,0xbabf3ab4,0x390ea580,0x3ab4ba57,0xbac8b99b,0xb9de37c1,0xbabf3ae1,
0x390eb905,0xb9acba81,0xbac8babf,0x38e0b93d,0xbac63ab4,0x390e390e,0x3ab4bac6,0x3ab40000,
0x3ae1,0x39dc3ae1,0xb93da580,0x25e0b93d,0xbac63a58,0xb9d1b987,0xba8bba10,0xbaf73ae1,
0x390e38e0,0x37c137c1,0x38e0390e,0xbac625e0,0x3ab4b9d1,0xbae3ba33,0xb987baf7,0x3aad39dc,
0x0,0x0,0x0,0x0,0x3dcc0000,0x3e973e4b,0x3ef53ec7,0x3f253f10,
0x3e4b0000,0x3f103ec7,0x3f573f37,0x3f7c3f6e,0x3e970000,0x3f483f10,0x3f7f3f6e,0x3f5e3f7a,
0x3ec70000,0x3f6e3f37,0x3f693f80,0x3eaf3f2e,0x3ef50000,0x3f7f3f57,0x3f1b3f69,0xbeaf3e19,
0x3f100000,0x3f7a3f6e,0x3e193f2e,0xbf5ebede,0x3f250000,0x3f5e3f7c,0xbeaf3eaf,0xbf7cbf5e,
0x3f370000,0x3f2e3f80,0xbf40bd4c,0xbf25bf7f,0x3f480000,0x3ede3f7a,0xbf7abede,0xbf48,
0x3f570000,0x3e193f69,0xbf76bf40,0x3f25be97,0x3f640000,0xbe193f50,0xbf37bf73,0x3f7c3e97,
0x3f6e0000,0xbede3f2e,0xbe97bf7f,0x3f5e3f48,0x3f760000,0xbf2e3f05,0x3e4bbf64,0x3eaf3f7f,
0x3f7c0000,0xbf5e3eaf,0x3f25bf25,0xbeaf3f5e,0x3f7f0000,0xbf7a3e19,0x3f6ebe97,0xbf5e3ede,
0x0,0x0,0x0,0x0,0x3f483f37,0x3f643f57,0x3f763f6e,0x3f7f3f7c,
0x3f7a3f80,0x3f503f69,0x3f053f2e,0x3e193eaf,0x3ede3f2e,0xbe193e19,0xbf2ebede,0xbf7abf5e,
0xbedebd4c,0xbf73bf40,0xbf64bf7f,0xbe97bf25,0xbf7abf40,0xbf37bf76,0x3e4bbe97,0x3f6e3f25,
0xbf48bf7f,0x3e97be97,0x3f7f3f48,0x3ede3f5e,0xbf25,0x3f7c3f25,0x3eaf3f5e,0xbf5ebeaf,
0x3f483dcc,0x3f1b3f7e,0xbf50be19,0xbf10bf7c,0x3f7a3f48,0xbede3ede,0xbf48bf7a,0x3f480000,
0x3ede3f7e,0xbf80bf05,0x3ec7bf10,0x3f2e3f7c,0xbede3f1b,0xbef5bf80,0x3f7e3f10,0xbf2e3eaf,
0xbf7abe19,0x3f10bf10,0x3e193f7a,0xbf48bf5e,0xbf48bf50,0x3f7e3ec7,0xbf693e19,0x3f10bf25,
0xbf7c,0x3eaf3f7c,0xbf25bf5e,0x3f5e3f25,0x3f48bf10,0xbf2e3f2e,0x3f10bf48,0xbede3f5e,
0x0,0x0,0x0,0x0,0xb82e0000,0xb92fb8c9,0xb989b979,0xbae53a57,
0xb8c90000,0x3a57b979,0x393f3a86,0x39e33a9b,0xb92f0000,0x3a183a57,0x3a913a9b,0xba98bad6,
0xb9790000,0x3a9b3a86,0x3ab1b9a3,0x396a39fe,0xb9890000,0x3a91393f,0xbada3ab1,0xb96ab9c3,
0x3a570000,0xbad63a9b,0xb9c339fe,0x3a98b998,0xbae50000,0xba9839e3,0xb96a396a,0xb9e33a98,
0x3a860000,0x39feb9a3,0x3965b82d,0x3ae5ba91,0x3a180000,0x3998bad6,0x3ad6b998,0xba18,
0x393f0000,0xb9c33ab1,0xbadd3965,0xbae5392f,0xba450000,0x39c3ba13,0xba863aa9,0x39e3b92f,
0x3a9b0000,0xb99839fe,0x392fba91,0xba983a18,0x3add0000,0xb9fe3afa,0xb8c93a45,0x396a3a91,
0x39e30000,0x3a98396a,0xbae53ae5,0xb96aba98,0x3a910000,0x3ad6b9c3,0x3a9b392f,0x3a983998,
0x0,0x0,0x0,0x0,0x3a183a86,0xba45393f,0x3add3a9b,0x3a9139e3,
0xbad6b9a3,0xba133ab1,0x3afa39fe,0xb9c3396a,0x399839fe,0x39c3b9c3,0xb9feb998,0x3ad63a98,
0xb998b82d,0x3aa93965,0x3a45ba91,0x392f3ae5,0x3ad63965,0xba86badd,0xb8c9392f,0x3a9bbae5,
0xba18ba91,0xb92f392f,0x3a913a18,0x3998ba98,0x3ae5,0x39e3bae5,0x396aba98,0x3a98b96a,
0x3a18b82e,0xbada3859,0x3a1339c3,0xba57b9e3,0xbad63a18,0xb9983998,0xba183ad6,0x3a180000,
0x39983859,0x39a3bafa,0xb979ba57,0x39fe39e3,0xb998bada,0x398939a3,0x38593a57,0xb9fe396a,
0x3ad639c3,0x3a57ba57,0xb9c3bad6,0xba183a98,0xba183a13,0x3859b979,0xbab1b9c3,0x3a573ae5,
0xb9e3,0x396a39e3,0x3ae53a98,0xba98bae5,0x3a18ba57,0xb9fe39fe,0x3a57ba18,0xb998ba98,
0x3f800000,0xbf7fbd4c,0x3f7e3dcc,0xbf7cbe19,0x3f7e0000,0xbf6ebe7d,0x3f503ef5,0xbf25bf2e,
0x3f7a0000,0xbf48bede,0x3ede3f48,0xbf7a,0x3f730000,0xbf10bf1b,0xbd4c3f76,0x3f25bf6e,
0x3f690000,0xbe97bf40,0xbf053f7e,0x3f7cbf10,0x3f5e0000,0xbf5e,0xbf5e3f5e,0x3f5e0000,
0x3f500000,0x3e97bf73,0xbf803f1b,0x3eaf3f10,0x3f400000,0x3f10bf7e,0xbf643e7d,0xbeaf3f6e,
0x3f2e0000,0x3f48bf7f,0xbf10be19,0xbf5e3f7a,0x3f1b0000,0x3f6ebf76,0xbdccbf05,0xbf7c3f2e,
0x3f050000,0x3f7fbf64,0x3ec7bf50,0xbf253e19,0x3ede0000,0x3f7abf48,0x3f48bf7a,0xbede,
0x3eaf0000,0x3f5ebf25,0x3f7cbf7c,0x3f25bf5e,0x3e7d0000,0x3f2ebef5,0x3f73bf57,0x3f7cbf7f,
0x3e190000,0x3edebe97,0x3f2ebf10,0x3f5ebf48,0x3d4c0000,0x3e19bdcc,0x3e7dbe4b,0x3eafbe97,
0x3f7a3e4b,0xbf76be7d,0x3f733e97,0xbf6ebeaf,0x3ede3f57,0xbe4bbf73,0xbd4c3f7f,0x3e97bf7c,
0xbede3f7a,0x3f48bf48,0xbf7a3ede,0x3f7a0000,0xbf7a3f05,0x3f693dcc,0xbef5bf2e,0xbe193f7c,
0xbf48be7d,0x3d4c3f64,0x3f37bf6e,0xbf7f3eaf,0xbf5e,0xbf5e3f5e,0x3f5e0000,0xbf5e,
0x3f48bf76,0xbf573d4c,0xbe7d3f6e,0x3f7fbf25,0x3f7abef5,0x3dccbf50,0xbf803f2e,0x3e193f25,
0x3ede3e97,0x3f6ebf6e,0xbe97bede,0xbf7a3f5e,0xbede3f64,0x3f40be4b,0x3f57bf7f,0xbe97beaf,
0xbf7a3f73,0xbe7d3f37,0x3f40be97,0x3f6ebf7c,0xbf483ede,0xbf7a3f7a,0xbede3f48,0x3ede0000,
0xbeaf,0xbf253eaf,0xbf7c3f5e,0xbf5e3f7c,0x3f48bf69,0x3ec7bf1b,0xbdccbe19,0xbf103eaf,
0x3f7abf6e,0x3f7fbf7f,0x3f6ebf7a,0x3f48bf5e,0x3edebec7,0x3f05bef5,0x3f1bbf10,0x3f2ebf25,
0xb9a30000,0xba91b82d,0x3859b82e,0xb9e339c3,0x38590000,0xba9b39a4,0xba13b989,0x3ae5b9fe,
0xbad60000,0xba18b998,0x39983a18,0x3ad6,0xbaa90000,0xba573ada,0xb82d3add,0xbae5ba9b,
0x3ab10000,0x392f3965,0xbafa3859,0x39e3ba57,0xba980000,0x3a98,0x3a98ba98,0xba980000,
0xba130000,0xb92f3aa9,0x39a3bada,0x396a3a57,0xb9650000,0x3a57b859,0x3a45b9a4,0xb96a3a9b,
0x39fe0000,0x3a18ba91,0xba5739c3,0x3a98bad6,0xbada0000,0x3a9bbadd,0x382ebafa,0xb9e339fe,
0x3afa0000,0x3a913a45,0xb9793a13,0x3ae5b9c3,0x39980000,0xbad6ba18,0x3a183ad6,0xb998,
0x396a0000,0xba983ae5,0x39e3b9e3,0xbae53a98,0xb9a40000,0x39fe3989,0xbaa9b93f,0x39e3ba91,
0xb9c30000,0x3998392f,0x39feba57,0xba98ba18,0x382d0000,0xb9c3382e,0xb9a438c9,0x396a392f,
0xbad6b8c9,0xbadd39a4,0xbaa9b92f,0xba9bb96a,0x3998393f,0x38c93aa9,0xb82d3a91,0xb92fb9e3,
0xb998bad6,0x3a18ba18,0x3ad63998,0xbad60000,0x3ad63afa,0x3ab1b82e,0x3989b9fe,0x39c339e3,
0xba1839a4,0x382dba45,0x3a86ba9b,0xba91396a,0x3a98,0x3a98ba98,0xba980000,0x3a98,
0x3a18badd,0xb93f382d,0x39a43a9b,0x3a913ae5,0xbad63989,0xb82e3a13,0x39a339fe,0xb9c3bae5,
0x3998b92f,0x3a9bba9b,0x392fb998,0x3ad6ba98,0xb998ba45,0xb96538c9,0x393fba91,0x392fb96a,
0x3ad6baa9,0x39a43a86,0xb965392f,0x3a9bb9e3,0xba183998,0x3ad6bad6,0xb9983a18,0x39980000,
0xb96a,0x3ae5396a,0xb9e3ba98,0x3a9839e3,0x3a18bab1,0xb9793ada,0x382e39c3,0xba57396a,
0xbad6ba9b,0x3a91ba91,0x3a9b3ad6,0x3a183a98,0x39983979,0x3afa3989,0xbadaba57,0x39fe3ae5,
};

typedef unsigned short u16;
typedef unsigned long long u64;
typedef __attribute__((ext_vector_type(8))) short bf16x8;
typedef __attribute__((ext_vector_type(4))) short s16x4;
typedef __attribute__((ext_vector_type(4))) float f32x4;
typedef __attribute__((ext_vector_type(2))) float f32x2;
typedef __attribute__((ext_vector_type(4))) unsigned u32x4;
typedef __attribute__((ext_vector_type(2))) __bf16 bf16x2_t;
typedef __attribute__((address_space(3))) s16x4* lds_s16x4_p;
typedef __attribute__((address_space(3))) void* lds_void_p;
#define DEVINL __device__ __forceinline__

#define STAMP(i)
#define STAMP_FLUSH()
#define STAMP2(a, b) do { if (s == 0) { STAMP(a); } else { STAMP(b); } } while (0)
typedef unsigned stv_t;

#define MFMA16(a, b, c) __builtin_amdgcn_mfma_f32_16x16x32_bf16((a), (b), (c), 0, 0, 0)
#define MFMAU(a, b, c) MFMA16(__builtin_bit_cast(bf16x8, a), __builtin_bit_cast(bf16x8, b), c)
#define SB0() __builtin_amdgcn_sched_barrier(0)
constexpr unsigned OOB = 0x7ffffff0u;
constexpr int SC1 = 16;

template <int N> struct IC { static constexpr int v = N; };
template <int... I, class F> DEVINL void static_for_impl(std::integer_sequence<int, I...>, F&& f) { (f(IC<I>{}), ...); }
template <int N, class F> DEVINL void static_for(F&& f) { static_for_impl(std::make_integer_sequence<int, N>{}, f); }
DEVINL void bar() { asm volatile("" ::: "memory"); __builtin_amdgcn_s_barrier(); asm volatile("" ::: "memory"); }
template <int N> DEVINL void wait_vm() { asm volatile("s_waitcnt vmcnt(%0)" ::"n"(N) : "memory"); }
DEVINL void wait_lds() { __builtin_amdgcn_s_waitcnt(0xc07f); }

constexpr int T0 = 16000, T2 = 4000;
constexpr int NT1 = 167;
constexpr int XST = 32768;
constexpr size_t X1_TB = 16 * XST, X1_G = 2 * X1_TB, X1_S = 16 * X1_G;
constexpr size_t X2_TB = 8 * XST, X2_G = 2 * X2_TB, X2_S = 16 * X2_G;
constexpr size_t WF_G = 1 << 20, WF_BYTES = 16 * WF_G;
constexpr size_t Y1_S = (size_t)128 * 65536, Y2_S = (size_t)64 * 65536;
constexpr size_t OFF_FLAG = 0;
constexpr int F_X = 0, F_W1 = 768, F_W2 = 1024, F_G1 = 1280, F_M = 1792, F_G2 = 2304, F_S = 2816, F_EPOCH = 2840, F_H = 512, F_END = 2848;
constexpr size_t OFF_WFT = OFF_FLAG + F_END * 128;
constexpr size_t OFF_WOT = OFF_WFT + 128 * 1024;
constexpr size_t OFF_GE = OFF_WOT + 128 * 1024;
constexpr size_t OFF_X1F = 4 << 20;
constexpr size_t OFF_W1F = OFF_X1F + 2 * X1_S;
constexpr size_t OFF_Y1F = OFF_W1F + WF_BYTES;
constexpr size_t OFF_X2F = OFF_Y1F + 2 * Y1_S;
constexpr size_t OFF_W2F = OFF_X2F + 2 * X2_S;
constexpr size_t OFF_Y2F = OFF_W2F + WF_BYTES;
constexpr size_t WS_USED = OFF_Y2F + 2 * Y2_S;
constexpr int TAB_F = 0, TAB_G = 8, TAB_T = 14;
constexpr int LDS_BYTES = 81920;

DEVINL unsigned pack_bf16(float a, float b) {
  f32x2 v = {a, b};
  bf16x2_t r = __builtin_convertvector(v, bf16x2_t);
  return __builtin_bit_cast(unsigned, r);
}
DEVINL u32x4 pack8(const f32x4& a, const f32x4& b) {
  return (u32x4){pack_bf16(a[0], a[1]), pack_bf16(a[2], a[3]), pack_bf16(b[0], b[1]), pack_bf16(b[2], b[3])};
}
DEVINL s16x4 tr_read(const char* p) { return __builtin_amdgcn_ds_read_tr16_b64_v4i16((lds_s16x4_p)(p)); }
DEVINL bf16x8 cat8(s16x4 a, s16x4 b) { return __builtin_shufflevector(a, b, 0, 1, 2, 3, 4, 5, 6, 7); }
DEVINL bf16x8 tr_frag(const char* p, int second) { return cat8(tr_read(p), tr_read(p + second)); }
template <int AUX> DEVINL void dma16(__amdgpu_buffer_rsrc_t rs, char* lds_dst, unsigned voff, unsigned soff) {
  const unsigned m = (unsigned)(size_t)(lds_void_p)lds_dst;
  if constexpr (AUX == 0) asm volatile("s_mov_b32 m0, %0\n\ts_nop 0\n\tbuffer_load_dwordx4 %1, %2, %3 offen lds" :: "s"(m), "v"(voff), "s"(rs), "s"(soff) : "memory");
  else asm volatile("s_mov_b32 m0, %0\n\ts_nop 0\n\tbuffer_load_dwordx4 %1, %2, %3 offen sc1 lds" :: "s"(m), "v"(voff), "s"(rs), "s"(soff) : "memory");
}
DEVINL __amdgpu_buffer_rsrc_t mkrsrc(const void* p, unsigned bytes) { return __builtin_amdgcn_make_buffer_rsrc((void*)p, 0, bytes, 0x00020000); }
DEVINL void st16(void* p, u32x4 v) { asm volatile("global_store_dwordx4 %0, %1, off sc1\n\ts_nop 1" ::"v"(p), "v"(v) : "memory"); }
template <int I, bool DEV> DEVINL void ldfrag(u32x4& d, const char* base, unsigned lane16) {
  const char* b = base + (I >> 3) * 8192 + 4096;
  if constexpr (DEV) asm volatile("global_load_dwordx4 %0, %1, %2 offset:%3 sc1" : "=v"(d) : "v"(lane16), "s"(b), "n"((I & 7) * 1024 - 4096) : "memory");
  else asm volatile("global_load_dwordx4 %0, %1, %2 offset:%3" : "=v"(d) : "v"(lane16), "s"(b), "n"((I & 7) * 1024 - 4096) : "memory");
}
template <int I, int BS = 16384> DEVINL void ldfrag_rot(u32x4& d, const char* base, int rot, unsigned lane16) {
  ldfrag<I & 7, true>(d, base + (((I >> 3) + rot) & 3) * BS, lane16);
}
DEVINL void tie(u32x4& a) { asm volatile("" : "+v"(a)); }
template <int N> DEVINL void tie(u32x4 (&a)[N]) {
#pragma unroll
  for (int i = 0; i < N; ++i) tie(a[i]);
}
DEVINL int lane_id() { int l; asm volatile("v_mbcnt_lo_u32_b32 %0, -1, 0\n\tv_mbcnt_hi_u32_b32 %0, -1, %0" : "=v"(l)); return l; }
DEVINL void sgpr_settle() { asm volatile("s_nop 4" ::: "memory"); }

#define CT_STEP 64
DEVINL void code_touch(unsigned& d, int slice) {
  u64 pc;
  unsigned len, off = slice * CT_STEP;
  asm volatile("s_getpc_b64 %0\n.Lpc%=:\n\ts_mov_b32 %1, .Lpc%=-k_all" : "=s"(pc), "=s"(len));
  pc -= len;
  asm volatile("s_mov_b32 %1, .Lk_end-k_all\n\ts_max_u32 %1, %1, .Lt_end-k_all\n"
               "1:\n\ts_load_dword %0, %3, %2\n\ts_add_u32 %2, %2, %4\n\ts_cmp_lt_u32 %2, %1\n\ts_cbranch_scc1 1b"
               : "=&s"(d), "=&s"(len), "+s"(off) : "s"(pc), "n"(128 * CT_STEP) : "scc", "memory");
}
DEVINL void tie(unsigned& a) { asm volatile("" : "+v"(a)); }

#define PRIO(x) __builtin_amdgcn_s_setprio(x)
#define PRIO_F 3
#define PRIO_G0 2
#define PRIO_GW 0
#define PRIO_G1 0
#define PRIO_G2A 0
#define PRIO_G2B 0
#define PRIO_JB 0
#define PRIO_T0 1
#define PRIO_T1 1
#define PRIO_T2 0
#define PRIO_MA 0
#define PRIO_MB 1
#define PRIO_JA 0
DEVINL void set_flag(char* ws, int flag, u64 tag, int w) {
  u64 d;
  unsigned o;
  asm volatile("s_cmp_lg_u32 %2, 0\n\ts_cbranch_scc1 1f\n\ts_mov_b64 exec, 1\n\ts_nop 1\n\tv_mov_b32 %1, %3\n\tv_mov_b64 %0, %5\n\t"
               "global_store_dwordx2 %1, %0, %4 sc1\n\ts_nop 1\n\ts_mov_b64 exec, -1\n1:"
               : "=&v"(d), "=&v"(o) : "s"(w), "s"(flag * 128), "s"(ws + OFF_FLAG), "s"(tag) : "scc", "memory");
}
template <int N = 0> DEVINL void publish(char* ws, int flag, u64 tag, int w) { wait_vm<N>(); PRIO(PRIO_F); bar(); set_flag(ws, flag, tag, w); }
#define POLL_SLEEP 1
DEVINL void poll(const char* ws, int flag, u64 mask, u64 tag) {
  const unsigned off = flag * 128;
  u64 v;
  unsigned cnt;
  asm volatile("s_mov_b64 exec, %6\n\ts_nop 4\n\ts_mov_b32 %1, 40000\n"
               "1:\n\tglobal_load_dwordx2 %0, %2, %3 sc1\n\ts_waitcnt vmcnt(0)\n\tv_cmp_ne_u64 vcc, %4, %0\n\ts_nop 1\n\ts_cbranch_vccz 2f\n\t"
               "s_cmp_eq_u64 vcc, %6\n\ts_cbranch_scc1 3f\n\t"
               "s_mov_b64 exec, vcc\n\ts_sub_u32 %1, %1, 1\n\ts_cbranch_scc1 2f\n\ts_sleep %5\n\ts_branch 1b\n"
               "3:\n\ts_mov_b64 exec, 1\n"
               "4:\n\ts_sleep %5\n\tglobal_load_dwordx2 %0, %2, %3 sc1\n\ts_waitcnt vmcnt(0)\n\tv_cmp_ne_u64 vcc, %4, %0\n\ts_sub_u32 %1, %1, 1\n\ts_cbranch_scc1 2f\n\ts_cbranch_vccnz 4b\n\t"
               "s_andn2_b64 exec, %6, 1\n\ts_branch 1b\n"
               "2:\n\ts_mov_b64 exec, -1"
               : "=&v"(v), "=&s"(cnt) : "v"(off), "s"(ws + OFF_FLAG), "s"(tag), "n"(POLL_SLEEP), "s"(mask) : "vcc", "scc", "memory");
}
constexpr u64 lanes(int n) { return (1ull << n) - 1; }
DEVINL int poll_vote(const char* ws, int flag, u64 tag) {
  const unsigned off = flag * 128;
  u64 v, t;
  unsigned cnt;
  asm volatile("s_mov_b64 exec, 0xffff\n\ts_nop 4\n\ts_mov_b32 %1, 40000\n"
               "1:\n\tglobal_load_dwordx2 %0, %3, %4 sc1\n\ts_waitcnt vmcnt(0)\n\tv_lshlrev_b64 %2, 1, %0\n\tv_cmp_ne_u64 vcc, %5, %2\n\ts_nop 1\n\ts_cbranch_vccz 2f\n\t"
               "s_mov_b64 exec, vcc\n\ts_sub_u32 %1, %1, 1\n\ts_cbranch_scc1 2f\n\ts_sleep %6\n\ts_branch 1b\n"
               "2:\n\ts_mov_b64 exec, 0xffff\n\ts_nop 1\n\tv_cmp_ne_u64 vcc, %7, %0\n\ts_mov_b64 exec, -1\n\ts_nop 1\n\ts_bcnt1_i32_b64 %1, vcc"
               : "=&v"(v), "=&s"(cnt), "=&v"(t) : "v"(off), "s"(ws + OFF_FLAG), "s"(tag << 1), "n"(POLL_SLEEP), "s"(tag) : "vcc", "scc", "memory");
  return cnt;
}
DEVINL int near_side(const char* p, int first) {
  u64 t0, t1, t2;
  unsigned d;
  asm volatile("s_memtime %0\n\ts_waitcnt lgkmcnt(0)\n\ts_load_dword %3, %4, 0x0\n\ts_waitcnt lgkmcnt(0)\n\t"
               "s_memtime %1\n\ts_waitcnt lgkmcnt(0)\n\ts_load_dword %3, %5, 0x0\n\ts_waitcnt lgkmcnt(0)\n\ts_memtime %2\n\ts_waitcnt lgkmcnt(0)"
               : "=&s"(t0), "=&s"(t1), "=&s"(t2), "=&s"(d) : "s"(p + first * 8192), "s"(p + (first ^ 1) * 8192) : "memory");
  return first ^ ((unsigned)t2 - (unsigned)t1 < (unsigned)t1 - (unsigned)t0);
}

template <int N> DEVINL void flag_peek(u64& v, const char* ws, unsigned off) {
  asm volatile("s_bfm_b64 exec, %3, 0\n\ts_nop 1\n\tglobal_load_dwordx2 %0, %1, %2 sc1\n\ts_mov_b64 exec, -1" : "=v"(v) : "v"(off), "s"(ws + OFF_FLAG), "n"(N) : "memory");
}
template <int N> DEVINL void flag_check(u64& v, const char* ws, unsigned off, u64 tag) {
  unsigned cnt;
  asm volatile("s_bfm_b64 exec, %5, 0\n\ts_mov_b32 %1, 40000\n"
               "1:\n\tv_cmp_ne_u64 vcc, %4, %0\n\ts_nop 1\n\ts_cbranch_vccz 2f\n\t"
               "s_mov_b64 exec, vcc\n\ts_sub_u32 %1, %1, 1\n\ts_cbranch_scc1 2f\n\tglobal_load_dwordx2 %0, %2, %3 sc1\n\ts_waitcnt vmcnt(0)\n\ts_branch 1b\n"
               "2:\n\ts_mov_b64 exec, -1"
               : "+v"(v), "=&s"(cnt) : "v"(off), "s"(ws + OFF_FLAG), "s"(tag), "n"(N) : "vcc", "scc", "memory");
}

constexpr float GC = 0.468668693f, GK = 4.91222954f;
DEVINL u32x4 gelu_pack8(const f32x4& a, const f32x4& b) {
  float y[8] = {a[0], a[1], a[2], a[3], b[0], b[1], b[2], b[3]}, t[8];
#pragma unroll
  for (int i = 0; i < 8; ++i) { t[i] = __builtin_fmaf(y[i], y[i], GK); asm("" : "+v"(t[i])); }
  SB0();
#pragma unroll
  for (int i = 0; i < 8; ++i) { t[i] *= y[i]; asm("" : "+v"(t[i])); }
  SB0();
#pragma unroll
  for (int i = 0; i < 8; ++i) t[i] = __builtin_amdgcn_exp2f(-t[i]);
  SB0();
#pragma unroll
  for (int i = 0; i < 8; ++i) { t[i] = __builtin_fmaf(t[i], GC, GC); asm("" : "+v"(t[i])); }
  SB0();
#pragma unroll
  for (int i = 0; i < 8; ++i) t[i] = __builtin_amdgcn_rcpf(t[i]);
  SB0();
#pragma unroll
  for (int i = 0; i < 8; ++i) { t[i] *= y[i]; asm("" : "+v"(t[i])); }
  SB0();
  return (u32x4){pack_bf16(t[0], t[1]), pack_bf16(t[2], t[3]), pack_bf16(t[4], t[5]), pack_bf16(t[6], t[7])};
}

#define X_PACE 16
#define WL_PACE 32
#define XB_SLEEP 0
#define XA_SLEEP 0
template <int RS, size_t XG>
DEVINL void fwd_unit(const char* la, const u32x4 (&F)[8], char* gd, unsigned lo) {
  bf16x8 A[2][2];
#pragma unroll
  for (int i = 0; i < 2; ++i)
#pragma unroll
    for (int ks = 0; ks < 2; ++ks) A[i][ks] = tr_frag(la + 8 * i + 32 * RS * ks, 4 * RS);
#pragma unroll
  for (int nt = 0; nt < 4; ++nt) {
    f32x4 c0 = {0, 0, 0, 0}, c1 = {0, 0, 0, 0};
#pragma unroll
    for (int ks = 0; ks < 2; ++ks) { c0 = MFMAU(A[0][ks], F[2 * nt + ks], c0); c1 = MFMAU(A[1][ks], F[2 * nt + ks], c1); }
    if constexpr (X_PACE < 16) wait_vm<X_PACE - 1>();
    st16(gd + (nt & 1) * 8 * XG + (lo ^ ((nt >> 1) << 13)), pack8(c0, c1));
  }
}
template <size_t XG> DEVINL unsigned x_lane_off(int lane, int tl, int row, unsigned pb) {
  const unsigned go = ((lane & 15) >> 1) * XG;
  return go + (((go >> 8) & 8192) ^ pb) + (row >> 6) * 16384 + (row & 63) * 128 + ((((lane & 1) * 4 + (lane >> 4)) ^ ((tl >> 1) & 7)) * 16);
}

DEVINL void w_issue(const u16* __restrict__ wsrc, char* reg, int q, int w, int lane, bool live) {
  const int co = q >> 2, cb = q & 3, par = co >> 5, c0 = (co & 31) * 8;
  __amdgpu_buffer_rsrc_t rs = mkrsrc(wsrc, 31 * 65536 * 2);
#pragma unroll
  for (int i = 0; i < 4; ++i) {
    const int cl = 2 * w + (i >> 1), tap = 8 * (i & 1) + (lane >> 3);
    const unsigned off = ((2 * tap + par) * 256 + c0 + cl) * 512 + cb * 128 + (((lane & 7) ^ (2 * (((tap >> 1) & 1) + 2 * (i & 1)))) * 16);
    dma16<0>(rs, reg + (w * 4 + i) * 1024, live ? off : OOB, 0);
  }
}
template <int PACE>
DEVINL void w_compute(const u32x4 (&Tm)[4], char* __restrict__ wf, const char* reg, int q, int w, int lane, unsigned pb) {
  const int co = q >> 2, cb = q & 3;
  const int G = lane >> 4, rq = (lane & 15) >> 2, p = lane & 3;
  char* dst = wf + (cb * 4 + w) * 65536 + (co >> 4) * 16384 + ((co >> 2) & 3) * 2048 + (co & 3) * 256 + (unsigned)((lane & 15) * 16 + 4 * G * WF_G);
  const char* la = reg + (8 * (G & 1) + rq) * 128 + (((2 * w + (p >> 1)) ^ (2 * ((rq >> 1) + 2 * (G & 1)))) * 16) + 8 * (p & 1);
  bf16x8 B[8];
#pragma unroll
  for (int cl = 0; cl < 8; ++cl) B[cl] = tr_frag(la + cl * 2048, 4 * 128);
#pragma unroll
  for (int mt = 0; mt < 4; ++mt) {
    f32x4 d[8];
#pragma unroll
    for (int cl = 0; cl < 8; ++cl) d[cl] = MFMAU(Tm[mt], B[cl], ((f32x4){0, 0, 0, 0}));
#pragma unroll
    for (int r = 0; r < 4; ++r) {
      u32x4 o = {pack_bf16(d[0][r], d[1][r]), pack_bf16(d[2][r], d[3][r]), pack_bf16(d[4][r], d[5][r]), pack_bf16(d[6][r], d[7][r])};
      if constexpr (PACE < 16) wait_vm<PACE>();
      st16(dst + (pb ^ (((mt & 1) ^ (r >> 1)) << 13)) + r * WF_G + (mt >> 1) * 1024, o);
    }
  }
}

constexpr float LOG2E2 = 2.0f * 1.4426950408889634f;
DEVINL void s_transpose(const u16* __restrict__ src, char* __restrict__ dst, char* lds, int ks, int half, int lane) {
  __amdgpu_buffer_rsrc_t rs = mkrsrc(src, 131072);
#pragma unroll
  for (int i = 0; i < 8; ++i) {
    const int row = 4 * i + (lane >> 4);
    dma16<0>(rs, lds + i * 1024, (ks * 32 + row) * 512 + half * 256 + (((lane & 15) ^ (4 * (row & 3))) * 16), 0);
  }
  wait_vm<0>();
  const int G = lane >> 4, rq = (lane & 15) >> 2, p = lane & 3;
#pragma unroll
  for (int ml = 0; ml < 8; ++ml) {
    const char* la = lds + (8 * G + rq) * 256 + (((4 * (ml >> 1) + p) ^ (4 * (rq & 3))) * 16) + 8 * (ml & 1);
    st16(dst + ((size_t)((8 * half + ml) * 8 + ks) * 64 + lane) * 16, __builtin_bit_cast(u32x4, tr_frag(la, 4 * 256)));
  }
}
DEVINL void s_ge(const u16* __restrict__ g, const u16* __restrict__ wg, float* __restrict__ ge, char* lds, int eb, int lane) {
  __amdgpu_buffer_rsrc_t rs = mkrsrc(wg, 131072);
#pragma unroll
  for (int i = 0; i < 8; ++i) dma16<0>(rs, lds + i * 1024, (i * 32 + (lane >> 1)) * 512 + eb * 32 + (lane & 1) * 16, 0);
  bf16x8 gb[8];
#pragma unroll
  for (int ks = 0; ks < 8; ++ks) gb[ks] = *(const bf16x8*)(g + (lane & 15) * 256 + ks * 32 + (lane >> 4) * 8);
  wait_vm<0>();
  const int G = lane >> 4, rq = (lane & 15) >> 2, p = lane & 3;
  f32x4 acc = {0, 0, 0, 0};
#pragma unroll
  for (int ks = 0; ks < 8; ++ks) acc = MFMA16(tr_frag(lds + (ks * 32 + 8 * G + rq) * 32 + 8 * p, 4 * 32), gb[ks], acc);
  acc *= LOG2E2;
  f32x4 ex;
#pragma unroll
  for (int r = 0; r < 4; ++r) ex[r] = __builtin_amdgcn_exp2f(acc[r] > 64.f ? 64.f : (acc[r] < -64.f ? -64.f : acc[r]));
  const int b = (lane >> 3) & 1, u = lane & 7;
  float* d = ge + ((b * 4 + (eb >> 2)) * 8 + u) * 64 + (eb & 3) * 16 + 4 * G;
  st16(d, __builtin_bit_cast(u32x4, acc));
  st16(d + 4096, __builtin_bit_cast(u32x4, ex));
}

#define G1_K 13
#define G1_P 19
#define G1_K2 23
#define GORD 0
#define GZ 0
#define GX 0
#define SPREAD 1
#define G1_WR 4
#define WPRE 16
#define PRO1 7
#define PRO2 7
#define RAMP 2
constexpr int RING = 7, SLOT = 11264, RING_DUMMY = RING * SLOT;
template <int PH> struct GPlan {
  static constexpr int NST = PH == 1 ? 32 : 8, PRO = PH == 1 ? PRO1 : PRO2;
  int first[NST + 1];
  int wfirst[NST + 1];
  int before[NST];
  int pos[NST];
  int posF, posF2, posY;
  constexpr GPlan() : first{}, wfirst{}, before{}, pos{}, posF(0), posF2(0), posY(0) {
    int n = 0, u = 0, k = 0;
    for (; u < PRO; ++u) { pos[u] = (n += 3); n += w1(u); }
    for (int t = 0; t < NST; ++t) {
      before[t] = n;
      first[t] = u;
      wfirst[t] = k;
      if (PH == 1 && t == G1_K - 2) posF = ++n;
      if (PH == 1 && t == G1_K2 - 2) posF2 = ++n;
      if (PH == 1 && t == 16) n += 6;
      for (; u <= t + RING - 1 && u < NST && (PH != 1 || u < 16 || t >= G1_K) && (u >= 16 || u < first[t] + RAMP); ++u) { pos[u] = (n += 3); n += w1(u); }
      if (PH == 1 && t == 16) posY = n;
      for (int i = 0; i < G1_WR && PH == 1 && t >= G1_K2 && k < 2 * (t - 16); ++i) { ++k; ++n; }
      for (; PH == 3 && k < 4 * t; ++k) ++n;
    }
    first[NST] = u;
    wfirst[NST] = k;
  }
  static constexpr int w1(int u) { return PH == 1 && u + WPRE < 16 ? 2 : 0; }
  constexpr int nx(int t) const { int n = 0; for (int u = first[t]; u < first[t + 1]; ++u) n += 3 + w1(u); return n; }
  constexpr int nreq(int t) const { return nx(t) + wfirst[t + 1] - wfirst[t]; }
  constexpr int xreq(int t, int j) const { int u = first[t]; while (j >= 3 + w1(u)) { j -= 3 + w1(u); ++u; } return 5 * u + j; }
  constexpr int slot(int t, int j) const {
    const int n = nreq(t);
    if (!SPREAD) return -1;
    if (n <= 6) return 4 * (j * 6 / n) + 1;
    if (n <= 12) return 2 * (j * 12 / n);
    return j * 24 / n;
  }
  constexpr int wait(int t) const {
    int need = pos[t + 1];
    if (PH == 1 && t == G1_K && posF > need) need = posF;
    if (PH == 1 && t == G1_K2 && posF2 > need) need = posF2;
    if (PH == 1 && t == G1_P && posY > need) need = posY;
    return before[t] - need;
  }
};
template <int PH>
DEVINL void g_phase(char* lds, char* ws, const char* xsrc, const char* wsrc1, const char* wsrc, u32x4 (&W)[32], char* ydst, unsigned yoff,
                    int fbase, int fbase2, int yflag, u64 tag, int rot, int w, stv_t& stv) {
  const int lane = lane_id();
  constexpr GPlan<PH> PL;
  constexpr int NST = PL.NST, NACC = PH == 1 ? 6 : 3, ROWS = PH == 1 ? 84 : 42;
  __amdgpu_buffer_rsrc_t xrs = mkrsrc(xsrc, PH == 1 ? X1_S + 16 * XST : 8 * XST);
  __amdgpu_buffer_rsrc_t yrs = mkrsrc(ydst, PH == 1 ? 2 * Y1_S : Y2_S);
  unsigned xo[3];
  int xd[3];
#pragma unroll
  for (int i = 0; i < 3; ++i) {
    const int piece = i * 4 + w, row = piece * 8 + (lane >> 3);
    xo[i] = row < 84 ? piece * 1024 + (i == 2 ? 8192 : 0) + lane * 16 : OOB;
    xd[i] = piece * 1024;
  }
  const unsigned lane16 = lane * 16;
  const bool dummy = (w == 3);
  unsigned xq[4];
#pragma unroll
  for (int i = 0; i < 4; ++i) xq[i] = ((i + rot) & 3) * ((PH == 1 ? 4 : 2) * XST);
  auto piece = [&](auto jc) {
    constexpr int u = decltype(jc)::v / 3, i = decltype(jc)::v % 3, slot = u % RING;
    constexpr int Q4 = PH == 1 ? 4 : 2;
    const unsigned so = xq[(u / Q4) & 3] + (u >= 16 ? X1_S : 0) + (u % Q4) * XST;
    dma16<SC1>(xrs, lds + (i == 2 && dummy ? RING_DUMMY : slot * SLOT + xd[i]), xo[i], so);
  };
  sgpr_settle();
  static_for<PL.PRO>([&](auto uc) {
    constexpr int u = decltype(uc)::v;
    static_for<3>([&](auto ic) { piece(IC<3 * u + decltype(ic)::v>{}); });
    static_for<PL.w1(u)>([&](auto ic) { constexpr int i = 2 * (u + WPRE) + decltype(ic)::v; ldfrag_rot<i>(W[i], wsrc1, rot, lane16); });
  });
  const int xa = (lane & 15) * 128 + (((lane >> 4) ^ ((lane & 15) >> 1)) * 16);
  int xb[4] = {xa, xa ^ 64, xa + 4 * SLOT, (xa ^ 64) + 4 * SLOT};
  asm volatile("" : "+v"(xb[2]), "+v"(xb[3]));
  const int za = (lane & 8) ? (xa & 255) + 512 + ((lane & 2) << 7) : xa;
  int xz[4] = {za, za ^ 64, za + 4 * SLOT, (za ^ 64) + 4 * SLOT};
  if (GZ) asm volatile("" : "+v"(xz[2]), "+v"(xz[3]));
  u32x4 xc[6], xs[6];
  auto ldx = [&](int pi, int slot) {
    const int ro = (PH == 1 ? pi * 16 : (pi / 3) * 42 + (pi % 3) * 16) * 128 + (slot & 3) * SLOT;
    if (GZ && PH == 1 && pi == 5) {
      xc[pi] = *(const u32x4*)(lds + xz[(slot >> 2) * 2] + ro);
      xs[pi] = *(const u32x4*)(lds + xz[(slot >> 2) * 2 + 1] + ro);
    } else {
      xc[pi] = *(const u32x4*)(lds + xb[(slot >> 2) * 2] + ro);
      xs[pi] = *(const u32x4*)(lds + xb[(slot >> 2) * 2 + 1] + ro);
    }
  };
  f32x4 R[NACC], Q[NACC];
  auto sty = [&](int n, unsigned base) {
    const int l = lane_id();
    const int tl = n * 16 + (l & 15);
    unsigned off = (tl >> 1) * 65536 + base + (tl & 1) * 64 + (l >> 4) * 16;
    off ^= (off >> 8) & 8192;
    __builtin_amdgcn_raw_buffer_store_b128(pack8(R[n], Q[n]), yrs, tl < ROWS ? off : OOB, 0, SC1);
  };
  wait_vm<PL.before[0] - PL.pos[0]>();
  static_for<PH == 1 ? 2 * WPRE : 32>([&](auto ic) { tie(W[decltype(ic)::v]); });
  bar();
  STAMP(PH == 1 ? 5 : (PH == 2 ? 14 : 18));
#pragma unroll
  for (int pi = 0; pi < 6; ++pi) ldx(pi, 0);
  u64 fv;
  unsigned foff;
  static_for<NST>([&](auto ic) {
    constexpr int st = decltype(ic)::v;
    constexpr bool MORE = st + 1 < NST;
    if constexpr (MORE) {
      wait_vm<PL.wait(st)>();
      if constexpr (PH == 1 && st + 1 >= WPRE && st + 1 < 16) { tie(W[2 * st + 2]); tie(W[2 * st + 3]); }
      if constexpr (PH == 1 && st == G1_K) { STAMP(8); flag_check<32>(fv, ws, foff, tag); }
      if constexpr (PH == 1 && st == G1_K2) { STAMP(21); flag_check<16>(fv, ws, foff, tag); STAMP(22); }
      if constexpr (!(GX & 1) && !((GX & 8) && (st & 1))) bar();
      if constexpr (PH == 1 && st == G1_K) STAMP(9);
      if constexpr (PH == 1 && st == 16) STAMP(6);
      if constexpr (PH == 1 && st == G1_P) { PRIO(PRIO_F); set_flag(ws, yflag, tag, w); PRIO(PRIO_G1); STAMP(7); }
      SB0();
      if constexpr (PH == 1 && st == G1_K - 2) { foff = (fbase + lane_id()) * 128; flag_peek<32>(fv, ws, foff); }
      if constexpr (PH == 1 && st == G1_K2 - 2) { foff = (fbase2 + lane_id()) * 128; flag_peek<16>(fv, ws, foff); }
    }
    auto req = [&](auto mc) {
      static_for<PL.nreq(st)>([&](auto jc) {
        constexpr int j = decltype(jc)::v, nx = PL.nx(st), xu = j < nx ? PL.xreq(st, j) / 5 : 0, xi = j < nx ? PL.xreq(st, j) % 5 : 0;
        if constexpr (PL.slot(st, j) == decltype(mc)::v && !(GX & 2)) {
          SB0();
          if constexpr (j < nx && xi < 3) piece(IC<3 * xu + xi>{});
          else if constexpr (j < nx) ldfrag_rot<2 * (xu + WPRE) + xi - 3>(W[2 * (xu + WPRE) + xi - 3], wsrc1, rot, lane16);
          else ldfrag_rot<PL.wfirst[st] + j - nx, PH == 1 ? 16384 : 8192>(W[PL.wfirst[st] + j - nx], wsrc, PH == 1 ? rot : 0, lane16);
          SB0();
        }
      });
    };
    req(IC<-1>{});
    u32x4 nw;
    constexpr f32x4 Z = {0, 0, 0, 0};
#define GM(a, b, c) MFMAU(a, b, c)
    if constexpr (GORD == 1 && PH == 1) {
      constexpr int ks = st & 15;
      static_for<24>([&](auto mc) {
        constexpr int m = decltype(mc)::v, n = m < 12 ? m >> 1 : (m - 12) % 6;
        if constexpr (st == 16 && m < 12 && !(m & 1)) sty(n, yoff);
        if constexpr (m < 12 && !(m & 1)) R[n] = GM(W[2 * ks], xc[n], ks == 0 ? Z : R[n]);
        else if constexpr (m < 12) Q[n] = GM(W[2 * ks], xs[n], ks == 0 ? Z : Q[n]);
        else if constexpr (m < 18) R[n] = GM(W[2 * ks + 1], xs[n], R[n]);
        else Q[n] = GM(nw, xc[n], Q[n]);
        if constexpr (m == 0) { tie(W[2 * ks + 1]); nw = W[2 * ks + 1] ^ 0x80008000u; }
        req(mc);
        if constexpr (MORE && m >= 12) {
          constexpr int slot = (st + 1) % RING, ro = n * 2048 + (slot & 3) * SLOT;
          if constexpr (m < 18) xs[n] = *(const u32x4*)(lds + xb[(slot >> 2) * 2 + 1] + ro);
          else xc[n] = *(const u32x4*)(lds + xb[(slot >> 2) * 2] + ro);
        }
        SB0();
      });
    } else
    static_for<6>([&](auto pc) {
      constexpr int pi = decltype(pc)::v, ks = PH == 1 ? st & 15 : 2 * st + pi / 3, n = PH == 1 ? pi : pi % 3;
      if constexpr (PH == 1 && st == 16) sty(n, yoff);
      if constexpr (GORD == 2) {
        if constexpr (n == 0) { tie(W[2 * ks + 1]); }
        R[n] = GM(W[2 * ks + 1], xs[pi], ks == 0 ? Z : R[n]);
        if constexpr (n == 0) { nw = W[2 * ks + 1] ^ 0x80008000u; }
        req(IC<4 * pi>{});
        Q[n] = GM(W[2 * ks], xs[pi], ks == 0 ? Z : Q[n]);
        req(IC<4 * pi + 1>{});
        R[n] = GM(W[2 * ks], xc[pi], R[n]);
        req(IC<4 * pi + 2>{});
        Q[n] = GM(nw, xc[pi], Q[n]);
        req(IC<4 * pi + 3>{});
      } else {
      R[n] = GM(W[2 * ks], xc[pi], ks == 0 ? Z : R[n]);
      if constexpr (n == 0) { tie(W[2 * ks + 1]); nw = W[2 * ks + 1] ^ 0x80008000u; }
      req(IC<4 * pi>{});
      Q[n] = GM(W[2 * ks], xs[pi], ks == 0 ? Z : Q[n]);
      req(IC<4 * pi + 1>{});
      R[n] = GM(W[2 * ks + 1], xs[pi], R[n]);
      req(IC<4 * pi + 2>{});
      Q[n] = GM(nw, xc[pi], Q[n]);
      req(IC<4 * pi + 3>{});
      }
      if constexpr (MORE && !(GX & 4)) ldx(pi, (st + 1) % RING);
      SB0();
    });
  });
  STAMP(PH == 1 ? 10 : (PH == 2 ? 15 : 19));
#pragma unroll
  for (int n = 0; n < NACC; ++n) sty(n, PH == 1 ? yoff + Y1_S : yoff);
  if constexpr (PH != 2) static_for<32 - PL.wfirst[NST]>([&](auto jc) { constexpr int i = PL.wfirst[NST] + decltype(jc)::v; ldfrag_rot<i, PH == 1 ? 16384 : 8192>(W[i], wsrc, PH == 1 ? rot : 0, lane16); });
}

DEVINL float jval(float r) {
  float t = __builtin_fmaf(-2.0f, r, 1.0f);
  asm("" : "+v"(t));
  return t;
}
template <bool FAST> DEVINL float jden(float f, float g) {
  float t = FAST ? __builtin_fmaf(f, g, 1.0f) : 1.0f + __builtin_amdgcn_exp2f(f + g);
  asm("" : "+v"(t));
  return t;
}
#define OUT_POL "sc1"
DEVINL void st_out(void* p, u32x4 v) { asm volatile("global_store_dwordx4 %0, %1, off " OUT_POL "\n\ts_nop 1" ::"v"(p), "v"(v) : "memory"); }
DEVINL void out_rows(char* orow, const u32x4& P0, const u32x4& P1) {
  u32x4 s1, s2;
#pragma unroll
  for (int i = 0; i < 4; ++i) {
    s1[i] = __builtin_amdgcn_update_dpp(P0[i], P1[i], 0x128, 0xf, 0xc, false);
    s2[i] = __builtin_amdgcn_update_dpp(P1[i], P0[i], 0x128, 0xf, 0x3, false);
  }
  st_out(orow, s1);
  st_out(orow + 8 * 4096, s2);
}
constexpr int J_Y = 32768, J_F = 65536, J_G = J_F + 8192;
constexpr int J_AH = 2;
template <bool FAST> DEVINL void j_make(const f32x4 (&fe)[4], char* lds, int u, int w, int lane) {
  const float* gl = (const float*)(lds + J_G + w * 2048) + 8 * (lane >> 4) + u * 64;
#pragma unroll
  for (int h = 0; h < 2; ++h) {
    const f32x4 g0 = *(const f32x4*)(gl + h * 32), g1 = *(const f32x4*)(gl + h * 32 + 4);
    f32x4 t0, t1;
#pragma unroll
    for (int r = 0; r < 4; ++r) {
      t0[r] = jval(__builtin_amdgcn_rcpf(jden<FAST>(fe[2 * h][r], g0[r])));
      t1[r] = jval(__builtin_amdgcn_rcpf(jden<FAST>(fe[2 * h + 1][r], g1[r])));
    }
    *(u32x4*)(lds + u * 8192 + (2 * w + h) * 1024 + lane * 16) = pack8(t0, t1);
  }
}
DEVINL void j_loop(u32x4 (&woa)[32], const f32x4 (&fe)[4], char* lds, int jw, int jstep, char* orow, int w, int lane, stv_t& stv) {
  constexpr int AH = J_AH;
  constexpr bool FAST = true;
  const float* gl = (const float*)(lds + J_G + w * 2048) + 8 * (lane >> 4);
  const char* jr = lds + lane * 16;
  f32x4 g[4];
  g[0] = *(const f32x4*)(gl + AH * 64); g[1] = *(const f32x4*)(gl + AH * 64 + 4);
  wait_lds();
  bar();
  STAMP(28);
  u32x4 B[8];
#pragma unroll
  for (int ks = 0; ks < 8; ++ks) B[ks] = *(const u32x4*)(jr + ks * 1024);
  f32x4 acc[4];
  u32x4 P0;
  static_for<8>([&](auto ic) {
    constexpr int u = decltype(ic)::v;
    constexpr bool FIRST = (u == 0), RD = (u + 1 < 8), J = (u + AH < 8);
    SB0();
    float d[16];
    unsigned jo[8];
    static_for<32>([&](auto qc) {
      constexpr int q = decltype(qc)::v, hf = q >> 4, ks = (q >> 1) & 7, m = 2 * hf + (q & 1);
      if constexpr (FIRST && q == 0) { wait_vm<16>(); static_for<16>([&](auto c) { tie(woa[decltype(c)::v]); }); }
      if constexpr (FIRST && q == 16) { wait_vm<0>(); static_for<16>([&](auto c) { tie(woa[16 + decltype(c)::v]); }); }
      if constexpr (RD && u >= 1 && q == 14) bar();
      if constexpr (ks == 0) acc[m] = MFMAU(woa[m * 8 + ks], B[ks], ((f32x4){0, 0, 0, 0}));
      else acc[m] = MFMAU(woa[m * 8 + ks], B[ks], acc[m]);
      if constexpr (RD && q >= 18 && !(q & 1)) B[ks - 1] = *(const u32x4*)(jr + (u + 1) * 8192 + (ks - 1) * 1024);
      if constexpr (!FIRST && q == 3) out_rows(orow + (u - 1) * 512, P0, pack8(acc[2], acc[3]));
      if constexpr (q == 19) P0 = pack8(acc[0], acc[1]);
      if constexpr (J) {
        if constexpr (q == 2) { g[2] = *(const f32x4*)(gl + (u + AH) * 64 + 32); g[3] = *(const f32x4*)(gl + (u + AH) * 64 + 36); }
        if constexpr (q >= 5 && q < 21) { constexpr int e = q - 5; d[e] = jden<FAST>(fe[e >> 2][e & 3], g[e >> 2][e & 3]); }
        if constexpr (q >= 7 && q < 23) { constexpr int e = q - 7; d[e] = __builtin_amdgcn_rcpf(d[e]); }
        if constexpr (q >= 9 && q < 25) { constexpr int e = q - 9; d[e] = jval(d[e]); }
        if constexpr (q >= 12 && q < 28 && !(q & 1)) { constexpr int e = q - 11; jo[e >> 1] = pack_bf16(d[e - 1], d[e]); }
        if constexpr (q == 19) *(u32x4*)(lds + jw) = (u32x4){jo[0], jo[1], jo[2], jo[3]};
        if constexpr (q == 27) { *(u32x4*)(lds + jw + 1024) = (u32x4){jo[4], jo[5], jo[6], jo[7]}; jw += jstep; asm("" : "+v"(jw)); }
        if constexpr (u + AH + 1 < 8 && q == 29) { g[0] = *(const f32x4*)(gl + (u + AH + 1) * 64); g[1] = *(const f32x4*)(gl + (u + AH + 1) * 64 + 4); }
      }
      SB0();
    });
    if constexpr (RD) B[7] = *(const u32x4*)(jr + (u + 1) * 8192 + 7 * 1024);
    SB0();
  });
  out_rows(orow + 7 * 512, P0, pack8(acc[2], acc[3]));
}

#define HINT 0
#define TAGLATE 0
#define XLOOP 1
#define ETOUCH 4096
#define RFIRST 1
#define G_ALL 0
#define T_ALL 0
#define W1_SLEEP 32
#define ROT 1
#define W2_PACE 1
constexpr int M_HS = 96;

template <int MK = 0> DEVINL void x_issue(const u16* __restrict__ x, char* reg, int s, int tp, int par, int ch, bool live, int lane) {
  __amdgpu_buffer_rsrc_t rs = mkrsrc(x + (size_t)s * T0 * 256, T0 * 512);
  const int t0 = 96 * tp - 111 + par + 2 * (lane >> 3);
  const int base = t0 * 512 + ch * 128 + (((lane & 7) ^ (4 * ((lane >> 4) & 1))) * 16);
  unsigned o0 = (tp == 0 || t0 < 0) ? OOB : (unsigned)base, o1 = tp == 0 ? OOB : (unsigned)(base + 8192), so;
  if (live) asm volatile("s_mov_b32 m0, %4\n\ts_mov_b32 %0, 6\n\ts_nop 2\n\tbuffer_load_dwordx4 %2, %3, 0 offen lds\n"
               "1:\n\ts_add_u32 m0, m0, 1024\n\ts_sub_u32 %0, %0, 1\n\tbuffer_load_dwordx4 %1, %3, 0 offen lds\n\tv_add_u32 %1, 0x2000, %1\n\ts_cbranch_scc0 1b ;@L7\n;@%5"
               : "=&s"(so), "+v"(o1) : "v"(o0), "s"(rs), "s"((unsigned)(size_t)(lds_void_p)reg), "n"(MK) : "scc", "memory");
}
#define G_GATE 0
#define T_GATE 0
DEVINL void gate(unsigned t0, unsigned n) {
  asm volatile("1:\n\ts_sleep 2\n\ts_memrealtime vcc\n\ts_waitcnt lgkmcnt(0)\n\ts_sub_u32 vcc_lo, vcc_lo, %0\n\ts_cmp_lt_u32 vcc_lo, %1\n\ts_cbranch_scc1 1b"
               :: "s"(t0), "s"(n) : "vcc", "scc", "memory");
}
DEVINL u64 load_tag(const char* ws) {
  u64 tag;
  asm volatile("s_load_dwordx2 %0, %1, 0x0\n\ts_waitcnt lgkmcnt(0)" : "=s"(tag) : "s"(ws + OFF_FLAG + F_EPOCH * 128) : "memory");
  return tag * 6364136223846793005ull + 1442695040888963407ull;
}

extern "C" __global__ __launch_bounds__(256, 2) void k_all(const u16* __restrict__ x, const u16* __restrict__ w1, const u16* __restrict__ w2, const u16* __restrict__ g_,
                                                const u16* __restrict__ wf, const u16* __restrict__ wg, const u16* __restrict__ wo,
                                                char* __restrict__ ws_, char* __restrict__ out) {
  constexpr unsigned pb = 0;
  char* const ws = ws_ + (-(size_t)ws_ & ((2 << 20) - 1));
  __shared__ __attribute__((aligned(16))) char lds[LDS_BYTES];
  int lane = threadIdx.x & 63;
  const int w = __builtin_amdgcn_readfirstlane(threadIdx.x >> 6);
  const int role = blockIdx.x >> 8, xl = blockIdx.x & 7, idx = (blockIdx.x >> 3) & 31;
  const int q = xl * 32 + idx;
  stv_t stv = {};
  STAMP(0);
  u64 tag;

  const int eu = w == (idx & 3) ? idx : ((idx & 3) == 0 && w == 2 ? 32 + (idx >> 2) : -1);
  const int tpv[2] = {xl * 21 + (idx >> 1), xl * 21 + 16 + (eu >> 3)}, parv[2] = {idx & 1, (eu >> 2) & 1}, chv[2] = {w, eu & 3};
  u32x4 F[8];
  static_for<4>([&](auto ic) { constexpr int i = decltype(ic)::v; ldfrag<i, false>(F[i], (const char*)TAB + (role ? TAB_F : TAB_T) * 1024, lane * 16); });
  asm volatile("s_cmp_eq_u32 %6, 0\n\ts_cbranch_scc1 1f\n\t;@f\n\tglobal_load_dwordx4 %0, %4, %5\n\tglobal_load_dwordx4 %1, %4, %5 offset:1024\n\t"
               "global_load_dwordx4 %2, %4, %5 offset:2048\n\tglobal_load_dwordx4 %3, %4, %5 offset:3072\n1:"
               : "=&v"(F[4]), "=&v"(F[5]), "=&v"(F[6]), "=&v"(F[7]) : "v"(lane * 16), "s"((const char*)TAB + (TAB_F + 4) * 1024), "s"(role) : "scc", "memory");
  asm volatile("" :: "s"(x), "s"(w1), "s"(w2), "s"(g_), "s"(wf), "s"(wg), "s"(wo), "s"(ws_));
  int role2 = role;
  if (role2 == 0) {
    asm volatile(";@g");
    PRIO(PRIO_G0);
    w_issue(w1, lds, q, w, lane, true);
    if (RFIRST) w_issue(w2, lds + 16384, q, w, lane, true);
  }
  asm volatile("" : "+s"(role2));
  if (role2 != 0) {
    asm volatile(";@t");
    PRIO(PRIO_T0);
    x_issue(x, lds + w * 8192, 0, tpv[0], parv[0], chv[0], true, lane);
    x_issue<1>(x, lds + 65536 + (eu >> 5) * 8192, 0, tpv[1], parv[1], chv[1], eu >= 0, lane);
  }
  if (RFIRST) x_issue<2>(x, lds + 32768 + w * 8192, 1, tpv[role == 0], parv[role == 0], chv[role == 0], role != 0 || eu >= 0, lane);
  unsigned t_start;
  asm volatile("s_memrealtime vcc\n\ts_waitcnt lgkmcnt(0)\n\ts_mov_b32 %0, vcc_lo" : "=s"(t_start) :: "vcc", "memory");
  unsigned per;
  {
    u64 pc;
    unsigned a, b;
    asm volatile("s_getpc_b64 %0\n.Lpe:\n\ts_mov_b32 %1, .Lg_beg-.Lpe\n\ts_mov_b32 %2, .Lt_beg-.Lpe" : "=s"(pc), "=s"(a), "=s"(b));
    const unsigned o = (idx * 4 + w) * 64;
    asm volatile("s_load_dword %0, %1, %2" : "=s"(per) : "s"(pc), "s"(((role ? b : a) + (o < ETOUCH ? o : 0)) & ~3u) : "memory");
  }
  asm volatile("" : "+s"(role2));

  u32x4 W[32];
  if (__builtin_expect(role2 == 0, 1)) {
    asm volatile(".Lg_beg:");
    const unsigned lane16 = lane * 16;
    {
      const int px = (xl + 1) & 7;
      const char* probe = ws + OFF_W2F + (px & 3) * 4 * WF_G + (idx & 15) * 65536 + (px >> 1) * 16384 + (px & 1) * 4096 + (idx >> 4) * 1024;
      if (!TAGLATE) { unsigned d; asm volatile("s_load_dword %0, %1, 0x100" : "=s"(d) : "s"(probe) : "memory"); tag = load_tag(ws); asm volatile("" :: "s"(d)); }
      unsigned ct = 0;
      if (RFIRST) {
        u64 pc;
        unsigned len;
        asm volatile("s_getpc_b64 %0\n.Lpc%=:\n\ts_mov_b32 %1, .Lpc%=-k_all" : "=s"(pc), "=s"(len));
        pc -= len;
        asm volatile("s_mov_b32 %0, .Lk_end-k_all\n\ts_max_u32 %0, %0, .Lt_end-k_all" : "=s"(len) :: "scc");
        const unsigned wv = idx * 4 + w, co = wv * 512 + lane * 128;
        const char* a = (const char*)pc + (co < len ? co : 0);
        if (lane >= 4) a = (const char*)(lane == 4 ? wf : (lane == 5 ? wg : wo)) + (q * 4 + w) * 128;
        if (lane == 7) a = wv < 64 ? (const char*)g_ + wv * 128 : (const char*)TAB + TAB_G * 1024 + (wv - 64) * 128;
        asm volatile("s_mov_b64 exec, 255\n\ts_nop 1\n\tglobal_load_dword %0, %1, off\n\ts_mov_b64 exec, -1" : "=v"(ct) : "v"(a) : "memory");
        wait_vm<G_ALL ? 0 : 13>();
        if (eu < 0) { wait_vm<5>(); asm volatile(";@e"); }
      } else wait_vm<0>();
      if (RFIRST) asm volatile(";@Ng2_ge\n;@r"); else asm volatile(";@Ng\n;@r");
      wait_lds();
      asm volatile("" :: "s"(per));
      if (W1_SLEEP) __builtin_amdgcn_s_sleep(W1_SLEEP);
      if (G_GATE) gate(t_start, G_GATE);
      static_for<4>([&](auto ic) { tie(F[decltype(ic)::v]); });
      const u32x4 Tm[4] = {F[0], F[1], F[2], F[3]};
      bar();
      STAMP(1);
      w_compute<16>(Tm, ws + OFF_W1F, lds, q, w, lane, pb);
      if (TAGLATE) { unsigned d; asm volatile("s_load_dword %0, %1, 0x100" : "=s"(d) : "s"(probe) : "memory"); tag = load_tag(ws); asm volatile("" :: "s"(d)); }
      int vote = 0;
      if (w == 0) vote = (xl >> 2) ^ near_side(probe, (idx >> 2) & 1);
      publish(ws, F_W1 + (q & 3) * 64 + (q >> 2), tag ^ ((u64)vote << 63), w);
      tie(ct);
      STAMP(2);
    }
    const int TB = (idx >> 2) & 1, cb = idx & 3;
    ((int*)lds)[w] = poll_vote(ws, F_W1 + cb * 64 + w * 16 + lane, tag);
    wait_lds();
    bar();
    const u32x4 votes = *(const u32x4*)lds;
    const int grp = (xl * 4 + (idx >> 3)) ^ (__builtin_amdgcn_readfirstlane(votes[0] + votes[1] + votes[2] + votes[3]) > 32 ? 16 : 0);
    const int gflag = (TB * 4 + cb) * 32 + grp, rot = ROT ? cb + 2 * TB : 0;
    const int gl = grp & 15;
    const unsigned sb = pb ^ ((grp >> 4) << 13);
    const unsigned yoff = (gl >> 2) * 16384 + sb + (gl & 3) * 2048 + (cb * 4 + w) * 128;
    const size_t woff = gl * WF_G + (cb * 4 + w) * 65536 + (sb ^ ((gl & 2) << 12));
    const size_t x1off = gl * X1_G + TB * X1_TB + (sb ^ ((gl & 2) << 12)), x2off = gl * X2_G + TB * X2_TB + (sb ^ ((gl & 4) << 11));
    {
      STAMP(3);
      PRIO(PRIO_GW);
      static_for<2 * WPRE>([&](auto ic) {
        constexpr int i = decltype(ic)::v;
        if constexpr (i >= WL_PACE) wait_vm<WL_PACE - 1>();
        ldfrag_rot<i>(W[i], ws + OFF_W1F + woff, rot, lane16);
      });
      PRIO(PRIO_F);
      poll(ws, F_X + TB * 128 + w * 32 + lane, lanes(32), tag);
      bar();
      if (HINT) set_flag(ws, F_H + q, tag, w);
      STAMP(4);
      PRIO(PRIO_G1);
      g_phase<1>(lds, ws, ws + OFF_X1F + x1off, ws + OFF_W1F + woff, ws + OFF_W2F + woff, W, ws + OFF_Y1F, yoff + TB * 42 * 65536,
                 F_X + 256 + TB * 128 + w * 32, F_W2 + cb * 64 + w * 16, F_G1 + gflag, tag, rot, w, stv);
      publish(ws, F_G1 + 256 + gflag, tag, w);
      STAMP(11);
    }
    lane = lane_id();
    poll(ws, F_M + TB * 112 + w * 28 + lane, lanes(28), tag);
    bar();
    STAMP(13);
    PRIO(PRIO_G2A);
    g_phase<2>(lds, ws, ws + OFF_X2F + x2off, nullptr, nullptr, W, ws + OFF_Y2F, yoff + TB * 21 * 65536, 0, 0, 0, tag, rot, w, stv);
    publish(ws, F_G2 + gflag, tag, w);
    STAMP(16);
    lane = lane_id();
    poll(ws, lane >= 28 ? F_S + w * 3 + lane - 28 : F_M + 256 + TB * 112 + w * 28 + lane, lanes(31), tag);
    bar();
    STAMP(17);
    PRIO(PRIO_G2B);
    g_phase<3>(lds, ws, ws + OFF_X2F + X2_S + x2off, nullptr, ws + OFF_WFT + w * 32768, W, ws + OFF_Y2F + Y2_S, yoff + TB * 21 * 65536, 0, 0, 0,
               tag, rot, w, stv);
    publish(ws, F_G2 + 256 + gflag, tag, w);
    STAMP(20);
    PRIO(PRIO_JB);
  } else {
    asm volatile(".Lt_beg:");
    const int G = lane >> 4, rq = (lane & 15) >> 2, p = lane & 3;
    const unsigned lane16 = lane * 16;
    {
      u32x4 Tm[4];
      tag = load_tag(ws);
      asm volatile("" :: "s"(per));
      wait_vm<T_ALL ? 0 : 8 + 8 * RFIRST>();
      if (eu < 0) { wait_vm<8 * RFIRST>(); asm volatile(";@e"); }
      if (RFIRST) asm volatile(";@Nft12_ft2e\n;@r"); else asm volatile(";@Nft1_fte\n;@r");
      tie(F);
      STAMP(1);
      if (XA_SLEEP) __builtin_amdgcn_s_sleep(XA_SLEEP);
      if (T_GATE) gate(t_start, T_GATE);
#pragma unroll 1
      for (int s = 0; s < 2; ++s) {
#pragma unroll 1
        for (int r = 0; r < (eu >= 0 ? 2 : 1); ++r) {
          if (r) wait_vm<8>();
          const int tl = (r ? tpv[1] : tpv[0]) - 84 * (xl >> 2);
          char* gd = ws + OFF_X1F + s * X1_S + (xl >> 2) * X1_TB + (size_t)(r ? parv[1] * 8 + chv[1] * 2 : parv[0] * 8 + chv[0] * 2) * XST;
          const unsigned lo = x_lane_off<X1_G>(lane, tl, tl, pb);
          const char* reg = lds + (r == s ? w * 8192 : (s ? 32768 + w * 8192 : 65536 + (eu >> 5) * 8192));
#pragma unroll
          for (int h = 0; h < 2; ++h) {
            const char* la = reg + (8 * G + rq) * 128 + (((4 * h + p) ^ (4 * ((rq >> 1) & 1))) * 16);
            fwd_unit<128, X1_G>(la, F, gd + h * XST, lo);
          }
        }
        publish(ws, F_X + s * 256 + q, tag, w);
        STAMP2(2, 4);
        if (s == 0) {
          PRIO(PRIO_T1);
          x_issue(x, lds + w * 8192, 1, tpv[1], parv[1], chv[1], eu >= 0, lane);
          w_issue(w2, lds + 65536, q, w, lane, true);
          if (XB_SLEEP) __builtin_amdgcn_s_sleep(XB_SLEEP);
          if (HINT) poll(ws, F_H + q, lanes(2), tag);
          STAMP(3);
        }
      }
      PRIO(PRIO_T2);
      static_for<4>([&](auto ic) { constexpr int i = decltype(ic)::v; ldfrag<TAB_T + i, false>(Tm[i], (const char*)TAB, lane16); });
      wait_vm<0>();
      tie(Tm);
      w_compute<W2_PACE>(Tm, ws + OFF_W2F, lds + 65536, q, w, lane, pb);
      publish(ws, F_W2 + (q & 3) * 64 + (q >> 2), tag, w);
      STAMP(5);
      if (idx >= 28 && (idx - 28) * 8 + xl < 12) {
        PRIO(PRIO_T2);
        const int sid = (idx - 28) * 8 + xl, t = sid * 4 + w;
        char* reg = lds + w * 8192;
        if (t < 16) s_transpose(wf, ws + OFF_WFT, reg, t >> 1, t & 1, lane);
        else if (t < 32) s_transpose(wo, ws + OFF_WOT, reg, (t - 16) >> 1, t & 1, lane);
        else s_ge(g_, wg, (float*)(ws + OFF_GE), reg, t - 32, lane);
        publish(ws, F_S + sid, tag, w);
      }
    }
    if (idx < 28) {
      u32x4 Gt[6], F[2];
      char* reg = lds + w * 8192;
      char* him = lds + 32768;
      const int pr = 3 * idx + w;
      static_for<6>([&](auto ic) { constexpr int i = decltype(ic)::v; ldfrag<TAB_G + i, false>(Gt[i], (const char*)TAB, lane16); });
      static_for<2>([&](auto ic) { constexpr int i = decltype(ic)::v; ldfrag<i, false>(F[i], (const char*)TAB + (TAB_F + 2 * w) * 1024, lane16); });
#pragma unroll 1
      for (int s = 0; s < 2; ++s) {
        poll(ws, F_G1 + s * 256 + ((idx >= 14 || lane >= 8 ? 4 : 0) + (xl >> 1)) * 32 + w * 8 + (lane & 7), idx == 13 ? lanes(16) : lanes(8), tag);
        bar();
        if (s == 0) { PRIO(PRIO_MA); } else { PRIO(PRIO_MB); }
        tie(Gt); tie(F);
        __amdgpu_buffer_rsrc_t rs = mkrsrc(ws + OFF_Y1F + s * Y1_S, 84 * 65536);
        sgpr_settle();
#pragma unroll
        for (int i = 0; i < 8; ++i) {
          const unsigned off = pr * 65536 + (i & 3) * 16384 + (((i >> 2) << 13) ^ ((pr & 32) << 8) ^ pb) + G * 2048 + xl * 256 + (((lane & 15) ^ ((G & 1) << 2) ^ (i & 1)) * 16);
          dma16<SC1>(rs, reg + i * 1024, off, 0);
        }
        STAMP2(6, 10);
        wait_vm<0>();
        STAMP2(7, 11);
        bf16x8 A[2][2][2];
#pragma unroll
        for (int tl = 0; tl < 2; ++tl)
#pragma unroll
          for (int i = 0; i < 2; ++i)
#pragma unroll
            for (int ks = 0; ks < 2; ++ks) {
              const int ch = (((p >> 1) * 8 + tl * 4 + 2 * (p & 1) + i) ^ ((rq >> 1) << 2) ^ (G & 1));
              A[tl][i][ks] = tr_frag(reg + (16 * ks + 4 * G + (rq >> 1)) * 256 + ch * 16 + (rq & 1) * 8, 512);
            }
        f32x4 c[2][2];
        auto idft = [&](int b) {
          constexpr f32x4 Z = {0, 0, 0, 0};
#pragma unroll
          for (int i = 0; i < 2; ++i) c[b & 1][i] = MFMAU(A[b / 3][i][0], Gt[2 * (b % 3)], Z);
#pragma unroll
          for (int i = 0; i < 2; ++i) c[b & 1][i] = MFMAU(A[b / 3][i][1], Gt[2 * (b % 3) + 1], c[b & 1][i]);
        };
        idft(0);
        const unsigned keep = pr == 83 ? 0u : ~0u;
#pragma unroll
        for (int b = 0; b < 6; ++b) {
          if (b + 1 < 6) idft(b + 1);
          SB0();
          u32x4 o = gelu_pack8(c[b & 1][0], c[b & 1][1]);
          if (b == 5) o &= keep;
          *(u32x4*)(him + (w * 96 + b * 16 + (lane & 15)) * M_HS + G * 16) = o;
          SB0();
        }
        wait_lds();
        bar();
        STAMP2(8, 12);
        const int TB2 = idx >= 14;
        char* gd = ws + OFF_X2F + s * X2_S + TB2 * X2_TB + (size_t)(xl >> 1) * XST + (size_t)(w & 1) * 8 * X2_G;
        const char* la = him + (33 + 2 * (8 * G + rq)) * M_HS + p * 16;
        bf16x8 B[6][2][2];
#pragma unroll
        for (int u = 0; u < 6; ++u)
#pragma unroll
          for (int i = 0; i < 2; ++i)
#pragma unroll
            for (int ks = 0; ks < 2; ++ks) B[u][i][ks] = tr_frag(la + ((u >> 1) * 96 + (u & 1) + 64 * ks) * M_HS + 8 * i, 8 * M_HS);
#pragma unroll
        for (int u = 0; u < 6; ++u) {
          f32x4 c0 = {0, 0, 0, 0}, c1 = {0, 0, 0, 0};
#pragma unroll
          for (int ks = 0; ks < 2; ++ks) { c0 = MFMAU(B[u][0][ks], F[ks], c0); c1 = MFMAU(B[u][1][ks], F[ks], c1); }
          const int tl = 3 * idx - 42 * TB2 + (u >> 1);
          st16(gd + x_lane_off<X2_G>(lane, tl, tl + 42 * (xl & 1), pb ^ ((w >> 1) << 13)) + (u & 1) * 4 * XST, pack8(c0, c1));
        }
        publish(ws, F_M + s * 256 + idx * 8 + xl, tag, w);
        STAMP2(9, 13);
      }
    }
    poll(ws, F_S + w * 3 + lane, lanes(3), tag);
    bar();
    PRIO(PRIO_JA);
    sgpr_settle();
    static_for<32>([&](auto ic) { constexpr int i = decltype(ic)::v; ldfrag<i, true>(W[i], ws + OFF_WFT + w * 32768, lane16); });
    wait_vm<0>();
    asm volatile(".Lt_end:");
  }
  asm volatile(";@Nr");
  tie(W);
  const int s = role ^ 1;
  lane = lane_id();
  const int G = lane >> 4, rq = (lane & 15) >> 2, p = lane & 3;
  const unsigned lane16 = lane * 16;

  const int jb = q;
  if (jb >= 250) {
    if (blockIdx.x == 255 && w == 0) {
#pragma unroll 1
      for (int i = 4; i < 8; ++i) poll(ws, F_G2 + i * 64 + lane, ~0ull, tag);
      set_flag(ws, F_EPOCH, tag, w);
    }
    STAMP_FLUSH();
    return;
  }
  {
    const int tile = jb / 3, vt = jb - 3 * tile;
    u32x4 woa[32], Gt[2];
    sgpr_settle();
    static_for<2>([&](auto ic) { constexpr int i = decltype(ic)::v; ldfrag<i, false>(Gt[i], (const char*)TAB + (TAB_G + 2 * vt) * 1024, lane16); });
    __amdgpu_buffer_rsrc_t grs = mkrsrc(ws + OFF_GE, 32768);
#pragma unroll
    for (int i = 0; i < 2; ++i) dma16<SC1>(grs, lds + J_G + w * 2048 + i * 1024, 16384 + (s * 4 + w) * 2048 + i * 1024 + lane16, 0);
    static_for<16>([&](auto ic) { constexpr int i = decltype(ic)::v; ldfrag<i, true>(woa[i], ws + OFF_WOT + w * 32768, lane16); });
    PRIO(PRIO_F);
    poll(ws, F_G2 + s * 256 + ((tile >= 42 ? 4 : 0) + w) * 32 + lane, lanes(32), tag);
    if (role) { PRIO(PRIO_JA); } else { PRIO(PRIO_JB); }
    tie(Gt);
    STAMP(24);
    char* reg = lds + J_Y + w * 8192;
    {
      __amdgpu_buffer_rsrc_t rs = mkrsrc(ws + OFF_Y2F + s * Y2_S + (size_t)(tile >> 1) * 65536, 65536);
#pragma unroll
      for (int i = 0; i < 8; ++i) {
        const int c = (lane & 15) ^ ((G & 1) << 3) ^ (i & 1);
        dma16<SC1>(rs, reg + i * 1024, (i & 3) * 16384 + (((i >> 2) << 13) ^ ((tile & 64) << 7) ^ pb) + G * 2048 + (4 * w + (c >> 2)) * 128 + (tile & 1) * 64 + (c & 3) * 16, 0);
      }
    }
    wait_vm<0>();
    STAMP(25);
#pragma unroll
    for (int h = 0; h < 2; ++h) {
      f32x4 c[2];
#pragma unroll
      for (int i = 0; i < 2; ++i) {
        const int ch = (8 * h + 2 * p + i) ^ ((rq >> 1) << 3) ^ (G & 1);
        const char* la = reg + (4 * G + (rq >> 1)) * 256 + ch * 16 + (rq & 1) * 8;
        c[i] = MFMAU(tr_frag(la, 512), Gt[0], ((f32x4){0, 0, 0, 0}));
        c[i] = MFMAU(tr_frag(la + 4096, 512), Gt[1], c[i]);
      }
      *(u32x4*)(lds + J_F + (2 * w + h) * 1024 + lane16) = gelu_pack8(c[0], c[1]);
    }
    wait_lds();
    bar();
    STAMP(26);
    f32x4 fe[4];
#pragma unroll
    for (int m = 0; m < 4; ++m) fe[m] = (f32x4){0, 0, 0, 0};
#pragma unroll
    for (int ks = 0; ks < 8; ++ks) {
      const u32x4 bfr = *(const u32x4*)(lds + J_F + ks * 1024 + lane16);
#pragma unroll
      for (int m = 0; m < 4; ++m) fe[m] = MFMAU(W[m * 8 + ks], bfr, fe[m]);
    }
    SB0();
    static_for<16>([&](auto ic) { constexpr int i = 16 + decltype(ic)::v; ldfrag<i, true>(woa[i], ws + OFF_WOT + w * 32768, lane16); });
    SB0();
    bool small = true;
#pragma unroll
    for (int m = 0; m < 4; ++m) {
      fe[m] *= LOG2E2;
#pragma unroll
      for (int r = 0; r < 4; ++r) small = small && (__builtin_fabsf(fe[m][r]) <= 32.0f);
    }
    STAMP(27);
    char* orow = out + ((size_t)(s * T2 + jb * 16 + (lane & 7)) * 8) * 512 + w * 128 + ((lane >> 3) & 1) * 64 + G * 16;
    const bool fast = __all(small);
    if (fast) {
#pragma unroll
      for (int m = 0; m < 4; ++m)
#pragma unroll
        for (int r = 0; r < 4; ++r) fe[m][r] = __builtin_amdgcn_exp2f(fe[m][r]);
#pragma unroll
      for (int u = 0; u < J_AH; ++u) j_make<true>(fe, lds, u, w, lane);
    } else {
#pragma unroll
      for (int i = 0; i < 2; ++i) dma16<SC1>(grs, lds + J_G + w * 2048 + i * 1024, (s * 4 + w) * 2048 + i * 1024 + lane16, 0);
      wait_vm<0>();
#pragma unroll 1
      for (int u = 0; u < 8; ++u) j_make<false>(fe, lds, u, w, lane);
    }
    j_loop(woa, fe, lds, (fast ? J_AH * 8192 : J_F) + 2 * w * 1024 + lane16, fast ? 8192 : 0, orow, w, lane, stv);
  }
  asm volatile(".Lk_end:");
  STAMP(29);
}

#define WS_SKIP 0
extern "C" void kernel_launch(void* const* d_in, const int* in_sizes, int n_in, void* d_out, int out_size, void* d_ws,
                              size_t ws_size, hipStream_t stream) {
  k_all<<<512, 256, 0, stream>>>((const u16*)d_in[0], (const u16*)d_in[1], (const u16*)d_in[2], (const u16*)d_in[3], (const u16*)d_in[4],
                                 (const u16*)d_in[5], (const u16*)d_in[6], (char*)d_ws + WS_SKIP, (char*)d_out);
}
```

```cpp
#include <hip/hip_runtime.h>
#include <hip/hip_bf16.h>
#include <utility>
__device__ const unsigned TAB[4608] __attribute__((aligned(128))) = {
0x3f803f80,0x3f803f80,0x3f803f80,0x3f803f80,0x0,0x0,0x0,0x0,
0x3f7f3f80,0x3f753f7b,0x3f613f6c,0x3f443f54,0x3dcc0000,0x3e973e4b,0x3ef53ec7,0x3f253f10,
0x3f7b3f80,0x3f543f6c,0x3f0b3f33,0x3e323ebb,0x3e4b0000,0x3f103ec7,0x3f573f37,0x3f7c3f6e,
0x3f753f80,0x3f203f54,0x3d993ebb,0xbf00be64,0x3e970000,0x3f483f10,0x3f7f3f6e,0x3f5e3f7a,
0x3f6c3f80,0x3ebb3f33,0xbed3bccc,0xbf71bf3c,0x3ec70000,0x3f6e3f37,0x3f693f80,0x3eaf3f2e,
0x3f613f80,0x3d993f0b,0xbf4cbed3,0xbf71bf7d,0x3ef50000,0x3f7f3f57,0x3f1b3f69,0xbeaf3e19,
0x3f543f80,0xbe643ebb,0xbf7dbf3c,0xbf00bf67,0x3f100000,0x3f7a3f6e,0x3e193f2e,0xbf5ebede,
0x3f443f80,0xbf003e32,0xbf71bf71,0x3e32bf00,0x3f250000,0x3f5e3f7c,0xbeaf3eaf,0xbf7cbf5e,
0x3f803f80,0x3f803f80,0x3f803f80,0x3f803f80,0x0,0x0,0x0,0x0,
0x3f203f33,0x3eea3f0b,0x3e8b3ebb,0x3d993e32,0x3f483f37,0x3f643f57,0x3f763f6e,0x3f7f3f7c,
0xbe64bccc,0xbf15bed3,0xbf5abf3c,0xbf7dbf71,0x3f7a3f80,0x3f503f69,0x3f053f2e,0x3e193eaf,
0xbf67bf3c,0xbf7dbf7d,0xbf3cbf67,0xbe64bf00,0x3ede3f2e,0xbe193e19,0xbf2ebede,0xbf7abf5e,
0xbf67bf80,0xbea3bf29,0x3eea3d99,0x3f753f44,0xbedebd4c,0xbf73bf40,0xbf64bf7f,0xbe97bf25,
0xbe64bf29,0x3f333e8b,0x3f7b3f75,0x3ebb3f44,0xbf7abf40,0xbf37bf76,0x3e4bbe97,0x3f6e3f25,
0x3f203d99,0x3f753f75,0x3d993f20,0xbf67bf00,0xbf48bf7f,0x3e97be97,0x3f7f3f48,0x3ede3f5e,
0x3f803f44,0x3e323f44,0xbf71bf00,0xbf00bf71,0xbf25,0x3f7c3f25,0x3eaf3f5e,0xbf5ebeaf,
0x3f803f80,0x3f803f80,0x3f803f80,0x3f803f80,0x0,0x0,0x0,0x0,
0xbdffbccc,0xbea3be64,0xbf00bed3,0xbf29bf15,0x3f7e3f80,0x3f733f7a,0x3f5e3f69,0x3f403f50,
0xbf78bf80,0xbf4cbf67,0xbf00bf29,0xbdffbea3,0xbe7dbd4c,0xbf1bbede,0xbf5ebf40,0xbf7ebf73,
0x3ebb3d99,0x3f543f20,0x3f803f75,0x3f543f75,0xbf6ebf7f,0xbf10bf48,0xbe97,0x3f103e97,
0x3f613f7f,0x3e8b3f20,0xbf00bdff,0xbf78bf4c,0x3ef53dcc,0x3f763f48,0x3f5e3f7e,0x3e7d3f1b,
0xbf15bdff,0xbf80bf67,0xbf00bf5a,0x3eeabccc,0x3f503f7e,0xbd4c3ede,0xbf5ebf05,0xbf64bf80,
0xbf3cbf7d,0x3ebbbe64,0x3f803f54,0x3ebb3f54,0xbf2ebe19,0xbf6ebf7a,0xbf10,0x3f6e3f10,
0x3f443e32,0x3f443f80,0xbf003e32,0xbf71bf71,0xbf25bf7c,0x3f250000,0x3f5e3f7c,0xbeaf3eaf,
0x3f803f80,0x3f803f80,0x3f803f80,0x3f803f80,0x0,0x0,0x0,0x0,
0xbf4cbf3c,0xbf67bf5a,0xbf78bf71,0xbf80bf7d,0x3f1b3f2e,0x3ede3f05,0x3e7d3eaf,0x3d4c3e19,
0x3e8b3d99,0x3f203eea,0x3f613f44,0x3f7f3f75,0xbf76bf7f,0xbf48bf64,0xbef5bf25,0xbdccbe97,
0x3ebb3f20,0xbe643d99,0xbf3cbf00,0xbf7dbf67,0x3f6e3f48,0x3f7a3f7f,0x3f2e3f5e,0x3e193ede,
0xbf5abf7d,0xbe64bf15,0x3f0b3e32,0x3f7b3f54,0xbf05be19,0xbf7abf50,0xbf57bf7c,0xbe4bbf10,
0x3f7f3f54,0x3f203f6c,0xbea33e32,0xbf78bf3c,0xbdccbf10,0x3f483ec7,0x3f733f7c,0x3e7d3f2e,
0xbf3cbe64,0xbf67bf7d,0x3d99bf00,0x3f753f20,0x3f2e3f7a,0xbede3e19,0xbf7fbf5e,0xbe97bf48,
0x3e32bf00,0x3f803f44,0x3e323f44,0xbf71bf00,0xbf7cbf5e,0xbf25,0x3f7c3f25,0x3eaf3f5e,
0x3f803f80,0x3f803f80,0x3f803f80,0x3f803f80,0x0,0x0,0x0,0x0,
0xbf7dbf80,0xbf71bf78,0xbf5abf67,0xbf3cbf4c,0xbe19bd4c,0xbeafbe7d,0xbf05bede,0xbf2ebf1b,
0x3f753f7f,0x3f443f61,0x3eea3f20,0x3d993e8b,0x3e973dcc,0x3f253ef5,0x3f643f48,0x3f7f3f76,
0xbf67bf7d,0xbf00bf3c,0x3d99be64,0x3f203ebb,0xbedebe19,0xbf5ebf2e,0xbf7fbf7a,0xbf48bf6e,
0x3f543f7b,0x3e323f0b,0xbf15be64,0xbf7dbf5a,0x3f103e4b,0x3f7c3f57,0x3f503f7a,0x3e193f05,
0xbf3cbf78,0x3e32bea3,0x3f6c3f20,0x3f543f7f,0xbf2ebe7d,0xbf7cbf73,0xbec7bf48,0x3f103dcc,
0x3f203f75,0xbf003d99,0xbf7dbf67,0xbe64bf3c,0x3f483e97,0x3f5e3f7f,0xbe193ede,0xbf7abf2e,
0xbf00bf71,0x3f443e32,0x3f443f80,0xbf003e32,0xbf5ebeaf,0xbf25bf7c,0x3f250000,0x3f5e3f7c,
0x3f803f80,0x3f803f80,0x3f803f80,0x3f803f80,0x0,0x0,0x0,0x0,
0xbf15bf29,0xbed3bf00,0xbe64bea3,0xbcccbdff,0xbf50bf40,0xbf69bf5e,0xbf7abf73,0xbf80bf7e,
0xbea3bdff,0xbf29bf00,0xbf67bf4c,0xbf80bf78,0x3f733f7e,0x3f403f5e,0x3ede3f1b,0x3d4c3e7d,
0x3f753f54,0x3f753f80,0x3f203f54,0x3d993ebb,0xbe97bf10,0x3e970000,0x3f483f10,0x3f7f3f6e,
0xbf4cbf78,0xbdffbf00,0x3f203e8b,0x3f7f3f61,0xbf1bbe7d,0xbf7ebf5e,0xbf48bf76,0xbdccbef5,
0xbccc3eea,0xbf5abf00,0xbf67bf80,0xbdffbf15,0x3f803f64,0x3f053f5e,0xbede3d4c,0xbf7ebf50,
0x3f543ebb,0x3f543f80,0xbe643ebb,0xbf7dbf3c,0xbf10bf6e,0x3f100000,0x3f7a3f6e,0x3e193f2e,
0xbf71bf71,0x3e32bf00,0x3f803f44,0x3e323f44,0xbeaf3eaf,0xbf7cbf5e,0xbf25,0x3f7c3f25,
0x3f803f80,0x3f803f80,0x3f803f80,0x3f803f80,0x0,0x0,0x0,0x0,
0x3e323d99,0x3ebb3e8b,0x3f0b3eea,0x3f333f20,0xbf7cbf7f,0xbf6ebf76,0xbf57bf64,0xbf37bf48,
0xbf71bf7d,0xbf3cbf5a,0xbed3bf15,0xbcccbe64,0xbeafbe19,0xbf2ebf05,0xbf69bf50,0xbf80bf7a,
0xbf00be64,0xbf67bf3c,0xbf7dbf7d,0xbf3cbf67,0x3f5e3f7a,0x3ede3f2e,0xbe193e19,0xbf2ebede,
0x3f443f75,0x3d993eea,0xbf29bea3,0xbf80bf67,0x3f253e97,0x3f7f3f64,0x3f403f73,0x3d4c3ede,
0x3f443ebb,0x3f753f7b,0x3e8b3f33,0xbf29be64,0xbf25bf6e,0x3e97be4b,0x3f763f37,0x3f403f7a,
0xbf00bf67,0x3f203d99,0x3f753f75,0x3d993f20,0xbf5ebede,0xbf48bf7f,0x3e97be97,0x3f7f3f48,
0xbf71bf00,0xbf00bf71,0x3f443e32,0x3f443f80,0x3eaf3f5e,0xbf5ebeaf,0xbf25bf7c,0x3f250000,
0x3f803f80,0x3f803f80,0x3f803f80,0x3f80,0x0,0x0,0x0,0x0,
0x3f543f44,0x3f6c3f61,0x3f7b3f75,0x3f7f,0xbf10bf25,0xbec7bef5,0xbe4bbe97,0xbdcc,
0x3ebb3e32,0x3f333f0b,0x3f6c3f54,0x3f7b,0xbf6ebf7c,0xbf37bf57,0xbec7bf10,0xbe4b,
0xbe64bf00,0x3ebb3d99,0x3f543f20,0x3f75,0xbf7abf5e,0xbf6ebf7f,0xbf10bf48,0xbe97,
0xbf3cbf71,0xbcccbed3,0x3f333ebb,0x3f6c,0xbf2ebeaf,0xbf80bf69,0xbf37bf6e,0xbec7,
0xbf7dbf71,0xbed3bf4c,0x3f0b3d99,0x3f61,0xbe193eaf,0xbf69bf1b,0xbf57bf7f,0xbef5,
0xbf67bf00,0xbf3cbf7d,0x3ebbbe64,0x3f54,0x3ede3f5e,0xbf2ebe19,0xbf6ebf7a,0xbf10,
0xbf003e32,0xbf71bf71,0x3e32bf00,0x3f44,0x3f5e3f7c,0xbeaf3eaf,0xbf7cbf5e,0xbf25,
0x3f333f80,0xbf3cbccc,0xbf29bf80,0x3f443d99,0x3f370000,0x3f2e3f80,0xbf40bd4c,0xbf25bf7f,
0x3f203f80,0xbf67be64,0xbe64bf67,0x3f803f20,0x3f480000,0x3ede3f7a,0xbf7abede,0xbf48,
0x3f0b3f80,0xbf7dbed3,0x3e8bbf29,0x3f443f75,0x3f570000,0x3e193f69,0xbf76bf40,0x3f25be97,
0x3eea3f80,0xbf7dbf15,0x3f33bea3,0x3e323f75,0x3f640000,0xbe193f50,0xbf37bf73,0x3f7c3e97,
0x3ebb3f80,0xbf67bf3c,0x3f753d99,0xbf003f20,0x3f6e0000,0xbede3f2e,0xbe97bf7f,0x3f5e3f48,
0x3e8b3f80,0xbf3cbf5a,0x3f7b3eea,0xbf713d99,0x3f760000,0xbf2e3f05,0x3e4bbf64,0x3eaf3f7f,
0x3e323f80,0xbf00bf71,0x3f443f44,0xbf71bf00,0x3f7c0000,0xbf5e3eaf,0x3f25bf25,0xbeaf3f5e,
0x3d993f80,0xbe64bf7d,0x3ebb3f75,0xbf00bf67,0x3f7f0000,0xbf7a3e19,0x3f6ebe97,0xbf5e3ede,
0x3f203f7f,0xbf4cbdff,0xbf15bf7d,0x3f543e32,0x3f483dcc,0x3f1b3f7e,0xbf50be19,0xbf10bf7c,
0xbe643f20,0xbf67bf67,0x3f20be64,0x3f203f80,0x3f7a3f48,0xbede3ede,0xbf48bf7a,0x3f480000,
0xbf67bdff,0xbcccbf5a,0x3f6c3f54,0xbf3c3e32,0x3ede3f7e,0xbf80bf05,0x3ec7bf10,0x3f2e3f7c,
0xbf67bf4c,0x3f61bccc,0xbdff3f54,0xbf3cbf71,0xbede3f1b,0xbef5bf80,0x3f7e3f10,0xbf2e3eaf,
0xbe64bf7d,0x3f543f54,0xbf7dbe64,0x3f20bf00,0xbf7abe19,0x3f10bf10,0x3e193f7a,0xbf48bf5e,
0x3f20bf15,0xbdff3f6c,0xbed3bf7d,0x3f543f44,0xbf48bf50,0x3f7e3ec7,0xbf693e19,0x3f10bf25,
0x3f803e32,0xbf713e32,0x3f44bf00,0xbf003f44,0xbf7c,0x3eaf3f7c,0xbf25bf5e,0x3f5e3f25,
0x3f203f54,0xbf3cbf3c,0x3f543f20,0xbf67bf00,0x3f48bf10,0xbf2e3f2e,0x3f10bf48,0xbede3f5e,
0x3f0b3f7b,0xbf5abe64,0xbf00bf78,0x3f613e8b,0x3f573e4b,0x3f053f7a,0xbf5ebe7d,0xbef5bf76,
0xbf67be64,0xbe64bf67,0x3f803f20,0xbe643f20,0x3ede3f7a,0xbf7abede,0xbf48,0x3f7a3f48,
0xbea3bf78,0x3f7f3f20,0xbf003eea,0xbf15bf80,0xbf73be7d,0x3dccbf48,0x3f5e3f64,0xbf503d4c,
0x3f7b3e8b,0xbed33f20,0xbf00bf80,0x3f7f3f0b,0xbe4bbf76,0x3f693f48,0xbf5e3d4c,0x3dccbf57,
0x3d993f75,0xbf3cbf67,0x3f803ebb,0xbf3c3ebb,0x3f7f3e97,0xbf2e3ede,0xbf6e,0x3f2e3f6e,
0xbf80bea3,0x3f61be64,0xbf003f33,0xbcccbf78,0xbd4c3f73,0xbef5bf7a,0x3f5e3f37,0xbf80be7d,
0x3e32bf71,0x3e323f80,0xbf00bf71,0x3f443f44,0xbf7cbeaf,0x3f7c0000,0xbf5e3eaf,0x3f25bf25,
0x3f753ebb,0xbf7dbe64,0x3f803d99,0xbf7d3d99,0x3e97bf6e,0xbe193f7a,0xbf7f,0x3e193f7f,
0x3eea3f75,0xbf67bea3,0xbed3bf71,0x3f6c3ebb,0x3f643e97,0x3ede3f73,0xbf69beaf,0xbec7bf6e,
0xbf67bf67,0x3f20be64,0x3f203f80,0xbf67be64,0xbede3ede,0xbf48bf7a,0x3f480000,0x3ede3f7a,
0x3f7b3ebb,0xbe643f33,0xbf4cbf71,0x3f613d99,0xbe4bbf6e,0x3f7a3f37,0xbf1b3eaf,0xbef5bf7f,
0xbf293ebb,0xbe64bf78,0x3f6c3f44,0xbf5a3d99,0x3f403f6e,0xbf7abe7d,0x3ec7bf25,0x3f053f7f,
0x3d99bf67,0x3f203f75,0xbf7dbf00,0x3f54be64,0xbf7fbede,0x3f48be97,0xbe193f5e,0xbf10bf7a,
0x3f0b3f75,0xbf67bf29,0x3f7f3e32,0xbf4c3ebb,0x3f57be97,0xbede3f40,0xbdccbf7c,0x3f1b3f6e,
0xbf71bf00,0x3f803e32,0xbf713e32,0x3f44bf00,0xbeaf3f5e,0xbf7c,0x3eaf3f7c,0xbf25bf5e,
0x3f75be64,0xbf673ebb,0x3f54bf00,0xbf3c3f20,0xbe97bf7a,0x3ede3f6e,0xbf10bf5e,0x3f2e3f48,
0x3ebb3f6c,0xbf71bed3,0xbea3bf67,0x3f753eea,0x3f6e3ec7,0x3eaf3f69,0xbf73bede,0xbe97bf64,
0xbe64bf67,0x3f803f20,0xbe643f20,0xbf67bf67,0xbf7abede,0xbf48,0x3f7a3f48,0xbede3ede,
0x3d993f61,0xbf71bf4c,0x3f33be64,0x3ebb3f7b,0x3f7f3ef5,0xbeaf3f1b,0xbf37bf7a,0x3f6e3e4b,
0x3d99bf5a,0x3f443f6c,0xbf78be64,0x3ebbbf29,0xbf7fbf05,0x3f25bec7,0x3e7d3f7a,0xbf6ebf40,
0xbe643f54,0xbf00bf7d,0x3f753f20,0xbf673d99,0x3f7a3f10,0xbf5e3e19,0x3e97bf48,0x3ede3f7f,
0x3ebbbf4c,0x3e323f7f,0xbf29bf67,0x3f753f0b,0xbf6ebf1b,0x3f7c3dcc,0xbf403ede,0x3e97bf57,
0xbf003f44,0x3e32bf71,0x3e323f80,0xbf00bf71,0x3f5e3f25,0xbf7cbeaf,0x3f7c0000,0xbf5e3eaf,
0x3f20bf3c,0xbf003f54,0x3ebbbf67,0xbe643f75,0xbf48bf2e,0x3f5e3f10,0xbf6ebede,0x3f7a3e97,
0x3e8b3f61,0xbf78bf00,0xbe64bf5a,0x3f7b3f0b,0x3f763ef5,0x3e7d3f5e,0xbf7abf05,0xbe4bbf57,
0x3f20be64,0x3f203f80,0xbf67be64,0xbe64bf67,0xbf48bf7a,0x3f480000,0x3ede3f7a,0xbf7abede,
0xbf80bf15,0x3eeabf00,0x3f203f7f,0xbf78bea3,0xbd4c3f50,0xbf64bf5e,0x3f48bdcc,0x3e7d3f73,
0x3f0b3f7f,0xbf80bf00,0x3f20bed3,0x3e8b3f7b,0x3f57bdcc,0xbd4c3f5e,0xbf48bf69,0x3f763e4b,
0x3ebbbf3c,0x3ebb3f80,0xbf67bf3c,0x3f753d99,0xbf6ebf2e,0x3f6e0000,0xbede3f2e,0xbe97bf7f,
0xbf78bccc,0x3f33bf00,0xbe643f61,0xbea3bf80,0x3e7d3f80,0xbf37bf5e,0x3f7a3ef5,0xbf733d4c,
0x3f443f44,0xbf71bf00,0x3f803e32,0xbf713e32,0x3f25bf25,0xbeaf3f5e,0xbf7c,0x3eaf3f7c,
0x3d99bf7d,0x3d993f80,0xbe64bf7d,0x3ebb3f75,0xbf7fbe19,0x3f7f0000,0xbf7a3e19,0x3f6ebe97,
0x3e323f54,0xbf7dbf15,0xbdffbf4c,0x3f7f3f20,0x3f7c3f10,0x3e193f50,0xbf7ebf1b,0xbdccbf48,
0x3f803f20,0xbe643f20,0xbf67bf67,0x3f20be64,0xbf48,0x3f7a3f48,0xbede3ede,0xbf48bf7a,
0x3e32bf3c,0x3f543f6c,0xbf5abccc,0xbdffbf67,0xbf7cbf2e,0x3f10bec7,0x3f053f80,0xbf7ebede,
0xbf71bf3c,0x3f54bdff,0xbccc3f61,0xbf4cbf67,0xbeaf3f2e,0xbf10bf7e,0x3f803ef5,0xbf1b3ede,
0xbf003f20,0xbe64bf7d,0x3f543f54,0xbf7dbe64,0x3f5e3f48,0xbf7abe19,0x3f10bf10,0x3e193f7a,
0x3f443f54,0xbf7dbed3,0x3f6cbdff,0xbf153f20,0x3f25bf10,0xbe193f69,0xbec7bf7e,0x3f503f48,
0x3f44bf00,0xbf003f44,0x3e32bf71,0x3e323f80,0xbf25bf5e,0x3f5e3f25,0xbf7cbeaf,0x3f7c0000,
0xbf00bf67,0x3f203f54,0xbf3cbf3c,0x3f543f20,0xbf5e3ede,0x3f48bf10,0xbf2e3f2e,0x3f10bf48,
0x3d993f44,0xbf80bf29,0xbcccbf3c,0x3f33,0x3f7f3f25,0x3d4c3f40,0xbf80bf2e,0xbf37,
0x3f203f80,0xbf67be64,0xbe64bf67,0x3f20,0x3f480000,0x3ede3f7a,0xbf7abede,0xbf48,
0x3f753f44,0xbf293e8b,0xbed3bf7d,0x3f0b,0x3e97bf25,0x3f403f76,0xbf69be19,0xbf57,
0x3f753e32,0xbea33f33,0xbf15bf7d,0x3eea,0xbe97bf7c,0x3f733f37,0xbf503e19,0xbf64,
0x3f20bf00,0x3d993f75,0xbf3cbf67,0x3ebb,0xbf48bf5e,0x3f7f3e97,0xbf2e3ede,0xbf6e,
0x3d99bf71,0x3eea3f7b,0xbf5abf3c,0x3e8b,0xbf7fbeaf,0x3f64be4b,0xbf053f2e,0xbf76,
0xbf00bf71,0x3f443f44,0xbf71bf00,0x3e32,0xbf5e3eaf,0x3f25bf25,0xbeaf3f5e,0xbf7c,
0xbf67bf00,0x3f753ebb,0xbf7dbe64,0x3d99,0xbede3f5e,0x3e97bf6e,0xbe193f7a,0xbf7f,
0xbccc3f80,0x3d99bf80,0xbdff3f7f,0x3e32bf7d,0x3f800000,0xbf7fbd4c,0x3f7e3dcc,0xbf7cbe19,
0xbdff3f80,0x3ebbbf78,0xbf153f61,0x3f44bf3c,0x3f7e0000,0xbf6ebe7d,0x3f503ef5,0xbf25bf2e,
0xbe643f80,0x3f20bf67,0xbf673f20,0x3f80be64,0x3f7a0000,0xbf48bede,0x3ede3f48,0xbf7a,
0xbea33f80,0x3f54bf4c,0xbf803e8b,0x3f443ebb,0x3f730000,0xbf10bf1b,0xbd4c3f76,0x3f25bf6e,
0xbed33f80,0x3f75bf29,0xbf5abdff,0x3e323f54,0x3f690000,0xbe97bf40,0xbf053f7e,0x3f7cbf10,
0xbf003f80,0x3f80bf00,0xbf00bf00,0xbf003f80,0x3f5e0000,0xbf5e,0xbf5e3f5e,0x3f5e0000,
0xbf153f80,0x3f75bea3,0xbcccbf4c,0xbf713f54,0x3f500000,0x3e97bf73,0xbf803f1b,0x3eaf3f10,
0xbf293f80,0x3f54bdff,0x3eeabf78,0xbf713ebb,0x3f400000,0x3f10bf7e,0xbf643e7d,0xbeaf3f6e,
0xbe643f7b,0x3e8bbf78,0xbea33f75,0x3ebbbf71,0x3f7a3e4b,0xbf76be7d,0x3f733e97,0xbf6ebeaf,
0xbf673f0b,0x3f7bbea3,0xbf803d99,0x3f753e32,0x3ede3f57,0xbe4bbf73,0xbd4c3f7f,0x3e97bf7c,
0xbf67be64,0x3f203f20,0xbe64bf67,0xbe643f80,0xbede3f7a,0x3f48bf48,0xbf7a3ede,0x3f7a0000,
0xbe64bf5a,0xbed33f7f,0x3f61bf3c,0xbf7d3e32,0xbf7a3f05,0x3f693dcc,0xbef5bf2e,0xbe193f7c,
0x3f20bf78,0xbf803eea,0x3f333ebb,0x3d99bf71,0xbf48be7d,0x3d4c3f64,0x3f37bf6e,0xbf7f3eaf,
0x3f80bf00,0xbf00bf00,0xbf003f80,0x3f80bf00,0xbf5e,0xbf5e3f5e,0x3f5e0000,0xbf5e,
0x3f203e8b,0x3f0bbf80,0xbf783ebb,0x3d993f44,0x3f48bf76,0xbf573d4c,0xbe7d3f6e,0x3f7fbf25,
0xbe643f61,0x3f7fbf15,0xbcccbf3c,0xbf7d3f44,0x3f7abef5,0x3dccbf50,0xbf803f2e,0x3e193f25,
0xbed33f6c,0x3eeabf67,0xbf003f61,0x3f0bbf5a,0x3f693ec7,0xbf64bede,0x3f5e3ef5,0xbf57bf05,
0xbf5abed3,0x3f333f20,0xbf00bf4c,0x3e8b3f6c,0xbf053f69,0x3f37bf48,0xbf5e3f1b,0x3f76bec7,
0x3f20bf67,0xbf673f20,0x3f80be64,0xbf67be64,0xbf48bede,0x3ede3f48,0xbf7a,0xbede3f7a,
0x3f333eea,0xbdffbf67,0xbf003f7b,0x3f6cbf29,0x3f37bf64,0xbf7e3ede,0x3f5e3e4b,0xbec7bf40,
0xbf4c3f61,0x3f7bbe64,0xbf00bf15,0xbea33f7f,0x3f1b3ef5,0x3e4bbf7a,0xbf5e3f50,0x3f73bdcc,
0xbf00bf00,0xbf003f80,0x3f80bf00,0xbf00bf00,0xbf5e3f5e,0x3f5e0000,0xbf5e,0xbf5e3f5e,
0x3f6cbf5a,0xbf29be64,0xbf003f7f,0x3f7bbed3,0xbec7bf05,0xbf403f7a,0x3f5ebdcc,0x3e4bbf69,
0x3e8b3f0b,0x3f6cbf67,0xbf00bea3,0xbf4c3f7b,0x3f76bf57,0xbec7bede,0xbf5e3f73,0x3f1b3e4b,
0xbf153f54,0x3f20bf4c,0xbf293f44,0x3f33bf3c,0x3f503f10,0xbf48bf1b,0x3f403f25,0xbf37bf2e,
0xbcccbf7d,0xbe643f7f,0x3eeabf71,0xbf293f54,0xbf803e19,0x3f7a3dcc,0xbf64beaf,0x3f403f10,
0x3f203f20,0xbe64bf67,0xbe643f80,0x3f20bf67,0x3f48bf48,0xbf7a3ede,0x3f7a0000,0xbf48bede,
0xbf783d99,0x3f203f0b,0xbcccbf71,0xbf153f75,0xbe7d3f7f,0x3f48bf57,0xbf803eaf,0x3f503e97,
0x3f6cbf3c,0xbf67bccc,0x3e8b3f44,0x3f0bbf7d,0xbec7bf2e,0xbede3f80,0x3f76bf25,0xbf57be19,
0xbf003f80,0x3f80bf00,0xbf00bf00,0xbf003f80,0x3f5e0000,0xbf5e,0xbf5e3f5e,0x3f5e0000,
0xbdffbf3c,0xbf673f61,0x3f333e32,0x3eeabf7d,0xbf7e3f2e,0x3ede3ef5,0x3f37bf7c,0xbf643e19,
0x3f333d99,0x3f20bf80,0xbf5a3e32,0xbed33f75,0x3f37bf7f,0xbf483d4c,0xbf053f7c,0x3f69be97,
0xbf3c3f33,0x3f44bf29,0xbf4c3f20,0x3f54bf15,0x3f2e3f37,0xbf25bf40,0x3f1b3f48,0xbf10bf50,
0x3f54bf29,0xbf713eea,0x3f7fbe64,0xbf7dbccc,0xbf10bf40,0x3eaf3f64,0xbdccbf7a,0xbe193f80,
0xbf673f20,0x3f80be64,0xbf67be64,0x3f203f20,0x3ede3f48,0xbf7a,0xbede3f7a,0x3f48bf48,
0x3f75bf15,0xbf71bccc,0x3f0b3f20,0x3d99bf78,0xbe97bf50,0xbeaf3f80,0x3f57bf48,0xbf7f3e7d,
0xbf7d3f0b,0x3f443e8b,0xbcccbf67,0xbf3c3f6c,0x3e193f57,0x3f25bf76,0xbf803ede,0x3f2e3ec7,
0x3f80bf00,0xbf00bf00,0xbf003f80,0x3f80bf00,0xbf5e,0xbf5e3f5e,0x3f5e0000,0xbf5e,
0xbf7d3eea,0x3e323f33,0x3f61bf67,0xbf3cbdff,0xbe193f64,0x3f7cbf37,0xbef5bede,0xbf2e3f7e,
0x3f75bed3,0x3e32bf5a,0xbf803f20,0x3d993f33,0x3e97bf69,0xbf7c3f05,0xbd4c3f48,0x3f7fbf37,
0xbf5a3f0b,0x3f61bf00,0xbf673eea,0x3f6cbed3,0x3f053f57,0xbef5bf5e,0x3ede3f64,0xbec7bf69,
0x3f6c3e8b,0xbf4cbf00,0x3f203f33,0xbed3bf5a,0x3ec7bf76,0xbf1b3f5e,0x3f48bf37,0xbf693f05,
0xbe64bf67,0xbe643f80,0x3f20bf67,0xbf673f20,0xbf7a3ede,0x3f7a0000,0xbf48bede,0x3ede3f48,
0xbf293f6c,0x3f7bbf00,0xbf67bdff,0x3eea3f33,0x3f403ec7,0xbe4bbf5e,0xbede3f7e,0x3f64bf37,
0x3f7fbea3,0xbf15bf00,0xbe643f7b,0x3f61bf4c,0x3dccbf73,0xbf503f5e,0x3f7abe4b,0xbef5bf1b,
0xbf00bf00,0xbf003f80,0x3f80bf00,0xbf00bf00,0xbf5e3f5e,0x3f5e0000,0xbf5e,0xbf5e3f5e,
0xbed33f7b,0x3f7fbf00,0xbe64bf29,0xbf5a3f6c,0x3f69be4b,0x3dccbf5e,0xbf7a3f40,0x3f053ec7,
0x3f7bbf4c,0xbea3bf00,0xbf673f6c,0x3f0b3e8b,0xbe4bbf1b,0xbf733f5e,0x3ede3ec7,0x3f57bf76,
0xbf713ebb,0x3f75bea3,0xbf783e8b,0x3f7bbe64,0x3eaf3f6e,0xbe97bf73,0x3e7d3f76,0xbe4bbf7a,
0x3e323f75,0x3d99bf80,0xbea33f7b,0x3f0bbf67,0x3f7cbe97,0xbf7f3d4c,0x3f733e4b,0xbf57bede,
0x3f80be64,0xbf67be64,0x3f203f20,0xbe64bf67,0xbf7a,0xbede3f7a,0x3f48bf48,0xbf7a3ede,
0x3e32bf7d,0xbf3c3f61,0x3f7fbed3,0xbf5abe64,0xbf7c3e19,0x3f2e3ef5,0xbdccbf69,0xbf053f7a,
0xbf713d99,0x3ebb3f33,0x3eeabf80,0xbf783f20,0xbeaf3f7f,0x3f6ebf37,0xbf64bd4c,0x3e7d3f48,
0xbf003f80,0x3f80bf00,0xbf00bf00,0xbf003f80,0x3f5e0000,0xbf5e,0xbf5e3f5e,0x3f5e0000,
0x3f443d99,0x3ebbbf78,0xbf803f0b,0x3e8b3f20,0x3f25bf7f,0xbf6e3e7d,0xbd4c3f57,0x3f76bf48,
0x3f44bf7d,0xbf3cbccc,0xbf153f7f,0x3f61be64,0xbf25be19,0xbf2e3f80,0x3f50bdcc,0x3ef5bf7a,
0xbf7d3e32,0x3f7fbdff,0xbf803d99,0xbccc,0x3e193f7c,0xbdccbf7e,0x3d4c3f7f,0xbf80,
0xbf3c3f44,0x3f61bf15,0xbf783ebb,0xbdff,0x3f2e3f25,0xbef5bf50,0x3e7d3f6e,0xbf7e,
0xbe643f80,0x3f20bf67,0xbf673f20,0xbe64,0x3f7a0000,0xbf48bede,0x3ede3f48,0xbf7a,
0x3ebb3f44,0x3e8bbf80,0xbf4c3f54,0xbea3,0x3f6ebf25,0xbf763d4c,0x3f1b3f10,0xbf73,
0x3f543e32,0xbdffbf5a,0xbf293f75,0xbed3,0x3f10bf7c,0xbf7e3f05,0x3f403e97,0xbf69,
0x3f80bf00,0xbf00bf00,0xbf003f80,0xbf00,0xbf5e,0xbf5e3f5e,0x3f5e0000,0xbf5e,
0x3f54bf71,0xbf4cbccc,0xbea33f75,0xbf15,0xbf10beaf,0xbf1b3f80,0x3f73be97,0xbf50,
0x3ebbbf71,0xbf783eea,0xbdff3f54,0xbf29,0xbf6e3eaf,0xbe7d3f64,0x3f7ebf10,0xbf40,
0xbf3c3f80,0x3f203d99,0x3f54bf7d,0xbf00be64,0x3f2e0000,0x3f48bf7f,0xbf10be19,0xbf5e3f7a,
0xbf4c3f80,0x3ebb3e8b,0x3f7fbf5a,0x3e32bf3c,0x3f1b0000,0x3f6ebf76,0xbdccbf05,0xbf7c3f2e,
0xbf5a3f80,0x3d993eea,0x3f6cbf15,0x3f44bf7d,0x3f050000,0x3f7fbf64,0x3ec7bf50,0xbf253e19,
0xbf673f80,0xbe643f20,0x3f20be64,0x3f80bf67,0x3ede0000,0x3f7abf48,0x3f48bf7a,0xbede,
0xbf713f80,0xbf003f44,0x3e323e32,0x3f44bf00,0x3eaf0000,0x3f5ebf25,0x3f7cbf7c,0x3f25bf5e,
0xbf783f80,0xbf3c3f61,0xbea33f0b,0x3e323d99,0x3e7d0000,0x3f2ebef5,0x3f73bf57,0x3f7cbf7f,
0xbf7d3f80,0xbf673f75,0xbf3c3f54,0xbf003f20,0x3e190000,0x3edebe97,0x3f2ebf10,0x3f5ebf48,
0xbf803f80,0xbf7d3f7f,0xbf783f7b,0xbf713f75,0x3d4c0000,0x3e19bdcc,0x3e7dbe4b,0x3eafbe97,
0xbf673f75,0x3ebb3ebb,0x3f75bf67,0xbe64bf00,0x3ede3e97,0x3f6ebf6e,0xbe97bede,0xbf7a3f5e,
0xbf673eea,0xbf293f7b,0x3f0b3d99,0x3f75bf71,0xbede3f64,0x3f40be4b,0x3f57bf7f,0xbe97beaf,
0xbe64bea3,0xbf783f33,0xbf293f75,0x3ebb3e32,0xbf7a3f73,0xbe7d3f37,0x3f40be97,0x3f6ebf7c,
0x3f20bf67,0xbe64be64,0xbf673f20,0xbf673f80,0xbf483ede,0xbf7a3f7a,0xbede3f48,0x3ede0000,
0x3f80bf71,0x3f44bf71,0x3e32bf00,0xbf003e32,0xbeaf,0xbf253eaf,0xbf7c3f5e,0xbf5e3f7c,
0x3f20bed3,0x3f6cbf4c,0x3f7fbf7d,0x3f54bf71,0x3f48bf69,0x3ec7bf1b,0xbdccbe19,0xbf103eaf,
0xbe643ebb,0x3d993d99,0x3ebbbe64,0x3f20bf00,0x3f7abf6e,0x3f7fbf7f,0x3f6ebf7a,0x3f48bf5e,
0xbf673f6c,0xbf5a3f61,0xbf4c3f54,0xbf3c3f44,0x3edebec7,0x3f05bef5,0x3f1bbf10,0x3f2ebf25,
0xbf7d3f54,0x3d993f20,0x3f80bf3c,0x3d99bf3c,0x3e193f10,0x3f7fbf48,0xbf2e,0xbf7f3f2e,
0xbcccbf15,0xbf783f20,0xbf003f6c,0x3f33bdff,0xbf803f50,0xbe7d3f48,0x3f5ebec7,0x3f37bf7e,
0x3f7fbf4c,0x3f0bbf67,0xbf00bccc,0xbf803f61,0x3dccbf1b,0xbf573ede,0xbf5e3f80,0x3d4c3ef5,
0xbe643f20,0x3f20be64,0x3f80bf67,0x3f20bf67,0x3f7abf48,0x3f48bf7a,0xbede,0xbf483ede,
0xbf713f44,0xbf713f80,0xbf003f44,0x3e323e32,0xbeaf3f25,0x3eaf0000,0x3f5ebf25,0x3f7cbf7c,
0x3eeabf29,0xbcccbe64,0xbf003e8b,0xbf5a3f33,0xbf643f40,0xbf803f7a,0xbf5e3f76,0xbf053f37,
0x3f54bf3c,0x3f75bf67,0x3f80bf7d,0x3f75bf7d,0x3f10bf2e,0x3e97bede,0xbe19,0xbe973e19,
0xbf293f33,0xbf153f20,0xbf003f0b,0xbed33eea,0x3f40bf37,0x3f50bf48,0x3f5ebf57,0x3f69bf64,
0xbf7d3f20,0xbe643f54,0x3f75bf00,0x3ebbbf67,0xbe193f48,0x3f7abf10,0x3e97bf5e,0xbf6e3ede,
0x3f61bf7d,0xbe64bed3,0xbf803f44,0xbea33f54,0xbef5be19,0xbf7a3f69,0xbd4c3f25,0x3f73bf10,
0xbed33f54,0x3f20bdff,0x3f7bbf71,0x3e8bbf3c,0x3f69bf10,0x3f48bf7e,0xbe4bbeaf,0xbf763f2e,
0xbe64be64,0xbf673f20,0xbf673f80,0xbe643f20,0xbf7a3f7a,0xbede3f48,0x3ede0000,0x3f7abf48,
0x3f44bf00,0x3f80bf71,0x3f44bf71,0x3e32bf00,0x3f25bf5e,0xbeaf,0xbf253eaf,0xbf7c3f5e,
0xbf803f75,0xbf673f7b,0xbf153f44,0xbdff3ebb,0xbd4c3e97,0x3edebe4b,0x3f50bf25,0x3f7ebf6e,
0x3f54bf67,0x3f20bf3c,0x3ebbbf00,0x3d99be64,0xbf103ede,0xbf483f2e,0xbf6e3f5e,0xbf7f3f7a,
0xbea33ebb,0xbe643e8b,0xbdff3e32,0xbccc3d99,0x3f73bf6e,0x3f7abf76,0x3f7ebf7c,0x3f80bf7f,
0xbf673ebb,0xbf003f75,0x3f54be64,0x3f20bf7d,0xbede3f6e,0x3f5ebe97,0x3f10bf7a,0xbf483e19,
0x3f54bea3,0x3f44bf80,0xbed3be64,0xbf7d3f61,0x3f10bf73,0xbf253d4c,0xbf693f7a,0x3e193ef5,
0xbf3c3e8b,0xbf713f7b,0xbdff3f20,0x3f54bed3,0xbf2e3f76,0x3eaf3e4b,0x3f7ebf48,0x3f10bf69,
0x3f20be64,0x3f80bf67,0x3f20bf67,0xbe64be64,0x3f48bf7a,0xbede,0xbf483ede,0xbf7a3f7a,
0xbf003e32,0xbf713f44,0xbf713f80,0xbf003f44,0xbf5e3f7c,0xbeaf3f25,0x3eaf0000,0x3f5ebf25,
0x3ebbbdff,0x3f44bf15,0x3f7bbf67,0x3f75bf80,0x3f6ebf7e,0x3f25bf50,0x3e4bbede,0xbe973d4c,
0xbe643d99,0xbf003ebb,0xbf3c3f20,0xbf673f54,0xbf7a3f7f,0xbf5e3f6e,0xbf2e3f48,0xbede3f10,
0x3d99bccc,0x3e32bdff,0x3e8bbe64,0x3ebbbea3,0x3f7fbf80,0x3f7cbf7e,0x3f76bf7a,0x3f6ebf73,
0xbf3c3d99,0xbf3c3f80,0x3f203d99,0x3f54bf7d,0xbf2e3f7f,0x3f2e0000,0x3f48bf7f,0xbf10be19,
0xbdff3f33,0x3f6cbf00,0x3f20bf78,0xbf15bccc,0x3f7ebf37,0x3ec7bf5e,0xbf483e7d,0xbf503f80,
0x3f61bf80,0xbcccbf00,0xbf673f0b,0xbf4c3f7f,0xbef5bd4c,0xbf803f5e,0xbede3f57,0x3f1bbdcc,
0xbf673f20,0xbf673f80,0xbe643f20,0x3f20be64,0xbede3f48,0x3ede0000,0x3f7abf48,0x3f48bf7a,
0x3e323e32,0x3f44bf00,0x3f80bf71,0x3f44bf71,0x3f7cbf7c,0x3f25bf5e,0xbeaf,0xbf253eaf,
0x3f33bf5a,0x3e8bbf00,0xbe64bccc,0xbf293eea,0xbf373f05,0xbf763f5e,0xbf7a3f80,0xbf403f64,
0xbf7d3f75,0xbf7d3f80,0xbf673f75,0xbf3c3f54,0xbe193e97,0x3e190000,0x3edebe97,0x3f2ebf10,
0x3eeabed3,0x3f0bbf00,0x3f20bf15,0x3f33bf29,0x3f64bf69,0x3f57bf5e,0x3f48bf50,0x3f37bf40,
0xbf00be64,0xbf673f75,0x3ebb3ebb,0x3f75bf67,0xbf5e3f7a,0x3ede3e97,0x3f6ebf6e,0xbe97bede,
0xbf713f75,0x3d993f0b,0x3f7bbf29,0x3eeabf67,0x3eaf3e97,0x3f7fbf57,0x3e4bbf40,0xbf643ede,
0x3e323ebb,0x3f75bf29,0x3f33bf78,0xbea3be64,0x3f7cbf6e,0x3e97bf40,0xbf373e7d,0xbf733f7a,
0x3f80bf67,0x3f20bf67,0xbe64be64,0xbf673f20,0xbede,0xbf483ede,0xbf7a3f7a,0xbede3f48,
0x3e32bf00,0xbf003e32,0xbf713f44,0xbf713f80,0xbf7c3f5e,0xbf5e3f7c,0xbeaf3f25,0x3eaf0000,
0xbf713f54,0xbf7d3f7f,0xbf4c3f6c,0xbed33f20,0xbeaf3f10,0x3e193dcc,0x3f1bbec7,0x3f69bf48,
0xbf003f20,0xbe643ebb,0x3d993d99,0x3ebbbe64,0x3f5ebf48,0x3f7abf6e,0x3f7fbf7f,0x3f6ebf7a,
0x3f44bf3c,0x3f54bf4c,0x3f61bf5a,0x3f6cbf67,0x3f25bf2e,0x3f10bf1b,0x3ef5bf05,0x3ec7bede,
0xbe64bf00,0xbf7d3f54,0x3d993f20,0xbf3c,0xbf7a3f5e,0x3e193f10,0x3f7fbf48,0xbf2e,
0xbf3c3e32,0xbf5a3f7f,0x3e8b3ebb,0xbf4c,0xbf2e3f7c,0x3f053dcc,0x3f76bf6e,0xbf1b,
0xbf7d3f44,0xbf153f6c,0x3eea3d99,0xbf5a,0xbe193f25,0x3f50bec7,0x3f64bf7f,0xbf05,
0xbf673f80,0xbe643f20,0x3f20be64,0xbf67,0x3ede0000,0x3f7abf48,0x3f48bf7a,0xbede,
0xbf003f44,0x3e323e32,0x3f44bf00,0xbf71,0x3f5ebf25,0x3f7cbf7c,0x3f25bf5e,0xbeaf,
0x3d993e32,0x3f0bbea3,0x3f61bf3c,0xbf78,0x3f7fbf7c,0x3f57bf73,0x3ef5bf2e,0xbe7d,
0x3f20bf00,0x3f54bf3c,0x3f75bf67,0xbf7d,0x3f48bf5e,0x3f10bf2e,0x3e97bede,0xbe19,
0x3f75bf71,0x3f7bbf78,0x3f7fbf7d,0xbf80,0x3e97beaf,0x3e4bbe7d,0x3dccbe19,0xbd4c,
0x3bf4,0x3c74,0x3c74,0x3c74,0x3bf4,0x3ac23c73,0x3b413c6f,0x3b903c69,
0x3bf4,0x3b413c6f,0x3bbd3c61,0x3c093c49,0x3bf4,0x3b903c69,0x3c093c49,0x3c3f3c18,
0x3bf4,0x3bbd3c61,0x3c2f3c2a,0x3c633bb2,0x3bf4,0x3be93c56,0x3c4d3c04,0x3c733a92,
0x3bf4,0x3c093c49,0x3c633bb2,0x3c6ebb59,0x3bf4,0x3c1d3c3b,0x3c703b29,0x3c53bbf4,
0x3bf4,0x3c2f3c2a,0x3c74b9c2,0x3c26bc33,0x3bf4,0x3c3f3c18,0x3c6ebb59,0x3bd4bc5c,
0x3bf4,0x3c4d3c04,0x3c5ebbc9,0x3b11bc71,0x3bf4,0x3c593bde,0x3c46bc0e,0xbb11bc71,
0x3bf4,0x3c633bb2,0x3c26bc33,0xbbd4bc5c,0x3bf4,0x3c6b3b84,0x3bfebc50,0xbc26bc33,
0x3bf4,0x3c703b29,0x3ba7bc65,0xbc53bbf4,0x3bf4,0x3c733a92,0x3b11bc71,0xbc6ebb59,
0x3c74,0x3c74,0x3c74,0x3c74,0x3bbd3c61,0x3be93c56,0x3c093c49,0x3c1d3c3b,
0x3c2f3c2a,0x3c4d3c04,0x3c633bb2,0x3c703b29,0x3c633bb2,0x3c733a92,0x3c6ebb59,0x3c53bbf4,
0x3c74b9c2,0x3c5ebbc9,0x3c26bc33,0x3ba7bc65,0x3c5ebbc9,0x3c13bc42,0x3b11bc71,0xbba7bc65,
0x3c26bc33,0x3b11bc71,0xbbd4bc5c,0xbc53bbf4,0x3ba7bc65,0xbba7bc65,0xbc53bbf4,0xbc703b29,
0xba42bc73,0xbc37bc21,0xbc733a92,0xbc1d3c3b,0xbbd4bc5c,0xbc6ebb59,0xbc3f3c18,0x3c74,
0xbc37bc21,0xbc6b3b84,0xbb903c69,0x3c1d3c3b,0xbc67bb9b,0xbc2f3c2a,0x3b903c69,0x3c703b29,
0xbc733a92,0xbb903c69,0x3c3f3c18,0x3c53bbf4,0xbc593bde,0x3b413c6f,0x3c733a92,0x3ba7bc65,
0xbc1d3c3b,0x3c1d3c3b,0x3c53bbf4,0xbba7bc65,0xbb903c69,0x3c633bb2,0x3bd4bc5c,0xbc53bbf4,
0x3c74,0x3c74,0x3c74,0x3c74,0x3c2f3c2a,0x3c3f3c18,0x3c4d3c04,0x3c593bde,
0x3c74b9c2,0x3c6ebb59,0x3c5ebbc9,0x3c46bc0e,0x3c26bc33,0x3bd4bc5c,0x3b11bc71,0xbb11bc71,
0xba42bc73,0xbbd4bc5c,0xbc37bc21,0xbc67bb9b,0xbc37bc21,0xbc6ebb59,0xbc6b3b84,0xbc2f3c2a,
0xbc733a92,0xbc3f3c18,0xbb903c69,0x3b903c69,0xbc1d3c3b,0x3c74,0x3c1d3c3b,0x3c703b29,
0x3ac23c73,0x3c3f3c18,0x3c72baf2,0x3c13bc42,0x3c3f3c18,0x3c6ebb59,0x3bd4bc5c,0xbbd4bc5c,
0x3c72baf2,0x3bd4bc5c,0xbbfebc50,0xbc74b9c2,0x3c13bc42,0xbbd4bc5c,0xbc74b9c2,0xbbe93c56,
0xbb11bc71,0xbc6ebb59,0xbc093c49,0x3c093c49,0xbc46bc0e,0xbc3f3c18,0x3bbd3c61,0x3c72baf2,
0xbc703b29,0x3c74,0x3c703b29,0x3ba7bc65,0xbc093c49,0x3c3f3c18,0x3c26bc33,0xbc26bc33,
0x3c74,0x3c74,0x3c74,0x3c74,0x3c633bb2,0x3c6b3b84,0x3c703b29,0x3c733a92,
0x3c26bc33,0x3bfebc50,0x3ba7bc65,0x3b11bc71,0xbbd4bc5c,0xbc26bc33,0xbc53bbf4,0xbc6ebb59,
0xbc733a92,0xbc593bde,0xbc1d3c3b,0xbb903c69,0xbb903c69,0x3b413c6f,0x3c1d3c3b,0x3c633bb2,
0x3c3f3c18,0x3c733a92,0x3c53bbf4,0x3bd4bc5c,0x3c53bbf4,0x3ba7bc65,0xbba7bc65,0xbc53bbf4,
0xbb11bc71,0xbc46bc0e,0xbc703b29,0xbc093c49,0xbc6ebb59,0xbc3f3c18,0x3c74,0x3c3f3c18,
0xbc093c49,0x3bbd3c61,0x3c703b29,0x3c26bc33,0x3c093c49,0x3c72baf2,0x3ba7bc65,0xbc26bc33,
0x3c6ebb59,0x3b11bc71,0xbc53bbf4,0xbc3f3c18,0x3b11bc71,0xbc5ebbc9,0xbc1d3c3b,0x3c093c49,
0xbc53bbf4,0xbc1d3c3b,0x3c1d3c3b,0x3c53bbf4,0xbc3f3c18,0x3c093c49,0x3c53bbf4,0xbbd4bc5c,
0x3c74,0x3c74,0x3c74,0x3c74,0x3c74b9c2,0x3c72baf2,0x3c6ebb59,0x3c67bb9b,
0xba42bc73,0xbb71bc6c,0xbbd4bc5c,0xbc13bc42,0xbc733a92,0xbc633bb2,0xbc3f3c18,0xbc093c49,
0x3ac23c73,0x3be93c56,0x3c3f3c18,0x3c6b3b84,0x3c72baf2,0x3c46bc0e,0x3bd4bc5c,0xba42bc73,
0xbb11bc71,0xbc26bc33,0xbc6ebb59,0xbc633bb2,0xbc703b29,0xbc1d3c3b,0x3c74,0x3c1d3c3b,
0x3b413c6f,0x3c4d3c04,0x3c6ebb59,0x3bfebc50,0x3c6ebb59,0x3bd4bc5c,0xbbd4bc5c,0xbc6ebb59,
0xbb71bc6c,0xbc67bb9b,0xbc3f3c18,0x3ac23c73,0xbc6b3b84,0xbb413c6f,0x3c3f3c18,0x3c5ebbc9,
0x3b903c69,0x3c733a92,0x3bd4bc5c,0xbc26bc33,0x3c67bb9b,0xba42bc73,0xbc6ebb59,0xbbe93c56,
0xbba7bc65,0xbc703b29,0x3c74,0x3c703b29,0xbc633bb2,0x3b903c69,0x3c6ebb59,0xbb11bc71,
0x3c74,0x3c74,0x3c74,0x3c74,0x3c5ebbc9,0x3c53bbf4,0x3c46bc0e,0x3c37bc21,
0xbc37bc21,0xbc53bbf4,0xbc67bb9b,0xbc72baf2,0xbb903c69,0x3c74,0x3b903c69,0x3c093c49,
0x3c72baf2,0x3c53bbf4,0x3c13bc42,0x3b71bc6c,0xbbfebc50,0xbc53bbf4,0xbc74b9c2,0xbc593bde,
0xbc093c49,0x3c74,0x3c093c49,0x3c633bb2,0x3c703b29,0x3c53bbf4,0x3ba7bc65,0xbba7bc65,
0xbb71bc6c,0xbc53bbf4,0xbc6b3b84,0xbbe93c56,0xbc3f3c18,0x3c74,0x3c3f3c18,0x3c6ebb59,
0x3c593bde,0x3c53bbf4,0x3a42bc73,0xbc46bc0e,0x3a42bc73,0xbc53bbf4,0xbc4d3c04,0x3ac23c73,
0xbc633bb2,0x3c74,0x3c633bb2,0x3c26bc33,0x3c2f3c2a,0x3c53bbf4,0xbb71bc6c,0xbc74b9c2,
0x3ba7bc65,0xbc53bbf4,0xbc1d3c3b,0x3c1d3c3b,0xbc733a92,0x3c74,0x3c733a92,0x3b11bc71,
0x3c74,0x3c74,0x3c74,0x3c74,0x3c26bc33,0x3c13bc42,0x3bfebc50,0x3bd4bc5c,
0xbc733a92,0xbc6b3b84,0xbc593bde,0xbc3f3c18,0x3c3f3c18,0x3c633bb2,0x3c733a92,0x3c6ebb59,
0xbb11bc71,0xbbfebc50,0xbc46bc0e,0xbc6ebb59,0xbc093c49,0xbac23c73,0x3bbd3c61,0x3c3f3c18,
0x3c6ebb59,0x3c26bc33,0x3b11bc71,0xbbd4bc5c,0xbc53bbf4,0xbc703b29,0xbc1d3c3b,0x3c74,
0x3b903c69,0x3c593bde,0x3c67bb9b,0x3bd4bc5c,0x3bd4bc5c,0xbbd4bc5c,0xbc6ebb59,0xbc3f3c18,
0xbc633bb2,0xbb413c6f,0x3c2f3c2a,0x3c6ebb59,0x3c633bb2,0x3c37bc21,0xbb71bc6c,0xbc6ebb59,
0xbbd4bc5c,0xbc733a92,0xbb903c69,0x3c3f3c18,0xbb903c69,0x3c4d3c04,0x3c37bc21,0xbbd4bc5c,
0x3c53bbf4,0xbba7bc65,0xbc703b29,0x3c74,0xbc6ebb59,0xbb903c69,0x3c633bb2,0x3bd4bc5c,
0x3c74,0x3c74,0x3c74,0x3c74,0x3ba7bc65,0x3b71bc6c,0x3b11bc71,0x3a42bc73,
0xbc1d3c3b,0xbbe93c56,0xbb903c69,0xbac23c73,0x3c53bbf4,0x3c26bc33,0x3bd4bc5c,0x3b11bc71,
0xbc703b29,0xbc4d3c04,0xbc093c49,0xbb413c6f,0x3c703b29,0x3c67bb9b,0x3c26bc33,0x3b71bc6c,
0xbc53bbf4,0xbc733a92,0xbc3f3c18,0xbb903c69,0x3c1d3c3b,0x3c703b29,0x3c53bbf4,0x3ba7bc65,
0xbba7bc65,0xbc5ebbc9,0xbc633bb2,0xbbbd3c61,0x3c74,0x3c3f3c18,0x3c6ebb59,0x3bd4bc5c,
0x3ba7bc65,0xbc13bc42,0xbc733a92,0xbbe93c56,0xbc1d3c3b,0x3bbd3c61,0x3c733a92,0x3bfebc50,
0x3c53bbf4,0xbb11bc71,0xbc6ebb59,0xbc093c49,0xbc703b29,0xbac23c73,0x3c633bb2,0x3c13bc42,
0x3c703b29,0x3ba7bc65,0xbc53bbf4,0xbc1d3c3b,0xbc53bbf4,0xbc093c49,0x3c3f3c18,0x3c26bc33,
0x3bf4,0x3c74b9c2,0xba42bc73,0xbc733a92,0x3bf4,0x3c72baf2,0xbb71bc6c,0xbc633bb2,
0x3bf4,0x3c6ebb59,0xbbd4bc5c,0xbc3f3c18,0x3bf4,0x3c67bb9b,0xbc13bc42,0xbc093c49,
0x3bf4,0x3c5ebbc9,0xbc37bc21,0xbb903c69,0x3bf4,0x3c53bbf4,0xbc53bbf4,0x3c74,
0x3bf4,0x3c46bc0e,0xbc67bb9b,0x3b903c69,0x3bf4,0x3c37bc21,0xbc72baf2,0x3c093c49,
0x3bf4,0x3c26bc33,0xbc733a92,0x3c3f3c18,0x3bf4,0x3c13bc42,0xbc6b3b84,0x3c633bb2,
0x3bf4,0x3bfebc50,0xbc593bde,0x3c733a92,0x3bf4,0x3bd4bc5c,0xbc3f3c18,0x3c6ebb59,
0x3bf4,0x3ba7bc65,0xbc1d3c3b,0x3c53bbf4,0x3bf4,0x3b71bc6c,0xbbe93c56,0x3c26bc33,
0x3bf4,0x3b11bc71,0xbb903c69,0x3bd4bc5c,0x3bf4,0x3a42bc73,0xbac23c73,0x3b11bc71,
0x3ac23c73,0x3c72baf2,0xbb11bc71,0xbc703b29,0x3be93c56,0x3c46bc0e,0xbc26bc33,0xbc1d3c3b,
0x3c3f3c18,0x3bd4bc5c,0xbc6ebb59,0x3c74,0x3c6b3b84,0xba42bc73,0xbc633bb2,0x3c1d3c3b,
0x3c72baf2,0xbbfebc50,0xbc093c49,0x3c703b29,0x3c53bbf4,0xbc53bbf4,0x3c74,0x3c53bbf4,
0x3c13bc42,0xbc74b9c2,0x3c093c49,0x3ba7bc65,0x3b71bc6c,0xbc593bde,0x3c633bb2,0xbba7bc65,
0xbb11bc71,0xbc093c49,0x3c6ebb59,0xbc53bbf4,0xbbfebc50,0xbac23c73,0x3c26bc33,0xbc703b29,
0xbc46bc0e,0x3bbd3c61,0x3b11bc71,0xbc1d3c3b,0xbc6ebb59,0x3c3f3c18,0xbbd4bc5c,0x3c74,
0xbc703b29,0x3c703b29,0xbc53bbf4,0x3c1d3c3b,0xbc4d3c04,0x3c67bb9b,0xbc733a92,0x3c703b29,
0xbc093c49,0x3c26bc33,0xbc3f3c18,0x3c53bbf4,0xbb413c6f,0x3b71bc6c,0xbb903c69,0x3ba7bc65,
0x3b413c6f,0x3c6ebb59,0xbb71bc6c,0xbc6b3b84,0x3c4d3c04,0x3bd4bc5c,0xbc67bb9b,0xbb413c6f,
0x3c6ebb59,0xbbd4bc5c,0xbc3f3c18,0x3c3f3c18,0x3bfebc50,0xbc6ebb59,0x3ac23c73,0x3c5ebbc9,
0xbb71bc6c,0xbc3f3c18,0x3c593bde,0x3a42bc73,0xbc53bbf4,0x3c74,0x3c53bbf4,0xbc53bbf4,
0xbc6b3b84,0x3c3f3c18,0x3a42bc73,0xbc4d3c04,0xbbe93c56,0x3c6ebb59,0xbc46bc0e,0x3ac23c73,
0x3b903c69,0x3bd4bc5c,0xbc633bb2,0x3c633bb2,0x3c593bde,0xbbd4bc5c,0xbb413c6f,0x3c37bc21,
0x3c67bb9b,0xbc6ebb59,0x3c2f3c2a,0xbb71bc6c,0x3bd4bc5c,0xbc3f3c18,0x3c6ebb59,0xbc6ebb59,
0xbba7bc65,0x3c74,0x3ba7bc65,0xbc1d3c3b,0xbc5ebbc9,0x3c3f3c18,0xbc13bc42,0x3bbd3c61,
0xbc633bb2,0x3c6ebb59,0xbc733a92,0x3c733a92,0xbbbd3c61,0x3bd4bc5c,0xbbe93c56,0x3bfebc50,
0x3b903c69,0x3c67bb9b,0xbba7bc65,0xbc633bb2,0x3c733a92,0xba42bc73,0xbc703b29,0x3b903c69,
0x3bd4bc5c,0xbc6ebb59,0x3c74,0x3c6ebb59,0xbc26bc33,0xbbe93c56,0x3c703b29,0xbb11bc71,
0xbc633bb2,0x3c2f3c2a,0x3ba7bc65,0xbc733a92,0x3c74,0x3c53bbf4,0xbc53bbf4,0x3c74,
0x3c633bb2,0xbb71bc6c,0xbc1d3c3b,0x3c733a92,0x3c26bc33,0xbc74b9c2,0x3c1d3c3b,0x3b11bc71,
0xbbd4bc5c,0xbb903c69,0x3c53bbf4,0xbc6ebb59,0xbc733a92,0x3c4d3c04,0xbba7bc65,0xbb903c69,
0xbb903c69,0x3c37bc21,0xbc703b29,0x3c633bb2,0x3c3f3c18,0xbbd4bc5c,0x3c74,0x3bd4bc5c,
0x3c53bbf4,0xbc703b29,0x3c703b29,0xbc53bbf4,0xbb11bc71,0xbac23c73,0x3ba7bc65,0xbc093c49,
0xbc6ebb59,0x3c633bb2,0xbc53bbf4,0x3c3f3c18,0xbc093c49,0x3c13bc42,0xbc1d3c3b,0x3c26bc33,
0x3bbd3c61,0x3c5ebbc9,0xbbd4bc5c,0xbc593bde,0x3c5ebbc9,0xbbfebc50,0xbc3f3c18,0x3c2f3c2a,
0xbbd4bc5c,0xbc3f3c18,0x3c3f3c18,0x3bd4bc5c,0xbc593bde,0x3c2f3c2a,0x3bd4bc5c,0xbc72baf2,
0x3be93c56,0x3c13bc42,0xbc6ebb59,0x3b413c6f,0x3c53bbf4,0xbc53bbf4,0x3c74,0x3c53bbf4,
0xbbfebc50,0xbbbd3c61,0x3c6ebb59,0xbc37bc21,0xbc4d3c04,0x3c6b3b84,0xbbd4bc5c,0xbbbd3c61,
0x3c093c49,0x3b11bc71,0xbc3f3c18,0x3c733a92,0x3c46bc0e,0xbc74b9c2,0x3c3f3c18,0xbb71bc6c,
0xbc13bc42,0x3ac23c73,0x3bd4bc5c,0xbc4d3c04,0xbc3f3c18,0x3c6ebb59,0xbc6ebb59,0x3c3f3c18,
0x3c1d3c3b,0xbba7bc65,0x3c74,0x3ba7bc65,0x3c37bc21,0xbc593bde,0x3c6ebb59,0xbc74b9c2,
0xbc26bc33,0x3c093c49,0xbbd4bc5c,0x3b903c69,0xbc2f3c2a,0x3c37bc21,0xbc3f3c18,0x3c46bc0e,
0x3be93c56,0x3c53bbf4,0xbbfebc50,0xbc4d3c04,0x3c13bc42,0xbc53bbf4,0xbbbd3c61,0x3c6b3b84,
0xbc6ebb59,0x3c74,0x3c6ebb59,0xbbd4bc5c,0x3b413c6f,0x3c53bbf4,0xbc37bc21,0xbbbd3c61,
0x3c46bc0e,0xbc53bbf4,0xbac23c73,0x3c67bb9b,0xbc53bbf4,0x3c74,0x3c53bbf4,0xbc53bbf4,
0xbac23c73,0x3c53bbf4,0xbc5ebbc9,0x3b413c6f,0x3c67bb9b,0xbc53bbf4,0x3b413c6f,0x3c13bc42,
0xbc26bc33,0x3c74,0x3c26bc33,0xbc733a92,0xbbbd3c61,0x3c53bbf4,0xbc72baf2,0x3c2f3c2a,
0x3c74b9c2,0xbc53bbf4,0x3be93c56,0x3a42bc73,0xbbd4bc5c,0x3c74,0x3bd4bc5c,0xbc3f3c18,
0xbc1d3c3b,0x3c53bbf4,0xbc703b29,0x3c703b29,0x3c6b3b84,0xbc53bbf4,0x3c2f3c2a,0xbbfebc50,
0xbb11bc71,0x3c74,0x3b11bc71,0xbb903c69,0xbc4d3c04,0x3c53bbf4,0xbc593bde,0x3c5ebbc9,
0x3c093c49,0x3c46bc0e,0xbc13bc42,0xbc3f3c18,0x3b11bc71,0xbc74b9c2,0x3ac23c73,0x3c6ebb59,
0xbc3f3c18,0x3c3f3c18,0x3bd4bc5c,0xbc6ebb59,0x3c733a92,0xbb71bc6c,0xbc4d3c04,0x3c3f3c18,
0xbc26bc33,0xbbbd3c61,0x3c74b9c2,0xbbd4bc5c,0x3c74,0x3c53bbf4,0xbc53bbf4,0x3c74,
0x3c26bc33,0xbc72baf2,0x3be93c56,0x3bd4bc5c,0xbc733a92,0x3c2f3c2a,0x3a42bc73,0xbc3f3c18,
0x3c3f3c18,0xbb11bc71,0xbc093c49,0x3c6ebb59,0xbb11bc71,0xbbe93c56,0x3c5ebbc9,0xbc6ebb59,
0xbc093c49,0x3c5ebbc9,0xbc72baf2,0x3c3f3c18,0x3c6ebb59,0xbc6ebb59,0x3c3f3c18,0xbbd4bc5c,
0xbc53bbf4,0x3c1d3c3b,0xbba7bc65,0x3c74,0x3b903c69,0xba42bc73,0xbb413c6f,0x3bd4bc5c,
0x3bd4bc5c,0xbc093c49,0x3c26bc33,0xbc3f3c18,0xbc633bb2,0x3c67bb9b,0xbc6b3b84,0x3c6ebb59,
0x3c1d3c3b,0x3c37bc21,0xbc26bc33,0xbc2f3c2a,0xbba7bc65,0xbc593bde,0x3c093c49,0x3c37bc21,
0x3c74,0x3c6ebb59,0xbbd4bc5c,0xbc3f3c18,0x3ba7bc65,0xbc74b9c2,0x3b903c69,0x3c46bc0e,
0xbc1d3c3b,0x3c6b3b84,0xbb11bc71,0xbc4d3c04,0x3c53bbf4,0xbc53bbf4,0x3c74,0x3c53bbf4,
0xbc703b29,0x3c2f3c2a,0x3b11bc71,0xbc593bde,0x3c703b29,0xbbfebc50,0xbb903c69,0x3c5ebbc9,
0xbc53bbf4,0x3b903c69,0x3bd4bc5c,0xbc633bb2,0x3c1d3c3b,0xba42bc73,0xbc093c49,0x3c67bb9b,
0xbba7bc65,0xbb413c6f,0x3c26bc33,0xbc6b3b84,0x3c74,0x3bd4bc5c,0xbc3f3c18,0x3c6ebb59,
0x3ba7bc65,0xbc1d3c3b,0x3c53bbf4,0xbc703b29,0xbc1d3c3b,0x3c46bc0e,0xbc633bb2,0x3c72baf2,
0x3c53bbf4,0xbc633bb2,0x3c6ebb59,0xbc733a92,0xbc703b29,0x3c72baf2,0xbc733a92,0x3c74b9c2,
0x3bf4,0xba42bc73,0x3ac23c73,0xbb11bc71,0x3bf4,0xbb11bc71,0x3b903c69,0xbbd4bc5c,
0x3bf4,0xbb71bc6c,0x3be93c56,0xbc26bc33,0x3bf4,0xbba7bc65,0x3c1d3c3b,0xbc53bbf4,
0x3bf4,0xbbd4bc5c,0x3c3f3c18,0xbc6ebb59,0x3bf4,0xbbfebc50,0x3c593bde,0xbc733a92,
0x3bf4,0xbc13bc42,0x3c6b3b84,0xbc633bb2,0x3bf4,0xbc26bc33,0x3c733a92,0xbc3f3c18,
0x3bf4,0xbc37bc21,0x3c72baf2,0xbc093c49,0x3bf4,0xbc46bc0e,0x3c67bb9b,0xbb903c69,
0x3bf4,0xbc53bbf4,0x3c53bbf4,0x3c74,0x3bf4,0xbc5ebbc9,0x3c37bc21,0x3b903c69,
0x3bf4,0xbc67bb9b,0x3c13bc42,0x3c093c49,0x3bf4,0xbc6ebb59,0x3bd4bc5c,0x3c3f3c18,
0x3bf4,0xbc72baf2,0x3b71bc6c,0x3c633bb2,0x3bf4,0xbc74b9c2,0x3a42bc73,0x3c733a92,
0x3b413c6f,0xbb71bc6c,0x3b903c69,0xbba7bc65,0x3c093c49,0xbc26bc33,0x3c3f3c18,0xbc53bbf4,
0x3c4d3c04,0xbc67bb9b,0x3c733a92,0xbc703b29,0x3c703b29,0xbc703b29,0x3c53bbf4,0xbc1d3c3b,
0x3c6ebb59,0xbc3f3c18,0x3bd4bc5c,0x3c74,0x3c46bc0e,0xbbbd3c61,0xbb11bc71,0x3c1d3c3b,
0x3bfebc50,0x3ac23c73,0xbc26bc33,0x3c703b29,0x3b11bc71,0x3c093c49,0xbc6ebb59,0x3c53bbf4,
0xbb71bc6c,0x3c593bde,0xbc633bb2,0x3ba7bc65,0xbc13bc42,0x3c74b9c2,0xbc093c49,0xbba7bc65,
0xbc53bbf4,0x3c53bbf4,0x3c74,0xbc53bbf4,0xbc72baf2,0x3bfebc50,0x3c093c49,0xbc703b29,
0xbc6b3b84,0x3a42bc73,0x3c633bb2,0xbc1d3c3b,0xbc3f3c18,0xbbd4bc5c,0x3c6ebb59,0x3c74,
0xbbe93c56,0xbc46bc0e,0x3c26bc33,0x3c1d3c3b,0xbac23c73,0xbc72baf2,0x3b11bc71,0x3c703b29,
0x3bbd3c61,0xbbd4bc5c,0x3be93c56,0xbbfebc50,0x3c633bb2,0xbc6ebb59,0x3c733a92,0xbc733a92,
0x3c5ebbc9,0xbc3f3c18,0x3c13bc42,0xbbbd3c61,0x3ba7bc65,0x3c74,0xbba7bc65,0x3c1d3c3b,
0xbbd4bc5c,0x3c3f3c18,0xbc6ebb59,0x3c6ebb59,0xbc67bb9b,0x3c6ebb59,0xbc2f3c2a,0x3b71bc6c,
0xbc593bde,0x3bd4bc5c,0x3b413c6f,0xbc37bc21,0xbb903c69,0xbbd4bc5c,0x3c633bb2,0xbc633bb2,
0x3be93c56,0xbc6ebb59,0x3c46bc0e,0xbac23c73,0x3c6b3b84,0xbc3f3c18,0xba42bc73,0x3c4d3c04,
0x3c53bbf4,0x3c74,0xbc53bbf4,0x3c53bbf4,0x3b71bc6c,0x3c3f3c18,0xbc593bde,0xba42bc73,
0xbbfebc50,0x3c6ebb59,0xbac23c73,0xbc5ebbc9,0xbc6ebb59,0x3bd4bc5c,0x3c3f3c18,0xbc3f3c18,
0xbc4d3c04,0xbbd4bc5c,0x3c67bb9b,0x3b413c6f,0xbb413c6f,0xbc6ebb59,0x3b71bc6c,0x3c6b3b84,
0x3c093c49,0xbc13bc42,0x3c1d3c3b,0xbc26bc33,0x3c6ebb59,0xbc633bb2,0x3c53bbf4,0xbc3f3c18,
0x3b11bc71,0x3ac23c73,0xbba7bc65,0x3c093c49,0xbc53bbf4,0x3c703b29,0xbc703b29,0x3c53bbf4,
0xbc3f3c18,0x3bd4bc5c,0x3c74,0xbbd4bc5c,0x3b903c69,0xbc37bc21,0x3c703b29,0xbc633bb2,
0x3c733a92,0xbc4d3c04,0x3ba7bc65,0x3b903c69,0x3bd4bc5c,0x3b903c69,0xbc53bbf4,0x3c6ebb59,
0xbc26bc33,0x3c74b9c2,0xbc1d3c3b,0xbb11bc71,0xbc633bb2,0x3b71bc6c,0x3c1d3c3b,0xbc733a92,
0x3c74,0xbc53bbf4,0x3c53bbf4,0x3c74,0x3c633bb2,0xbc2f3c2a,0xbba7bc65,0x3c733a92,
0x3c26bc33,0x3be93c56,0xbc703b29,0x3b11bc71,0xbbd4bc5c,0x3c6ebb59,0x3c74,0xbc6ebb59,
0xbc733a92,0x3a42bc73,0x3c703b29,0xbb903c69,0xbb903c69,0xbc67bb9b,0x3ba7bc65,0x3c633bb2,
0x3c2f3c2a,0xbc37bc21,0x3c3f3c18,0xbc46bc0e,0x3c26bc33,0xbc093c49,0x3bd4bc5c,0xbb903c69,
0xbc37bc21,0x3c593bde,0xbc6ebb59,0x3c74b9c2,0xbc1d3c3b,0x3ba7bc65,0x3c74,0xbba7bc65,
0x3c3f3c18,0xbc6ebb59,0x3c6ebb59,0xbc3f3c18,0x3c13bc42,0xbac23c73,0xbbd4bc5c,0x3c4d3c04,
0xbc46bc0e,0x3c74b9c2,0xbc3f3c18,0x3b71bc6c,0xbc093c49,0xbb11bc71,0x3c3f3c18,0xbc733a92,
0x3c4d3c04,0xbc6b3b84,0x3bd4bc5c,0x3bbd3c61,0x3bfebc50,0x3bbd3c61,0xbc6ebb59,0x3c37bc21,
0xbc53bbf4,0x3c53bbf4,0x3c74,0xbc53bbf4,0xbbe93c56,0xbc13bc42,0x3c6ebb59,0xbb413c6f,
0x3c593bde,0xbc2f3c2a,0xbbd4bc5c,0x3c72baf2,0x3bd4bc5c,0x3c3f3c18,0xbc3f3c18,0xbbd4bc5c,
0xbc5ebbc9,0x3bfebc50,0x3c3f3c18,0xbc2f3c2a,0xbbbd3c61,0xbc5ebbc9,0x3bd4bc5c,0x3c593bde,
0x3c4d3c04,0xbc53bbf4,0x3c593bde,0xbc5ebbc9,0x3b11bc71,0x3c74,0xbb11bc71,0x3b903c69,
0xbc6b3b84,0x3c53bbf4,0xbc2f3c2a,0x3bfebc50,0x3c1d3c3b,0xbc53bbf4,0x3c703b29,0xbc703b29,
0x3bd4bc5c,0x3c74,0xbbd4bc5c,0x3c3f3c18,0xbc74b9c2,0x3c53bbf4,0xbbe93c56,0xba42bc73,
0x3bbd3c61,0xbc53bbf4,0x3c72baf2,0xbc2f3c2a,0x3c26bc33,0x3c74,0xbc26bc33,0x3c733a92,
0xbc67bb9b,0x3c53bbf4,0xbb413c6f,0xbc13bc42,0x3ac23c73,0xbc53bbf4,0x3c5ebbc9,0xbb413c6f,
0x3c53bbf4,0x3c74,0xbc53bbf4,0x3c53bbf4,0xbc46bc0e,0x3c53bbf4,0x3ac23c73,0xbc67bb9b,
0xbb413c6f,0xbc53bbf4,0x3c37bc21,0x3bbd3c61,0x3c6ebb59,0x3c74,0xbc6ebb59,0x3bd4bc5c,
0xbc13bc42,0x3c53bbf4,0x3bbd3c61,0xbc6b3b84,0xbbe93c56,0xbc53bbf4,0x3bfebc50,0x3c4d3c04,
0x3c633bb2,0xbc67bb9b,0x3c6b3b84,0xbc6ebb59,0xbbd4bc5c,0x3c093c49,0xbc26bc33,0x3c3f3c18,
0xbb903c69,0x3a42bc73,0x3b413c6f,0xbbd4bc5c,0x3c53bbf4,0xbc1d3c3b,0x3ba7bc65,0x3c74,
0xbc6ebb59,0x3c6ebb59,0xbc3f3c18,0x3bd4bc5c,0x3c093c49,0xbc5ebbc9,0x3c72baf2,0xbc3f3c18,
0x3b11bc71,0x3be93c56,0xbc5ebbc9,0x3c6ebb59,0xbc3f3c18,0x3b11bc71,0x3c093c49,0xbc6ebb59,
0x3c733a92,0xbc2f3c2a,0xba42bc73,0x3c3f3c18,0xbc26bc33,0x3c72baf2,0xbbe93c56,0xbbd4bc5c,
0x3c74,0xbc53bbf4,0x3c53bbf4,0x3c74,0x3c26bc33,0x3bbd3c61,0xbc74b9c2,0x3bd4bc5c,
0xbc733a92,0x3b71bc6c,0x3c4d3c04,0xbc3f3c18,0x3c3f3c18,0xbc3f3c18,0xbbd4bc5c,0x3c6ebb59,
0xbb11bc71,0x3c74b9c2,0xbac23c73,0xbc6ebb59,0xbc093c49,0xbc46bc0e,0x3c13bc42,0x3c3f3c18,
0x3c703b29,0xbc72baf2,0x3c733a92,0xbc74b9c2,0xbc53bbf4,0x3c633bb2,0xbc6ebb59,0x3c733a92,
0x3c1d3c3b,0xbc46bc0e,0x3c633bb2,0xbc72baf2,0xbba7bc65,0x3c1d3c3b,0xbc53bbf4,0x3c703b29,
0x3c74,0xbbd4bc5c,0x3c3f3c18,0xbc6ebb59,0x3ba7bc65,0x3b413c6f,0xbc26bc33,0x3c6b3b84,
0xbc1d3c3b,0x3a42bc73,0x3c093c49,0xbc67bb9b,0x3c53bbf4,0xbb903c69,0xbbd4bc5c,0x3c633bb2,
0xbc703b29,0x3bfebc50,0x3b903c69,0xbc5ebbc9,0x3c703b29,0xbc2f3c2a,0xbb11bc71,0x3c593bde,
0xbc53bbf4,0x3c53bbf4,0x3c74,0xbc53bbf4,0x3c1d3c3b,0xbc6b3b84,0x3b11bc71,0x3c4d3c04,
0xbba7bc65,0x3c74b9c2,0xbb903c69,0xbc46bc0e,0x3c74,0xbc6ebb59,0x3bd4bc5c,0x3c3f3c18,
0x3ba7bc65,0x3c593bde,0xbc093c49,0xbc37bc21,0xbc1d3c3b,0xbc37bc21,0x3c26bc33,0x3c2f3c2a,
0x3f803f80,0x3f803f80,0x3f803f80,0x3f803f80,0x3f7f3f80,0x3f753f7b,0x3f613f6c,0x3f443f54,
0x3f7b3f80,0x3f543f6c,0x3f0b3f33,0x3e323ebb,0x3f753f80,0x3f203f54,0x3d993ebb,0xbf00be64,
0x3f6c3f80,0x3ebb3f33,0xbed3bccc,0xbf71bf3c,0x3f613f80,0x3d993f0b,0xbf4cbed3,0xbf71bf7d,
0x3f543f80,0xbe643ebb,0xbf7dbf3c,0xbf00bf67,0x3f443f80,0xbf003e32,0xbf71bf71,0x3e32bf00,
0x3f333f80,0xbf3cbccc,0xbf29bf80,0x3f443d99,0x3f203f80,0xbf67be64,0xbe64bf67,0x3f803f20,
0x3f0b3f80,0xbf7dbed3,0x3e8bbf29,0x3f443f75,0x3eea3f80,0xbf7dbf15,0x3f33bea3,0x3e323f75,
0x3ebb3f80,0xbf67bf3c,0x3f753d99,0xbf003f20,0x3e8b3f80,0xbf3cbf5a,0x3f7b3eea,0xbf713d99,
0x3e323f80,0xbf00bf71,0x3f443f44,0xbf71bf00,0x3d993f80,0xbe64bf7d,0x3ebb3f75,0xbf00bf67,
0x3f803f80,0x3f803f80,0x3f803f80,0x3f803f80,0x3f203f33,0x3eea3f0b,0x3e8b3ebb,0x3d993e32,
0xbe64bccc,0xbf15bed3,0xbf5abf3c,0xbf7dbf71,0xbf67bf3c,0xbf7dbf7d,0xbf3cbf67,0xbe64bf00,
0xbf67bf80,0xbea3bf29,0x3eea3d99,0x3f753f44,0xbe64bf29,0x3f333e8b,0x3f7b3f75,0x3ebb3f44,
0x3f203d99,0x3f753f75,0x3d993f20,0xbf67bf00,0x3f803f44,0x3e323f44,0xbf71bf00,0xbf00bf71,
0x3f203f7f,0xbf4cbdff,0xbf15bf7d,0x3f543e32,0xbe643f20,0xbf67bf67,0x3f20be64,0x3f203f80,
0xbf67bdff,0xbcccbf5a,0x3f6c3f54,0xbf3c3e32,0xbf67bf4c,0x3f61bccc,0xbdff3f54,0xbf3cbf71,
0xbe64bf7d,0x3f543f54,0xbf7dbe64,0x3f20bf00,0x3f20bf15,0xbdff3f6c,0xbed3bf7d,0x3f543f44,
0x3f803e32,0xbf713e32,0x3f44bf00,0xbf003f44,0x3f203f54,0xbf3cbf3c,0x3f543f20,0xbf67bf00,
0x0,0x0,0x0,0x0,0xba8b0000,0xbabfb99b,0xba33b9d1,0x39dcbaf7,
0xb99b0000,0xbaf7b9d1,0xb9deba81,0xb93d38e0,0xbabf0000,0xbac6baf7,0x37c138e0,0xa580390e,
0xb9d10000,0x38e0ba81,0x3a58b7ee,0x3ae13aad,0xba330000,0x37c1b9de,0xb9873a58,0x3ae1ba10,
0xbaf70000,0x390e38e0,0xba103aad,0x25e03ab4,0x39dc0000,0xa580b93d,0x3ae13ae1,0xb93d25e0,
0xba810000,0x3aadb7ee,0xbac83aa3,0x39dc37c1,0xbac60000,0x3ab4390e,0x390e3ab4,0xbac6,
0xb9de0000,0xba103a58,0xba29bac8,0x39dcbabf,0xba570000,0xba10bae0,0xba81b905,0xb93dbabf,
0x38e00000,0x3ab43aad,0xbabf37c1,0xa580bac6,0xba290000,0x3aadbae3,0xb99bba57,0x3ae137c1,
0xb93d0000,0x25e03ae1,0x39dc39dc,0x3ae1a580,0x37c10000,0x390eba10,0x38e0babf,0x25e03ab4,
0x0,0x0,0x0,0x0,0xbac6ba81,0xba57b9de,0xba2938e0,0x37c1b93d,
0x390eb7ee,0xbae03a58,0xbae33aad,0xba103ae1,0x3ab43aad,0xba10ba10,0x3aad3ab4,0x390e25e0,
0x3ab43aa3,0xb905bac8,0xba5737c1,0xbabf39dc,0x390ebac8,0xba81ba29,0xb99bbabf,0x38e039dc,
0xbac637c1,0xbabfbabf,0x37c1bac6,0x3ab4a580,0x39dc,0xb93d39dc,0x3ae1a580,0x25e03ae1,
0xbac6ba8b,0xb9873930,0xbae0ba10,0xbaf7b93d,0x390ebac6,0x3ab43ab4,0xbac6390e,0xbac60000,
0x3ab43930,0xb7eebae3,0xb9d1baf7,0x3aadb93d,0x3ab4b987,0xba33b7ee,0x3930baf7,0x3aad3ae1,
0x390eba10,0xbaf7baf7,0xba10390e,0xbac625e0,0xbac6bae0,0x3930b9d1,0x3a58ba10,0xbaf739dc,
0xb93d,0x3ae1b93d,0x39dc25e0,0xa58039dc,0xbac6baf7,0x3aad3aad,0xbaf7bac6,0x3ab4a580,
0xbccc3f80,0x3d99bf80,0xbdff3f7f,0x3e32bf7d,0xbdff3f80,0x3ebbbf78,0xbf153f61,0x3f44bf3c,
0xbe643f80,0x3f20bf67,0xbf673f20,0x3f80be64,0xbea33f80,0x3f54bf4c,0xbf803e8b,0x3f443ebb,
0xbed33f80,0x3f75bf29,0xbf5abdff,0x3e323f54,0xbf003f80,0x3f80bf00,0xbf00bf00,0xbf003f80,
0xbf153f80,0x3f75bea3,0xbcccbf4c,0xbf713f54,0xbf293f80,0x3f54bdff,0x3eeabf78,0xbf713ebb,
0xbf3c3f80,0x3f203d99,0x3f54bf7d,0xbf00be64,0xbf4c3f80,0x3ebb3e8b,0x3f7fbf5a,0x3e32bf3c,
0xbf5a3f80,0x3d993eea,0x3f6cbf15,0x3f44bf7d,0xbf673f80,0xbe643f20,0x3f20be64,0x3f80bf67,
0xbf713f80,0xbf003f44,0x3e323e32,0x3f44bf00,0xbf783f80,0xbf3c3f61,0xbea33f0b,0x3e323d99,
0xbf7d3f80,0xbf673f75,0xbf3c3f54,0xbf003f20,0xbf803f80,0xbf7d3f7f,0xbf783f7b,0xbf713f75,
0xbe643f7b,0x3e8bbf78,0xbea33f75,0x3ebbbf71,0xbf673f0b,0x3f7bbea3,0xbf803d99,0x3f753e32,
0xbf67be64,0x3f203f20,0xbe64bf67,0xbe643f80,0xbe64bf5a,0xbed33f7f,0x3f61bf3c,0xbf7d3e32,
0x3f20bf78,0xbf803eea,0x3f333ebb,0x3d99bf71,0x3f80bf00,0xbf00bf00,0xbf003f80,0x3f80bf00,
0x3f203e8b,0x3f0bbf80,0xbf783ebb,0x3d993f44,0xbe643f61,0x3f7fbf15,0xbcccbf3c,0xbf7d3f44,
0xbf673f75,0x3ebb3ebb,0x3f75bf67,0xbe64bf00,0xbf673eea,0xbf293f7b,0x3f0b3d99,0x3f75bf71,
0xbe64bea3,0xbf783f33,0xbf293f75,0x3ebb3e32,0x3f20bf67,0xbe64be64,0xbf673f20,0xbf673f80,
0x3f80bf71,0x3f44bf71,0x3e32bf00,0xbf003e32,0x3f20bed3,0x3f6cbf4c,0x3f7fbf7d,0x3f54bf71,
0xbe643ebb,0x3d993d99,0x3ebbbe64,0x3f20bf00,0xbf673f6c,0xbf5a3f61,0xbf4c3f54,0xbf3c3f44,
0xb7ee0000,0x37c13aa3,0x3930ba8b,0xb93dba10,0x39300000,0x38e0b9ac,0xbae0ba33,0x39dc3aad,
0x390e0000,0xbac63ab4,0x3ab4bac6,0x390e,0xb9050000,0xbaf7b987,0x3aa3ba29,0x39dc38e0,
0x3a580000,0xbabfbac8,0xbae33930,0xb93dbaf7,0xa5800000,0x25e0,0x25e0a580,0xa5800000,
0xbae00000,0xbabfb905,0xb7eeb987,0x3ae1baf7,0xbac80000,0xbaf73930,0xba57b9ac,0x3ae138e0,
0x3aad0000,0xbac637c1,0xbaf7ba10,0x25e0390e,0xb9870000,0x38e0ba29,0xba8bbae3,0xb93d3aad,
0xbae30000,0x37c1ba57,0xb9d1bae0,0x39dcba10,0x3ab40000,0x390ebac6,0xbac6390e,0x3ab4,
0x3ae10000,0xa58039dc,0xb93db93d,0x39dc25e0,0xb9ac0000,0x3aadba33,0xb905b9de,0xb93d37c1,
0xba100000,0x3ab4babf,0x3aadbaf7,0xa580bac6,0x3aa30000,0xba10ba8b,0xb9acb99b,0x3ae1babf,
0x390eb99b,0xba29b9ac,0xb905babf,0x38e03ae1,0x3ab4b9de,0xb99bb905,0x3aa337c1,0xbabfb93d,
0x3ab4390e,0xbac6bac6,0x390e3ab4,0x390e0000,0x390ebae3,0x3a58ba8b,0xba333aad,0xba10b93d,
0xbac6b9ac,0x3aa3ba57,0xba8138e0,0x37c13ae1,0x25e0,0x25e0a580,0xa5800000,0x25e0,
0xbac6ba29,0xb9de3aa3,0xb9ac38e0,0x37c139dc,0x390eba33,0xba8bbae0,0xb7ee3aad,0xba1039dc,
0x3ab4babf,0x38e038e0,0xbabf3ab4,0x390ea580,0x3ab4ba57,0xbac8b99b,0xb9de37c1,0xbabf3ae1,
0x390eb905,0xb9acba81,0xbac8babf,0x38e0b93d,0xbac63ab4,0x390e390e,0x3ab4bac6,0x3ab40000,
0x3ae1,0x39dc3ae1,0xb93da580,0x25e0b93d,0xbac63a58,0xb9d1b987,0xba8bba10,0xbaf73ae1,
0x390e38e0,0x37c137c1,0x38e0390e,0xbac625e0,0x3ab4b9d1,0xbae3ba33,0xb987baf7,0x3aad39dc,
0x0,0x0,0x0,0x0,0x3dcc0000,0x3e973e4b,0x3ef53ec7,0x3f253f10,
0x3e4b0000,0x3f103ec7,0x3f573f37,0x3f7c3f6e,0x3e970000,0x3f483f10,0x3f7f3f6e,0x3f5e3f7a,
0x3ec70000,0x3f6e3f37,0x3f693f80,0x3eaf3f2e,0x3ef50000,0x3f7f3f57,0x3f1b3f69,0xbeaf3e19,
0x3f100000,0x3f7a3f6e,0x3e193f2e,0xbf5ebede,0x3f250000,0x3f5e3f7c,0xbeaf3eaf,0xbf7cbf5e,
0x3f370000,0x3f2e3f80,0xbf40bd4c,0xbf25bf7f,0x3f480000,0x3ede3f7a,0xbf7abede,0xbf48,
0x3f570000,0x3e193f69,0xbf76bf40,0x3f25be97,0x3f640000,0xbe193f50,0xbf37bf73,0x3f7c3e97,
0x3f6e0000,0xbede3f2e,0xbe97bf7f,0x3f5e3f48,0x3f760000,0xbf2e3f05,0x3e4bbf64,0x3eaf3f7f,
0x3f7c0000,0xbf5e3eaf,0x3f25bf25,0xbeaf3f5e,0x3f7f0000,0xbf7a3e19,0x3f6ebe97,0xbf5e3ede,
0x0,0x0,0x0,0x0,0x3f483f37,0x3f643f57,0x3f763f6e,0x3f7f3f7c,
0x3f7a3f80,0x3f503f69,0x3f053f2e,0x3e193eaf,0x3ede3f2e,0xbe193e19,0xbf2ebede,0xbf7abf5e,
0xbedebd4c,0xbf73bf40,0xbf64bf7f,0xbe97bf25,0xbf7abf40,0xbf37bf76,0x3e4bbe97,0x3f6e3f25,
0xbf48bf7f,0x3e97be97,0x3f7f3f48,0x3ede3f5e,0xbf25,0x3f7c3f25,0x3eaf3f5e,0xbf5ebeaf,
0x3f483dcc,0x3f1b3f7e,0xbf50be19,0xbf10bf7c,0x3f7a3f48,0xbede3ede,0xbf48bf7a,0x3f480000,
0x3ede3f7e,0xbf80bf05,0x3ec7bf10,0x3f2e3f7c,0xbede3f1b,0xbef5bf80,0x3f7e3f10,0xbf2e3eaf,
0xbf7abe19,0x3f10bf10,0x3e193f7a,0xbf48bf5e,0xbf48bf50,0x3f7e3ec7,0xbf693e19,0x3f10bf25,
0xbf7c,0x3eaf3f7c,0xbf25bf5e,0x3f5e3f25,0x3f48bf10,0xbf2e3f2e,0x3f10bf48,0xbede3f5e,
0x0,0x0,0x0,0x0,0xb82e0000,0xb92fb8c9,0xb989b979,0xbae53a57,
0xb8c90000,0x3a57b979,0x393f3a86,0x39e33a9b,0xb92f0000,0x3a183a57,0x3a913a9b,0xba98bad6,
0xb9790000,0x3a9b3a86,0x3ab1b9a3,0x396a39fe,0xb9890000,0x3a91393f,0xbada3ab1,0xb96ab9c3,
0x3a570000,0xbad63a9b,0xb9c339fe,0x3a98b998,0xbae50000,0xba9839e3,0xb96a396a,0xb9e33a98,
0x3a860000,0x39feb9a3,0x3965b82d,0x3ae5ba91,0x3a180000,0x3998bad6,0x3ad6b998,0xba18,
0x393f0000,0xb9c33ab1,0xbadd3965,0xbae5392f,0xba450000,0x39c3ba13,0xba863aa9,0x39e3b92f,
0x3a9b0000,0xb99839fe,0x392fba91,0xba983a18,0x3add0000,0xb9fe3afa,0xb8c93a45,0x396a3a91,
0x39e30000,0x3a98396a,0xbae53ae5,0xb96aba98,0x3a910000,0x3ad6b9c3,0x3a9b392f,0x3a983998,
0x0,0x0,0x0,0x0,0x3a183a86,0xba45393f,0x3add3a9b,0x3a9139e3,
0xbad6b9a3,0xba133ab1,0x3afa39fe,0xb9c3396a,0x399839fe,0x39c3b9c3,0xb9feb998,0x3ad63a98,
0xb998b82d,0x3aa93965,0x3a45ba91,0x392f3ae5,0x3ad63965,0xba86badd,0xb8c9392f,0x3a9bbae5,
0xba18ba91,0xb92f392f,0x3a913a18,0x3998ba98,0x3ae5,0x39e3bae5,0x396aba98,0x3a98b96a,
0x3a18b82e,0xbada3859,0x3a1339c3,0xba57b9e3,0xbad63a18,0xb9983998,0xba183ad6,0x3a180000,
0x39983859,0x39a3bafa,0xb979ba57,0x39fe39e3,0xb998bada,0x398939a3,0x38593a57,0xb9fe396a,
0x3ad639c3,0x3a57ba57,0xb9c3bad6,0xba183a98,0xba183a13,0x3859b979,0xbab1b9c3,0x3a573ae5,
0xb9e3,0x396a39e3,0x3ae53a98,0xba98bae5,0x3a18ba57,0xb9fe39fe,0x3a57ba18,0xb998ba98,
0x3f800000,0xbf7fbd4c,0x3f7e3dcc,0xbf7cbe19,0x3f7e0000,0xbf6ebe7d,0x3f503ef5,0xbf25bf2e,
0x3f7a0000,0xbf48bede,0x3ede3f48,0xbf7a,0x3f730000,0xbf10bf1b,0xbd4c3f76,0x3f25bf6e,
0x3f690000,0xbe97bf40,0xbf053f7e,0x3f7cbf10,0x3f5e0000,0xbf5e,0xbf5e3f5e,0x3f5e0000,
0x3f500000,0x3e97bf73,0xbf803f1b,0x3eaf3f10,0x3f400000,0x3f10bf7e,0xbf643e7d,0xbeaf3f6e,
0x3f2e0000,0x3f48bf7f,0xbf10be19,0xbf5e3f7a,0x3f1b0000,0x3f6ebf76,0xbdccbf05,0xbf7c3f2e,
0x3f050000,0x3f7fbf64,0x3ec7bf50,0xbf253e19,0x3ede0000,0x3f7abf48,0x3f48bf7a,0xbede,
0x3eaf0000,0x3f5ebf25,0x3f7cbf7c,0x3f25bf5e,0x3e7d0000,0x3f2ebef5,0x3f73bf57,0x3f7cbf7f,
0x3e190000,0x3edebe97,0x3f2ebf10,0x3f5ebf48,0x3d4c0000,0x3e19bdcc,0x3e7dbe4b,0x3eafbe97,
0x3f7a3e4b,0xbf76be7d,0x3f733e97,0xbf6ebeaf,0x3ede3f57,0xbe4bbf73,0xbd4c3f7f,0x3e97bf7c,
0xbede3f7a,0x3f48bf48,0xbf7a3ede,0x3f7a0000,0xbf7a3f05,0x3f693dcc,0xbef5bf2e,0xbe193f7c,
0xbf48be7d,0x3d4c3f64,0x3f37bf6e,0xbf7f3eaf,0xbf5e,0xbf5e3f5e,0x3f5e0000,0xbf5e,
0x3f48bf76,0xbf573d4c,0xbe7d3f6e,0x3f7fbf25,0x3f7abef5,0x3dccbf50,0xbf803f2e,0x3e193f25,
0x3ede3e97,0x3f6ebf6e,0xbe97bede,0xbf7a3f5e,0xbede3f64,0x3f40be4b,0x3f57bf7f,0xbe97beaf,
0xbf7a3f73,0xbe7d3f37,0x3f40be97,0x3f6ebf7c,0xbf483ede,0xbf7a3f7a,0xbede3f48,0x3ede0000,
0xbeaf,0xbf253eaf,0xbf7c3f5e,0xbf5e3f7c,0x3f48bf69,0x3ec7bf1b,0xbdccbe19,0xbf103eaf,
0x3f7abf6e,0x3f7fbf7f,0x3f6ebf7a,0x3f48bf5e,0x3edebec7,0x3f05bef5,0x3f1bbf10,0x3f2ebf25,
0xb9a30000,0xba91b82d,0x3859b82e,0xb9e339c3,0x38590000,0xba9b39a4,0xba13b989,0x3ae5b9fe,
0xbad60000,0xba18b998,0x39983a18,0x3ad6,0xbaa90000,0xba573ada,0xb82d3add,0xbae5ba9b,
0x3ab10000,0x392f3965,0xbafa3859,0x39e3ba57,0xba980000,0x3a98,0x3a98ba98,0xba980000,
0xba130000,0xb92f3aa9,0x39a3bada,0x396a3a57,0xb9650000,0x3a57b859,0x3a45b9a4,0xb96a3a9b,
0x39fe0000,0x3a18ba91,0xba5739c3,0x3a98bad6,0xbada0000,0x3a9bbadd,0x382ebafa,0xb9e339fe,
0x3afa0000,0x3a913a45,0xb9793a13,0x3ae5b9c3,0x39980000,0xbad6ba18,0x3a183ad6,0xb998,
0x396a0000,0xba983ae5,0x39e3b9e3,0xbae53a98,0xb9a40000,0x39fe3989,0xbaa9b93f,0x39e3ba91,
0xb9c30000,0x3998392f,0x39feba57,0xba98ba18,0x382d0000,0xb9c3382e,0xb9a438c9,0x396a392f,
0xbad6b8c9,0xbadd39a4,0xbaa9b92f,0xba9bb96a,0x3998393f,0x38c93aa9,0xb82d3a91,0xb92fb9e3,
0xb998bad6,0x3a18ba18,0x3ad63998,0xbad60000,0x3ad63afa,0x3ab1b82e,0x3989b9fe,0x39c339e3,
0xba1839a4,0x382dba45,0x3a86ba9b,0xba91396a,0x3a98,0x3a98ba98,0xba980000,0x3a98,
0x3a18badd,0xb93f382d,0x39a43a9b,0x3a913ae5,0xbad63989,0xb82e3a13,0x39a339fe,0xb9c3bae5,
0x3998b92f,0x3a9bba9b,0x392fb998,0x3ad6ba98,0xb998ba45,0xb96538c9,0x393fba91,0x392fb96a,
0x3ad6baa9,0x39a43a86,0xb965392f,0x3a9bb9e3,0xba183998,0x3ad6bad6,0xb9983a18,0x39980000,
0xb96a,0x3ae5396a,0xb9e3ba98,0x3a9839e3,0x3a18bab1,0xb9793ada,0x382e39c3,0xba57396a,
0xbad6ba9b,0x3a91ba91,0x3a9b3ad6,0x3a183a98,0x39983979,0x3afa3989,0xbadaba57,0x39fe3ae5,
};

typedef unsigned short u16;
typedef unsigned long long u64;
typedef __attribute__((ext_vector_type(8))) short bf16x8;
typedef __attribute__((ext_vector_type(4))) short s16x4;
typedef __attribute__((ext_vector_type(4))) float f32x4;
typedef __attribute__((ext_vector_type(2))) float f32x2;
typedef __attribute__((ext_vector_type(4))) unsigned u32x4;
typedef __attribute__((ext_vector_type(2))) __bf16 bf16x2_t;
typedef __attribute__((address_space(3))) s16x4* lds_s16x4_p;
typedef __attribute__((address_space(3))) void* lds_void_p;
#define DEVINL __device__ __forceinline__

#define STAMP(i)
#define STAMP_FLUSH()
#define STAMP2(a, b) do { if (s == 0) { STAMP(a); } else { STAMP(b); } } while (0)
typedef unsigned stv_t;

#define IN_POL ""
#define ST_POL "sc1"
#define MFMA16(a, b, c) __builtin_amdgcn_mfma_f32_16x16x32_bf16((a), (b), (c), 0, 0, 0)
#define MFMAU(a, b, c) MFMA16(__builtin_bit_cast(bf16x8, a), __builtin_bit_cast(bf16x8, b), c)
#define SB0() __builtin_amdgcn_sched_barrier(0)
constexpr unsigned OOB = 0x7ffffff0u;
constexpr int SC1 = 16;

template <int N> struct IC { static constexpr int v = N; };
template <int... I, class F> DEVINL void static_for_impl(std::integer_sequence<int, I...>, F&& f) { (f(IC<I>{}), ...); }
template <int N, class F> DEVINL void static_for(F&& f) { static_for_impl(std::make_integer_sequence<int, N>{}, f); }
DEVINL void bar() { asm volatile("" ::: "memory"); __builtin_amdgcn_s_barrier(); asm volatile("" ::: "memory"); }
template <int N> DEVINL void wait_vm() { asm volatile("s_waitcnt vmcnt(%0)" ::"n"(N) : "memory"); }
DEVINL void wait_lds() { __builtin_amdgcn_s_waitcnt(0xc07f); }

constexpr int T0 = 16000, T2 = 4000;
constexpr int NT1 = 167;
constexpr int XST = 32768;
constexpr size_t X1_TB = 16 * XST, X1_G = 2 * X1_TB, X1_S = 16 * X1_G;
constexpr size_t X2_TB = 8 * XST, X2_G = 2 * X2_TB, X2_S = 16 * X2_G;
constexpr size_t WF_G = 1 << 20, WF_BYTES = 16 * WF_G;
constexpr size_t Y1_S = (size_t)128 * 65536, Y2_S = (size_t)64 * 65536;
constexpr size_t OFF_FLAG = 0;
constexpr int F_X = 0, F_W1 = 768, F_W2 = 1024, F_G1 = 1280, F_M = 1792, F_G2 = 2304, F_S = 2816, F_EPOCH = 2840, F_XH = 512, F_W1H = 2848, F_END = 3104;
constexpr size_t OFF_PROBE = 1 << 20;
constexpr size_t OFF_WFT = OFF_FLAG + 4096 * 128;
constexpr size_t OFF_WOT = OFF_WFT + 128 * 1024;
constexpr size_t OFF_GE = OFF_WOT + 128 * 1024;
constexpr size_t OFF_X1F = 4 << 20;
constexpr size_t OFF_W1F = OFF_X1F + 2 * X1_S;
constexpr size_t OFF_Y1F = OFF_W1F + WF_BYTES;
constexpr size_t OFF_X2F = OFF_Y1F + 2 * Y1_S;
constexpr size_t OFF_W2F = OFF_X2F + 2 * X2_S;
constexpr size_t OFF_Y2F = OFF_W2F + WF_BYTES;
constexpr size_t WS_USED = OFF_Y2F + 2 * Y2_S;
constexpr int TAB_F = 0, TAB_G = 8, TAB_T = 14;
constexpr int LDS_BYTES = 81920;

DEVINL unsigned pack_bf16(float a, float b) {
  f32x2 v = {a, b};
  bf16x2_t r = __builtin_convertvector(v, bf16x2_t);
  return __builtin_bit_cast(unsigned, r);
}
DEVINL u32x4 pack8(const f32x4& a, const f32x4& b) {
  return (u32x4){pack_bf16(a[0], a[1]), pack_bf16(a[2], a[3]), pack_bf16(b[0], b[1]), pack_bf16(b[2], b[3])};
}
DEVINL s16x4 tr_read(const char* p) { return __builtin_amdgcn_ds_read_tr16_b64_v4i16((lds_s16x4_p)(p)); }
DEVINL bf16x8 cat8(s16x4 a, s16x4 b) { return __builtin_shufflevector(a, b, 0, 1, 2, 3, 4, 5, 6, 7); }
DEVINL bf16x8 tr_frag(const char* p, int second) { return cat8(tr_read(p), tr_read(p + second)); }
template <int AUX> DEVINL void dma16(__amdgpu_buffer_rsrc_t rs, char* lds_dst, unsigned voff, unsigned soff) {
  const unsigned m = (unsigned)(size_t)(lds_void_p)lds_dst;
  if constexpr (AUX == 0) asm volatile("s_mov_b32 m0, %0\n\ts_nop 0\n\tbuffer_load_dwordx4 %1, %2, %3 offen " IN_POL " lds" :: "s"(m), "v"(voff), "s"(rs), "s"(soff) : "memory");
  else asm volatile("s_mov_b32 m0, %0\n\ts_nop 0\n\tbuffer_load_dwordx4 %1, %2, %3 offen sc1 lds" :: "s"(m), "v"(voff), "s"(rs), "s"(soff) : "memory");
}
DEVINL __amdgpu_buffer_rsrc_t mkrsrc(const void* p, unsigned bytes) { return __builtin_amdgcn_make_buffer_rsrc((void*)p, 0, bytes, 0x00020000); }
DEVINL void st16(void* p, u32x4 v) { asm volatile("global_store_dwordx4 %0, %1, off " ST_POL "\n\ts_nop 1" ::"v"(p), "v"(v) : "memory"); }
template <int I, bool DEV> DEVINL void ldfrag(u32x4& d, const char* base, unsigned lane16) {
  const char* b = base + (I >> 3) * 8192 + 4096;
  if constexpr (DEV) asm volatile("global_load_dwordx4 %0, %1, %2 offset:%3 sc1" : "=v"(d) : "v"(lane16), "s"(b), "n"((I & 7) * 1024 - 4096) : "memory");
  else asm volatile("global_load_dwordx4 %0, %1, %2 offset:%3" : "=v"(d) : "v"(lane16), "s"(b), "n"((I & 7) * 1024 - 4096) : "memory");
}
template <int I, int BS = 16384> DEVINL void ldfrag_rot(u32x4& d, const char* base, int rot, unsigned lane16) {
  ldfrag<I & 7, true>(d, base + (((I >> 3) + rot) & 3) * BS, lane16);
}
template <int I> DEVINL void ldfrag_rot1(u32x4& d, const char* base, int rot, unsigned lane16) {
  const int b = ((I >> 2) + rot) & 3;
  ldfrag<I & 3, true>(d, base + (I >> 4) * 32768 + (b >> 1) * 16384 + (b & 1) * 4096, lane16);
}
DEVINL void tie(u32x4& a) { asm volatile("" : "+v"(a)); }
template <int N> DEVINL void tie(u32x4 (&a)[N]) {
#pragma unroll
  for (int i = 0; i < N; ++i) tie(a[i]);
}
DEVINL int lane_id() { int l; asm volatile("v_mbcnt_lo_u32_b32 %0, -1, 0\n\tv_mbcnt_hi_u32_b32 %0, -1, %0" : "=v"(l)); return l; }
DEVINL void sgpr_settle() { asm volatile("s_nop 4" ::: "memory"); }

#define CT_STEP 64
DEVINL void code_touch(unsigned& d, int slice) {
  u64 pc;
  unsigned len, off = slice * CT_STEP;
  asm volatile("s_getpc_b64 %0\n.Lpc%=:\n\ts_mov_b32 %1, .Lpc%=-k_all" : "=s"(pc), "=s"(len));
  pc -= len;
  asm volatile("s_mov_b32 %1, .Lk_end-k_all\n\ts_max_u32 %1, %1, .Lt_end-k_all\n"
               "1:\n\ts_load_dword %0, %3, %2\n\ts_add_u32 %2, %2, %4\n\ts_cmp_lt_u32 %2, %1\n\ts_cbranch_scc1 1b"
               : "=&s"(d), "=&s"(len), "+s"(off) : "s"(pc), "n"(128 * CT_STEP) : "scc", "memory");
}
DEVINL void tie(unsigned& a) { asm volatile("" : "+v"(a)); }

#define PRIO(x) __builtin_amdgcn_s_setprio(x)
#define PRIO_F 3
#define PRIO_G0 2
#define PRIO_GW 0
#define PRIO_G1 0
#define PRIO_G2A 0
#define PRIO_G2B 0
#define PRIO_JB 0
#define PRIO_T0 1
#define PRIO_T1 1
#define PRIO_T2 0
#define PRIO_MA 0
#define PRIO_MB 1
#define PRIO_JA 0
#define FLAG_LANES 8
DEVINL void set_flag(char* ws, int flag, u64 tag, int w) {
  u64 d;
  unsigned o;
  asm volatile("s_cmp_lg_u32 %2, 0\n\ts_cbranch_scc1 1f\n\ts_bfm_b64 exec, %6, 0\n\ts_nop 1\n\tv_mbcnt_lo_u32_b32 %1, -1, 0\n\tv_mov_b64 %0, %5\n\tv_lshl_add_u32 %1, %1, 3, %3\n\t"
               "global_store_dwordx2 %1, %0, %4 sc1\n\ts_nop 1\n\ts_mov_b64 exec, -1\n1:"
               : "=&v"(d), "=&v"(o) : "s"(w), "s"(flag * 128), "s"(ws + OFF_FLAG), "s"(tag), "n"(FLAG_LANES) : "scc", "memory");
  return;
  asm volatile("s_cmp_lg_u32 %2, 0\n\ts_cbranch_scc1 1f\n\ts_mov_b64 exec, 1\n\ts_nop 1\n\tv_mov_b32 %1, %3\n\tv_mov_b64 %0, %5\n\t"
               "global_store_dwordx2 %1, %0, %4 sc1\n\ts_nop 1\n\ts_mov_b64 exec, -1\n1:"
               : "=&v"(d), "=&v"(o) : "s"(w), "s"(flag * 128), "s"(ws + OFF_FLAG), "s"(tag) : "scc", "memory");
}
template <int N = 0> DEVINL void publish(char* ws, int flag, u64 tag, int w) { wait_vm<N>(); PRIO(PRIO_F); bar(); set_flag(ws, flag, tag, w); }
#define POLL_SLEEP 1
DEVINL void poll(const char* ws, int flag, u64 mask, u64 tag) {
  const unsigned off = flag * 128;
  u64 v;
  unsigned cnt;
  asm volatile("s_mov_b64 exec, %6\n\ts_nop 4\n\ts_mov_b32 %1, 40000\n"
               "1:\n\tglobal_load_dwordx2 %0, %2, %3 sc1\n\ts_waitcnt vmcnt(0)\n\tv_cmp_ne_u64 vcc, %4, %0\n\ts_nop 1\n\ts_cbranch_vccz 2f\n\t"
               "s_cmp_eq_u64 vcc, %6\n\ts_cbranch_scc1 3f\n\t"
               "s_mov_b64 exec, vcc\n\ts_sub_u32 %1, %1, 1\n\ts_cbranch_scc1 2f\n\ts_sleep %5\n\ts_branch 1b\n"
               "3:\n\ts_mov_b64 exec, 1\n"
               "4:\n\ts_sleep %5\n\tglobal_load_dwordx2 %0, %2, %3 sc1\n\ts_waitcnt vmcnt(0)\n\tv_cmp_ne_u64 vcc, %4, %0\n\ts_sub_u32 %1, %1, 1\n\ts_cbranch_scc1 2f\n\ts_cbranch_vccnz 4b\n\t"
               "s_andn2_b64 exec, %6, 1\n\ts_branch 1b\n"
               "2:\n\ts_mov_b64 exec, -1"
               : "=&v"(v), "=&s"(cnt) : "v"(off), "s"(ws + OFF_FLAG), "s"(tag), "n"(POLL_SLEEP), "s"(mask) : "vcc", "scc", "memory");
}
constexpr u64 lanes(int n) { return (1ull << n) - 1; }
DEVINL int poll_vote(const char* ws, int flag, u64 tag) {
  const unsigned off = flag * 128;
  u64 v, t;
  unsigned cnt;
  asm volatile("s_mov_b64 exec, 0xffff\n\ts_nop 4\n\ts_mov_b32 %1, 40000\n"
               "1:\n\tglobal_load_dwordx2 %0, %3, %4 sc1\n\ts_waitcnt vmcnt(0)\n\tv_lshlrev_b64 %2, 1, %0\n\tv_cmp_ne_u64 vcc, %5, %2\n\ts_nop 1\n\ts_cbranch_vccz 2f\n\t"
               "s_mov_b64 exec, vcc\n\ts_sub_u32 %1, %1, 1\n\ts_cbranch_scc1 2f\n\ts_sleep %6\n\ts_branch 1b\n"
               "2:\n\ts_mov_b64 exec, 0xffff\n\ts_nop 1\n\tv_cmp_ne_u64 vcc, %7, %0\n\ts_mov_b64 exec, -1\n\ts_nop 1\n\ts_bcnt1_i32_b64 %1, vcc"
               : "=&v"(v), "=&s"(cnt), "=&v"(t) : "v"(off), "s"(ws + OFF_FLAG), "s"(tag << 1), "n"(POLL_SLEEP), "s"(tag) : "vcc", "scc", "memory");
  return cnt;
}
DEVINL int near_side(const char* p, int first) {
  u64 t0, t1, t2;
  unsigned d;
  asm volatile("s_memtime %0\n\ts_waitcnt lgkmcnt(0)\n\ts_load_dword %3, %4, 0x0\n\ts_waitcnt lgkmcnt(0)\n\t"
               "s_memtime %1\n\ts_waitcnt lgkmcnt(0)\n\ts_load_dword %3, %5, 0x0\n\ts_waitcnt lgkmcnt(0)\n\ts_memtime %2\n\ts_waitcnt lgkmcnt(0)"
               : "=&s"(t0), "=&s"(t1), "=&s"(t2), "=&s"(d) : "s"(p + first * 8192), "s"(p + (first ^ 1) * 8192) : "memory");
  return first ^ ((unsigned)t2 - (unsigned)t1 < (unsigned)t1 - (unsigned)t0);
}

template <int N> DEVINL void flag_peek(u64& v, const char* ws, unsigned off) {
  asm volatile("s_bfm_b64 exec, %3, 0\n\ts_nop 1\n\tglobal_load_dwordx2 %0, %1, %2 sc1\n\ts_mov_b64 exec, -1" : "=v"(v) : "v"(off), "s"(ws + OFF_FLAG), "n"(N) : "memory");
}
template <int N> DEVINL void flag_check(u64& v, const char* ws, unsigned off, u64 tag) {
  unsigned cnt;
  asm volatile("s_bfm_b64 exec, %5, 0\n\ts_mov_b32 %1, 40000\n"
               "1:\n\tv_cmp_ne_u64 vcc, %4, %0\n\ts_nop 1\n\ts_cbranch_vccz 2f\n\t"
               "s_mov_b64 exec, vcc\n\ts_sub_u32 %1, %1, 1\n\ts_cbranch_scc1 2f\n\tglobal_load_dwordx2 %0, %2, %3 sc1\n\ts_waitcnt vmcnt(0)\n\ts_branch 1b\n"
               "2:\n\ts_mov_b64 exec, -1"
               : "+v"(v), "=&s"(cnt) : "v"(off), "s"(ws + OFF_FLAG), "s"(tag), "n"(N) : "vcc", "scc", "memory");
}

constexpr float GC = 0.468668693f, GK = 4.91222954f;
DEVINL u32x4 gelu_pack8(const f32x4& a, const f32x4& b) {
  float y[8] = {a[0], a[1], a[2], a[3], b[0], b[1], b[2], b[3]}, t[8];
#pragma unroll
  for (int i = 0; i < 8; ++i) { t[i] = __builtin_fmaf(y[i], y[i], GK); asm("" : "+v"(t[i])); }
  SB0();
#pragma unroll
  for (int i = 0; i < 8; ++i) { t[i] *= y[i]; asm("" : "+v"(t[i])); }
  SB0();
#pragma unroll
  for (int i = 0; i < 8; ++i) t[i] = __builtin_amdgcn_exp2f(-t[i]);
  SB0();
#pragma unroll
  for (int i = 0; i < 8; ++i) { t[i] = __builtin_fmaf(t[i], GC, GC); asm("" : "+v"(t[i])); }
  SB0();
#pragma unroll
  for (int i = 0; i < 8; ++i) t[i] = __builtin_amdgcn_rcpf(t[i]);
  SB0();
#pragma unroll
  for (int i = 0; i < 8; ++i) { t[i] *= y[i]; asm("" : "+v"(t[i])); }
  SB0();
  return (u32x4){pack_bf16(t[0], t[1]), pack_bf16(t[2], t[3]), pack_bf16(t[4], t[5]), pack_bf16(t[6], t[7])};
}

#define X_PACE 16
#define WL_PACE 32
#define XB_SLEEP 0
#define XA_SLEEP 16
template <int RS, size_t XG>
DEVINL void fwd_unit(const char* la, const u32x4 (&F)[8], char* gd, unsigned lo) {
  bf16x8 A[2][2];
#pragma unroll
  for (int i = 0; i < 2; ++i)
#pragma unroll
    for (int ks = 0; ks < 2; ++ks) A[i][ks] = tr_frag(la + 8 * i + 32 * RS * ks, 4 * RS);
#pragma unroll
  for (int nt = 0; nt < 4; ++nt) {
    f32x4 c0 = {0, 0, 0, 0}, c1 = {0, 0, 0, 0};
#pragma unroll
    for (int ks = 0; ks < 2; ++ks) { c0 = MFMAU(A[0][ks], F[2 * nt + ks], c0); c1 = MFMAU(A[1][ks], F[2 * nt + ks], c1); }
    if constexpr (X_PACE < 16) wait_vm<X_PACE - 1>();
    st16(gd + (nt & 1) * 8 * XG + (lo ^ ((nt >> 1) << 13)), pack8(c0, c1));
  }
}
template <size_t XG> DEVINL unsigned x_lane_off(int lane, int tl, int row, unsigned pb) {
  const unsigned go = ((lane & 15) >> 1) * XG;
  return go + (((go >> 8) & 8192) ^ pb) + (row >> 6) * 16384 + (row & 63) * 128 + ((((lane & 1) * 4 + (lane >> 4)) ^ ((tl >> 1) & 7)) * 16);
}

DEVINL void w_issue(const u16* __restrict__ wsrc, char* reg, int co, int cb, int w, int lane) {
  const int par = co >> 5, c0 = (co & 31) * 8;
  __amdgpu_buffer_rsrc_t rs = mkrsrc(wsrc, 31 * 65536 * 2);
#pragma unroll
  for (int i = 0; i < 4; ++i) {
    const int cl = 2 * w + (i >> 1), tap = 8 * (i & 1) + (lane >> 3);
    const unsigned off = ((2 * tap + par) * 256 + c0 + cl) * 512 + cb * 128 + (((lane & 7) ^ (2 * (((tap >> 1) & 1) + 2 * (i & 1)))) * 16);
    dma16<0>(rs, reg + (w * 4 + i) * 1024, off, 0);
  }
}
template <int PACE, int NM>
DEVINL void w_compute(const u32x4 (&Tm)[NM], char* __restrict__ wf, const char* reg, int co, int cb, int w, int lane, unsigned pb) {
  const int G = lane >> 4, rq = (lane & 15) >> 2, p = lane & 3;
  char* dst = wf + (cb * 4 + w) * 65536 + (co >> 4) * 16384 + ((co >> 2) & 3) * 2048 + (co & 3) * 256 + (unsigned)((lane & 15) * 16 + 4 * G * WF_G);
  const char* la = reg + (8 * (G & 1) + rq) * 128 + (((2 * w + (p >> 1)) ^ (2 * ((rq >> 1) + 2 * (G & 1)))) * 16) + 8 * (p & 1);
  bf16x8 B[8];
#pragma unroll
  for (int cl = 0; cl < 8; ++cl) B[cl] = tr_frag(la + cl * 2048, 4 * 128);
#pragma unroll
  for (int mt = 0; mt < NM; ++mt) {
    f32x4 d[8];
#pragma unroll
    for (int cl = 0; cl < 8; ++cl) d[cl] = MFMAU(Tm[mt], B[cl], ((f32x4){0, 0, 0, 0}));
#pragma unroll
    for (int r = 0; r < 4; ++r) {
      u32x4 o = {pack_bf16(d[0][r], d[1][r]), pack_bf16(d[2][r], d[3][r]), pack_bf16(d[4][r], d[5][r]), pack_bf16(d[6][r], d[7][r])};
      if constexpr (PACE < 16) wait_vm<PACE>();
      st16(dst + (pb ^ ((((NM == 4) & mt) ^ (r >> 1)) << 13)) + r * WF_G + (NM == 4 ? mt >> 1 : mt) * 1024, o);
    }
  }
}

constexpr float LOG2E2 = 2.0f * 1.4426950408889634f;
DEVINL void s_transpose(const u16* __restrict__ src, char* __restrict__ dst, char* lds, int ks, int half, int lane) {
  __amdgpu_buffer_rsrc_t rs = mkrsrc(src, 131072);
#pragma unroll
  for (int i = 0; i < 8; ++i) {
    const int row = 4 * i + (lane >> 4);
    dma16<0>(rs, lds + i * 1024, (ks * 32 + row) * 512 + half * 256 + (((lane & 15) ^ (4 * (row & 3))) * 16), 0);
  }
  wait_vm<0>();
  const int G = lane >> 4, rq = (lane & 15) >> 2, p = lane & 3;
#pragma unroll
  for (int ml = 0; ml < 8; ++ml) {
    const char* la = lds + (8 * G + rq) * 256 + (((4 * (ml >> 1) + p) ^ (4 * (rq & 3))) * 16) + 8 * (ml & 1);
    st16(dst + ((size_t)((8 * half + ml) * 8 + ks) * 64 + lane) * 16, __builtin_bit_cast(u32x4, tr_frag(la, 4 * 256)));
  }
}
DEVINL void s_ge(const u16* __restrict__ g, const u16* __restrict__ wg, float* __restrict__ ge, char* lds, int eb, int lane) {
  __amdgpu_buffer_rsrc_t rs = mkrsrc(wg, 131072);
#pragma unroll
  for (int i = 0; i < 8; ++i) dma16<0>(rs, lds + i * 1024, (i * 32 + (lane >> 1)) * 512 + eb * 32 + (lane & 1) * 16, 0);
  bf16x8 gb[8];
#pragma unroll
  for (int ks = 0; ks < 8; ++ks) gb[ks] = *(const bf16x8*)(g + (lane & 15) * 256 + ks * 32 + (lane >> 4) * 8);
  wait_vm<0>();
  const int G = lane >> 4, rq = (lane & 15) >> 2, p = lane & 3;
  f32x4 acc = {0, 0, 0, 0};
#pragma unroll
  for (int ks = 0; ks < 8; ++ks) acc = MFMA16(tr_frag(lds + (ks * 32 + 8 * G + rq) * 32 + 8 * p, 4 * 32), gb[ks], acc);
  acc *= LOG2E2;
  f32x4 ex;
#pragma unroll
  for (int r = 0; r < 4; ++r) ex[r] = __builtin_amdgcn_exp2f(acc[r] > 64.f ? 64.f : (acc[r] < -64.f ? -64.f : acc[r]));
  const int b = (lane >> 3) & 1, u = lane & 7;
  float* d = ge + ((b * 4 + (eb >> 2)) * 8 + u) * 64 + (eb & 3) * 16 + 4 * G;
  st16(d, __builtin_bit_cast(u32x4, acc));
  st16(d + 4096, __builtin_bit_cast(u32x4, ex));
}

#define G1_K 13
#define G1_P 19
#define G1_K2 27
#define GORD 0
#define GZ 0
#define GX 0
#define SPREAD 1
#define G1_WR 6
#define G1_K0 3
#define PRO1 7
#define PRO2 7
#define RAMP 2
constexpr int RING = 7, SLOT = 11264, RING_DUMMY = RING * SLOT;
template <int PH> struct GPlan {
  static constexpr int NST = PH == 1 ? 32 : 8, PRO = PH == 1 ? PRO1 : PRO2;
  int first[NST + 1];
  int wfirst[NST + 1];
  int before[NST];
  int pos[NST];
  int posF0, posF, posF2, posY;
  constexpr GPlan() : first{}, wfirst{}, before{}, pos{}, posF0(0), posF(0), posF2(0), posY(0) {
    int n = 0, u = 0, k = 0;
    for (; u < PRO; ++u) pos[u] = (n += 3);
    for (int t = 0; t < NST; ++t) {
      before[t] = n;
      first[t] = u;
      wfirst[t] = k;
      if (PH == 1 && t == G1_K0 - 2) posF0 = ++n;
      if (PH == 1 && t == G1_K - 2) posF = ++n;
      if (PH == 1 && t == G1_K2 - 2) posF2 = ++n;
      if (PH == 1 && t == 16) n += 6;
      for (; u <= t + RING - 1 && u < NST && (PH != 1 || ((u < 8 || t >= G1_K0) && (u < 16 || t >= G1_K))) && (u >= 16 || u < first[t] + RAMP); ++u) pos[u] = (n += 3 + w1(u));
      if (PH == 1 && t == 16) posY = n;
      for (int i = 0; i < G1_WR && PH == 1 && t >= G1_K2 && k < 2 * (t - 16); ++i) { ++k; ++n; }
      for (; PH == 3 && k < 4 * t; ++k) ++n;
    }
    first[NST] = u;
    wfirst[NST] = k;
  }
  static constexpr int w1(int u) { return PH == 1 && u >= 8 && u < 16 ? 2 : 0; }
  constexpr int nx(int t) const { int n = 0; for (int u = first[t]; u < first[t + 1]; ++u) n += 3 + w1(u); return n; }
  constexpr int nreq(int t) const { return nx(t) + wfirst[t + 1] - wfirst[t]; }
  constexpr int xreq(int t, int j) const { int u = first[t]; while (j >= 3 + w1(u)) { j -= 3 + w1(u); ++u; } return 5 * u + j; }
  constexpr int slot(int t, int j) const {
    const int n = nreq(t);
    if (!SPREAD) return -1;
    if (n <= 6) return 4 * (j * 6 / n) + 1;
    if (n <= 12) return 2 * (j * 12 / n);
    return j * 24 / n;
  }
  constexpr int wait(int t) const {
    int need = pos[t + 1];
    if (PH == 1 && t == G1_K0 && posF0 > need) need = posF0;
    if (PH == 1 && t == G1_K && posF > need) need = posF;
    if (PH == 1 && t == G1_K2 && posF2 > need) need = posF2;
    if (PH == 1 && t == G1_P && posY > need) need = posY;
    return before[t] - need;
  }
};
template <int PH>
DEVINL void g_phase(char* lds, char* ws, const char* xsrc, const char* wsrc1, const char* wsrc, u32x4 (&W)[32], char* ydst, unsigned yoff,
                    int fbase, int fbase2, int yflag, u64 tag, int rot, int w, stv_t& stv) {
  const int lane = lane_id();
  constexpr GPlan<PH> PL;
  constexpr int NST = PL.NST, NACC = PH == 1 ? 6 : 3, ROWS = PH == 1 ? 84 : 42;
  __amdgpu_buffer_rsrc_t xrs = mkrsrc(xsrc, PH == 1 ? X1_S + 16 * XST : 8 * XST);
  __amdgpu_buffer_rsrc_t yrs = mkrsrc(ydst, PH == 1 ? 2 * Y1_S : Y2_S);
  unsigned xo[3];
  int xd[3];
#pragma unroll
  for (int i = 0; i < 3; ++i) {
    const int piece = i * 4 + w, row = piece * 8 + (lane >> 3);
    xo[i] = row < 84 ? piece * 1024 + (i == 2 ? 8192 : 0) + lane * 16 : OOB;
    xd[i] = piece * 1024;
  }
  const unsigned lane16 = lane * 16;
  const bool dummy = (w == 3);
  unsigned xq[4];
#pragma unroll
  for (int i = 0; i < 4; ++i) xq[i] = ((i + rot) & 3) * (2 * XST);
  auto piece = [&](auto jc) {
    constexpr int u = decltype(jc)::v / 3, i = decltype(jc)::v % 3, slot = u % RING;
    const unsigned so = xq[(u >> 1) & 3] + (u >= 16 ? X1_S : 0) + (u & 9) * XST;
    dma16<SC1>(xrs, lds + (i == 2 && dummy ? RING_DUMMY : slot * SLOT + xd[i]), xo[i], so);
  };
  sgpr_settle();
  static_for<PL.PRO>([&](auto uc) {
    constexpr int u = decltype(uc)::v;
    static_for<3>([&](auto ic) { piece(IC<3 * u + decltype(ic)::v>{}); });
  });
  const int xa = (lane & 15) * 128 + (((lane >> 4) ^ ((lane & 15) >> 1)) * 16);
  int xb[4] = {xa, xa ^ 64, xa + 4 * SLOT, (xa ^ 64) + 4 * SLOT};
  asm volatile("" : "+v"(xb[2]), "+v"(xb[3]));
  const int za = (lane & 8) ? (xa & 255) + 512 + ((lane & 2) << 7) : xa;
  int xz[4] = {za, za ^ 64, za + 4 * SLOT, (za ^ 64) + 4 * SLOT};
  if (GZ) asm volatile("" : "+v"(xz[2]), "+v"(xz[3]));
  u32x4 xc[6], xs[6];
  auto ldx = [&](int pi, int slot) {
    const int ro = (PH == 1 ? pi * 16 : (pi / 3) * 42 + (pi % 3) * 16) * 128 + (slot & 3) * SLOT;
    if (GZ && PH == 1 && pi == 5) {
      xc[pi] = *(const u32x4*)(lds + xz[(slot >> 2) * 2] + ro);
      xs[pi] = *(const u32x4*)(lds + xz[(slot >> 2) * 2 + 1] + ro);
    } else {
      xc[pi] = *(const u32x4*)(lds + xb[(slot >> 2) * 2] + ro);
      xs[pi] = *(const u32x4*)(lds + xb[(slot >> 2) * 2 + 1] + ro);
    }
  };
  f32x4 R[NACC], Q[NACC];
  auto sty = [&](int n, unsigned base) {
    const int l = lane_id();
    const int tl = n * 16 + (l & 15);
    unsigned off = (tl >> 1) * 65536 + base + (tl & 1) * 64 + (l >> 4) * 16;
    off ^= (off >> 8) & 8192;
    __builtin_amdgcn_raw_buffer_store_b128(pack8(R[n], Q[n]), yrs, tl < ROWS ? off : OOB, 0, SC1);
  };
  wait_vm<PL.before[0] - PL.pos[0]>();
  static_for<PH == 1 ? 16 : 32>([&](auto ic) { tie(W[decltype(ic)::v]); });
  bar();
  STAMP(PH == 1 ? 5 : (PH == 2 ? 14 : 18));
#pragma unroll
  for (int pi = 0; pi < 6; ++pi) ldx(pi, 0);
  u64 fv;
  unsigned foff;
  static_for<NST>([&](auto ic) {
    constexpr int st = decltype(ic)::v;
    constexpr bool MORE = st + 1 < NST;
    if constexpr (MORE) {
      wait_vm<PL.wait(st)>();
      if constexpr (PH == 1 && st + 1 >= 8 && st + 1 < 16) { tie(W[2 * st + 2]); tie(W[2 * st + 3]); }
      if constexpr (PH == 1 && st == G1_K0) { STAMP(12); flag_check<48>(fv, ws, foff, tag); STAMP(23); }
      if constexpr (PH == 1 && st == G1_K) { STAMP(8); flag_check<32>(fv, ws, foff, tag); }
      if constexpr (PH == 1 && st == G1_K2) { STAMP(21); flag_check<16>(fv, ws, foff, tag); STAMP(22); }
      if constexpr (!(GX & 1) && !((GX & 8) && (st & 1))) bar();
      if constexpr (PH == 1 && st == G1_K) STAMP(9);
      if constexpr (PH == 1 && st == 16) STAMP(6);
      if constexpr (PH == 1 && st == G1_P) { PRIO(PRIO_F); set_flag(ws, yflag, tag, w); PRIO(PRIO_G1); STAMP(7); }
      SB0();
      if constexpr (PH == 1 && st == G1_K0 - 2) {
        const int l = lane_id();
        foff = (l + (l < 32 ? fbase + F_XH - F_X - 256 : fbase2 + F_W1H - F_W2 - 32)) * 128;
        flag_peek<48>(fv, ws, foff);
      }
      if constexpr (PH == 1 && st == G1_K - 2) { foff = (fbase + lane_id()) * 128; flag_peek<32>(fv, ws, foff); }
      if constexpr (PH == 1 && st == G1_K2 - 2) { foff = (fbase2 + lane_id()) * 128; flag_peek<16>(fv, ws, foff); }
    }
    auto req = [&](auto mc) {
      static_for<PL.nreq(st)>([&](auto jc) {
        constexpr int j = decltype(jc)::v, nx = PL.nx(st), xu = j < nx ? PL.xreq(st, j) / 5 : 0, xi = j < nx ? PL.xreq(st, j) % 5 : 0;
        if constexpr (PL.slot(st, j) == decltype(mc)::v && !(GX & 2)) {
          SB0();
          if constexpr (j < nx && xi < 3) piece(IC<3 * xu + xi>{});
          else if constexpr (j < nx) ldfrag_rot1<2 * xu + xi - 3>(W[2 * xu + xi - 3], wsrc1, rot, lane16);
          else ldfrag_rot<PL.wfirst[st] + j - nx, PH == 1 ? 16384 : 8192>(W[PL.wfirst[st] + j - nx], wsrc, PH == 1 ? rot : 0, lane16);
          SB0();
        }
      });
    };
    req(IC<-1>{});
    u32x4 nw;
    constexpr f32x4 Z = {0, 0, 0, 0};
#define GM(a, b, c) MFMAU(a, b, c)
    if constexpr (GORD == 1 && PH == 1) {
      constexpr int ks = st & 15;
      static_for<24>([&](auto mc) {
        constexpr int m = decltype(mc)::v, n = m < 12 ? m >> 1 : (m - 12) % 6;
        if constexpr (st == 16 && m < 12 && !(m & 1)) sty(n, yoff);
        if constexpr (m < 12 && !(m & 1)) R[n] = GM(W[2 * ks], xc[n], ks == 0 ? Z : R[n]);
        else if constexpr (m < 12) Q[n] = GM(W[2 * ks], xs[n], ks == 0 ? Z : Q[n]);
        else if constexpr (m < 18) R[n] = GM(W[2 * ks + 1], xs[n], R[n]);
        else Q[n] = GM(nw, xc[n], Q[n]);
        if constexpr (m == 0) { tie(W[2 * ks + 1]); nw = W[2 * ks + 1] ^ 0x80008000u; }
        req(mc);
        if constexpr (MORE && m >= 12) {
          constexpr int slot = (st + 1) % RING, ro = n * 2048 + (slot & 3) * SLOT;
          if constexpr (m < 18) xs[n] = *(const u32x4*)(lds + xb[(slot >> 2) * 2 + 1] + ro);
          else xc[n] = *(const u32x4*)(lds + xb[(slot >> 2) * 2] + ro);
        }
        SB0();
      });
    } else
    static_for<6>([&](auto pc) {
      constexpr int pi = decltype(pc)::v, ks = PH == 1 ? st & 15 : 2 * st + pi / 3, n = PH == 1 ? pi : pi % 3;
      if constexpr (PH == 1 && st == 16) sty(n, yoff);
      if constexpr (GORD == 2) {
        if constexpr (n == 0) { tie(W[2 * ks + 1]); }
        R[n] = GM(W[2 * ks + 1], xs[pi], ks == 0 ? Z : R[n]);
        if constexpr (n == 0) { nw = W[2 * ks + 1] ^ 0x80008000u; }
        req(IC<4 * pi>{});
        Q[n] = GM(W[2 * ks], xs[pi], ks == 0 ? Z : Q[n]);
        req(IC<4 * pi + 1>{});
        R[n] = GM(W[2 * ks], xc[pi], R[n]);
        req(IC<4 * pi + 2>{});
        Q[n] = GM(nw, xc[pi], Q[n]);
        req(IC<4 * pi + 3>{});
      } else {
      R[n] = GM(W[2 * ks], xc[pi], ks == 0 ? Z : R[n]);
      if constexpr (n == 0) { tie(W[2 * ks + 1]); nw = W[2 * ks + 1] ^ 0x80008000u; }
      req(IC<4 * pi>{});
      Q[n] = GM(W[2 * ks], xs[pi], ks == 0 ? Z : Q[n]);
      req(IC<4 * pi + 1>{});
      R[n] = GM(W[2 * ks + 1], xs[pi], R[n]);
      req(IC<4 * pi + 2>{});
      Q[n] = GM(nw, xc[pi], Q[n]);
      req(IC<4 * pi + 3>{});
      }
      if constexpr (MORE && !(GX & 4)) ldx(pi, (st + 1) % RING);
      SB0();
    });
  });
  STAMP(PH == 1 ? 10 : (PH == 2 ? 15 : 19));
#pragma unroll
  for (int n = 0; n < NACC; ++n) sty(n, PH == 1 ? yoff + Y1_S : yoff);
  if constexpr (PH != 2) static_for<32 - PL.wfirst[NST]>([&](auto jc) { constexpr int i = PL.wfirst[NST] + decltype(jc)::v; ldfrag_rot<i, PH == 1 ? 16384 : 8192>(W[i], wsrc, PH == 1 ? rot : 0, lane16); });
}

DEVINL float jval(float r) {
  float t = __builtin_fmaf(-2.0f, r, 1.0f);
  asm("" : "+v"(t));
  return t;
}
template <bool FAST> DEVINL float jden(float f, float g) {
  float t = FAST ? __builtin_fmaf(f, g, 1.0f) : 1.0f + __builtin_amdgcn_exp2f(f + g);
  asm("" : "+v"(t));
  return t;
}
#define OUT_POL "sc1"
DEVINL void st_out(void* p, u32x4 v) { asm volatile("global_store_dwordx4 %0, %1, off " OUT_POL "\n\ts_nop 1" ::"v"(p), "v"(v) : "memory"); }
DEVINL void out_rows(char* orow, const u32x4& P0, const u32x4& P1) {
  u32x4 s1, s2;
#pragma unroll
  for (int i = 0; i < 4; ++i) {
    s1[i] = __builtin_amdgcn_update_dpp(P0[i], P1[i], 0x128, 0xf, 0xc, false);
    s2[i] = __builtin_amdgcn_update_dpp(P1[i], P0[i], 0x128, 0xf, 0x3, false);
  }
  st_out(orow, s1);
  st_out(orow + 8 * 4096, s2);
}
constexpr int J_Y = 32768, J_F = 65536, J_G = J_F + 8192;
constexpr int J_AH = 2;
template <bool FAST> DEVINL void j_make(const f32x4 (&fe)[4], char* lds, int u, int w, int lane) {
  const float* gl = (const float*)(lds + J_G + w * 2048) + 8 * (lane >> 4) + u * 64;
#pragma unroll
  for (int h = 0; h < 2; ++h) {
    const f32x4 g0 = *(const f32x4*)(gl + h * 32), g1 = *(const f32x4*)(gl + h * 32 + 4);
    f32x4 t0, t1;
#pragma unroll
    for (int r = 0; r < 4; ++r) {
      t0[r] = jval(__builtin_amdgcn_rcpf(jden<FAST>(fe[2 * h][r], g0[r])));
      t1[r] = jval(__builtin_amdgcn_rcpf(jden<FAST>(fe[2 * h + 1][r], g1[r])));
    }
    *(u32x4*)(lds + u * 8192 + (2 * w + h) * 1024 + lane * 16) = pack8(t0, t1);
  }
}
DEVINL void j_loop(u32x4 (&woa)[32], const f32x4 (&fe)[4], char* lds, int jw, int jstep, char* orow, int w, int lane, stv_t& stv) {
  constexpr int AH = J_AH;
  constexpr bool FAST = true;
  const float* gl = (const float*)(lds + J_G + w * 2048) + 8 * (lane >> 4);
  const char* jr = lds + lane * 16;
  f32x4 g[4];
  g[0] = *(const f32x4*)(gl + AH * 64); g[1] = *(const f32x4*)(gl + AH * 64 + 4);
  wait_lds();
  bar();
  STAMP(28);
  u32x4 B[8];
#pragma unroll
  for (int ks = 0; ks < 8; ++ks) B[ks] = *(const u32x4*)(jr + ks * 1024);
  f32x4 acc[4];
  u32x4 P0;
  static_for<8>([&](auto ic) {
    constexpr int u = decltype(ic)::v;
    constexpr bool FIRST = (u == 0), RD = (u + 1 < 8), J = (u + AH < 8);
    SB0();
    float d[16];
    unsigned jo[8];
    static_for<32>([&](auto qc) {
      constexpr int q = decltype(qc)::v, hf = q >> 4, ks = (q >> 1) & 7, m = 2 * hf + (q & 1);
      if constexpr (FIRST && q == 0) { wait_vm<16>(); static_for<16>([&](auto c) { tie(woa[decltype(c)::v]); }); }
      if constexpr (FIRST && q == 16) { wait_vm<0>(); static_for<16>([&](auto c) { tie(woa[16 + decltype(c)::v]); }); }
      if constexpr (RD && u >= 1 && q == 14) bar();
      if constexpr (ks == 0) acc[m] = MFMAU(woa[m * 8 + ks], B[ks], ((f32x4){0, 0, 0, 0}));
      else acc[m] = MFMAU(woa[m * 8 + ks], B[ks], acc[m]);
      if constexpr (RD && q >= 18 && !(q & 1)) B[ks - 1] = *(const u32x4*)(jr + (u + 1) * 8192 + (ks - 1) * 1024);
      if constexpr (!FIRST && q == 3) out_rows(orow + (u - 1) * 512, P0, pack8(acc[2], acc[3]));
      if constexpr (q == 19) P0 = pack8(acc[0], acc[1]);
      if constexpr (J) {
        if constexpr (q == 2) { g[2] = *(const f32x4*)(gl + (u + AH) * 64 + 32); g[3] = *(const f32x4*)(gl + (u + AH) * 64 + 36); }
        if constexpr (q >= 5 && q < 21) { constexpr int e = q - 5; d[e] = jden<FAST>(fe[e >> 2][e & 3], g[e >> 2][e & 3]); }
        if constexpr (q >= 7 && q < 23) { constexpr int e = q - 7; d[e] = __builtin_amdgcn_rcpf(d[e]); }
        if constexpr (q >= 9 && q < 25) { constexpr int e = q - 9; d[e] = jval(d[e]); }
        if constexpr (q >= 12 && q < 28 && !(q & 1)) { constexpr int e = q - 11; jo[e >> 1] = pack_bf16(d[e - 1], d[e]); }
        if constexpr (q == 19) *(u32x4*)(lds + jw) = (u32x4){jo[0], jo[1], jo[2], jo[3]};
        if constexpr (q == 27) { *(u32x4*)(lds + jw + 1024) = (u32x4){jo[4], jo[5], jo[6], jo[7]}; jw += jstep; asm("" : "+v"(jw)); }
        if constexpr (u + AH + 1 < 8 && q == 29) { g[0] = *(const f32x4*)(gl + (u + AH + 1) * 64); g[1] = *(const f32x4*)(gl + (u + AH + 1) * 64 + 4); }
      }
      SB0();
    });
    if constexpr (RD) B[7] = *(const u32x4*)(jr + (u + 1) * 8192 + 7 * 1024);
    SB0();
  });
  out_rows(orow + 7 * 512, P0, pack8(acc[2], acc[3]));
}

#define HINT 0
#define TAGLATE 0
#define XLOOP 1
#define ETOUCH 4096
#define RFIRST 1
#define PTOUCH 1
#define FTOUCH 1
#define LATE_WF 1
#define G_ALL 1
#define T_ALL 1
#define W1_SLEEP 16
#define ROT 1
#define W2_PACE 2
constexpr int M_HS = 96;

template <int MK = 0> DEVINL void x_issue(const u16* __restrict__ x, char* reg, int s, int tp, int par, int ch, bool live, int lane) {
  __amdgpu_buffer_rsrc_t rs = mkrsrc(x + (size_t)s * T0 * 256, T0 * 512);
  const int t0 = 96 * tp - 111 + par + 2 * (lane >> 3);
  const int base = t0 * 512 + ch * 128 + (((lane & 7) ^ (4 * ((lane >> 4) & 1))) * 16);
  unsigned o0 = (tp == 0 || t0 < 0) ? OOB : (unsigned)base, o1 = tp == 0 ? OOB : (unsigned)(base + 8192), so;
  if (live) asm volatile("s_mov_b32 m0, %4\n\ts_mov_b32 %0, 6\n\ts_nop 2\n\tbuffer_load_dwordx4 %2, %3, 0 offen " IN_POL " lds\n"
               "1:\n\ts_add_u32 m0, m0, 1024\n\ts_sub_u32 %0, %0, 1\n\tbuffer_load_dwordx4 %1, %3, 0 offen " IN_POL " lds\n\tv_add_u32 %1, 0x2000, %1\n\ts_cbranch_scc0 1b ;@L7\n;@%5"
               : "=&s"(so), "+v"(o1) : "v"(o0), "s"(rs), "s"((unsigned)(size_t)(lds_void_p)reg), "n"(MK) : "scc", "memory");
}
#define G_GATE 0
#define T_GATE 0
DEVINL void gate(unsigned t0, unsigned n) {
  asm volatile("1:\n\ts_sleep 2\n\ts_memrealtime vcc\n\ts_waitcnt lgkmcnt(0)\n\ts_sub_u32 vcc_lo, vcc_lo, %0\n\ts_cmp_lt_u32 vcc_lo, %1\n\ts_cbranch_scc1 1b"
               :: "s"(t0), "s"(n) : "vcc", "scc", "memory");
}
DEVINL u64 load_tag(const char* ws) {
  u64 tag;
  asm volatile("s_load_dwordx2 %0, %1, 0x0\n\ts_waitcnt lgkmcnt(0)" : "=s"(tag) : "s"(ws + OFF_FLAG + F_EPOCH * 128) : "memory");
  return tag * 6364136223846793005ull + 1442695040888963407ull;
}

extern "C" __global__ __launch_bounds__(256, 2) void k_all(const u16* __restrict__ x, const u16* __restrict__ w1, const u16* __restrict__ w2, const u16* __restrict__ g_,
                                                const u16* __restrict__ wf, const u16* __restrict__ wg, const u16* __restrict__ wo,
                                                char* __restrict__ ws_, char* __restrict__ out) {
  constexpr unsigned pb = 0;
  char* const ws = ws_ + (-(size_t)ws_ & ((2 << 20) - 1));
  __shared__ __attribute__((aligned(16))) char lds[LDS_BYTES];
  int lane = threadIdx.x & 63;
  const int w = __builtin_amdgcn_readfirstlane(threadIdx.x >> 6);
  const int role = blockIdx.x >> 8, xl = blockIdx.x & 7, idx = (blockIdx.x >> 3) & 31;
  const int q = xl * 32 + idx;
  stv_t stv = {};
  STAMP(0);
  u64 tag;

  const int eu = w == (idx & 3) ? idx : ((idx & 3) == 0 && w == 2 ? 32 + (idx >> 2) : -1);
  const int tpv[2] = {xl * 21 + (idx >> 1), xl * 21 + 16 + (eu >> 3)}, parv[2] = {idx & 1, (eu >> 2) & 1}, chv[2] = {w, eu & 3};
  u32x4 F[8];
  static_for<4>([&](auto ic) { constexpr int i = decltype(ic)::v; ldfrag<i, false>(F[i], (const char*)TAB + (role ? TAB_F : TAB_T) * 1024, lane * 16); });
  asm volatile("s_cmp_eq_u32 %6, 0\n\ts_cbranch_scc1 1f\n\t;@f\n\tglobal_load_dwordx4 %0, %4, %5\n\tglobal_load_dwordx4 %1, %4, %5 offset:1024\n\t"
               "global_load_dwordx4 %2, %4, %5 offset:2048\n\tglobal_load_dwordx4 %3, %4, %5 offset:3072\n1:"
               : "=&v"(F[4]), "=&v"(F[5]), "=&v"(F[6]), "=&v"(F[7]) : "v"(lane * 16), "s"((const char*)TAB + (TAB_F + 4) * 1024), "s"(role) : "scc", "memory");
  asm volatile("" :: "s"(x), "s"(w1), "s"(w2), "s"(g_), "s"(wf), "s"(wg), "s"(wo), "s"(ws_));
  int role2 = role;
  if (role2 == 0) {
    asm volatile(";@g");
    PRIO(PRIO_G0);
    w_issue(w1, lds, (q >> 5) * 8 + ((q >> 3) & 3), q & 3, w, lane);
    w_issue(w1, lds + 16384, (q >> 5) * 8 + 4 + ((q >> 3) & 3), q & 3, w, lane);
    w_issue(w2, lds + 65536, q >> 2, q & 3, w, lane);
  }
  asm volatile("" : "+s"(role2));
  if (role2 != 0) {
    asm volatile(";@t");
    PRIO(PRIO_T0);
    x_issue(x, lds + w * 8192, 0, tpv[0], parv[0], chv[0], true, lane);
    x_issue<1>(x, lds + 65536 + (eu >> 5) * 8192, 0, tpv[1], parv[1], chv[1], eu >= 0, lane);
  }
  if (RFIRST) x_issue<2>(x, lds + 32768 + w * 8192, 1, tpv[role == 0], parv[role == 0], chv[role == 0], role != 0 || eu >= 0, lane);
  unsigned t_start;
  asm volatile("s_memrealtime vcc\n\ts_waitcnt lgkmcnt(0)\n\ts_mov_b32 %0, vcc_lo" : "=s"(t_start) :: "vcc", "memory");
  unsigned per;
  {
    u64 pc;
    unsigned a, b;
    asm volatile("s_getpc_b64 %0\n.Lpe:\n\ts_mov_b32 %1, .Lg_beg-.Lpe\n\ts_mov_b32 %2, .Lt_beg-.Lpe" : "=s"(pc), "=s"(a), "=s"(b));
    const unsigned o = (idx * 4 + w) * 64;
    asm volatile("s_load_dword %0, %1, %2" : "=s"(per) : "s"(pc), "s"(((role ? b : a) + (o < ETOUCH ? o : 0)) & ~3u) : "memory");
  }
  asm volatile("" : "+s"(role2));

  u32x4 W[32];
  if (__builtin_expect(role2 == 0, 1)) {
    asm volatile(".Lg_beg:");
    const unsigned lane16 = lane * 16;
    {
      const char* probe = ws + OFF_PROBE + (q >> 4) * 16384 + (q & 15) * 512;
      unsigned ct = 0;
      if (RFIRST) {
        u64 pc;
        unsigned len;
        asm volatile("s_getpc_b64 %0\n.Lpc%=:\n\ts_mov_b32 %1, .Lpc%=-k_all" : "=s"(pc), "=s"(len));
        pc -= len;
        asm volatile("s_mov_b32 %0, .Lk_end-k_all\n\ts_max_u32 %0, %0, .Lt_end-k_all" : "=s"(len) :: "scc");
        const unsigned wv = idx * 4 + w, co = wv * 512 + lane * 64, l2 = lane >> 1;
        const char* a = (const char*)pc + (co < len ? co : 0);
        if (l2 >= 4) a = (const char*)(l2 == 4 ? wf : (l2 == 5 ? wg : wo)) + (q * 4 + w) * 128 + (lane & 1) * 64;
        if (l2 == 7) a = (wv < 64 ? (const char*)g_ + wv * 128 : (const char*)TAB + TAB_G * 1024 + (wv - 64) * 128) + (lane & 1) * 64;
        if (lane >= 16) a = ws + OFF_FLAG + (q * 16 + w * 4 + lane - 16) * 128;
        if (lane == 20) a = ws + OFF_PROBE + (((q >> 4) * 16384 + 32768) & 0x3ffff) + (q & 15) * 512 + (w & 1) * 8192;
        asm volatile("s_bfm_b64 exec, %2, 0\n\ts_nop 1\n\tglobal_load_dword %0, %1, off sc1\n\ts_mov_b64 exec, -1" : "=v"(ct) : "v"(a), "n"(PTOUCH ? 21 : (FTOUCH ? 20 : 16)) : "memory");
        if (!TAGLATE) { unsigned d; asm volatile("s_load_dword %0, %1, 0x100" : "=s"(d) : "s"(probe) : "memory"); tag = load_tag(ws); asm volatile("" :: "s"(d)); }
        wait_vm<G_ALL ? 0 : 13>();
        if (eu < 0) { wait_vm<5>(); asm volatile(";@e"); }
      } else wait_vm<0>();
      if (RFIRST) asm volatile(";@Ng2_ge\n;@r"); else asm volatile(";@Ng\n;@r");
      wait_lds();
      asm volatile("" :: "s"(per));
      if (W1_SLEEP) __builtin_amdgcn_s_sleep(W1_SLEEP);
      if (G_GATE) gate(t_start, G_GATE);
      static_for<4>([&](auto ic) { tie(F[decltype(ic)::v]); });
      const bool sd = q & 4;
      const u32x4 Tm[2] = {sd ? F[1] : F[0], sd ? F[3] : F[2]};
      bar();
      STAMP(1);
      w_compute<16>(Tm, ws + OFF_W1F, lds, q >> 3, q & 3, w, lane, pb ^ (sd << 13));
      if (TAGLATE) { unsigned d; asm volatile("s_load_dword %0, %1, 0x100" : "=s"(d) : "s"(probe) : "memory"); tag = load_tag(ws); asm volatile("" :: "s"(d)); }
      int vote = 0;
      if (w == 0) vote = (xl >> 2) ^ near_side(probe, (idx >> 2) & 1);
      publish(ws, F_W1 + (q & 3) * 64 + (q >> 2), tag ^ ((u64)vote << 63), w);
      tie(ct);
      STAMP(2);
      PRIO(PRIO_G0);
      w_compute<16>(Tm, ws + OFF_W1F, lds + 16384, 32 + (q >> 3), q & 3, w, lane, pb ^ (sd << 13));
      publish(ws, F_W1H + (q & 3) * 64 + (q >> 2), tag, w);
      STAMP(31);
    }
    const int TB = (idx >> 2) & 1, cb = idx & 3;
    ((int*)lds)[w] = poll_vote(ws, F_W1 + cb * 64 + w * 16 + lane, tag);
    wait_lds();
    bar();
    const u32x4 votes = *(const u32x4*)lds;
    const int grp = (xl * 4 + (idx >> 3)) ^ (__builtin_amdgcn_readfirstlane(votes[0] + votes[1] + votes[2] + votes[3]) > 32 ? 16 : 0);
    const int gflag = (TB * 4 + cb) * 32 + grp, rot = ROT ? cb + 2 * TB : 0;
    const int gl = grp & 15;
    const unsigned sb = pb ^ ((grp >> 4) << 13);
    const unsigned yoff = (gl >> 2) * 16384 + sb + (gl & 3) * 2048 + (cb * 4 + w) * 128;
    const size_t woff = gl * WF_G + (cb * 4 + w) * 65536 + (sb ^ ((gl & 2) << 12));
    const size_t x1off = gl * X1_G + TB * X1_TB + (sb ^ ((gl & 2) << 12)), x2off = gl * X2_G + TB * X2_TB + (sb ^ ((gl & 4) << 11));
    {
      STAMP(3);
      PRIO(PRIO_GW);
      static_for<16>([&](auto ic) { constexpr int i = decltype(ic)::v; ldfrag_rot1<i>(W[i], ws + OFF_W1F + woff, rot, lane16); });
      PRIO(PRIO_F);
      poll(ws, F_X + TB * 128 + w * 32 + lane, lanes(32), tag);
      bar();
      STAMP(4);
      PRIO(PRIO_G1);
      g_phase<1>(lds, ws, ws + OFF_X1F + x1off, ws + OFF_W1F + woff, ws + OFF_W2F + woff, W, ws + OFF_Y1F, yoff + TB * 42 * 65536,
                 F_X + 256 + TB * 128 + w * 32, F_W2 + cb * 64 + w * 16, F_G1 + gflag, tag, rot, w, stv);
      publish(ws, F_G1 + 256 + gflag, tag, w);
      STAMP(11);
    }
    lane = lane_id();
    poll(ws, F_M + TB * 112 + w * 28 + lane, lanes(28), tag);
    bar();
    STAMP(13);
    PRIO(PRIO_G2A);
    g_phase<2>(lds, ws, ws + OFF_X2F + x2off, nullptr, nullptr, W, ws + OFF_Y2F, yoff + TB * 21 * 65536, 0, 0, 0, tag, rot, w, stv);
    publish(ws, F_G2 + gflag, tag, w);
    STAMP(16);
    lane = lane_id();
    poll(ws, lane >= 28 ? F_S + w * 3 + lane - 28 : F_M + 256 + TB * 112 + w * 28 + lane, lanes(31), tag);
    bar();
    STAMP(17);
    PRIO(PRIO_G2B);
    g_phase<3>(lds, ws, ws + OFF_X2F + X2_S + x2off, nullptr, ws + OFF_WFT + w * 32768, W, ws + OFF_Y2F + Y2_S, yoff + TB * 21 * 65536, 0, 0, 0,
               tag, rot, w, stv);
    publish(ws, F_G2 + 256 + gflag, tag, w);
    STAMP(20);
    PRIO(PRIO_JB);
  } else {
    asm volatile(".Lt_beg:");
    const int G = lane >> 4, rq = (lane & 15) >> 2, p = lane & 3;
    const unsigned lane16 = lane * 16;
    {
      u32x4 Tm[4];
      tag = load_tag(ws);
      asm volatile("" :: "s"(per));
      wait_vm<T_ALL ? 0 : 8 + 8 * RFIRST>();
      if (eu < 0) { wait_vm<8 * RFIRST>(); asm volatile(";@e"); }
      if (RFIRST) asm volatile(";@Nft12_ft2e\n;@r"); else asm volatile(";@Nft1_fte\n;@r");
      tie(F);
      STAMP(1);
      if (XA_SLEEP) __builtin_amdgcn_s_sleep(XA_SLEEP);
      if (T_GATE) gate(t_start, T_GATE);
#pragma unroll 1
      for (int i = 0; i < 8; ++i) {
        const int s = i >> 2, r = s ? (i >> 1) & 1 : i & 1, h = s ? i & 1 : i >> 1;
        if (r == 0 || eu >= 0) {
          if (r && !h) wait_vm<12>();
          const int tl = (r ? tpv[1] : tpv[0]) - 84 * (xl >> 2);
          char* gd = ws + OFF_X1F + s * X1_S + (xl >> 2) * X1_TB + (size_t)(8 * h + (r ? parv[1] * 4 + chv[1] : parv[0] * 4 + chv[0])) * XST;
          const char* reg = lds + (r == s ? w * 8192 : (s ? 32768 + w * 8192 : 65536 + (eu >> 5) * 8192));
          fwd_unit<128, X1_G>(reg + (8 * G + rq) * 128 + (((4 * h + p) ^ (4 * ((rq >> 1) & 1))) * 16), F, gd, x_lane_off<X1_G>(lane, tl, tl, pb));
        }
        if ((0x8a >> i) & 1) {
          publish(ws, (i == 3 ? F_XH : F_X + s * 256) + q, tag, w);
          PRIO(PRIO_T1);
          if (i == 1) STAMP(2);
          if (i == 7) STAMP(4);
        }
        if (i == 3) {
          STAMP(3);
          x_issue(x, lds + w * 8192, 1, tpv[1], parv[1], chv[1], eu >= 0, lane);
          w_issue(w2, lds + 65536, q >> 2, q & 3, w, lane);
          if (XB_SLEEP) __builtin_amdgcn_s_sleep(XB_SLEEP);
        }
      }
      PRIO(PRIO_T2);
      static_for<4>([&](auto ic) { constexpr int i = decltype(ic)::v; ldfrag<TAB_T + i, false>(Tm[i], (const char*)TAB, lane16); });
      wait_vm<0>();
      tie(Tm);
      w_compute<W2_PACE>(Tm, ws + OFF_W2F, lds + 65536, q >> 2, q & 3, w, lane, pb);
      publish(ws, F_W2 + (q & 3) * 64 + (q >> 2), tag, w);
      STAMP(5);
      if (idx >= 28 && (idx - 28) * 8 + xl < 12) {
        PRIO(PRIO_T2);
        const int sid = (idx - 28) * 8 + xl, t = sid * 4 + w;
        char* reg = lds + w * 8192;
        if (t < 16) s_transpose(wf, ws + OFF_WFT, reg, t >> 1, t & 1, lane);
        else if (t < 32) s_transpose(wo, ws + OFF_WOT, reg, (t - 16) >> 1, t & 1, lane);
        else s_ge(g_, wg, (float*)(ws + OFF_GE), reg, t - 32, lane);
        publish(ws, F_S + sid, tag, w);
      }
    }
    if (idx < 28) {
      u32x4 Gt[6], F[2];
      char* reg = lds + w * 8192;
      char* him = lds + 32768;
      const int pr = 3 * idx + w;
      static_for<6>([&](auto ic) { constexpr int i = decltype(ic)::v; ldfrag<TAB_G + i, false>(Gt[i], (const char*)TAB, lane16); });
      static_for<2>([&](auto ic) { constexpr int i = decltype(ic)::v; ldfrag<i, false>(F[i], (const char*)TAB + (TAB_F + 2 * w) * 1024, lane16); });
#pragma unroll 1
      for (int s = 0; s < 2; ++s) {
        poll(ws, F_G1 + s * 256 + ((idx >= 14 || lane >= 8 ? 4 : 0) + (xl >> 1)) * 32 + w * 8 + (lane & 7), idx == 13 ? lanes(16) : lanes(8), tag);
        bar();
        if (s == 0) { PRIO(PRIO_MA); } else { PRIO(PRIO_MB); }
        tie(Gt); tie(F);
        __amdgpu_buffer_rsrc_t rs = mkrsrc(ws + OFF_Y1F + s * Y1_S, 84 * 65536);
        sgpr_settle();
#pragma unroll
        for (int i = 0; i < 8; ++i) {
          const unsigned off = pr * 65536 + (i & 3) * 16384 + (((i >> 2) << 13) ^ ((pr & 32) << 8) ^ pb) + G * 2048 + xl * 256 + (((lane & 15) ^ ((G & 1) << 2) ^ (i & 1)) * 16);
          dma16<SC1>(rs, reg + i * 1024, off, 0);
        }
        STAMP2(6, 10);
        wait_vm<0>();
        STAMP2(7, 11);
        bf16x8 A[2][2][2];
#pragma unroll
        for (int tl = 0; tl < 2; ++tl)
#pragma unroll
          for (int i = 0; i < 2; ++i)
#pragma unroll
            for (int ks = 0; ks < 2; ++ks) {
              const int ch = (((p >> 1) * 8 + tl * 4 + 2 * (p & 1) + i) ^ ((rq >> 1) << 2) ^ (G & 1));
              A[tl][i][ks] = tr_frag(reg + (16 * ks + 4 * G + (rq >> 1)) * 256 + ch * 16 + (rq & 1) * 8, 512);
            }
        f32x4 c[2][2];
        auto idft = [&](int b) {
          constexpr f32x4 Z = {0, 0, 0, 0};
#pragma unroll
          for (int i = 0; i < 2; ++i) c[b & 1][i] = MFMAU(A[b / 3][i][0], Gt[2 * (b % 3)], Z);
#pragma unroll
          for (int i = 0; i < 2; ++i) c[b & 1][i] = MFMAU(A[b / 3][i][1], Gt[2 * (b % 3) + 1], c[b & 1][i]);
        };
        idft(0);
        const unsigned keep = pr == 83 ? 0u : ~0u;
#pragma unroll
        for (int b = 0; b < 6; ++b) {
          if (b + 1 < 6) idft(b + 1);
          SB0();
          u32x4 o = gelu_pack8(c[b & 1][0], c[b & 1][1]);
          if (b == 5) o &= keep;
          *(u32x4*)(him + (w * 96 + b * 16 + (lane & 15)) * M_HS + G * 16) = o;
          SB0();
        }
        wait_lds();
        bar();
        STAMP2(8, 12);
        const int TB2 = idx >= 14;
        char* gd = ws + OFF_X2F + s * X2_S + TB2 * X2_TB + (size_t)(xl >> 1) * XST + (size_t)(w & 1) * 8 * X2_G;
        const char* la = him + (33 + 2 * (8 * G + rq)) * M_HS + p * 16;
        bf16x8 B[6][2][2];
#pragma unroll
        for (int u = 0; u < 6; ++u)
#pragma unroll
          for (int i = 0; i < 2; ++i)
#pragma unroll
            for (int ks = 0; ks < 2; ++ks) B[u][i][ks] = tr_frag(la + ((u >> 1) * 96 + (u & 1) + 64 * ks) * M_HS + 8 * i, 8 * M_HS);
#pragma unroll
        for (int u = 0; u < 6; ++u) {
          f32x4 c0 = {0, 0, 0, 0}, c1 = {0, 0, 0, 0};
#pragma unroll
          for (int ks = 0; ks < 2; ++ks) { c0 = MFMAU(B[u][0][ks], F[ks], c0); c1 = MFMAU(B[u][1][ks], F[ks], c1); }
          const int tl = 3 * idx - 42 * TB2 + (u >> 1);
          st16(gd + x_lane_off<X2_G>(lane, tl, tl + 42 * (xl & 1), pb ^ ((w >> 1) << 13)) + (u & 1) * 4 * XST, pack8(c0, c1));
        }
        publish(ws, F_M + s * 256 + idx * 8 + xl, tag, w);
        STAMP2(9, 13);
      }
    }
    if (idx >= 28 && LATE_WF) poll(ws, F_G1 + 256 + (idx - 24) * 32 + w * 8 + lane, lanes(8), tag);
    poll(ws, F_S + w * 3 + lane, lanes(3), tag);
    bar();
    PRIO(PRIO_JA);
    sgpr_settle();
    static_for<32>([&](auto ic) { constexpr int i = decltype(ic)::v; ldfrag<i, true>(W[i], ws + OFF_WFT + w * 32768, lane16); });
    wait_vm<0>();
    asm volatile(".Lt_end:");
  }
  asm volatile(";@Nr");
  tie(W);
  const int s = role ^ 1;
  lane = lane_id();
  const int G = lane >> 4, rq = (lane & 15) >> 2, p = lane & 3;
  const unsigned lane16 = lane * 16;

  const int jb = q;
  if (jb >= 250) {
    if (blockIdx.x == 255 && w == 0) {
#pragma unroll 1
      for (int i = 4; i < 8; ++i) poll(ws, F_G2 + i * 64 + lane, ~0ull, tag);
      set_flag(ws, F_EPOCH, tag, w);
    }
    STAMP_FLUSH();
    return;
  }
  {
    const int tile = jb / 3, vt = jb - 3 * tile;
    u32x4 woa[32], Gt[2];
    sgpr_settle();
    static_for<2>([&](auto ic) { constexpr int i = decltype(ic)::v; ldfrag<i, false>(Gt[i], (const char*)TAB + (TAB_G + 2 * vt) * 1024, lane16); });
    __amdgpu_buffer_rsrc_t grs = mkrsrc(ws + OFF_GE, 32768);
#pragma unroll
    for (int i = 0; i < 2; ++i) dma16<SC1>(grs, lds + J_G + w * 2048 + i * 1024, 16384 + (s * 4 + w) * 2048 + i * 1024 + lane16, 0);
    static_for<16>([&](auto ic) { constexpr int i = decltype(ic)::v; ldfrag<i, true>(woa[i], ws + OFF_WOT + w * 32768, lane16); });
    PRIO(PRIO_F);
    poll(ws, F_G2 + s * 256 + ((tile >= 42 ? 4 : 0) + w) * 32 + lane, lanes(32), tag);
    if (role) { PRIO(PRIO_JA); } else { PRIO(PRIO_JB); }
    tie(Gt);
    STAMP(24);
    char* reg = lds + J_Y + w * 8192;
    {
      __amdgpu_buffer_rsrc_t rs = mkrsrc(ws + OFF_Y2F + s * Y2_S + (size_t)(tile >> 1) * 65536, 65536);
#pragma unroll
      for (int i = 0; i < 8; ++i) {
        const int c = (lane & 15) ^ ((G & 1) << 3) ^ (i & 1);
        dma16<SC1>(rs, reg + i * 1024, (i & 3) * 16384 + (((i >> 2) << 13) ^ ((tile & 64) << 7) ^ pb) + G * 2048 + (4 * w + (c >> 2)) * 128 + (tile & 1) * 64 + (c & 3) * 16, 0);
      }
    }
    wait_vm<0>();
    STAMP(25);
#pragma unroll
    for (int h = 0; h < 2; ++h) {
      f32x4 c[2];
#pragma unroll
      for (int i = 0; i < 2; ++i) {
        const int ch = (8 * h + 2 * p + i) ^ ((rq >> 1) << 3) ^ (G & 1);
        const char* la = reg + (4 * G + (rq >> 1)) * 256 + ch * 16 + (rq & 1) * 8;
        c[i] = MFMAU(tr_frag(la, 512), Gt[0], ((f32x4){0, 0, 0, 0}));
        c[i] = MFMAU(tr_frag(la + 4096, 512), Gt[1], c[i]);
      }
      *(u32x4*)(lds + J_F + (2 * w + h) * 1024 + lane16) = gelu_pack8(c[0], c[1]);
    }
    wait_lds();
    bar();
    STAMP(26);
    f32x4 fe[4];
#pragma unroll
    for (int m = 0; m < 4; ++m) fe[m] = (f32x4){0, 0, 0, 0};
#pragma unroll
    for (int ks = 0; ks < 8; ++ks) {
      const u32x4 bfr = *(const u32x4*)(lds + J_F + ks * 1024 + lane16);
#pragma unroll
      for (int m = 0; m < 4; ++m) fe[m] = MFMAU(W[m * 8 + ks], bfr, fe[m]);
    }
    SB0();
    static_for<16>([&](auto ic) { constexpr int i = 16 + decltype(ic)::v; ldfrag<i, true>(woa[i], ws + OFF_WOT + w * 32768, lane16); });
    SB0();
    bool small = true;
#pragma unroll
    for (int m = 0; m < 4; ++m) {
      fe[m] *= LOG2E2;
#pragma unroll
      for (int r = 0; r < 4; ++r) small = small && (__builtin_fabsf(fe[m][r]) <= 32.0f);
    }
    STAMP(27);
    char* orow = out + ((size_t)(s * T2 + jb * 16 + (lane & 7)) * 8) * 512 + w * 128 + ((lane >> 3) & 1) * 64 + G * 16;
    const bool fast = __all(small);
    if (fast) {
#pragma unroll
      for (int m = 0; m < 4; ++m)
#pragma unroll
        for (int r = 0; r < 4; ++r) fe[m][r] = __builtin_amdgcn_exp2f(fe[m][r]);
#pragma unroll
      for (int u = 0; u < J_AH; ++u) j_make<true>(fe, lds, u, w, lane);
    } else {
#pragma unroll
      for (int i = 0; i < 2; ++i) dma16<SC1>(grs, lds + J_G + w * 2048 + i * 1024, (s * 4 + w) * 2048 + i * 1024 + lane16, 0);
      wait_vm<0>();
#pragma unroll 1
      for (int u = 0; u < 8; ++u) j_make<false>(fe, lds, u, w, lane);
    }
    j_loop(woa, fe, lds, (fast ? J_AH * 8192 : J_F) + 2 * w * 1024 + lane16, fast ? 8192 : 0, orow, w, lane, stv);
  }
  asm volatile(".Lk_end:");
  STAMP(29);
}

#define WS_SKIP 0
extern "C" void kernel_launch(void* const* d_in, const int* in_sizes, int n_in, void* d_out, int out_size, void* d_ws,
                              size_t ws_size, hipStream_t stream) {
  k_all<<<512, 256, 0, stream>>>((const u16*)d_in[0], (const u16*)d_in[1], (const u16*)d_in[2], (const u16*)d_in[3], (const u16*)d_in[4],
                                 (const u16*)d_in[5], (const u16*)d_in[6], (char*)d_ws + WS_SKIP, (char*)d_out);
}
```

```cpp
#include <hip/hip_runtime.h>
#include <hip/hip_bf16.h>
#include <utility>
__device__ const unsigned TAB[4608] __attribute__((aligned(128))) = {
0x3f803f80,0x3f803f80,0x3f803f80,0x3f803f80,0x0,0x0,0x0,0x0,
0x3f7f3f80,0x3f753f7b,0x3f613f6c,0x3f443f54,0x3dcc0000,0x3e973e4b,0x3ef53ec7,0x3f253f10,
0x3f7b3f80,0x3f543f6c,0x3f0b3f33,0x3e323ebb,0x3e4b0000,0x3f103ec7,0x3f573f37,0x3f7c3f6e,
0x3f753f80,0x3f203f54,0x3d993ebb,0xbf00be64,0x3e970000,0x3f483f10,0x3f7f3f6e,0x3f5e3f7a,
0x3f6c3f80,0x3ebb3f33,0xbed3bccc,0xbf71bf3c,0x3ec70000,0x3f6e3f37,0x3f693f80,0x3eaf3f2e,
0x3f613f80,0x3d993f0b,0xbf4cbed3,0xbf71bf7d,0x3ef50000,0x3f7f3f57,0x3f1b3f69,0xbeaf3e19,
0x3f543f80,0xbe643ebb,0xbf7dbf3c,0xbf00bf67,0x3f100000,0x3f7a3f6e,0x3e193f2e,0xbf5ebede,
0x3f443f80,0xbf003e32,0xbf71bf71,0x3e32bf00,0x3f250000,0x3f5e3f7c,0xbeaf3eaf,0xbf7cbf5e,
0x3f803f80,0x3f803f80,0x3f803f80,0x3f803f80,0x0,0x0,0x0,0x0,
0x3f203f33,0x3eea3f0b,0x3e8b3ebb,0x3d993e32,0x3f483f37,0x3f643f57,0x3f763f6e,0x3f7f3f7c,
0xbe64bccc,0xbf15bed3,0xbf5abf3c,0xbf7dbf71,0x3f7a3f80,0x3f503f69,0x3f053f2e,0x3e193eaf,
0xbf67bf3c,0xbf7dbf7d,0xbf3cbf67,0xbe64bf00,0x3ede3f2e,0xbe193e19,0xbf2ebede,0xbf7abf5e,
0xbf67bf80,0xbea3bf29,0x3eea3d99,0x3f753f44,0xbedebd4c,0xbf73bf40,0xbf64bf7f,0xbe97bf25,
0xbe64bf29,0x3f333e8b,0x3f7b3f75,0x3ebb3f44,0xbf7abf40,0xbf37bf76,0x3e4bbe97,0x3f6e3f25,
0x3f203d99,0x3f753f75,0x3d993f20,0xbf67bf00,0xbf48bf7f,0x3e97be97,0x3f7f3f48,0x3ede3f5e,
0x3f803f44,0x3e323f44,0xbf71bf00,0xbf00bf71,0xbf25,0x3f7c3f25,0x3eaf3f5e,0xbf5ebeaf,
0x3f803f80,0x3f803f80,0x3f803f80,0x3f803f80,0x0,0x0,0x0,0x0,
0xbdffbccc,0xbea3be64,0xbf00bed3,0xbf29bf15,0x3f7e3f80,0x3f733f7a,0x3f5e3f69,0x3f403f50,
0xbf78bf80,0xbf4cbf67,0xbf00bf29,0xbdffbea3,0xbe7dbd4c,0xbf1bbede,0xbf5ebf40,0xbf7ebf73,
0x3ebb3d99,0x3f543f20,0x3f803f75,0x3f543f75,0xbf6ebf7f,0xbf10bf48,0xbe97,0x3f103e97,
0x3f613f7f,0x3e8b3f20,0xbf00bdff,0xbf78bf4c,0x3ef53dcc,0x3f763f48,0x3f5e3f7e,0x3e7d3f1b,
0xbf15bdff,0xbf80bf67,0xbf00bf5a,0x3eeabccc,0x3f503f7e,0xbd4c3ede,0xbf5ebf05,0xbf64bf80,
0xbf3cbf7d,0x3ebbbe64,0x3f803f54,0x3ebb3f54,0xbf2ebe19,0xbf6ebf7a,0xbf10,0x3f6e3f10,
0x3f443e32,0x3f443f80,0xbf003e32,0xbf71bf71,0xbf25bf7c,0x3f250000,0x3f5e3f7c,0xbeaf3eaf,
0x3f803f80,0x3f803f80,0x3f803f80,0x3f803f80,0x0,0x0,0x0,0x0,
0xbf4cbf3c,0xbf67bf5a,0xbf78bf71,0xbf80bf7d,0x3f1b3f2e,0x3ede3f05,0x3e7d3eaf,0x3d4c3e19,
0x3e8b3d99,0x3f203eea,0x3f613f44,0x3f7f3f75,0xbf76bf7f,0xbf48bf64,0xbef5bf25,0xbdccbe97,
0x3ebb3f20,0xbe643d99,0xbf3cbf00,0xbf7dbf67,0x3f6e3f48,0x3f7a3f7f,0x3f2e3f5e,0x3e193ede,
0xbf5abf7d,0xbe64bf15,0x3f0b3e32,0x3f7b3f54,0xbf05be19,0xbf7abf50,0xbf57bf7c,0xbe4bbf10,
0x3f7f3f54,0x3f203f6c,0xbea33e32,0xbf78bf3c,0xbdccbf10,0x3f483ec7,0x3f733f7c,0x3e7d3f2e,
0xbf3cbe64,0xbf67bf7d,0x3d99bf00,0x3f753f20,0x3f2e3f7a,0xbede3e19,0xbf7fbf5e,0xbe97bf48,
0x3e32bf00,0x3f803f44,0x3e323f44,0xbf71bf00,0xbf7cbf5e,0xbf25,0x3f7c3f25,0x3eaf3f5e,
0x3f803f80,0x3f803f80,0x3f803f80,0x3f803f80,0x0,0x0,0x0,0x0,
0xbf7dbf80,0xbf71bf78,0xbf5abf67,0xbf3cbf4c,0xbe19bd4c,0xbeafbe7d,0xbf05bede,0xbf2ebf1b,
0x3f753f7f,0x3f443f61,0x3eea3f20,0x3d993e8b,0x3e973dcc,0x3f253ef5,0x3f643f48,0x3f7f3f76,
0xbf67bf7d,0xbf00bf3c,0x3d99be64,0x3f203ebb,0xbedebe19,0xbf5ebf2e,0xbf7fbf7a,0xbf48bf6e,
0x3f543f7b,0x3e323f0b,0xbf15be64,0xbf7dbf5a,0x3f103e4b,0x3f7c3f57,0x3f503f7a,0x3e193f05,
0xbf3cbf78,0x3e32bea3,0x3f6c3f20,0x3f543f7f,0xbf2ebe7d,0xbf7cbf73,0xbec7bf48,0x3f103dcc,
0x3f203f75,0xbf003d99,0xbf7dbf67,0xbe64bf3c,0x3f483e97,0x3f5e3f7f,0xbe193ede,0xbf7abf2e,
0xbf00bf71,0x3f443e32,0x3f443f80,0xbf003e32,0xbf5ebeaf,0xbf25bf7c,0x3f250000,0x3f5e3f7c,
0x3f803f80,0x3f803f80,0x3f803f80,0x3f803f80,0x0,0x0,0x0,0x0,
0xbf15bf29,0xbed3bf00,0xbe64bea3,0xbcccbdff,0xbf50bf40,0xbf69bf5e,0xbf7abf73,0xbf80bf7e,
0xbea3bdff,0xbf29bf00,0xbf67bf4c,0xbf80bf78,0x3f733f7e,0x3f403f5e,0x3ede3f1b,0x3d4c3e7d,
0x3f753f54,0x3f753f80,0x3f203f54,0x3d993ebb,0xbe97bf10,0x3e970000,0x3f483f10,0x3f7f3f6e,
0xbf4cbf78,0xbdffbf00,0x3f203e8b,0x3f7f3f61,0xbf1bbe7d,0xbf7ebf5e,0xbf48bf76,0xbdccbef5,
0xbccc3eea,0xbf5abf00,0xbf67bf80,0xbdffbf15,0x3f803f64,0x3f053f5e,0xbede3d4c,0xbf7ebf50,
0x3f543ebb,0x3f543f80,0xbe643ebb,0xbf7dbf3c,0xbf10bf6e,0x3f100000,0x3f7a3f6e,0x3e193f2e,
0xbf71bf71,0x3e32bf00,0x3f803f44,0x3e323f44,0xbeaf3eaf,0xbf7cbf5e,0xbf25,0x3f7c3f25,
0x3f803f80,0x3f803f80,0x3f803f80,0x3f803f80,0x0,0x0,0x0,0x0,
0x3e323d99,0x3ebb3e8b,0x3f0b3eea,0x3f333f20,0xbf7cbf7f,0xbf6ebf76,0xbf57bf64,0xbf37bf48,
0xbf71bf7d,0xbf3cbf5a,0xbed3bf15,0xbcccbe64,0xbeafbe19,0xbf2ebf05,0xbf69bf50,0xbf80bf7a,
0xbf00be64,0xbf67bf3c,0xbf7dbf7d,0xbf3cbf67,0x3f5e3f7a,0x3ede3f2e,0xbe193e19,0xbf2ebede,
0x3f443f75,0x3d993eea,0xbf29bea3,0xbf80bf67,0x3f253e97,0x3f7f3f64,0x3f403f73,0x3d4c3ede,
0x3f443ebb,0x3f753f7b,0x3e8b3f33,0xbf29be64,0xbf25bf6e,0x3e97be4b,0x3f763f37,0x3f403f7a,
0xbf00bf67,0x3f203d99,0x3f753f75,0x3d993f20,0xbf5ebede,0xbf48bf7f,0x3e97be97,0x3f7f3f48,
0xbf71bf00,0xbf00bf71,0x3f443e32,0x3f443f80,0x3eaf3f5e,0xbf5ebeaf,0xbf25bf7c,0x3f250000,
0x3f803f80,0x3f803f80,0x3f803f80,0x3f80,0x0,0x0,0x0,0x0,
0x3f543f44,0x3f6c3f61,0x3f7b3f75,0x3f7f,0xbf10bf25,0xbec7bef5,0xbe4bbe97,0xbdcc,
0x3ebb3e32,0x3f333f0b,0x3f6c3f54,0x3f7b,0xbf6ebf7c,0xbf37bf57,0xbec7bf10,0xbe4b,
0xbe64bf00,0x3ebb3d99,0x3f543f20,0x3f75,0xbf7abf5e,0xbf6ebf7f,0xbf10bf48,0xbe97,
0xbf3cbf71,0xbcccbed3,0x3f333ebb,0x3f6c,0xbf2ebeaf,0xbf80bf69,0xbf37bf6e,0xbec7,
0xbf7dbf71,0xbed3bf4c,0x3f0b3d99,0x3f61,0xbe193eaf,0xbf69bf1b,0xbf57bf7f,0xbef5,
0xbf67bf00,0xbf3cbf7d,0x3ebbbe64,0x3f54,0x3ede3f5e,0xbf2ebe19,0xbf6ebf7a,0xbf10,
0xbf003e32,0xbf71bf71,0x3e32bf00,0x3f44,0x3f5e3f7c,0xbeaf3eaf,0xbf7cbf5e,0xbf25,
0x3f333f80,0xbf3cbccc,0xbf29bf80,0x3f443d99,0x3f370000,0x3f2e3f80,0xbf40bd4c,0xbf25bf7f,
0x3f203f80,0xbf67be64,0xbe64bf67,0x3f803f20,0x3f480000,0x3ede3f7a,0xbf7abede,0xbf48,
0x3f0b3f80,0xbf7dbed3,0x3e8bbf29,0x3f443f75,0x3f570000,0x3e193f69,0xbf76bf40,0x3f25be97,
0x3eea3f80,0xbf7dbf15,0x3f33bea3,0x3e323f75,0x3f640000,0xbe193f50,0xbf37bf73,0x3f7c3e97,
0x3ebb3f80,0xbf67bf3c,0x3f753d99,0xbf003f20,0x3f6e0000,0xbede3f2e,0xbe97bf7f,0x3f5e3f48,
0x3e8b3f80,0xbf3cbf5a,0x3f7b3eea,0xbf713d99,0x3f760000,0xbf2e3f05,0x3e4bbf64,0x3eaf3f7f,
0x3e323f80,0xbf00bf71,0x3f443f44,0xbf71bf00,0x3f7c0000,0xbf5e3eaf,0x3f25bf25,0xbeaf3f5e,
0x3d993f80,0xbe64bf7d,0x3ebb3f75,0xbf00bf67,0x3f7f0000,0xbf7a3e19,0x3f6ebe97,0xbf5e3ede,
0x3f203f7f,0xbf4cbdff,0xbf15bf7d,0x3f543e32,0x3f483dcc,0x3f1b3f7e,0xbf50be19,0xbf10bf7c,
0xbe643f20,0xbf67bf67,0x3f20be64,0x3f203f80,0x3f7a3f48,0xbede3ede,0xbf48bf7a,0x3f480000,
0xbf67bdff,0xbcccbf5a,0x3f6c3f54,0xbf3c3e32,0x3ede3f7e,0xbf80bf05,0x3ec7bf10,0x3f2e3f7c,
0xbf67bf4c,0x3f61bccc,0xbdff3f54,0xbf3cbf71,0xbede3f1b,0xbef5bf80,0x3f7e3f10,0xbf2e3eaf,
0xbe64bf7d,0x3f543f54,0xbf7dbe64,0x3f20bf00,0xbf7abe19,0x3f10bf10,0x3e193f7a,0xbf48bf5e,
0x3f20bf15,0xbdff3f6c,0xbed3bf7d,0x3f543f44,0xbf48bf50,0x3f7e3ec7,0xbf693e19,0x3f10bf25,
0x3f803e32,0xbf713e32,0x3f44bf00,0xbf003f44,0xbf7c,0x3eaf3f7c,0xbf25bf5e,0x3f5e3f25,
0x3f203f54,0xbf3cbf3c,0x3f543f20,0xbf67bf00,0x3f48bf10,0xbf2e3f2e,0x3f10bf48,0xbede3f5e,
0x3f0b3f7b,0xbf5abe64,0xbf00bf78,0x3f613e8b,0x3f573e4b,0x3f053f7a,0xbf5ebe7d,0xbef5bf76,
0xbf67be64,0xbe64bf67,0x3f803f20,0xbe643f20,0x3ede3f7a,0xbf7abede,0xbf48,0x3f7a3f48,
0xbea3bf78,0x3f7f3f20,0xbf003eea,0xbf15bf80,0xbf73be7d,0x3dccbf48,0x3f5e3f64,0xbf503d4c,
0x3f7b3e8b,0xbed33f20,0xbf00bf80,0x3f7f3f0b,0xbe4bbf76,0x3f693f48,0xbf5e3d4c,0x3dccbf57,
0x3d993f75,0xbf3cbf67,0x3f803ebb,0xbf3c3ebb,0x3f7f3e97,0xbf2e3ede,0xbf6e,0x3f2e3f6e,
0xbf80bea3,0x3f61be64,0xbf003f33,0xbcccbf78,0xbd4c3f73,0xbef5bf7a,0x3f5e3f37,0xbf80be7d,
0x3e32bf71,0x3e323f80,0xbf00bf71,0x3f443f44,0xbf7cbeaf,0x3f7c0000,0xbf5e3eaf,0x3f25bf25,
0x3f753ebb,0xbf7dbe64,0x3f803d99,0xbf7d3d99,0x3e97bf6e,0xbe193f7a,0xbf7f,0x3e193f7f,
0x3eea3f75,0xbf67bea3,0xbed3bf71,0x3f6c3ebb,0x3f643e97,0x3ede3f73,0xbf69beaf,0xbec7bf6e,
0xbf67bf67,0x3f20be64,0x3f203f80,0xbf67be64,0xbede3ede,0xbf48bf7a,0x3f480000,0x3ede3f7a,
0x3f7b3ebb,0xbe643f33,0xbf4cbf71,0x3f613d99,0xbe4bbf6e,0x3f7a3f37,0xbf1b3eaf,0xbef5bf7f,
0xbf293ebb,0xbe64bf78,0x3f6c3f44,0xbf5a3d99,0x3f403f6e,0xbf7abe7d,0x3ec7bf25,0x3f053f7f,
0x3d99bf67,0x3f203f75,0xbf7dbf00,0x3f54be64,0xbf7fbede,0x3f48be97,0xbe193f5e,0xbf10bf7a,
0x3f0b3f75,0xbf67bf29,0x3f7f3e32,0xbf4c3ebb,0x3f57be97,0xbede3f40,0xbdccbf7c,0x3f1b3f6e,
0xbf71bf00,0x3f803e32,0xbf713e32,0x3f44bf00,0xbeaf3f5e,0xbf7c,0x3eaf3f7c,0xbf25bf5e,
0x3f75be64,0xbf673ebb,0x3f54bf00,0xbf3c3f20,0xbe97bf7a,0x3ede3f6e,0xbf10bf5e,0x3f2e3f48,
0x3ebb3f6c,0xbf71bed3,0xbea3bf67,0x3f753eea,0x3f6e3ec7,0x3eaf3f69,0xbf73bede,0xbe97bf64,
0xbe64bf67,0x3f803f20,0xbe643f20,0xbf67bf67,0xbf7abede,0xbf48,0x3f7a3f48,0xbede3ede,
0x3d993f61,0xbf71bf4c,0x3f33be64,0x3ebb3f7b,0x3f7f3ef5,0xbeaf3f1b,0xbf37bf7a,0x3f6e3e4b,
0x3d99bf5a,0x3f443f6c,0xbf78be64,0x3ebbbf29,0xbf7fbf05,0x3f25bec7,0x3e7d3f7a,0xbf6ebf40,
0xbe643f54,0xbf00bf7d,0x3f753f20,0xbf673d99,0x3f7a3f10,0xbf5e3e19,0x3e97bf48,0x3ede3f7f,
0x3ebbbf4c,0x3e323f7f,0xbf29bf67,0x3f753f0b,0xbf6ebf1b,0x3f7c3dcc,0xbf403ede,0x3e97bf57,
0xbf003f44,0x3e32bf71,0x3e323f80,0xbf00bf71,0x3f5e3f25,0xbf7cbeaf,0x3f7c0000,0xbf5e3eaf,
0x3f20bf3c,0xbf003f54,0x3ebbbf67,0xbe643f75,0xbf48bf2e,0x3f5e3f10,0xbf6ebede,0x3f7a3e97,
0x3e8b3f61,0xbf78bf00,0xbe64bf5a,0x3f7b3f0b,0x3f763ef5,0x3e7d3f5e,0xbf7abf05,0xbe4bbf57,
0x3f20be64,0x3f203f80,0xbf67be64,0xbe64bf67,0xbf48bf7a,0x3f480000,0x3ede3f7a,0xbf7abede,
0xbf80bf15,0x3eeabf00,0x3f203f7f,0xbf78bea3,0xbd4c3f50,0xbf64bf5e,0x3f48bdcc,0x3e7d3f73,
0x3f0b3f7f,0xbf80bf00,0x3f20bed3,0x3e8b3f7b,0x3f57bdcc,0xbd4c3f5e,0xbf48bf69,0x3f763e4b,
0x3ebbbf3c,0x3ebb3f80,0xbf67bf3c,0x3f753d99,0xbf6ebf2e,0x3f6e0000,0xbede3f2e,0xbe97bf7f,
0xbf78bccc,0x3f33bf00,0xbe643f61,0xbea3bf80,0x3e7d3f80,0xbf37bf5e,0x3f7a3ef5,0xbf733d4c,
0x3f443f44,0xbf71bf00,0x3f803e32,0xbf713e32,0x3f25bf25,0xbeaf3f5e,0xbf7c,0x3eaf3f7c,
0x3d99bf7d,0x3d993f80,0xbe64bf7d,0x3ebb3f75,0xbf7fbe19,0x3f7f0000,0xbf7a3e19,0x3f6ebe97,
0x3e323f54,0xbf7dbf15,0xbdffbf4c,0x3f7f3f20,0x3f7c3f10,0x3e193f50,0xbf7ebf1b,0xbdccbf48,
0x3f803f20,0xbe643f20,0xbf67bf67,0x3f20be64,0xbf48,0x3f7a3f48,0xbede3ede,0xbf48bf7a,
0x3e32bf3c,0x3f543f6c,0xbf5abccc,0xbdffbf67,0xbf7cbf2e,0x3f10bec7,0x3f053f80,0xbf7ebede,
0xbf71bf3c,0x3f54bdff,0xbccc3f61,0xbf4cbf67,0xbeaf3f2e,0xbf10bf7e,0x3f803ef5,0xbf1b3ede,
0xbf003f20,0xbe64bf7d,0x3f543f54,0xbf7dbe64,0x3f5e3f48,0xbf7abe19,0x3f10bf10,0x3e193f7a,
0x3f443f54,0xbf7dbed3,0x3f6cbdff,0xbf153f20,0x3f25bf10,0xbe193f69,0xbec7bf7e,0x3f503f48,
0x3f44bf00,0xbf003f44,0x3e32bf71,0x3e323f80,0xbf25bf5e,0x3f5e3f25,0xbf7cbeaf,0x3f7c0000,
0xbf00bf67,0x3f203f54,0xbf3cbf3c,0x3f543f20,0xbf5e3ede,0x3f48bf10,0xbf2e3f2e,0x3f10bf48,
0x3d993f44,0xbf80bf29,0xbcccbf3c,0x3f33,0x3f7f3f25,0x3d4c3f40,0xbf80bf2e,0xbf37,
0x3f203f80,0xbf67be64,0xbe64bf67,0x3f20,0x3f480000,0x3ede3f7a,0xbf7abede,0xbf48,
0x3f753f44,0xbf293e8b,0xbed3bf7d,0x3f0b,0x3e97bf25,0x3f403f76,0xbf69be19,0xbf57,
0x3f753e32,0xbea33f33,0xbf15bf7d,0x3eea,0xbe97bf7c,0x3f733f37,0xbf503e19,0xbf64,
0x3f20bf00,0x3d993f75,0xbf3cbf67,0x3ebb,0xbf48bf5e,0x3f7f3e97,0xbf2e3ede,0xbf6e,
0x3d99bf71,0x3eea3f7b,0xbf5abf3c,0x3e8b,0xbf7fbeaf,0x3f64be4b,0xbf053f2e,0xbf76,
0xbf00bf71,0x3f443f44,0xbf71bf00,0x3e32,0xbf5e3eaf,0x3f25bf25,0xbeaf3f5e,0xbf7c,
0xbf67bf00,0x3f753ebb,0xbf7dbe64,0x3d99,0xbede3f5e,0x3e97bf6e,0xbe193f7a,0xbf7f,
0xbccc3f80,0x3d99bf80,0xbdff3f7f,0x3e32bf7d,0x3f800000,0xbf7fbd4c,0x3f7e3dcc,0xbf7cbe19,
0xbdff3f80,0x3ebbbf78,0xbf153f61,0x3f44bf3c,0x3f7e0000,0xbf6ebe7d,0x3f503ef5,0xbf25bf2e,
0xbe643f80,0x3f20bf67,0xbf673f20,0x3f80be64,0x3f7a0000,0xbf48bede,0x3ede3f48,0xbf7a,
0xbea33f80,0x3f54bf4c,0xbf803e8b,0x3f443ebb,0x3f730000,0xbf10bf1b,0xbd4c3f76,0x3f25bf6e,
0xbed33f80,0x3f75bf29,0xbf5abdff,0x3e323f54,0x3f690000,0xbe97bf40,0xbf053f7e,0x3f7cbf10,
0xbf003f80,0x3f80bf00,0xbf00bf00,0xbf003f80,0x3f5e0000,0xbf5e,0xbf5e3f5e,0x3f5e0000,
0xbf153f80,0x3f75bea3,0xbcccbf4c,0xbf713f54,0x3f500000,0x3e97bf73,0xbf803f1b,0x3eaf3f10,
0xbf293f80,0x3f54bdff,0x3eeabf78,0xbf713ebb,0x3f400000,0x3f10bf7e,0xbf643e7d,0xbeaf3f6e,
0xbe643f7b,0x3e8bbf78,0xbea33f75,0x3ebbbf71,0x3f7a3e4b,0xbf76be7d,0x3f733e97,0xbf6ebeaf,
0xbf673f0b,0x3f7bbea3,0xbf803d99,0x3f753e32,0x3ede3f57,0xbe4bbf73,0xbd4c3f7f,0x3e97bf7c,
0xbf67be64,0x3f203f20,0xbe64bf67,0xbe643f80,0xbede3f7a,0x3f48bf48,0xbf7a3ede,0x3f7a0000,
0xbe64bf5a,0xbed33f7f,0x3f61bf3c,0xbf7d3e32,0xbf7a3f05,0x3f693dcc,0xbef5bf2e,0xbe193f7c,
0x3f20bf78,0xbf803eea,0x3f333ebb,0x3d99bf71,0xbf48be7d,0x3d4c3f64,0x3f37bf6e,0xbf7f3eaf,
0x3f80bf00,0xbf00bf00,0xbf003f80,0x3f80bf00,0xbf5e,0xbf5e3f5e,0x3f5e0000,0xbf5e,
0x3f203e8b,0x3f0bbf80,0xbf783ebb,0x3d993f44,0x3f48bf76,0xbf573d4c,0xbe7d3f6e,0x3f7fbf25,
0xbe643f61,0x3f7fbf15,0xbcccbf3c,0xbf7d3f44,0x3f7abef5,0x3dccbf50,0xbf803f2e,0x3e193f25,
0xbed33f6c,0x3eeabf67,0xbf003f61,0x3f0bbf5a,0x3f693ec7,0xbf64bede,0x3f5e3ef5,0xbf57bf05,
0xbf5abed3,0x3f333f20,0xbf00bf4c,0x3e8b3f6c,0xbf053f69,0x3f37bf48,0xbf5e3f1b,0x3f76bec7,
0x3f20bf67,0xbf673f20,0x3f80be64,0xbf67be64,0xbf48bede,0x3ede3f48,0xbf7a,0xbede3f7a,
0x3f333eea,0xbdffbf67,0xbf003f7b,0x3f6cbf29,0x3f37bf64,0xbf7e3ede,0x3f5e3e4b,0xbec7bf40,
0xbf4c3f61,0x3f7bbe64,0xbf00bf15,0xbea33f7f,0x3f1b3ef5,0x3e4bbf7a,0xbf5e3f50,0x3f73bdcc,
0xbf00bf00,0xbf003f80,0x3f80bf00,0xbf00bf00,0xbf5e3f5e,0x3f5e0000,0xbf5e,0xbf5e3f5e,
0x3f6cbf5a,0xbf29be64,0xbf003f7f,0x3f7bbed3,0xbec7bf05,0xbf403f7a,0x3f5ebdcc,0x3e4bbf69,
0x3e8b3f0b,0x3f6cbf67,0xbf00bea3,0xbf4c3f7b,0x3f76bf57,0xbec7bede,0xbf5e3f73,0x3f1b3e4b,
0xbf153f54,0x3f20bf4c,0xbf293f44,0x3f33bf3c,0x3f503f10,0xbf48bf1b,0x3f403f25,0xbf37bf2e,
0xbcccbf7d,0xbe643f7f,0x3eeabf71,0xbf293f54,0xbf803e19,0x3f7a3dcc,0xbf64beaf,0x3f403f10,
0x3f203f20,0xbe64bf67,0xbe643f80,0x3f20bf67,0x3f48bf48,0xbf7a3ede,0x3f7a0000,0xbf48bede,
0xbf783d99,0x3f203f0b,0xbcccbf71,0xbf153f75,0xbe7d3f7f,0x3f48bf57,0xbf803eaf,0x3f503e97,
0x3f6cbf3c,0xbf67bccc,0x3e8b3f44,0x3f0bbf7d,0xbec7bf2e,0xbede3f80,0x3f76bf25,0xbf57be19,
0xbf003f80,0x3f80bf00,0xbf00bf00,0xbf003f80,0x3f5e0000,0xbf5e,0xbf5e3f5e,0x3f5e0000,
0xbdffbf3c,0xbf673f61,0x3f333e32,0x3eeabf7d,0xbf7e3f2e,0x3ede3ef5,0x3f37bf7c,0xbf643e19,
0x3f333d99,0x3f20bf80,0xbf5a3e32,0xbed33f75,0x3f37bf7f,0xbf483d4c,0xbf053f7c,0x3f69be97,
0xbf3c3f33,0x3f44bf29,0xbf4c3f20,0x3f54bf15,0x3f2e3f37,0xbf25bf40,0x3f1b3f48,0xbf10bf50,
0x3f54bf29,0xbf713eea,0x3f7fbe64,0xbf7dbccc,0xbf10bf40,0x3eaf3f64,0xbdccbf7a,0xbe193f80,
0xbf673f20,0x3f80be64,0xbf67be64,0x3f203f20,0x3ede3f48,0xbf7a,0xbede3f7a,0x3f48bf48,
0x3f75bf15,0xbf71bccc,0x3f0b3f20,0x3d99bf78,0xbe97bf50,0xbeaf3f80,0x3f57bf48,0xbf7f3e7d,
0xbf7d3f0b,0x3f443e8b,0xbcccbf67,0xbf3c3f6c,0x3e193f57,0x3f25bf76,0xbf803ede,0x3f2e3ec7,
0x3f80bf00,0xbf00bf00,0xbf003f80,0x3f80bf00,0xbf5e,0xbf5e3f5e,0x3f5e0000,0xbf5e,
0xbf7d3eea,0x3e323f33,0x3f61bf67,0xbf3cbdff,0xbe193f64,0x3f7cbf37,0xbef5bede,0xbf2e3f7e,
0x3f75bed3,0x3e32bf5a,0xbf803f20,0x3d993f33,0x3e97bf69,0xbf7c3f05,0xbd4c3f48,0x3f7fbf37,
0xbf5a3f0b,0x3f61bf00,0xbf673eea,0x3f6cbed3,0x3f053f57,0xbef5bf5e,0x3ede3f64,0xbec7bf69,
0x3f6c3e8b,0xbf4cbf00,0x3f203f33,0xbed3bf5a,0x3ec7bf76,0xbf1b3f5e,0x3f48bf37,0xbf693f05,
0xbe64bf67,0xbe643f80,0x3f20bf67,0xbf673f20,0xbf7a3ede,0x3f7a0000,0xbf48bede,0x3ede3f48,
0xbf293f6c,0x3f7bbf00,0xbf67bdff,0x3eea3f33,0x3f403ec7,0xbe4bbf5e,0xbede3f7e,0x3f64bf37,
0x3f7fbea3,0xbf15bf00,0xbe643f7b,0x3f61bf4c,0x3dccbf73,0xbf503f5e,0x3f7abe4b,0xbef5bf1b,
0xbf00bf00,0xbf003f80,0x3f80bf00,0xbf00bf00,0xbf5e3f5e,0x3f5e0000,0xbf5e,0xbf5e3f5e,
0xbed33f7b,0x3f7fbf00,0xbe64bf29,0xbf5a3f6c,0x3f69be4b,0x3dccbf5e,0xbf7a3f40,0x3f053ec7,
0x3f7bbf4c,0xbea3bf00,0xbf673f6c,0x3f0b3e8b,0xbe4bbf1b,0xbf733f5e,0x3ede3ec7,0x3f57bf76,
0xbf713ebb,0x3f75bea3,0xbf783e8b,0x3f7bbe64,0x3eaf3f6e,0xbe97bf73,0x3e7d3f76,0xbe4bbf7a,
0x3e323f75,0x3d99bf80,0xbea33f7b,0x3f0bbf67,0x3f7cbe97,0xbf7f3d4c,0x3f733e4b,0xbf57bede,
0x3f80be64,0xbf67be64,0x3f203f20,0xbe64bf67,0xbf7a,0xbede3f7a,0x3f48bf48,0xbf7a3ede,
0x3e32bf7d,0xbf3c3f61,0x3f7fbed3,0xbf5abe64,0xbf7c3e19,0x3f2e3ef5,0xbdccbf69,0xbf053f7a,
0xbf713d99,0x3ebb3f33,0x3eeabf80,0xbf783f20,0xbeaf3f7f,0x3f6ebf37,0xbf64bd4c,0x3e7d3f48,
0xbf003f80,0x3f80bf00,0xbf00bf00,0xbf003f80,0x3f5e0000,0xbf5e,0xbf5e3f5e,0x3f5e0000,
0x3f443d99,0x3ebbbf78,0xbf803f0b,0x3e8b3f20,0x3f25bf7f,0xbf6e3e7d,0xbd4c3f57,0x3f76bf48,
0x3f44bf7d,0xbf3cbccc,0xbf153f7f,0x3f61be64,0xbf25be19,0xbf2e3f80,0x3f50bdcc,0x3ef5bf7a,
0xbf7d3e32,0x3f7fbdff,0xbf803d99,0xbccc,0x3e193f7c,0xbdccbf7e,0x3d4c3f7f,0xbf80,
0xbf3c3f44,0x3f61bf15,0xbf783ebb,0xbdff,0x3f2e3f25,0xbef5bf50,0x3e7d3f6e,0xbf7e,
0xbe643f80,0x3f20bf67,0xbf673f20,0xbe64,0x3f7a0000,0xbf48bede,0x3ede3f48,0xbf7a,
0x3ebb3f44,0x3e8bbf80,0xbf4c3f54,0xbea3,0x3f6ebf25,0xbf763d4c,0x3f1b3f10,0xbf73,
0x3f543e32,0xbdffbf5a,0xbf293f75,0xbed3,0x3f10bf7c,0xbf7e3f05,0x3f403e97,0xbf69,
0x3f80bf00,0xbf00bf00,0xbf003f80,0xbf00,0xbf5e,0xbf5e3f5e,0x3f5e0000,0xbf5e,
0x3f54bf71,0xbf4cbccc,0xbea33f75,0xbf15,0xbf10beaf,0xbf1b3f80,0x3f73be97,0xbf50,
0x3ebbbf71,0xbf783eea,0xbdff3f54,0xbf29,0xbf6e3eaf,0xbe7d3f64,0x3f7ebf10,0xbf40,
0xbf3c3f80,0x3f203d99,0x3f54bf7d,0xbf00be64,0x3f2e0000,0x3f48bf7f,0xbf10be19,0xbf5e3f7a,
0xbf4c3f80,0x3ebb3e8b,0x3f7fbf5a,0x3e32bf3c,0x3f1b0000,0x3f6ebf76,0xbdccbf05,0xbf7c3f2e,
0xbf5a3f80,0x3d993eea,0x3f6cbf15,0x3f44bf7d,0x3f050000,0x3f7fbf64,0x3ec7bf50,0xbf253e19,
0xbf673f80,0xbe643f20,0x3f20be64,0x3f80bf67,0x3ede0000,0x3f7abf48,0x3f48bf7a,0xbede,
0xbf713f80,0xbf003f44,0x3e323e32,0x3f44bf00,0x3eaf0000,0x3f5ebf25,0x3f7cbf7c,0x3f25bf5e,
0xbf783f80,0xbf3c3f61,0xbea33f0b,0x3e323d99,0x3e7d0000,0x3f2ebef5,0x3f73bf57,0x3f7cbf7f,
0xbf7d3f80,0xbf673f75,0xbf3c3f54,0xbf003f20,0x3e190000,0x3edebe97,0x3f2ebf10,0x3f5ebf48,
0xbf803f80,0xbf7d3f7f,0xbf783f7b,0xbf713f75,0x3d4c0000,0x3e19bdcc,0x3e7dbe4b,0x3eafbe97,
0xbf673f75,0x3ebb3ebb,0x3f75bf67,0xbe64bf00,0x3ede3e97,0x3f6ebf6e,0xbe97bede,0xbf7a3f5e,
0xbf673eea,0xbf293f7b,0x3f0b3d99,0x3f75bf71,0xbede3f64,0x3f40be4b,0x3f57bf7f,0xbe97beaf,
0xbe64bea3,0xbf783f33,0xbf293f75,0x3ebb3e32,0xbf7a3f73,0xbe7d3f37,0x3f40be97,0x3f6ebf7c,
0x3f20bf67,0xbe64be64,0xbf673f20,0xbf673f80,0xbf483ede,0xbf7a3f7a,0xbede3f48,0x3ede0000,
0x3f80bf71,0x3f44bf71,0x3e32bf00,0xbf003e32,0xbeaf,0xbf253eaf,0xbf7c3f5e,0xbf5e3f7c,
0x3f20bed3,0x3f6cbf4c,0x3f7fbf7d,0x3f54bf71,0x3f48bf69,0x3ec7bf1b,0xbdccbe19,0xbf103eaf,
0xbe643ebb,0x3d993d99,0x3ebbbe64,0x3f20bf00,0x3f7abf6e,0x3f7fbf7f,0x3f6ebf7a,0x3f48bf5e,
0xbf673f6c,0xbf5a3f61,0xbf4c3f54,0xbf3c3f44,0x3edebec7,0x3f05bef5,0x3f1bbf10,0x3f2ebf25,
0xbf7d3f54,0x3d993f20,0x3f80bf3c,0x3d99bf3c,0x3e193f10,0x3f7fbf48,0xbf2e,0xbf7f3f2e,
0xbcccbf15,0xbf783f20,0xbf003f6c,0x3f33bdff,0xbf803f50,0xbe7d3f48,0x3f5ebec7,0x3f37bf7e,
0x3f7fbf4c,0x3f0bbf67,0xbf00bccc,0xbf803f61,0x3dccbf1b,0xbf573ede,0xbf5e3f80,0x3d4c3ef5,
0xbe643f20,0x3f20be64,0x3f80bf67,0x3f20bf67,0x3f7abf48,0x3f48bf7a,0xbede,0xbf483ede,
0xbf713f44,0xbf713f80,0xbf003f44,0x3e323e32,0xbeaf3f25,0x3eaf0000,0x3f5ebf25,0x3f7cbf7c,
0x3eeabf29,0xbcccbe64,0xbf003e8b,0xbf5a3f33,0xbf643f40,0xbf803f7a,0xbf5e3f76,0xbf053f37,
0x3f54bf3c,0x3f75bf67,0x3f80bf7d,0x3f75bf7d,0x3f10bf2e,0x3e97bede,0xbe19,0xbe973e19,
0xbf293f33,0xbf153f20,0xbf003f0b,0xbed33eea,0x3f40bf37,0x3f50bf48,0x3f5ebf57,0x3f69bf64,
0xbf7d3f20,0xbe643f54,0x3f75bf00,0x3ebbbf67,0xbe193f48,0x3f7abf10,0x3e97bf5e,0xbf6e3ede,
0x3f61bf7d,0xbe64bed3,0xbf803f44,0xbea33f54,0xbef5be19,0xbf7a3f69,0xbd4c3f25,0x3f73bf10,
0xbed33f54,0x3f20bdff,0x3f7bbf71,0x3e8bbf3c,0x3f69bf10,0x3f48bf7e,0xbe4bbeaf,0xbf763f2e,
0xbe64be64,0xbf673f20,0xbf673f80,0xbe643f20,0xbf7a3f7a,0xbede3f48,0x3ede0000,0x3f7abf48,
0x3f44bf00,0x3f80bf71,0x3f44bf71,0x3e32bf00,0x3f25bf5e,0xbeaf,0xbf253eaf,0xbf7c3f5e,
0xbf803f75,0xbf673f7b,0xbf153f44,0xbdff3ebb,0xbd4c3e97,0x3edebe4b,0x3f50bf25,0x3f7ebf6e,
0x3f54bf67,0x3f20bf3c,0x3ebbbf00,0x3d99be64,0xbf103ede,0xbf483f2e,0xbf6e3f5e,0xbf7f3f7a,
0xbea33ebb,0xbe643e8b,0xbdff3e32,0xbccc3d99,0x3f73bf6e,0x3f7abf76,0x3f7ebf7c,0x3f80bf7f,
0xbf673ebb,0xbf003f75,0x3f54be64,0x3f20bf7d,0xbede3f6e,0x3f5ebe97,0x3f10bf7a,0xbf483e19,
0x3f54bea3,0x3f44bf80,0xbed3be64,0xbf7d3f61,0x3f10bf73,0xbf253d4c,0xbf693f7a,0x3e193ef5,
0xbf3c3e8b,0xbf713f7b,0xbdff3f20,0x3f54bed3,0xbf2e3f76,0x3eaf3e4b,0x3f7ebf48,0x3f10bf69,
0x3f20be64,0x3f80bf67,0x3f20bf67,0xbe64be64,0x3f48bf7a,0xbede,0xbf483ede,0xbf7a3f7a,
0xbf003e32,0xbf713f44,0xbf713f80,0xbf003f44,0xbf5e3f7c,0xbeaf3f25,0x3eaf0000,0x3f5ebf25,
0x3ebbbdff,0x3f44bf15,0x3f7bbf67,0x3f75bf80,0x3f6ebf7e,0x3f25bf50,0x3e4bbede,0xbe973d4c,
0xbe643d99,0xbf003ebb,0xbf3c3f20,0xbf673f54,0xbf7a3f7f,0xbf5e3f6e,0xbf2e3f48,0xbede3f10,
0x3d99bccc,0x3e32bdff,0x3e8bbe64,0x3ebbbea3,0x3f7fbf80,0x3f7cbf7e,0x3f76bf7a,0x3f6ebf73,
0xbf3c3d99,0xbf3c3f80,0x3f203d99,0x3f54bf7d,0xbf2e3f7f,0x3f2e0000,0x3f48bf7f,0xbf10be19,
0xbdff3f33,0x3f6cbf00,0x3f20bf78,0xbf15bccc,0x3f7ebf37,0x3ec7bf5e,0xbf483e7d,0xbf503f80,
0x3f61bf80,0xbcccbf00,0xbf673f0b,0xbf4c3f7f,0xbef5bd4c,0xbf803f5e,0xbede3f57,0x3f1bbdcc,
0xbf673f20,0xbf673f80,0xbe643f20,0x3f20be64,0xbede3f48,0x3ede0000,0x3f7abf48,0x3f48bf7a,
0x3e323e32,0x3f44bf00,0x3f80bf71,0x3f44bf71,0x3f7cbf7c,0x3f25bf5e,0xbeaf,0xbf253eaf,
0x3f33bf5a,0x3e8bbf00,0xbe64bccc,0xbf293eea,0xbf373f05,0xbf763f5e,0xbf7a3f80,0xbf403f64,
0xbf7d3f75,0xbf7d3f80,0xbf673f75,0xbf3c3f54,0xbe193e97,0x3e190000,0x3edebe97,0x3f2ebf10,
0x3eeabed3,0x3f0bbf00,0x3f20bf15,0x3f33bf29,0x3f64bf69,0x3f57bf5e,0x3f48bf50,0x3f37bf40,
0xbf00be64,0xbf673f75,0x3ebb3ebb,0x3f75bf67,0xbf5e3f7a,0x3ede3e97,0x3f6ebf6e,0xbe97bede,
0xbf713f75,0x3d993f0b,0x3f7bbf29,0x3eeabf67,0x3eaf3e97,0x3f7fbf57,0x3e4bbf40,0xbf643ede,
0x3e323ebb,0x3f75bf29,0x3f33bf78,0xbea3be64,0x3f7cbf6e,0x3e97bf40,0xbf373e7d,0xbf733f7a,
0x3f80bf67,0x3f20bf67,0xbe64be64,0xbf673f20,0xbede,0xbf483ede,0xbf7a3f7a,0xbede3f48,
0x3e32bf00,0xbf003e32,0xbf713f44,0xbf713f80,0xbf7c3f5e,0xbf5e3f7c,0xbeaf3f25,0x3eaf0000,
0xbf713f54,0xbf7d3f7f,0xbf4c3f6c,0xbed33f20,0xbeaf3f10,0x3e193dcc,0x3f1bbec7,0x3f69bf48,
0xbf003f20,0xbe643ebb,0x3d993d99,0x3ebbbe64,0x3f5ebf48,0x3f7abf6e,0x3f7fbf7f,0x3f6ebf7a,
0x3f44bf3c,0x3f54bf4c,0x3f61bf5a,0x3f6cbf67,0x3f25bf2e,0x3f10bf1b,0x3ef5bf05,0x3ec7bede,
0xbe64bf00,0xbf7d3f54,0x3d993f20,0xbf3c,0xbf7a3f5e,0x3e193f10,0x3f7fbf48,0xbf2e,
0xbf3c3e32,0xbf5a3f7f,0x3e8b3ebb,0xbf4c,0xbf2e3f7c,0x3f053dcc,0x3f76bf6e,0xbf1b,
0xbf7d3f44,0xbf153f6c,0x3eea3d99,0xbf5a,0xbe193f25,0x3f50bec7,0x3f64bf7f,0xbf05,
0xbf673f80,0xbe643f20,0x3f20be64,0xbf67,0x3ede0000,0x3f7abf48,0x3f48bf7a,0xbede,
0xbf003f44,0x3e323e32,0x3f44bf00,0xbf71,0x3f5ebf25,0x3f7cbf7c,0x3f25bf5e,0xbeaf,
0x3d993e32,0x3f0bbea3,0x3f61bf3c,0xbf78,0x3f7fbf7c,0x3f57bf73,0x3ef5bf2e,0xbe7d,
0x3f20bf00,0x3f54bf3c,0x3f75bf67,0xbf7d,0x3f48bf5e,0x3f10bf2e,0x3e97bede,0xbe19,
0x3f75bf71,0x3f7bbf78,0x3f7fbf7d,0xbf80,0x3e97beaf,0x3e4bbe7d,0x3dccbe19,0xbd4c,
0x3bf4,0x3c74,0x3c74,0x3c74,0x3bf4,0x3ac23c73,0x3b413c6f,0x3b903c69,
0x3bf4,0x3b413c6f,0x3bbd3c61,0x3c093c49,0x3bf4,0x3b903c69,0x3c093c49,0x3c3f3c18,
0x3bf4,0x3bbd3c61,0x3c2f3c2a,0x3c633bb2,0x3bf4,0x3be93c56,0x3c4d3c04,0x3c733a92,
0x3bf4,0x3c093c49,0x3c633bb2,0x3c6ebb59,0x3bf4,0x3c1d3c3b,0x3c703b29,0x3c53bbf4,
0x3bf4,0x3c2f3c2a,0x3c74b9c2,0x3c26bc33,0x3bf4,0x3c3f3c18,0x3c6ebb59,0x3bd4bc5c,
0x3bf4,0x3c4d3c04,0x3c5ebbc9,0x3b11bc71,0x3bf4,0x3c593bde,0x3c46bc0e,0xbb11bc71,
0x3bf4,0x3c633bb2,0x3c26bc33,0xbbd4bc5c,0x3bf4,0x3c6b3b84,0x3bfebc50,0xbc26bc33,
0x3bf4,0x3c703b29,0x3ba7bc65,0xbc53bbf4,0x3bf4,0x3c733a92,0x3b11bc71,0xbc6ebb59,
0x3c74,0x3c74,0x3c74,0x3c74,0x3bbd3c61,0x3be93c56,0x3c093c49,0x3c1d3c3b,
0x3c2f3c2a,0x3c4d3c04,0x3c633bb2,0x3c703b29,0x3c633bb2,0x3c733a92,0x3c6ebb59,0x3c53bbf4,
0x3c74b9c2,0x3c5ebbc9,0x3c26bc33,0x3ba7bc65,0x3c5ebbc9,0x3c13bc42,0x3b11bc71,0xbba7bc65,
0x3c26bc33,0x3b11bc71,0xbbd4bc5c,0xbc53bbf4,0x3ba7bc65,0xbba7bc65,0xbc53bbf4,0xbc703b29,
0xba42bc73,0xbc37bc21,0xbc733a92,0xbc1d3c3b,0xbbd4bc5c,0xbc6ebb59,0xbc3f3c18,0x3c74,
0xbc37bc21,0xbc6b3b84,0xbb903c69,0x3c1d3c3b,0xbc67bb9b,0xbc2f3c2a,0x3b903c69,0x3c703b29,
0xbc733a92,0xbb903c69,0x3c3f3c18,0x3c53bbf4,0xbc593bde,0x3b413c6f,0x3c733a92,0x3ba7bc65,
0xbc1d3c3b,0x3c1d3c3b,0x3c53bbf4,0xbba7bc65,0xbb903c69,0x3c633bb2,0x3bd4bc5c,0xbc53bbf4,
0x3c74,0x3c74,0x3c74,0x3c74,0x3c2f3c2a,0x3c3f3c18,0x3c4d3c04,0x3c593bde,
0x3c74b9c2,0x3c6ebb59,0x3c5ebbc9,0x3c46bc0e,0x3c26bc33,0x3bd4bc5c,0x3b11bc71,0xbb11bc71,
0xba42bc73,0xbbd4bc5c,0xbc37bc21,0xbc67bb9b,0xbc37bc21,0xbc6ebb59,0xbc6b3b84,0xbc2f3c2a,
0xbc733a92,0xbc3f3c18,0xbb903c69,0x3b903c69,0xbc1d3c3b,0x3c74,0x3c1d3c3b,0x3c703b29,
0x3ac23c73,0x3c3f3c18,0x3c72baf2,0x3c13bc42,0x3c3f3c18,0x3c6ebb59,0x3bd4bc5c,0xbbd4bc5c,
0x3c72baf2,0x3bd4bc5c,0xbbfebc50,0xbc74b9c2,0x3c13bc42,0xbbd4bc5c,0xbc74b9c2,0xbbe93c56,
0xbb11bc71,0xbc6ebb59,0xbc093c49,0x3c093c49,0xbc46bc0e,0xbc3f3c18,0x3bbd3c61,0x3c72baf2,
0xbc703b29,0x3c74,0x3c703b29,0x3ba7bc65,0xbc093c49,0x3c3f3c18,0x3c26bc33,0xbc26bc33,
0x3c74,0x3c74,0x3c74,0x3c74,0x3c633bb2,0x3c6b3b84,0x3c703b29,0x3c733a92,
0x3c26bc33,0x3bfebc50,0x3ba7bc65,0x3b11bc71,0xbbd4bc5c,0xbc26bc33,0xbc53bbf4,0xbc6ebb59,
0xbc733a92,0xbc593bde,0xbc1d3c3b,0xbb903c69,0xbb903c69,0x3b413c6f,0x3c1d3c3b,0x3c633bb2,
0x3c3f3c18,0x3c733a92,0x3c53bbf4,0x3bd4bc5c,0x3c53bbf4,0x3ba7bc65,0xbba7bc65,0xbc53bbf4,
0xbb11bc71,0xbc46bc0e,0xbc703b29,0xbc093c49,0xbc6ebb59,0xbc3f3c18,0x3c74,0x3c3f3c18,
0xbc093c49,0x3bbd3c61,0x3c703b29,0x3c26bc33,0x3c093c49,0x3c72baf2,0x3ba7bc65,0xbc26bc33,
0x3c6ebb59,0x3b11bc71,0xbc53bbf4,0xbc3f3c18,0x3b11bc71,0xbc5ebbc9,0xbc1d3c3b,0x3c093c49,
0xbc53bbf4,0xbc1d3c3b,0x3c1d3c3b,0x3c53bbf4,0xbc3f3c18,0x3c093c49,0x3c53bbf4,0xbbd4bc5c,
0x3c74,0x3c74,0x3c74,0x3c74,0x3c74b9c2,0x3c72baf2,0x3c6ebb59,0x3c67bb9b,
0xba42bc73,0xbb71bc6c,0xbbd4bc5c,0xbc13bc42,0xbc733a92,0xbc633bb2,0xbc3f3c18,0xbc093c49,
0x3ac23c73,0x3be93c56,0x3c3f3c18,0x3c6b3b84,0x3c72baf2,0x3c46bc0e,0x3bd4bc5c,0xba42bc73,
0xbb11bc71,0xbc26bc33,0xbc6ebb59,0xbc633bb2,0xbc703b29,0xbc1d3c3b,0x3c74,0x3c1d3c3b,
0x3b413c6f,0x3c4d3c04,0x3c6ebb59,0x3bfebc50,0x3c6ebb59,0x3bd4bc5c,0xbbd4bc5c,0xbc6ebb59,
0xbb71bc6c,0xbc67bb9b,0xbc3f3c18,0x3ac23c73,0xbc6b3b84,0xbb413c6f,0x3c3f3c18,0x3c5ebbc9,
0x3b903c69,0x3c733a92,0x3bd4bc5c,0xbc26bc33,0x3c67bb9b,0xba42bc73,0xbc6ebb59,0xbbe93c56,
0xbba7bc65,0xbc703b29,0x3c74,0x3c703b29,0xbc633bb2,0x3b903c69,0x3c6ebb59,0xbb11bc71,
0x3c74,0x3c74,0x3c74,0x3c74,0x3c5ebbc9,0x3c53bbf4,0x3c46bc0e,0x3c37bc21,
0xbc37bc21,0xbc53bbf4,0xbc67bb9b,0xbc72baf2,0xbb903c69,0x3c74,0x3b903c69,0x3c093c49,
0x3c72baf2,0x3c53bbf4,0x3c13bc42,0x3b71bc6c,0xbbfebc50,0xbc53bbf4,0xbc74b9c2,0xbc593bde,
0xbc093c49,0x3c74,0x3c093c49,0x3c633bb2,0x3c703b29,0x3c53bbf4,0x3ba7bc65,0xbba7bc65,
0xbb71bc6c,0xbc53bbf4,0xbc6b3b84,0xbbe93c56,0xbc3f3c18,0x3c74,0x3c3f3c18,0x3c6ebb59,
0x3c593bde,0x3c53bbf4,0x3a42bc73,0xbc46bc0e,0x3a42bc73,0xbc53bbf4,0xbc4d3c04,0x3ac23c73,
0xbc633bb2,0x3c74,0x3c633bb2,0x3c26bc33,0x3c2f3c2a,0x3c53bbf4,0xbb71bc6c,0xbc74b9c2,
0x3ba7bc65,0xbc53bbf4,0xbc1d3c3b,0x3c1d3c3b,0xbc733a92,0x3c74,0x3c733a92,0x3b11bc71,
0x3c74,0x3c74,0x3c74,0x3c74,0x3c26bc33,0x3c13bc42,0x3bfebc50,0x3bd4bc5c,
0xbc733a92,0xbc6b3b84,0xbc593bde,0xbc3f3c18,0x3c3f3c18,0x3c633bb2,0x3c733a92,0x3c6ebb59,
0xbb11bc71,0xbbfebc50,0xbc46bc0e,0xbc6ebb59,0xbc093c49,0xbac23c73,0x3bbd3c61,0x3c3f3c18,
0x3c6ebb59,0x3c26bc33,0x3b11bc71,0xbbd4bc5c,0xbc53bbf4,0xbc703b29,0xbc1d3c3b,0x3c74,
0x3b903c69,0x3c593bde,0x3c67bb9b,0x3bd4bc5c,0x3bd4bc5c,0xbbd4bc5c,0xbc6ebb59,0xbc3f3c18,
0xbc633bb2,0xbb413c6f,0x3c2f3c2a,0x3c6ebb59,0x3c633bb2,0x3c37bc21,0xbb71bc6c,0xbc6ebb59,
0xbbd4bc5c,0xbc733a92,0xbb903c69,0x3c3f3c18,0xbb903c69,0x3c4d3c04,0x3c37bc21,0xbbd4bc5c,
0x3c53bbf4,0xbba7bc65,0xbc703b29,0x3c74,0xbc6ebb59,0xbb903c69,0x3c633bb2,0x3bd4bc5c,
0x3c74,0x3c74,0x3c74,0x3c74,0x3ba7bc65,0x3b71bc6c,0x3b11bc71,0x3a42bc73,
0xbc1d3c3b,0xbbe93c56,0xbb903c69,0xbac23c73,0x3c53bbf4,0x3c26bc33,0x3bd4bc5c,0x3b11bc71,
0xbc703b29,0xbc4d3c04,0xbc093c49,0xbb413c6f,0x3c703b29,0x3c67bb9b,0x3c26bc33,0x3b71bc6c,
0xbc53bbf4,0xbc733a92,0xbc3f3c18,0xbb903c69,0x3c1d3c3b,0x3c703b29,0x3c53bbf4,0x3ba7bc65,
0xbba7bc65,0xbc5ebbc9,0xbc633bb2,0xbbbd3c61,0x3c74,0x3c3f3c18,0x3c6ebb59,0x3bd4bc5c,
0x3ba7bc65,0xbc13bc42,0xbc733a92,0xbbe93c56,0xbc1d3c3b,0x3bbd3c61,0x3c733a92,0x3bfebc50,
0x3c53bbf4,0xbb11bc71,0xbc6ebb59,0xbc093c49,0xbc703b29,0xbac23c73,0x3c633bb2,0x3c13bc42,
0x3c703b29,0x3ba7bc65,0xbc53bbf4,0xbc1d3c3b,0xbc53bbf4,0xbc093c49,0x3c3f3c18,0x3c26bc33,
0x3bf4,0x3c74b9c2,0xba42bc73,0xbc733a92,0x3bf4,0x3c72baf2,0xbb71bc6c,0xbc633bb2,
0x3bf4,0x3c6ebb59,0xbbd4bc5c,0xbc3f3c18,0x3bf4,0x3c67bb9b,0xbc13bc42,0xbc093c49,
0x3bf4,0x3c5ebbc9,0xbc37bc21,0xbb903c69,0x3bf4,0x3c53bbf4,0xbc53bbf4,0x3c74,
0x3bf4,0x3c46bc0e,0xbc67bb9b,0x3b903c69,0x3bf4,0x3c37bc21,0xbc72baf2,0x3c093c49,
0x3bf4,0x3c26bc33,0xbc733a92,0x3c3f3c18,0x3bf4,0x3c13bc42,0xbc6b3b84,0x3c633bb2,
0x3bf4,0x3bfebc50,0xbc593bde,0x3c733a92,0x3bf4,0x3bd4bc5c,0xbc3f3c18,0x3c6ebb59,
0x3bf4,0x3ba7bc65,0xbc1d3c3b,0x3c53bbf4,0x3bf4,0x3b71bc6c,0xbbe93c56,0x3c26bc33,
0x3bf4,0x3b11bc71,0xbb903c69,0x3bd4bc5c,0x3bf4,0x3a42bc73,0xbac23c73,0x3b11bc71,
0x3ac23c73,0x3c72baf2,0xbb11bc71,0xbc703b29,0x3be93c56,0x3c46bc0e,0xbc26bc33,0xbc1d3c3b,
0x3c3f3c18,0x3bd4bc5c,0xbc6ebb59,0x3c74,0x3c6b3b84,0xba42bc73,0xbc633bb2,0x3c1d3c3b,
0x3c72baf2,0xbbfebc50,0xbc093c49,0x3c703b29,0x3c53bbf4,0xbc53bbf4,0x3c74,0x3c53bbf4,
0x3c13bc42,0xbc74b9c2,0x3c093c49,0x3ba7bc65,0x3b71bc6c,0xbc593bde,0x3c633bb2,0xbba7bc65,
0xbb11bc71,0xbc093c49,0x3c6ebb59,0xbc53bbf4,0xbbfebc50,0xbac23c73,0x3c26bc33,0xbc703b29,
0xbc46bc0e,0x3bbd3c61,0x3b11bc71,0xbc1d3c3b,0xbc6ebb59,0x3c3f3c18,0xbbd4bc5c,0x3c74,
0xbc703b29,0x3c703b29,0xbc53bbf4,0x3c1d3c3b,0xbc4d3c04,0x3c67bb9b,0xbc733a92,0x3c703b29,
0xbc093c49,0x3c26bc33,0xbc3f3c18,0x3c53bbf4,0xbb413c6f,0x3b71bc6c,0xbb903c69,0x3ba7bc65,
0x3b413c6f,0x3c6ebb59,0xbb71bc6c,0xbc6b3b84,0x3c4d3c04,0x3bd4bc5c,0xbc67bb9b,0xbb413c6f,
0x3c6ebb59,0xbbd4bc5c,0xbc3f3c18,0x3c3f3c18,0x3bfebc50,0xbc6ebb59,0x3ac23c73,0x3c5ebbc9,
0xbb71bc6c,0xbc3f3c18,0x3c593bde,0x3a42bc73,0xbc53bbf4,0x3c74,0x3c53bbf4,0xbc53bbf4,
0xbc6b3b84,0x3c3f3c18,0x3a42bc73,0xbc4d3c04,0xbbe93c56,0x3c6ebb59,0xbc46bc0e,0x3ac23c73,
0x3b903c69,0x3bd4bc5c,0xbc633bb2,0x3c633bb2,0x3c593bde,0xbbd4bc5c,0xbb413c6f,0x3c37bc21,
0x3c67bb9b,0xbc6ebb59,0x3c2f3c2a,0xbb71bc6c,0x3bd4bc5c,0xbc3f3c18,0x3c6ebb59,0xbc6ebb59,
0xbba7bc65,0x3c74,0x3ba7bc65,0xbc1d3c3b,0xbc5ebbc9,0x3c3f3c18,0xbc13bc42,0x3bbd3c61,
0xbc633bb2,0x3c6ebb59,0xbc733a92,0x3c733a92,0xbbbd3c61,0x3bd4bc5c,0xbbe93c56,0x3bfebc50,
0x3b903c69,0x3c67bb9b,0xbba7bc65,0xbc633bb2,0x3c733a92,0xba42bc73,0xbc703b29,0x3b903c69,
0x3bd4bc5c,0xbc6ebb59,0x3c74,0x3c6ebb59,0xbc26bc33,0xbbe93c56,0x3c703b29,0xbb11bc71,
0xbc633bb2,0x3c2f3c2a,0x3ba7bc65,0xbc733a92,0x3c74,0x3c53bbf4,0xbc53bbf4,0x3c74,
0x3c633bb2,0xbb71bc6c,0xbc1d3c3b,0x3c733a92,0x3c26bc33,0xbc74b9c2,0x3c1d3c3b,0x3b11bc71,
0xbbd4bc5c,0xbb903c69,0x3c53bbf4,0xbc6ebb59,0xbc733a92,0x3c4d3c04,0xbba7bc65,0xbb903c69,
0xbb903c69,0x3c37bc21,0xbc703b29,0x3c633bb2,0x3c3f3c18,0xbbd4bc5c,0x3c74,0x3bd4bc5c,
0x3c53bbf4,0xbc703b29,0x3c703b29,0xbc53bbf4,0xbb11bc71,0xbac23c73,0x3ba7bc65,0xbc093c49,
0xbc6ebb59,0x3c633bb2,0xbc53bbf4,0x3c3f3c18,0xbc093c49,0x3c13bc42,0xbc1d3c3b,0x3c26bc33,
0x3bbd3c61,0x3c5ebbc9,0xbbd4bc5c,0xbc593bde,0x3c5ebbc9,0xbbfebc50,0xbc3f3c18,0x3c2f3c2a,
0xbbd4bc5c,0xbc3f3c18,0x3c3f3c18,0x3bd4bc5c,0xbc593bde,0x3c2f3c2a,0x3bd4bc5c,0xbc72baf2,
0x3be93c56,0x3c13bc42,0xbc6ebb59,0x3b413c6f,0x3c53bbf4,0xbc53bbf4,0x3c74,0x3c53bbf4,
0xbbfebc50,0xbbbd3c61,0x3c6ebb59,0xbc37bc21,0xbc4d3c04,0x3c6b3b84,0xbbd4bc5c,0xbbbd3c61,
0x3c093c49,0x3b11bc71,0xbc3f3c18,0x3c733a92,0x3c46bc0e,0xbc74b9c2,0x3c3f3c18,0xbb71bc6c,
0xbc13bc42,0x3ac23c73,0x3bd4bc5c,0xbc4d3c04,0xbc3f3c18,0x3c6ebb59,0xbc6ebb59,0x3c3f3c18,
0x3c1d3c3b,0xbba7bc65,0x3c74,0x3ba7bc65,0x3c37bc21,0xbc593bde,0x3c6ebb59,0xbc74b9c2,
0xbc26bc33,0x3c093c49,0xbbd4bc5c,0x3b903c69,0xbc2f3c2a,0x3c37bc21,0xbc3f3c18,0x3c46bc0e,
0x3be93c56,0x3c53bbf4,0xbbfebc50,0xbc4d3c04,0x3c13bc42,0xbc53bbf4,0xbbbd3c61,0x3c6b3b84,
0xbc6ebb59,0x3c74,0x3c6ebb59,0xbbd4bc5c,0x3b413c6f,0x3c53bbf4,0xbc37bc21,0xbbbd3c61,
0x3c46bc0e,0xbc53bbf4,0xbac23c73,0x3c67bb9b,0xbc53bbf4,0x3c74,0x3c53bbf4,0xbc53bbf4,
0xbac23c73,0x3c53bbf4,0xbc5ebbc9,0x3b413c6f,0x3c67bb9b,0xbc53bbf4,0x3b413c6f,0x3c13bc42,
0xbc26bc33,0x3c74,0x3c26bc33,0xbc733a92,0xbbbd3c61,0x3c53bbf4,0xbc72baf2,0x3c2f3c2a,
0x3c74b9c2,0xbc53bbf4,0x3be93c56,0x3a42bc73,0xbbd4bc5c,0x3c74,0x3bd4bc5c,0xbc3f3c18,
0xbc1d3c3b,0x3c53bbf4,0xbc703b29,0x3c703b29,0x3c6b3b84,0xbc53bbf4,0x3c2f3c2a,0xbbfebc50,
0xbb11bc71,0x3c74,0x3b11bc71,0xbb903c69,0xbc4d3c04,0x3c53bbf4,0xbc593bde,0x3c5ebbc9,
0x3c093c49,0x3c46bc0e,0xbc13bc42,0xbc3f3c18,0x3b11bc71,0xbc74b9c2,0x3ac23c73,0x3c6ebb59,
0xbc3f3c18,0x3c3f3c18,0x3bd4bc5c,0xbc6ebb59,0x3c733a92,0xbb71bc6c,0xbc4d3c04,0x3c3f3c18,
0xbc26bc33,0xbbbd3c61,0x3c74b9c2,0xbbd4bc5c,0x3c74,0x3c53bbf4,0xbc53bbf4,0x3c74,
0x3c26bc33,0xbc72baf2,0x3be93c56,0x3bd4bc5c,0xbc733a92,0x3c2f3c2a,0x3a42bc73,0xbc3f3c18,
0x3c3f3c18,0xbb11bc71,0xbc093c49,0x3c6ebb59,0xbb11bc71,0xbbe93c56,0x3c5ebbc9,0xbc6ebb59,
0xbc093c49,0x3c5ebbc9,0xbc72baf2,0x3c3f3c18,0x3c6ebb59,0xbc6ebb59,0x3c3f3c18,0xbbd4bc5c,
0xbc53bbf4,0x3c1d3c3b,0xbba7bc65,0x3c74,0x3b903c69,0xba42bc73,0xbb413c6f,0x3bd4bc5c,
0x3bd4bc5c,0xbc093c49,0x3c26bc33,0xbc3f3c18,0xbc633bb2,0x3c67bb9b,0xbc6b3b84,0x3c6ebb59,
0x3c1d3c3b,0x3c37bc21,0xbc26bc33,0xbc2f3c2a,0xbba7bc65,0xbc593bde,0x3c093c49,0x3c37bc21,
0x3c74,0x3c6ebb59,0xbbd4bc5c,0xbc3f3c18,0x3ba7bc65,0xbc74b9c2,0x3b903c69,0x3c46bc0e,
0xbc1d3c3b,0x3c6b3b84,0xbb11bc71,0xbc4d3c04,0x3c53bbf4,0xbc53bbf4,0x3c74,0x3c53bbf4,
0xbc703b29,0x3c2f3c2a,0x3b11bc71,0xbc593bde,0x3c703b29,0xbbfebc50,0xbb903c69,0x3c5ebbc9,
0xbc53bbf4,0x3b903c69,0x3bd4bc5c,0xbc633bb2,0x3c1d3c3b,0xba42bc73,0xbc093c49,0x3c67bb9b,
0xbba7bc65,0xbb413c6f,0x3c26bc33,0xbc6b3b84,0x3c74,0x3bd4bc5c,0xbc3f3c18,0x3c6ebb59,
0x3ba7bc65,0xbc1d3c3b,0x3c53bbf4,0xbc703b29,0xbc1d3c3b,0x3c46bc0e,0xbc633bb2,0x3c72baf2,
0x3c53bbf4,0xbc633bb2,0x3c6ebb59,0xbc733a92,0xbc703b29,0x3c72baf2,0xbc733a92,0x3c74b9c2,
0x3bf4,0xba42bc73,0x3ac23c73,0xbb11bc71,0x3bf4,0xbb11bc71,0x3b903c69,0xbbd4bc5c,
0x3bf4,0xbb71bc6c,0x3be93c56,0xbc26bc33,0x3bf4,0xbba7bc65,0x3c1d3c3b,0xbc53bbf4,
0x3bf4,0xbbd4bc5c,0x3c3f3c18,0xbc6ebb59,0x3bf4,0xbbfebc50,0x3c593bde,0xbc733a92,
0x3bf4,0xbc13bc42,0x3c6b3b84,0xbc633bb2,0x3bf4,0xbc26bc33,0x3c733a92,0xbc3f3c18,
0x3bf4,0xbc37bc21,0x3c72baf2,0xbc093c49,0x3bf4,0xbc46bc0e,0x3c67bb9b,0xbb903c69,
0x3bf4,0xbc53bbf4,0x3c53bbf4,0x3c74,0x3bf4,0xbc5ebbc9,0x3c37bc21,0x3b903c69,
0x3bf4,0xbc67bb9b,0x3c13bc42,0x3c093c49,0x3bf4,0xbc6ebb59,0x3bd4bc5c,0x3c3f3c18,
0x3bf4,0xbc72baf2,0x3b71bc6c,0x3c633bb2,0x3bf4,0xbc74b9c2,0x3a42bc73,0x3c733a92,
0x3b413c6f,0xbb71bc6c,0x3b903c69,0xbba7bc65,0x3c093c49,0xbc26bc33,0x3c3f3c18,0xbc53bbf4,
0x3c4d3c04,0xbc67bb9b,0x3c733a92,0xbc703b29,0x3c703b29,0xbc703b29,0x3c53bbf4,0xbc1d3c3b,
0x3c6ebb59,0xbc3f3c18,0x3bd4bc5c,0x3c74,0x3c46bc0e,0xbbbd3c61,0xbb11bc71,0x3c1d3c3b,
0x3bfebc50,0x3ac23c73,0xbc26bc33,0x3c703b29,0x3b11bc71,0x3c093c49,0xbc6ebb59,0x3c53bbf4,
0xbb71bc6c,0x3c593bde,0xbc633bb2,0x3ba7bc65,0xbc13bc42,0x3c74b9c2,0xbc093c49,0xbba7bc65,
0xbc53bbf4,0x3c53bbf4,0x3c74,0xbc53bbf4,0xbc72baf2,0x3bfebc50,0x3c093c49,0xbc703b29,
0xbc6b3b84,0x3a42bc73,0x3c633bb2,0xbc1d3c3b,0xbc3f3c18,0xbbd4bc5c,0x3c6ebb59,0x3c74,
0xbbe93c56,0xbc46bc0e,0x3c26bc33,0x3c1d3c3b,0xbac23c73,0xbc72baf2,0x3b11bc71,0x3c703b29,
0x3bbd3c61,0xbbd4bc5c,0x3be93c56,0xbbfebc50,0x3c633bb2,0xbc6ebb59,0x3c733a92,0xbc733a92,
0x3c5ebbc9,0xbc3f3c18,0x3c13bc42,0xbbbd3c61,0x3ba7bc65,0x3c74,0xbba7bc65,0x3c1d3c3b,
0xbbd4bc5c,0x3c3f3c18,0xbc6ebb59,0x3c6ebb59,0xbc67bb9b,0x3c6ebb59,0xbc2f3c2a,0x3b71bc6c,
0xbc593bde,0x3bd4bc5c,0x3b413c6f,0xbc37bc21,0xbb903c69,0xbbd4bc5c,0x3c633bb2,0xbc633bb2,
0x3be93c56,0xbc6ebb59,0x3c46bc0e,0xbac23c73,0x3c6b3b84,0xbc3f3c18,0xba42bc73,0x3c4d3c04,
0x3c53bbf4,0x3c74,0xbc53bbf4,0x3c53bbf4,0x3b71bc6c,0x3c3f3c18,0xbc593bde,0xba42bc73,
0xbbfebc50,0x3c6ebb59,0xbac23c73,0xbc5ebbc9,0xbc6ebb59,0x3bd4bc5c,0x3c3f3c18,0xbc3f3c18,
0xbc4d3c04,0xbbd4bc5c,0x3c67bb9b,0x3b413c6f,0xbb413c6f,0xbc6ebb59,0x3b71bc6c,0x3c6b3b84,
0x3c093c49,0xbc13bc42,0x3c1d3c3b,0xbc26bc33,0x3c6ebb59,0xbc633bb2,0x3c53bbf4,0xbc3f3c18,
0x3b11bc71,0x3ac23c73,0xbba7bc65,0x3c093c49,0xbc53bbf4,0x3c703b29,0xbc703b29,0x3c53bbf4,
0xbc3f3c18,0x3bd4bc5c,0x3c74,0xbbd4bc5c,0x3b903c69,0xbc37bc21,0x3c703b29,0xbc633bb2,
0x3c733a92,0xbc4d3c04,0x3ba7bc65,0x3b903c69,0x3bd4bc5c,0x3b903c69,0xbc53bbf4,0x3c6ebb59,
0xbc26bc33,0x3c74b9c2,0xbc1d3c3b,0xbb11bc71,0xbc633bb2,0x3b71bc6c,0x3c1d3c3b,0xbc733a92,
0x3c74,0xbc53bbf4,0x3c53bbf4,0x3c74,0x3c633bb2,0xbc2f3c2a,0xbba7bc65,0x3c733a92,
0x3c26bc33,0x3be93c56,0xbc703b29,0x3b11bc71,0xbbd4bc5c,0x3c6ebb59,0x3c74,0xbc6ebb59,
0xbc733a92,0x3a42bc73,0x3c703b29,0xbb903c69,0xbb903c69,0xbc67bb9b,0x3ba7bc65,0x3c633bb2,
0x3c2f3c2a,0xbc37bc21,0x3c3f3c18,0xbc46bc0e,0x3c26bc33,0xbc093c49,0x3bd4bc5c,0xbb903c69,
0xbc37bc21,0x3c593bde,0xbc6ebb59,0x3c74b9c2,0xbc1d3c3b,0x3ba7bc65,0x3c74,0xbba7bc65,
0x3c3f3c18,0xbc6ebb59,0x3c6ebb59,0xbc3f3c18,0x3c13bc42,0xbac23c73,0xbbd4bc5c,0x3c4d3c04,
0xbc46bc0e,0x3c74b9c2,0xbc3f3c18,0x3b71bc6c,0xbc093c49,0xbb11bc71,0x3c3f3c18,0xbc733a92,
0x3c4d3c04,0xbc6b3b84,0x3bd4bc5c,0x3bbd3c61,0x3bfebc50,0x3bbd3c61,0xbc6ebb59,0x3c37bc21,
0xbc53bbf4,0x3c53bbf4,0x3c74,0xbc53bbf4,0xbbe93c56,0xbc13bc42,0x3c6ebb59,0xbb413c6f,
0x3c593bde,0xbc2f3c2a,0xbbd4bc5c,0x3c72baf2,0x3bd4bc5c,0x3c3f3c18,0xbc3f3c18,0xbbd4bc5c,
0xbc5ebbc9,0x3bfebc50,0x3c3f3c18,0xbc2f3c2a,0xbbbd3c61,0xbc5ebbc9,0x3bd4bc5c,0x3c593bde,
0x3c4d3c04,0xbc53bbf4,0x3c593bde,0xbc5ebbc9,0x3b11bc71,0x3c74,0xbb11bc71,0x3b903c69,
0xbc6b3b84,0x3c53bbf4,0xbc2f3c2a,0x3bfebc50,0x3c1d3c3b,0xbc53bbf4,0x3c703b29,0xbc703b29,
0x3bd4bc5c,0x3c74,0xbbd4bc5c,0x3c3f3c18,0xbc74b9c2,0x3c53bbf4,0xbbe93c56,0xba42bc73,
0x3bbd3c61,0xbc53bbf4,0x3c72baf2,0xbc2f3c2a,0x3c26bc33,0x3c74,0xbc26bc33,0x3c733a92,
0xbc67bb9b,0x3c53bbf4,0xbb413c6f,0xbc13bc42,0x3ac23c73,0xbc53bbf4,0x3c5ebbc9,0xbb413c6f,
0x3c53bbf4,0x3c74,0xbc53bbf4,0x3c53bbf4,0xbc46bc0e,0x3c53bbf4,0x3ac23c73,0xbc67bb9b,
0xbb413c6f,0xbc53bbf4,0x3c37bc21,0x3bbd3c61,0x3c6ebb59,0x3c74,0xbc6ebb59,0x3bd4bc5c,
0xbc13bc42,0x3c53bbf4,0x3bbd3c61,0xbc6b3b84,0xbbe93c56,0xbc53bbf4,0x3bfebc50,0x3c4d3c04,
0x3c633bb2,0xbc67bb9b,0x3c6b3b84,0xbc6ebb59,0xbbd4bc5c,0x3c093c49,0xbc26bc33,0x3c3f3c18,
0xbb903c69,0x3a42bc73,0x3b413c6f,0xbbd4bc5c,0x3c53bbf4,0xbc1d3c3b,0x3ba7bc65,0x3c74,
0xbc6ebb59,0x3c6ebb59,0xbc3f3c18,0x3bd4bc5c,0x3c093c49,0xbc5ebbc9,0x3c72baf2,0xbc3f3c18,
0x3b11bc71,0x3be93c56,0xbc5ebbc9,0x3c6ebb59,0xbc3f3c18,0x3b11bc71,0x3c093c49,0xbc6ebb59,
0x3c733a92,0xbc2f3c2a,0xba42bc73,0x3c3f3c18,0xbc26bc33,0x3c72baf2,0xbbe93c56,0xbbd4bc5c,
0x3c74,0xbc53bbf4,0x3c53bbf4,0x3c74,0x3c26bc33,0x3bbd3c61,0xbc74b9c2,0x3bd4bc5c,
0xbc733a92,0x3b71bc6c,0x3c4d3c04,0xbc3f3c18,0x3c3f3c18,0xbc3f3c18,0xbbd4bc5c,0x3c6ebb59,
0xbb11bc71,0x3c74b9c2,0xbac23c73,0xbc6ebb59,0xbc093c49,0xbc46bc0e,0x3c13bc42,0x3c3f3c18,
0x3c703b29,0xbc72baf2,0x3c733a92,0xbc74b9c2,0xbc53bbf4,0x3c633bb2,0xbc6ebb59,0x3c733a92,
0x3c1d3c3b,0xbc46bc0e,0x3c633bb2,0xbc72baf2,0xbba7bc65,0x3c1d3c3b,0xbc53bbf4,0x3c703b29,
0x3c74,0xbbd4bc5c,0x3c3f3c18,0xbc6ebb59,0x3ba7bc65,0x3b413c6f,0xbc26bc33,0x3c6b3b84,
0xbc1d3c3b,0x3a42bc73,0x3c093c49,0xbc67bb9b,0x3c53bbf4,0xbb903c69,0xbbd4bc5c,0x3c633bb2,
0xbc703b29,0x3bfebc50,0x3b903c69,0xbc5ebbc9,0x3c703b29,0xbc2f3c2a,0xbb11bc71,0x3c593bde,
0xbc53bbf4,0x3c53bbf4,0x3c74,0xbc53bbf4,0x3c1d3c3b,0xbc6b3b84,0x3b11bc71,0x3c4d3c04,
0xbba7bc65,0x3c74b9c2,0xbb903c69,0xbc46bc0e,0x3c74,0xbc6ebb59,0x3bd4bc5c,0x3c3f3c18,
0x3ba7bc65,0x3c593bde,0xbc093c49,0xbc37bc21,0xbc1d3c3b,0xbc37bc21,0x3c26bc33,0x3c2f3c2a,
0x3f803f80,0x3f803f80,0x3f803f80,0x3f803f80,0x3f7f3f80,0x3f753f7b,0x3f613f6c,0x3f443f54,
0x3f7b3f80,0x3f543f6c,0x3f0b3f33,0x3e323ebb,0x3f753f80,0x3f203f54,0x3d993ebb,0xbf00be64,
0x3f6c3f80,0x3ebb3f33,0xbed3bccc,0xbf71bf3c,0x3f613f80,0x3d993f0b,0xbf4cbed3,0xbf71bf7d,
0x3f543f80,0xbe643ebb,0xbf7dbf3c,0xbf00bf67,0x3f443f80,0xbf003e32,0xbf71bf71,0x3e32bf00,
0x3f333f80,0xbf3cbccc,0xbf29bf80,0x3f443d99,0x3f203f80,0xbf67be64,0xbe64bf67,0x3f803f20,
0x3f0b3f80,0xbf7dbed3,0x3e8bbf29,0x3f443f75,0x3eea3f80,0xbf7dbf15,0x3f33bea3,0x3e323f75,
0x3ebb3f80,0xbf67bf3c,0x3f753d99,0xbf003f20,0x3e8b3f80,0xbf3cbf5a,0x3f7b3eea,0xbf713d99,
0x3e323f80,0xbf00bf71,0x3f443f44,0xbf71bf00,0x3d993f80,0xbe64bf7d,0x3ebb3f75,0xbf00bf67,
0x3f803f80,0x3f803f80,0x3f803f80,0x3f803f80,0x3f203f33,0x3eea3f0b,0x3e8b3ebb,0x3d993e32,
0xbe64bccc,0xbf15bed3,0xbf5abf3c,0xbf7dbf71,0xbf67bf3c,0xbf7dbf7d,0xbf3cbf67,0xbe64bf00,
0xbf67bf80,0xbea3bf29,0x3eea3d99,0x3f753f44,0xbe64bf29,0x3f333e8b,0x3f7b3f75,0x3ebb3f44,
0x3f203d99,0x3f753f75,0x3d993f20,0xbf67bf00,0x3f803f44,0x3e323f44,0xbf71bf00,0xbf00bf71,
0x3f203f7f,0xbf4cbdff,0xbf15bf7d,0x3f543e32,0xbe643f20,0xbf67bf67,0x3f20be64,0x3f203f80,
0xbf67bdff,0xbcccbf5a,0x3f6c3f54,0xbf3c3e32,0xbf67bf4c,0x3f61bccc,0xbdff3f54,0xbf3cbf71,
0xbe64bf7d,0x3f543f54,0xbf7dbe64,0x3f20bf00,0x3f20bf15,0xbdff3f6c,0xbed3bf7d,0x3f543f44,
0x3f803e32,0xbf713e32,0x3f44bf00,0xbf003f44,0x3f203f54,0xbf3cbf3c,0x3f543f20,0xbf67bf00,
0x0,0x0,0x0,0x0,0xba8b0000,0xbabfb99b,0xba33b9d1,0x39dcbaf7,
0xb99b0000,0xbaf7b9d1,0xb9deba81,0xb93d38e0,0xbabf0000,0xbac6baf7,0x37c138e0,0xa580390e,
0xb9d10000,0x38e0ba81,0x3a58b7ee,0x3ae13aad,0xba330000,0x37c1b9de,0xb9873a58,0x3ae1ba10,
0xbaf70000,0x390e38e0,0xba103aad,0x25e03ab4,0x39dc0000,0xa580b93d,0x3ae13ae1,0xb93d25e0,
0xba810000,0x3aadb7ee,0xbac83aa3,0x39dc37c1,0xbac60000,0x3ab4390e,0x390e3ab4,0xbac6,
0xb9de0000,0xba103a58,0xba29bac8,0x39dcbabf,0xba570000,0xba10bae0,0xba81b905,0xb93dbabf,
0x38e00000,0x3ab43aad,0xbabf37c1,0xa580bac6,0xba290000,0x3aadbae3,0xb99bba57,0x3ae137c1,
0xb93d0000,0x25e03ae1,0x39dc39dc,0x3ae1a580,0x37c10000,0x390eba10,0x38e0babf,0x25e03ab4,
0x0,0x0,0x0,0x0,0xbac6ba81,0xba57b9de,0xba2938e0,0x37c1b93d,
0x390eb7ee,0xbae03a58,0xbae33aad,0xba103ae1,0x3ab43aad,0xba10ba10,0x3aad3ab4,0x390e25e0,
0x3ab43aa3,0xb905bac8,0xba5737c1,0xbabf39dc,0x390ebac8,0xba81ba29,0xb99bbabf,0x38e039dc,
0xbac637c1,0xbabfbabf,0x37c1bac6,0x3ab4a580,0x39dc,0xb93d39dc,0x3ae1a580,0x25e03ae1,
0xbac6ba8b,0xb9873930,0xbae0ba10,0xbaf7b93d,0x390ebac6,0x3ab43ab4,0xbac6390e,0xbac60000,
0x3ab43930,0xb7eebae3,0xb9d1baf7,0x3aadb93d,0x3ab4b987,0xba33b7ee,0x3930baf7,0x3aad3ae1,
0x390eba10,0xbaf7baf7,0xba10390e,0xbac625e0,0xbac6bae0,0x3930b9d1,0x3a58ba10,0xbaf739dc,
0xb93d,0x3ae1b93d,0x39dc25e0,0xa58039dc,0xbac6baf7,0x3aad3aad,0xbaf7bac6,0x3ab4a580,
0xbccc3f80,0x3d99bf80,0xbdff3f7f,0x3e32bf7d,0xbdff3f80,0x3ebbbf78,0xbf153f61,0x3f44bf3c,
0xbe643f80,0x3f20bf67,0xbf673f20,0x3f80be64,0xbea33f80,0x3f54bf4c,0xbf803e8b,0x3f443ebb,
0xbed33f80,0x3f75bf29,0xbf5abdff,0x3e323f54,0xbf003f80,0x3f80bf00,0xbf00bf00,0xbf003f80,
0xbf153f80,0x3f75bea3,0xbcccbf4c,0xbf713f54,0xbf293f80,0x3f54bdff,0x3eeabf78,0xbf713ebb,
0xbf3c3f80,0x3f203d99,0x3f54bf7d,0xbf00be64,0xbf4c3f80,0x3ebb3e8b,0x3f7fbf5a,0x3e32bf3c,
0xbf5a3f80,0x3d993eea,0x3f6cbf15,0x3f44bf7d,0xbf673f80,0xbe643f20,0x3f20be64,0x3f80bf67,
0xbf713f80,0xbf003f44,0x3e323e32,0x3f44bf00,0xbf783f80,0xbf3c3f61,0xbea33f0b,0x3e323d99,
0xbf7d3f80,0xbf673f75,0xbf3c3f54,0xbf003f20,0xbf803f80,0xbf7d3f7f,0xbf783f7b,0xbf713f75,
0xbe643f7b,0x3e8bbf78,0xbea33f75,0x3ebbbf71,0xbf673f0b,0x3f7bbea3,0xbf803d99,0x3f753e32,
0xbf67be64,0x3f203f20,0xbe64bf67,0xbe643f80,0xbe64bf5a,0xbed33f7f,0x3f61bf3c,0xbf7d3e32,
0x3f20bf78,0xbf803eea,0x3f333ebb,0x3d99bf71,0x3f80bf00,0xbf00bf00,0xbf003f80,0x3f80bf00,
0x3f203e8b,0x3f0bbf80,0xbf783ebb,0x3d993f44,0xbe643f61,0x3f7fbf15,0xbcccbf3c,0xbf7d3f44,
0xbf673f75,0x3ebb3ebb,0x3f75bf67,0xbe64bf00,0xbf673eea,0xbf293f7b,0x3f0b3d99,0x3f75bf71,
0xbe64bea3,0xbf783f33,0xbf293f75,0x3ebb3e32,0x3f20bf67,0xbe64be64,0xbf673f20,0xbf673f80,
0x3f80bf71,0x3f44bf71,0x3e32bf00,0xbf003e32,0x3f20bed3,0x3f6cbf4c,0x3f7fbf7d,0x3f54bf71,
0xbe643ebb,0x3d993d99,0x3ebbbe64,0x3f20bf00,0xbf673f6c,0xbf5a3f61,0xbf4c3f54,0xbf3c3f44,
0xb7ee0000,0x37c13aa3,0x3930ba8b,0xb93dba10,0x39300000,0x38e0b9ac,0xbae0ba33,0x39dc3aad,
0x390e0000,0xbac63ab4,0x3ab4bac6,0x390e,0xb9050000,0xbaf7b987,0x3aa3ba29,0x39dc38e0,
0x3a580000,0xbabfbac8,0xbae33930,0xb93dbaf7,0xa5800000,0x25e0,0x25e0a580,0xa5800000,
0xbae00000,0xbabfb905,0xb7eeb987,0x3ae1baf7,0xbac80000,0xbaf73930,0xba57b9ac,0x3ae138e0,
0x3aad0000,0xbac637c1,0xbaf7ba10,0x25e0390e,0xb9870000,0x38e0ba29,0xba8bbae3,0xb93d3aad,
0xbae30000,0x37c1ba57,0xb9d1bae0,0x39dcba10,0x3ab40000,0x390ebac6,0xbac6390e,0x3ab4,
0x3ae10000,0xa58039dc,0xb93db93d,0x39dc25e0,0xb9ac0000,0x3aadba33,0xb905b9de,0xb93d37c1,
0xba100000,0x3ab4babf,0x3aadbaf7,0xa580bac6,0x3aa30000,0xba10ba8b,0xb9acb99b,0x3ae1babf,
0x390eb99b,0xba29b9ac,0xb905babf,0x38e03ae1,0x3ab4b9de,0xb99bb905,0x3aa337c1,0xbabfb93d,
0x3ab4390e,0xbac6bac6,0x390e3ab4,0x390e0000,0x390ebae3,0x3a58ba8b,0xba333aad,0xba10b93d,
0xbac6b9ac,0x3aa3ba57,0xba8138e0,0x37c13ae1,0x25e0,0x25e0a580,0xa5800000,0x25e0,
0xbac6ba29,0xb9de3aa3,0xb9ac38e0,0x37c139dc,0x390eba33,0xba8bbae0,0xb7ee3aad,0xba1039dc,
0x3ab4babf,0x38e038e0,0xbabf3ab4,0x390ea580,0x3ab4ba57,0xbac8b99b,0xb9de37c1,0xbabf3ae1,
0x390eb905,0xb9acba81,0xbac8babf,0x38e0b93d,0xbac63ab4,0x390e390e,0x3ab4bac6,0x3ab40000,
0x3ae1,0x39dc3ae1,0xb93da580,0x25e0b93d,0xbac63a58,0xb9d1b987,0xba8bba10,0xbaf73ae1,
0x390e38e0,0x37c137c1,0x38e0390e,0xbac625e0,0x3ab4b9d1,0xbae3ba33,0xb987baf7,0x3aad39dc,
0x0,0x0,0x0,0x0,0x3dcc0000,0x3e973e4b,0x3ef53ec7,0x3f253f10,
0x3e4b0000,0x3f103ec7,0x3f573f37,0x3f7c3f6e,0x3e970000,0x3f483f10,0x3f7f3f6e,0x3f5e3f7a,
0x3ec70000,0x3f6e3f37,0x3f693f80,0x3eaf3f2e,0x3ef50000,0x3f7f3f57,0x3f1b3f69,0xbeaf3e19,
0x3f100000,0x3f7a3f6e,0x3e193f2e,0xbf5ebede,0x3f250000,0x3f5e3f7c,0xbeaf3eaf,0xbf7cbf5e,
0x3f370000,0x3f2e3f80,0xbf40bd4c,0xbf25bf7f,0x3f480000,0x3ede3f7a,0xbf7abede,0xbf48,
0x3f570000,0x3e193f69,0xbf76bf40,0x3f25be97,0x3f640000,0xbe193f50,0xbf37bf73,0x3f7c3e97,
0x3f6e0000,0xbede3f2e,0xbe97bf7f,0x3f5e3f48,0x3f760000,0xbf2e3f05,0x3e4bbf64,0x3eaf3f7f,
0x3f7c0000,0xbf5e3eaf,0x3f25bf25,0xbeaf3f5e,0x3f7f0000,0xbf7a3e19,0x3f6ebe97,0xbf5e3ede,
0x0,0x0,0x0,0x0,0x3f483f37,0x3f643f57,0x3f763f6e,0x3f7f3f7c,
0x3f7a3f80,0x3f503f69,0x3f053f2e,0x3e193eaf,0x3ede3f2e,0xbe193e19,0xbf2ebede,0xbf7abf5e,
0xbedebd4c,0xbf73bf40,0xbf64bf7f,0xbe97bf25,0xbf7abf40,0xbf37bf76,0x3e4bbe97,0x3f6e3f25,
0xbf48bf7f,0x3e97be97,0x3f7f3f48,0x3ede3f5e,0xbf25,0x3f7c3f25,0x3eaf3f5e,0xbf5ebeaf,
0x3f483dcc,0x3f1b3f7e,0xbf50be19,0xbf10bf7c,0x3f7a3f48,0xbede3ede,0xbf48bf7a,0x3f480000,
0x3ede3f7e,0xbf80bf05,0x3ec7bf10,0x3f2e3f7c,0xbede3f1b,0xbef5bf80,0x3f7e3f10,0xbf2e3eaf,
0xbf7abe19,0x3f10bf10,0x3e193f7a,0xbf48bf5e,0xbf48bf50,0x3f7e3ec7,0xbf693e19,0x3f10bf25,
0xbf7c,0x3eaf3f7c,0xbf25bf5e,0x3f5e3f25,0x3f48bf10,0xbf2e3f2e,0x3f10bf48,0xbede3f5e,
0x0,0x0,0x0,0x0,0xb82e0000,0xb92fb8c9,0xb989b979,0xbae53a57,
0xb8c90000,0x3a57b979,0x393f3a86,0x39e33a9b,0xb92f0000,0x3a183a57,0x3a913a9b,0xba98bad6,
0xb9790000,0x3a9b3a86,0x3ab1b9a3,0x396a39fe,0xb9890000,0x3a91393f,0xbada3ab1,0xb96ab9c3,
0x3a570000,0xbad63a9b,0xb9c339fe,0x3a98b998,0xbae50000,0xba9839e3,0xb96a396a,0xb9e33a98,
0x3a860000,0x39feb9a3,0x3965b82d,0x3ae5ba91,0x3a180000,0x3998bad6,0x3ad6b998,0xba18,
0x393f0000,0xb9c33ab1,0xbadd3965,0xbae5392f,0xba450000,0x39c3ba13,0xba863aa9,0x39e3b92f,
0x3a9b0000,0xb99839fe,0x392fba91,0xba983a18,0x3add0000,0xb9fe3afa,0xb8c93a45,0x396a3a91,
0x39e30000,0x3a98396a,0xbae53ae5,0xb96aba98,0x3a910000,0x3ad6b9c3,0x3a9b392f,0x3a983998,
0x0,0x0,0x0,0x0,0x3a183a86,0xba45393f,0x3add3a9b,0x3a9139e3,
0xbad6b9a3,0xba133ab1,0x3afa39fe,0xb9c3396a,0x399839fe,0x39c3b9c3,0xb9feb998,0x3ad63a98,
0xb998b82d,0x3aa93965,0x3a45ba91,0x392f3ae5,0x3ad63965,0xba86badd,0xb8c9392f,0x3a9bbae5,
0xba18ba91,0xb92f392f,0x3a913a18,0x3998ba98,0x3ae5,0x39e3bae5,0x396aba98,0x3a98b96a,
0x3a18b82e,0xbada3859,0x3a1339c3,0xba57b9e3,0xbad63a18,0xb9983998,0xba183ad6,0x3a180000,
0x39983859,0x39a3bafa,0xb979ba57,0x39fe39e3,0xb998bada,0x398939a3,0x38593a57,0xb9fe396a,
0x3ad639c3,0x3a57ba57,0xb9c3bad6,0xba183a98,0xba183a13,0x3859b979,0xbab1b9c3,0x3a573ae5,
0xb9e3,0x396a39e3,0x3ae53a98,0xba98bae5,0x3a18ba57,0xb9fe39fe,0x3a57ba18,0xb998ba98,
0x3f800000,0xbf7fbd4c,0x3f7e3dcc,0xbf7cbe19,0x3f7e0000,0xbf6ebe7d,0x3f503ef5,0xbf25bf2e,
0x3f7a0000,0xbf48bede,0x3ede3f48,0xbf7a,0x3f730000,0xbf10bf1b,0xbd4c3f76,0x3f25bf6e,
0x3f690000,0xbe97bf40,0xbf053f7e,0x3f7cbf10,0x3f5e0000,0xbf5e,0xbf5e3f5e,0x3f5e0000,
0x3f500000,0x3e97bf73,0xbf803f1b,0x3eaf3f10,0x3f400000,0x3f10bf7e,0xbf643e7d,0xbeaf3f6e,
0x3f2e0000,0x3f48bf7f,0xbf10be19,0xbf5e3f7a,0x3f1b0000,0x3f6ebf76,0xbdccbf05,0xbf7c3f2e,
0x3f050000,0x3f7fbf64,0x3ec7bf50,0xbf253e19,0x3ede0000,0x3f7abf48,0x3f48bf7a,0xbede,
0x3eaf0000,0x3f5ebf25,0x3f7cbf7c,0x3f25bf5e,0x3e7d0000,0x3f2ebef5,0x3f73bf57,0x3f7cbf7f,
0x3e190000,0x3edebe97,0x3f2ebf10,0x3f5ebf48,0x3d4c0000,0x3e19bdcc,0x3e7dbe4b,0x3eafbe97,
0x3f7a3e4b,0xbf76be7d,0x3f733e97,0xbf6ebeaf,0x3ede3f57,0xbe4bbf73,0xbd4c3f7f,0x3e97bf7c,
0xbede3f7a,0x3f48bf48,0xbf7a3ede,0x3f7a0000,0xbf7a3f05,0x3f693dcc,0xbef5bf2e,0xbe193f7c,
0xbf48be7d,0x3d4c3f64,0x3f37bf6e,0xbf7f3eaf,0xbf5e,0xbf5e3f5e,0x3f5e0000,0xbf5e,
0x3f48bf76,0xbf573d4c,0xbe7d3f6e,0x3f7fbf25,0x3f7abef5,0x3dccbf50,0xbf803f2e,0x3e193f25,
0x3ede3e97,0x3f6ebf6e,0xbe97bede,0xbf7a3f5e,0xbede3f64,0x3f40be4b,0x3f57bf7f,0xbe97beaf,
0xbf7a3f73,0xbe7d3f37,0x3f40be97,0x3f6ebf7c,0xbf483ede,0xbf7a3f7a,0xbede3f48,0x3ede0000,
0xbeaf,0xbf253eaf,0xbf7c3f5e,0xbf5e3f7c,0x3f48bf69,0x3ec7bf1b,0xbdccbe19,0xbf103eaf,
0x3f7abf6e,0x3f7fbf7f,0x3f6ebf7a,0x3f48bf5e,0x3edebec7,0x3f05bef5,0x3f1bbf10,0x3f2ebf25,
0xb9a30000,0xba91b82d,0x3859b82e,0xb9e339c3,0x38590000,0xba9b39a4,0xba13b989,0x3ae5b9fe,
0xbad60000,0xba18b998,0x39983a18,0x3ad6,0xbaa90000,0xba573ada,0xb82d3add,0xbae5ba9b,
0x3ab10000,0x392f3965,0xbafa3859,0x39e3ba57,0xba980000,0x3a98,0x3a98ba98,0xba980000,
0xba130000,0xb92f3aa9,0x39a3bada,0x396a3a57,0xb9650000,0x3a57b859,0x3a45b9a4,0xb96a3a9b,
0x39fe0000,0x3a18ba91,0xba5739c3,0x3a98bad6,0xbada0000,0x3a9bbadd,0x382ebafa,0xb9e339fe,
0x3afa0000,0x3a913a45,0xb9793a13,0x3ae5b9c3,0x39980000,0xbad6ba18,0x3a183ad6,0xb998,
0x396a0000,0xba983ae5,0x39e3b9e3,0xbae53a98,0xb9a40000,0x39fe3989,0xbaa9b93f,0x39e3ba91,
0xb9c30000,0x3998392f,0x39feba57,0xba98ba18,0x382d0000,0xb9c3382e,0xb9a438c9,0x396a392f,
0xbad6b8c9,0xbadd39a4,0xbaa9b92f,0xba9bb96a,0x3998393f,0x38c93aa9,0xb82d3a91,0xb92fb9e3,
0xb998bad6,0x3a18ba18,0x3ad63998,0xbad60000,0x3ad63afa,0x3ab1b82e,0x3989b9fe,0x39c339e3,
0xba1839a4,0x382dba45,0x3a86ba9b,0xba91396a,0x3a98,0x3a98ba98,0xba980000,0x3a98,
0x3a18badd,0xb93f382d,0x39a43a9b,0x3a913ae5,0xbad63989,0xb82e3a13,0x39a339fe,0xb9c3bae5,
0x3998b92f,0x3a9bba9b,0x392fb998,0x3ad6ba98,0xb998ba45,0xb96538c9,0x393fba91,0x392fb96a,
0x3ad6baa9,0x39a43a86,0xb965392f,0x3a9bb9e3,0xba183998,0x3ad6bad6,0xb9983a18,0x39980000,
0xb96a,0x3ae5396a,0xb9e3ba98,0x3a9839e3,0x3a18bab1,0xb9793ada,0x382e39c3,0xba57396a,
0xbad6ba9b,0x3a91ba91,0x3a9b3ad6,0x3a183a98,0x39983979,0x3afa3989,0xbadaba57,0x39fe3ae5,
};

typedef unsigned short u16;
typedef unsigned long long u64;
typedef __attribute__((ext_vector_type(8))) short bf16x8;
typedef __attribute__((ext_vector_type(4))) short s16x4;
typedef __attribute__((ext_vector_type(4))) float f32x4;
typedef __attribute__((ext_vector_type(2))) float f32x2;
typedef __attribute__((ext_vector_type(4))) unsigned u32x4;
typedef __attribute__((ext_vector_type(2))) __bf16 bf16x2_t;
typedef __attribute__((address_space(3))) s16x4* lds_s16x4_p;
typedef __attribute__((address_space(3))) void* lds_void_p;
#define DEVINL __device__ __forceinline__

#define STAMP(i)
#define STAMP_FLUSH()
#define STAMP2(a, b) do { if (s == 0) { STAMP(a); } else { STAMP(b); } } while (0)
typedef unsigned stv_t;

#define IN_POL ""
#define ST_POL "sc1"
#define MFMA16(a, b, c) __builtin_amdgcn_mfma_f32_16x16x32_bf16((a), (b), (c), 0, 0, 0)
#define MFMAU(a, b, c) MFMA16(__builtin_bit_cast(bf16x8, a), __builtin_bit_cast(bf16x8, b), c)
#define SB0() __builtin_amdgcn_sched_barrier(0)
constexpr unsigned OOB = 0x7ffffff0u;
constexpr int SC1 = 16;

template <int N> struct IC { static constexpr int v = N; };
template <int... I, class F> DEVINL void static_for_impl(std::integer_sequence<int, I...>, F&& f) { (f(IC<I>{}), ...); }
template <int N, class F> DEVINL void static_for(F&& f) { static_for_impl(std::make_integer_sequence<int, N>{}, f); }
DEVINL void bar() { asm volatile("" ::: "memory"); __builtin_amdgcn_s_barrier(); asm volatile("" ::: "memory"); }
template <int N> DEVINL void wait_vm() { asm volatile("s_waitcnt vmcnt(%0)" ::"n"(N) : "memory"); }
DEVINL void wait_lds() { __builtin_amdgcn_s_waitcnt(0xc07f); }

constexpr int T0 = 16000, T2 = 4000;
constexpr int NT1 = 167;
constexpr int XST = 32768;
constexpr size_t X1_TB = 16 * XST, X1_G = 2 * X1_TB, X1_S = 16 * X1_G;
constexpr size_t X2_TB = 8 * XST, X2_G = 2 * X2_TB, X2_S = 16 * X2_G;
constexpr size_t WF_G = 1 << 20, WF_BYTES = 16 * WF_G;
constexpr size_t Y1_S = (size_t)128 * 65536, Y2_S = (size_t)64 * 65536;
constexpr size_t OFF_FLAG = 0;
constexpr int F_X = 0, F_W1 = 768, F_W2 = 1024, F_G1 = 1280, F_M = 1792, F_G2 = 2304, F_S = 2816, F_EPOCH = 2840, F_XH = 512, F_W1H = 2848, F_END = 3104;
constexpr size_t OFF_PROBE = 1 << 20;
constexpr size_t OFF_WFT = OFF_FLAG + 4096 * 128;
constexpr size_t OFF_WOT = OFF_WFT + 128 * 1024;
constexpr size_t OFF_GE = OFF_WOT + 128 * 1024;
constexpr size_t OFF_X1F = 4 << 20;
constexpr size_t OFF_W1F = OFF_X1F + 2 * X1_S;
constexpr size_t OFF_Y1F = OFF_W1F + WF_BYTES;
constexpr size_t OFF_X2F = OFF_Y1F + 2 * Y1_S;
constexpr size_t OFF_W2F = OFF_X2F + 2 * X2_S;
constexpr size_t OFF_Y2F = OFF_W2F + WF_BYTES;
constexpr size_t WS_USED = OFF_Y2F + 2 * Y2_S;
constexpr int TAB_F = 0, TAB_G = 8, TAB_T = 14;
constexpr int LDS_BYTES = 81920;

DEVINL unsigned pack_bf16(float a, float b) {
  f32x2 v = {a, b};
  bf16x2_t r = __builtin_convertvector(v, bf16x2_t);
  return __builtin_bit_cast(unsigned, r);
}
DEVINL u32x4 pack8(const f32x4& a, const f32x4& b) {
  return (u32x4){pack_bf16(a[0], a[1]), pack_bf16(a[2], a[3]), pack_bf16(b[0], b[1]), pack_bf16(b[2], b[3])};
}
DEVINL s16x4 tr_read(const char* p) { return __builtin_amdgcn_ds_read_tr16_b64_v4i16((lds_s16x4_p)(p)); }
DEVINL bf16x8 cat8(s16x4 a, s16x4 b) { return __builtin_shufflevector(a, b, 0, 1, 2, 3, 4, 5, 6, 7); }
DEVINL bf16x8 tr_frag(const char* p, int second) { return cat8(tr_read(p), tr_read(p + second)); }
template <int AUX> DEVINL void dma16(__amdgpu_buffer_rsrc_t rs, char* lds_dst, unsigned voff, unsigned soff) {
  const unsigned m = (unsigned)(size_t)(lds_void_p)lds_dst;
  if constexpr (AUX == 0) asm volatile("s_mov_b32 m0, %0\n\ts_nop 0\n\tbuffer_load_dwordx4 %1, %2, %3 offen " IN_POL " lds" :: "s"(m), "v"(voff), "s"(rs), "s"(soff) : "memory");
  else asm volatile("s_mov_b32 m0, %0\n\ts_nop 0\n\tbuffer_load_dwordx4 %1, %2, %3 offen sc1 lds" :: "s"(m), "v"(voff), "s"(rs), "s"(soff) : "memory");
}
DEVINL __amdgpu_buffer_rsrc_t mkrsrc(const void* p, unsigned bytes) { return __builtin_amdgcn_make_buffer_rsrc((void*)p, 0, bytes, 0x00020000); }
DEVINL void st16(void* p, u32x4 v) { asm volatile("global_store_dwordx4 %0, %1, off " ST_POL "\n\ts_nop 1" ::"v"(p), "v"(v) : "memory"); }
template <int I, bool DEV> DEVINL void ldfrag(u32x4& d, const char* base, unsigned lane16) {
  const char* b = base + (I >> 3) * 8192 + 4096;
  if constexpr (DEV) asm volatile("global_load_dwordx4 %0, %1, %2 offset:%3 sc1" : "=v"(d) : "v"(lane16), "s"(b), "n"((I & 7) * 1024 - 4096) : "memory");
  else asm volatile("global_load_dwordx4 %0, %1, %2 offset:%3" : "=v"(d) : "v"(lane16), "s"(b), "n"((I & 7) * 1024 - 4096) : "memory");
}
template <int I, int BS = 16384> DEVINL void ldfrag_rot(u32x4& d, const char* base, int rot, unsigned lane16) {
  ldfrag<I & 7, true>(d, base + (((I >> 3) + rot) & 3) * BS, lane16);
}
template <int I> DEVINL void ldfrag_rot1(u32x4& d, const char* base, int rot, unsigned lane16) {
  const int b = ((I >> 2) + rot) & 3;
  ldfrag<I & 3, true>(d, base + (I >> 4) * 32768 + (b >> 1) * 16384 + (b & 1) * 4096, lane16);
}
DEVINL void tie(u32x4& a) { asm volatile("" : "+v"(a)); }
template <int N> DEVINL void tie(u32x4 (&a)[N]) {
#pragma unroll
  for (int i = 0; i < N; ++i) tie(a[i]);
}
DEVINL int lane_id() { int l; asm volatile("v_mbcnt_lo_u32_b32 %0, -1, 0\n\tv_mbcnt_hi_u32_b32 %0, -1, %0" : "=v"(l)); return l; }
DEVINL void sgpr_settle() { asm volatile("s_nop 4" ::: "memory"); }

#define CT_STEP 64
DEVINL void code_touch(unsigned& d, int slice) {
  u64 pc;
  unsigned len, off = slice * CT_STEP;
  asm volatile("s_getpc_b64 %0\n.Lpc%=:\n\ts_mov_b32 %1, .Lpc%=-k_all" : "=s"(pc), "=s"(len));
  pc -= len;
  asm volatile("s_mov_b32 %1, .Lk_end-k_all\n\ts_max_u32 %1, %1, .Lt_end-k_all\n"
               "1:\n\ts_load_dword %0, %3, %2\n\ts_add_u32 %2, %2, %4\n\ts_cmp_lt_u32 %2, %1\n\ts_cbranch_scc1 1b"
               : "=&s"(d), "=&s"(len), "+s"(off) : "s"(pc), "n"(128 * CT_STEP) : "scc", "memory");
}
DEVINL void tie(unsigned& a) { asm volatile("" : "+v"(a)); }

#define PRIO(x) __builtin_amdgcn_s_setprio(x)
#define PRIO_JB0 0
#define PRIO_JAL PRIO_JA
#define PRIO_JBL PRIO_JB
#define PRIO_F 3
#define PRIO_G0 2
#define PRIO_GW 0
#define PRIO_G1 0
#define PRIO_G2A 0
#define PRIO_G2B 0
#define PRIO_JB 0
#define PRIO_T0 1
#define PRIO_T1 1
#define PRIO_T2 0
#define PRIO_MA 0
#define PRIO_MB 1
#define PRIO_JA 0
#define FS 128
#define FLAG_LANES (FS >= 64 ? 8 : FS / 8)
DEVINL void set_flag(char* ws, int flag, u64 tag, int w) {
  u64 d;
  unsigned o;
#if FLAG_LANES > 1
  asm volatile("s_cmp_lg_u32 %2, 0\n\ts_cbranch_scc1 1f\n\ts_bfm_b64 exec, %6, 0\n\ts_nop 1\n\tv_mbcnt_lo_u32_b32 %1, -1, 0\n\tv_mov_b64 %0, %5\n\tv_lshl_add_u32 %1, %1, 3, %3\n\t"
               "global_store_dwordx2 %1, %0, %4 sc1\n\ts_nop 1\n\ts_mov_b64 exec, -1\n1:"
               : "=&v"(d), "=&v"(o) : "s"(w), "s"(flag * FS), "s"(ws + OFF_FLAG), "s"(tag), "n"(FLAG_LANES) : "scc", "memory");
  return;
#endif
  asm volatile("s_cmp_lg_u32 %2, 0\n\ts_cbranch_scc1 1f\n\ts_mov_b64 exec, 1\n\ts_nop 1\n\tv_mov_b32 %1, %3\n\tv_mov_b64 %0, %5\n\t"
               "global_store_dwordx2 %1, %0, %4 sc1\n\ts_nop 1\n\ts_mov_b64 exec, -1\n1:"
               : "=&v"(d), "=&v"(o) : "s"(w), "s"(flag * FS), "s"(ws + OFF_FLAG), "s"(tag) : "scc", "memory");
}
template <int N = 0> DEVINL void publish(char* ws, int flag, u64 tag, int w) { wait_vm<N>(); PRIO(PRIO_F); bar(); set_flag(ws, flag, tag, w); }
#define POLL_SLEEP 1
DEVINL void poll(const char* ws, int flag, u64 mask, u64 tag) {
  const unsigned off = flag * FS;
  u64 v;
  unsigned cnt;
  asm volatile("s_mov_b64 exec, %6\n\ts_nop 4\n\ts_mov_b32 %1, 40000\n"
               "1:\n\tglobal_load_dwordx2 %0, %2, %3 sc1\n\ts_waitcnt vmcnt(0)\n\tv_cmp_ne_u64 vcc, %4, %0\n\ts_nop 1\n\ts_cbranch_vccz 2f\n\t"
               "s_cmp_eq_u64 vcc, %6\n\ts_cbranch_scc1 3f\n\t"
               "s_mov_b64 exec, vcc\n\ts_sub_u32 %1, %1, 1\n\ts_cbranch_scc1 2f\n\ts_sleep %5\n\ts_branch 1b\n"
               "3:\n\ts_mov_b64 exec, 1\n"
               "4:\n\ts_sleep %5\n\tglobal_load_dwordx2 %0, %2, %3 sc1\n\ts_waitcnt vmcnt(0)\n\tv_cmp_ne_u64 vcc, %4, %0\n\ts_sub_u32 %1, %1, 1\n\ts_cbranch_scc1 2f\n\ts_cbranch_vccnz 4b\n\t"
               "s_andn2_b64 exec, %6, 1\n\ts_branch 1b\n"
               "2:\n\ts_mov_b64 exec, -1"
               : "=&v"(v), "=&s"(cnt) : "v"(off), "s"(ws + OFF_FLAG), "s"(tag), "n"(POLL_SLEEP), "s"(mask) : "vcc", "scc", "memory");
}
DEVINL void poll_from(u64 v, const char* ws, int flag, u64 mask, u64 tag) {
  const unsigned off = flag * FS;
  unsigned cnt;
  asm volatile("s_mov_b64 exec, %6\n\ts_nop 4\n\ts_mov_b32 %1, 40000\n\ts_branch 5f\n"
               "1:\n\tglobal_load_dwordx2 %0, %2, %3 sc1\n\ts_waitcnt vmcnt(0)\n5:\n\tv_cmp_ne_u64 vcc, %4, %0\n\ts_nop 1\n\ts_cbranch_vccz 2f\n\t"
               "s_cmp_eq_u64 vcc, %6\n\ts_cbranch_scc1 3f\n\t"
               "s_mov_b64 exec, vcc\n\ts_sub_u32 %1, %1, 1\n\ts_cbranch_scc1 2f\n\ts_sleep %5\n\ts_branch 1b\n"
               "3:\n\ts_mov_b64 exec, 1\n"
               "4:\n\ts_sleep %5\n\tglobal_load_dwordx2 %0, %2, %3 sc1\n\ts_waitcnt vmcnt(0)\n\tv_cmp_ne_u64 vcc, %4, %0\n\ts_sub_u32 %1, %1, 1\n\ts_cbranch_scc1 2f\n\ts_cbranch_vccnz 4b\n\t"
               "s_andn2_b64 exec, %6, 1\n\ts_branch 1b\n"
               "2:\n\ts_mov_b64 exec, -1"
               : "+v"(v), "=&s"(cnt) : "v"(off), "s"(ws + OFF_FLAG), "s"(tag), "n"(POLL_SLEEP), "s"(mask) : "vcc", "scc", "memory");
}
constexpr u64 lanes(int n) { return (1ull << n) - 1; }
template <int N> DEVINL int poll_vote(u64 v, const char* ws, unsigned off, u64 tag) {
  u64 t;
  unsigned cnt;
  asm volatile("s_bfm_b64 exec, %8, 0\n\ts_nop 4\n\ts_mov_b32 %1, 40000\n\ts_branch 5f\n"
               "1:\n\tglobal_load_dwordx2 %0, %3, %4 sc1\n\ts_waitcnt vmcnt(0)\n5:\n\tv_lshlrev_b64 %2, 1, %0\n\tv_cmp_ne_u64 vcc, %5, %2\n\ts_nop 1\n\ts_cbranch_vccz 2f\n\t"
               "s_mov_b64 exec, vcc\n\ts_sub_u32 %1, %1, 1\n\ts_cbranch_scc1 2f\n\ts_sleep %6\n\ts_branch 1b\n"
               "2:\n\ts_bfm_b64 exec, %8, 0\n\ts_nop 1\n\tv_cmp_ne_u64 vcc, %7, %0\n\ts_mov_b64 exec, -1\n\ts_nop 1\n\ts_bcnt1_i32_b64 %1, vcc"
               : "+v"(v), "=&s"(cnt), "=&v"(t) : "v"(off), "s"(ws + OFF_FLAG), "s"(tag << 1), "n"(POLL_SLEEP), "s"(tag), "n"(N) : "vcc", "scc", "memory");
  return cnt;
}
DEVINL int near_side(const char* p, int first) {
  u64 t0, t1, t2;
  unsigned d;
  asm volatile("s_memtime %0\n\ts_waitcnt lgkmcnt(0)\n\ts_load_dword %3, %4, 0x0\n\ts_waitcnt lgkmcnt(0)\n\t"
               "s_memtime %1\n\ts_waitcnt lgkmcnt(0)\n\ts_load_dword %3, %5, 0x0\n\ts_waitcnt lgkmcnt(0)\n\ts_memtime %2\n\ts_waitcnt lgkmcnt(0)"
               : "=&s"(t0), "=&s"(t1), "=&s"(t2), "=&s"(d) : "s"(p + first * 8192), "s"(p + (first ^ 1) * 8192) : "memory");
  return first ^ ((unsigned)t2 - (unsigned)t1 < (unsigned)t1 - (unsigned)t0);
}

template <int N> DEVINL void flag_peek(u64& v, const char* ws, unsigned off) {
  asm volatile("s_bfm_b64 exec, %3, 0\n\ts_nop 1\n\tglobal_load_dwordx2 %0, %1, %2 sc1\n\ts_mov_b64 exec, -1" : "=v"(v) : "v"(off), "s"(ws + OFF_FLAG), "n"(N) : "memory");
}
template <int N> DEVINL void flag_check(u64& v, const char* ws, unsigned off, u64 tag) {
  unsigned cnt;
  asm volatile("s_bfm_b64 exec, %5, 0\n\ts_mov_b32 %1, 40000\n"
               "1:\n\tv_cmp_ne_u64 vcc, %4, %0\n\ts_nop 1\n\ts_cbranch_vccz 2f\n\t"
               "s_mov_b64 exec, vcc\n\ts_sub_u32 %1, %1, 1\n\ts_cbranch_scc1 2f\n\tglobal_load_dwordx2 %0, %2, %3 sc1\n\ts_waitcnt vmcnt(0)\n\ts_branch 1b\n"
               "2:\n\ts_mov_b64 exec, -1"
               : "+v"(v), "=&s"(cnt) : "v"(off), "s"(ws + OFF_FLAG), "s"(tag), "n"(N) : "vcc", "scc", "memory");
}

constexpr float GC = 0.468668693f, GK = 4.91222954f;
template <class H> DEVINL u32x4 gelu_pack8(const f32x4& a, const f32x4& b, H&& between) {
  float y[8] = {a[0], a[1], a[2], a[3], b[0], b[1], b[2], b[3]}, t[8];
#pragma unroll
  for (int i = 0; i < 8; ++i) { t[i] = __builtin_fmaf(y[i], y[i], GK); asm("" : "+v"(t[i])); }
  SB0(); between(0); SB0();
#pragma unroll
  for (int i = 0; i < 8; ++i) { t[i] *= y[i]; asm("" : "+v"(t[i])); }
  SB0(); between(1); SB0();
#pragma unroll
  for (int i = 0; i < 8; ++i) t[i] = __builtin_amdgcn_exp2f(-t[i]);
  SB0(); between(2); SB0();
#pragma unroll
  for (int i = 0; i < 8; ++i) { t[i] = __builtin_fmaf(t[i], GC, GC); asm("" : "+v"(t[i])); }
  SB0(); between(3); SB0();
#pragma unroll
  for (int i = 0; i < 8; ++i) t[i] = __builtin_amdgcn_rcpf(t[i]);
  SB0();
#pragma unroll
  for (int i = 0; i < 8; ++i) { t[i] *= y[i]; asm("" : "+v"(t[i])); }
  SB0();
  return (u32x4){pack_bf16(t[0], t[1]), pack_bf16(t[2], t[3]), pack_bf16(t[4], t[5]), pack_bf16(t[6], t[7])};
}
DEVINL u32x4 gelu_pack8(const f32x4& a, const f32x4& b) { return gelu_pack8(a, b, [](int) {}); }

#define X_PACE 16
#define WL_PACE 32
#define XB_SLEEP 0
#define XA_SLEEP 16
template <int RS, size_t XG>
DEVINL void fwd_unit(const char* la, const u32x4 (&F)[8], char* gd, unsigned lo) {
  bf16x8 A[2][2];
#pragma unroll
  for (int i = 0; i < 2; ++i)
#pragma unroll
    for (int ks = 0; ks < 2; ++ks) A[i][ks] = tr_frag(la + 8 * i + 32 * RS * ks, 4 * RS);
#pragma unroll
  for (int nt = 0; nt < 4; ++nt) {
    f32x4 c0 = {0, 0, 0, 0}, c1 = {0, 0, 0, 0};
#pragma unroll
    for (int ks = 0; ks < 2; ++ks) { c0 = MFMAU(A[0][ks], F[2 * nt + ks], c0); c1 = MFMAU(A[1][ks], F[2 * nt + ks], c1); }
    if constexpr (X_PACE < 16) wait_vm<X_PACE - 1>();
    st16(gd + (nt & 1) * 8 * XG + (lo ^ ((nt >> 1) << 13)), pack8(c0, c1));
  }
}
#define IDXX 0
#define EU_A (idx & 3)
#define EU_B 2
#define EU_I 0
#define SG_W ((idx & 3) ^ 1)
#define WSD (w & 1)
#define XSW 1
template <size_t XG> DEVINL unsigned x_lane_off(int lane, int tl, int row, unsigned pb) {
  const unsigned go = ((lane & 15) >> 1) * XG;
  return go + (((go >> 8) & 8192) ^ pb) + (row >> 6) * 16384 + (((row & 63) * 128) ^ (XSW ? (lane & 14) << 9 : 0)) + ((((lane & 1) * 4 + (lane >> 4)) ^ ((tl >> 1) & 7)) * 16);
}

DEVINL void w_issue(const u16* __restrict__ wsrc, char* reg, int co, int cb, int w, int lane) {
  const int par = co >> 5, c0 = (co & 31) * 8;
  __amdgpu_buffer_rsrc_t rs = mkrsrc(wsrc, 31 * 65536 * 2);
#pragma unroll
  for (int i = 0; i < 4; ++i) {
    const int cl = 2 * w + (i >> 1), tap = 8 * (i & 1) + (lane >> 3);
    const unsigned off = ((2 * tap + par) * 256 + c0 + cl) * 512 + cb * 128 + (((lane & 7) ^ (2 * (((tap >> 1) & 1) + 2 * (i & 1)))) * 16);
    dma16<0>(rs, reg + (w * 4 + i) * 1024, off, 0);
  }
}
template <int PACE, int NM>
DEVINL void w_compute(const u32x4 (&Tm)[NM], char* __restrict__ wf, const char* reg, int co, int cb, int w, int lane, unsigned pb) {
  const int G = lane >> 4, rq = (lane & 15) >> 2, p = lane & 3;
  char* dst = wf + (cb * 4 + w) * 65536 + (co >> 4) * 16384 + ((co >> 2) & 3) * 2048 + (co & 3) * 256 + (unsigned)((lane & 15) * 16 + 4 * G * WF_G);
  const char* la = reg + (8 * (G & 1) + rq) * 128 + (((2 * w + (p >> 1)) ^ (2 * ((rq >> 1) + 2 * (G & 1)))) * 16) + 8 * (p & 1);
  bf16x8 B[8];
#pragma unroll
  for (int cl = 0; cl < 8; ++cl) B[cl] = tr_frag(la + cl * 2048, 4 * 128);
#pragma unroll
  for (int mt = 0; mt < NM; ++mt) {
    f32x4 d[8];
#pragma unroll
    for (int cl = 0; cl < 8; ++cl) d[cl] = MFMAU(Tm[mt], B[cl], ((f32x4){0, 0, 0, 0}));
#pragma unroll
    for (int r = 0; r < 4; ++r) {
      u32x4 o = {pack_bf16(d[0][r], d[1][r]), pack_bf16(d[2][r], d[3][r]), pack_bf16(d[4][r], d[5][r]), pack_bf16(d[6][r], d[7][r])};
      if constexpr (PACE < 16) wait_vm<PACE>();
      st16(dst + (pb ^ ((((NM == 4) & mt) ^ (r >> 1)) << 13)) + r * WF_G + (NM == 4 ? mt >> 1 : mt) * 1024, o);
    }
  }
}

constexpr float LOG2E2 = 2.0f * 1.4426950408889634f;
DEVINL void s_gather(u32x4& d, const u16* __restrict__ wf, const u16* __restrict__ wo, bool o, int wv, int f, int lane) {
  const int mt = f >> 3;
  const unsigned offf = ((f & 7) * 32 + 8 * (lane >> 4)) * 512 + ((mt >> 1) * 32 + ((lane >> 2) & 3) * 8 + (mt & 1) * 4 + (lane & 3)) * 2;
  const unsigned offo = ((f >> 1) * 16 + 8 * (lane >> 5)) * 512 + ((f & 1) * 32 + ((lane >> 2) & 1) * 16 + ((lane >> 3) & 3) * 4 + (lane & 3)) * 2;
  const char* src = (const char*)(o ? wo : wf) + (o ? offo : offf) + wv * 128;
  unsigned t[8];
#pragma unroll
  for (int j = 0; j < 8; ++j) {
    t[j] = *(const u16*)(src + j * 512);
    if (o) t[j] = __builtin_bit_cast(unsigned, 2.0f * __builtin_bit_cast(float, t[j] << 16)) >> 16;
  }
  d = (u32x4){t[0] | t[1] << 16, t[2] | t[3] << 16, t[4] | t[5] << 16, t[6] | t[7] << 16};
}
DEVINL void s_ge(const u16* __restrict__ g, const u16* __restrict__ wg, float* __restrict__ ge, char* lds, int eb, int lane) {
  __amdgpu_buffer_rsrc_t rs = mkrsrc(wg, 131072);
#pragma unroll
  for (int i = 0; i < 8; ++i) dma16<0>(rs, lds + i * 1024, (i * 32 + (lane >> 1)) * 512 + eb * 32 + (lane & 1) * 16, 0);
  bf16x8 gb[8];
#pragma unroll
  for (int ks = 0; ks < 8; ++ks) gb[ks] = *(const bf16x8*)(g + (lane & 15) * 256 + ks * 32 + (lane >> 4) * 8);
  wait_vm<0>();
  const int G = lane >> 4, rq = (lane & 15) >> 2, p = lane & 3;
  f32x4 acc = {0, 0, 0, 0};
#pragma unroll
  for (int ks = 0; ks < 8; ++ks) acc = MFMA16(gb[ks], tr_frag(lds + (ks * 32 + 8 * G + rq) * 32 + 8 * p, 4 * 32), acc);
  acc *= LOG2E2;
  f32x4 ex;
#pragma unroll
  for (int r = 0; r < 4; ++r) ex[r] = __builtin_amdgcn_exp2f(acc[r] > 64.f ? 64.f : (acc[r] < -64.f ? -64.f : acc[r]));
  const int e = (eb & 3) * 16 + (lane & 15);
  float* d = ge + ((G >> 1) * 4 + (eb >> 2)) * 512 + (G & 1) * 256 + (((e >> 3) & 3) * 16 + (e >> 5) * 8 + (e & 7)) * 4;
  st16(d, __builtin_bit_cast(u32x4, acc));
  st16(d + 4096, __builtin_bit_cast(u32x4, ex));
}

#define G1_K 13
#define G1_P 19
#define G1_K2 27
#define GORD 0
#define GZ 0
#define GX 0
#define SPREAD 1
#define TRAIL 10
#define TR_PACE 32
#define PRIO_TR 0
#define G3_WR 4
#define G1_WR 6
#define OPENV 0
#define WPRE (OPENV & 1 ? 0 : 4)
#define NOPEEK 0
#define G1_K0 3
#define PRO1 5
#define PRO2 7
#define RAMP 2
constexpr int RING = 7, SLOT = 11264, RING_DUMMY = RING * SLOT;
template <int PH> struct GPlan {
  static constexpr int NST = PH == 1 ? 32 : 8, PRO = PH == 1 ? PRO1 : PRO2;
  int first[NST + 1];
  int wfirst[NST + 1];
  int before[NST];
  int pos[NST];
  int posF0, posF, posF2, posY;
  constexpr GPlan() : first{}, wfirst{}, before{}, pos{}, posF0(0), posF(0), posF2(0), posY(0) {
    int n = 0, u = 0, k = 0;
    for (; u < PRO; ++u) pos[u] = (n += 3 + w1(u));
    for (int t = 0; t < NST; ++t) {
      before[t] = n;
      first[t] = u;
      wfirst[t] = k;
      if (PH == 1 && t == G1_K0 - 2) posF0 = ++n;
      if (PH == 1 && t == G1_K - 2) posF = ++n;
      if (PH == 1 && t == G1_K2 - 2) posF2 = ++n;
      if (PH == 1 && t == 16) n += 6;
      for (; u <= t + RING - 1 && u < NST && (PH != 1 || ((u < 8 || t >= G1_K0) && (u < 16 || t >= G1_K))) && (u >= 16 || u < first[t] + RAMP); ++u) pos[u] = (n += 3 + w1(u));
      if (PH == 1 && t == 16) posY = n;
      for (int i = 0; i < G1_WR && PH == 1 && t >= G1_K2 && k < 2 * (t - 16); ++i) { ++k; ++n; }
      for (; PH == 3 && k < G3_WR * t; ++k) ++n;
    }
    first[NST] = u;
    wfirst[NST] = k;
  }
  static constexpr int w1(int u) { return PH == 1 && u >= WPRE && u < 16 ? 2 : 0; }
  constexpr int nx(int t) const { int n = 0; for (int u = first[t]; u < first[t + 1]; ++u) n += 3 + w1(u); return n; }
  constexpr int nreq(int t) const { return nx(t) + wfirst[t + 1] - wfirst[t]; }
  constexpr int xreq(int t, int j) const { int u = first[t]; while (j >= 3 + w1(u)) { j -= 3 + w1(u); ++u; } return 5 * u + j; }
  constexpr int slot(int t, int j) const {
    const int n = nreq(t);
    if (!SPREAD) return -1;
    if (n <= 6) return 4 * (j * 6 / n) + 1;
    if (n <= 12) return 2 * (j * 12 / n);
    return j * 24 / n;
  }
  constexpr int wait(int t) const {
    int need = pos[t + 1];
    if (PH == 1 && t == G1_K0 && posF0 > need) need = posF0;
    if (PH == 1 && t == G1_K && posF > need) need = posF;
    if (PH == 1 && t == G1_K2 && posF2 > need) need = posF2;
    if (PH == 1 && t == G1_P && posY > need) need = posY;
    return before[t] - need;
  }
};
template <int PH>
DEVINL void g_phase(char* lds, char* ws, const char* xsrc, const char* wsrc1, const char* wsrc, u32x4 (&W)[32], char* ydst, unsigned yoff,
                    int fbase, int fbase2, int yflag, int pflag, u64 tag, int rot, int g7, int w, stv_t& stv) {
  const int lane = lane_id();
  constexpr GPlan<PH> PL;
  constexpr int NST = PL.NST, NACC = PH == 1 ? 6 : 3, ROWS = PH == 1 ? 84 : 42;
  __amdgpu_buffer_rsrc_t xrs = mkrsrc(xsrc, PH == 1 ? X1_S + 16 * XST : 8 * XST);
  __amdgpu_buffer_rsrc_t yrs = mkrsrc(ydst, PH == 1 ? 2 * Y1_S : Y2_S);
  unsigned xo[3];
  int xd[3];
#pragma unroll
  for (int i = 0; i < 3; ++i) {
    const int piece = i * 4 + w, row = piece * 8 + (lane >> 3);
    xo[i] = row < 84 ? ((piece & 7) ^ (XSW ? g7 : 0)) * 1024 + (i == 2 ? 16384 : 0) + lane * 16 : OOB;
    xd[i] = piece * 1024;
  }
  const unsigned lane16 = lane * 16;
  const bool dummy = (w == 3);
  unsigned xq[4];
#pragma unroll
  for (int i = 0; i < 4; ++i) xq[i] = ((i + rot) & 3) * (2 * XST);
  auto piece = [&](auto jc) {
    constexpr int u = decltype(jc)::v / 3, i = decltype(jc)::v % 3, slot = u % RING;
    const unsigned so = xq[(u >> 1) & 3] + (u >= 16 ? X1_S : 0) + (u & 9) * XST;
    dma16<SC1>(xrs, lds + (i == 2 && dummy ? RING_DUMMY : slot * SLOT + xd[i]), xo[i], so);
  };
  sgpr_settle();
  static_for<PL.PRO>([&](auto uc) {
    constexpr int u = decltype(uc)::v;
    static_for<3>([&](auto ic) { piece(IC<3 * u + decltype(ic)::v>{}); });
    static_for<PL.w1(u)>([&](auto ic) { constexpr int i = 2 * u + decltype(ic)::v; ldfrag_rot1<i>(W[i], wsrc1, rot, lane16); });
  });
  const int xa = (lane & 15) * 128 + (((lane >> 4) ^ ((lane & 15) >> 1)) * 16);
  int xb[4] = {xa, xa ^ 64, xa + 4 * SLOT, (xa ^ 64) + 4 * SLOT};
  asm volatile("" : "+v"(xb[2]), "+v"(xb[3]));
  const int za = (lane & 8) ? (xa & 255) + 512 + ((lane & 2) << 7) : xa;
  int xz[4] = {za, za ^ 64, za + 4 * SLOT, (za ^ 64) + 4 * SLOT};
  if (GZ) asm volatile("" : "+v"(xz[2]), "+v"(xz[3]));
  u32x4 xc[6], xs[6];
  auto ldx = [&](int pi, int slot) {
    const int ro = (PH == 1 ? pi * 16 : (pi / 3) * 42 + (pi % 3) * 16) * 128 + (slot & 3) * SLOT;
    if (GZ && PH == 1 && pi == 5) {
      xc[pi] = *(const u32x4*)(lds + xz[(slot >> 2) * 2] + ro);
      xs[pi] = *(const u32x4*)(lds + xz[(slot >> 2) * 2 + 1] + ro);
    } else {
      xc[pi] = *(const u32x4*)(lds + xb[(slot >> 2) * 2] + ro);
      xs[pi] = *(const u32x4*)(lds + xb[(slot >> 2) * 2 + 1] + ro);
    }
  };
  f32x4 R[NACC], Q[NACC];
  auto sty = [&](int n, unsigned base) {
    const int l = lane_id();
    const int tl = n * 16 + (l & 15);
    unsigned off = (tl >> 1) * 65536 + base + (tl & 1) * 64 + (l >> 4) * 16;
    off ^= (off >> 8) & 8192;
    __builtin_amdgcn_raw_buffer_store_b128(pack8(R[n], Q[n]), yrs, tl < ROWS ? off : OOB, 0, SC1);
  };
  wait_vm<PL.before[0] - PL.pos[0]>();
  static_for<PH == 1 ? (WPRE ? 2 * WPRE : 2) : 32>([&](auto ic) { tie(W[decltype(ic)::v]); });
  bar();
  STAMP(PH == 1 ? 5 : (PH == 2 ? 14 : 18));
#pragma unroll
  for (int pi = 0; pi < 6; ++pi) ldx(pi, 0);
  u64 fv;
  unsigned foff;
  static_for<NST>([&](auto ic) {
    constexpr int st = decltype(ic)::v;
    constexpr bool MORE = st + 1 < NST;
    if constexpr (MORE) {
      wait_vm<PL.wait(st)>();
      if constexpr (PH == 1 && st + 1 >= WPRE && st + 1 < 16) { tie(W[2 * st + 2]); tie(W[2 * st + 3]); }
      if constexpr (PH == 1 && st == G1_K0) { STAMP(12); flag_check<NOPEEK ? 1 : 48>(fv, ws, foff, tag); STAMP(23); }
      if constexpr (PH == 1 && st == G1_K) { STAMP(8); flag_check<NOPEEK ? 1 : 32>(fv, ws, foff, tag); }
      if constexpr (PH == 1 && st == G1_K2) { STAMP(21); flag_check<NOPEEK ? 1 : 16>(fv, ws, foff, tag); STAMP(22); }
      if constexpr (!(GX & 1) && !((GX & 8) && (st & 1))) bar();
      if constexpr (PH == 1 && st == G1_K) STAMP(9);
      if constexpr (PH == 1 && st == 16) STAMP(6);
      if constexpr (PH == 1 && st == G1_P) { PRIO(PRIO_F); set_flag(ws, yflag, tag, w); PRIO(PRIO_G1); STAMP(7); }
      SB0();
      if constexpr (PH == 1 && st == G1_K0 - 2) {
        const int l = lane_id();
        foff = (l + (l < 32 ? fbase + F_XH - F_X - 256 : fbase2 + F_W1H - F_W2 - 32)) * FS;
        flag_peek<NOPEEK ? 1 : 48>(fv, ws, foff);
      }
      if constexpr (PH == 1 && st == G1_K - 2) { foff = (fbase + lane_id()) * FS; flag_peek<NOPEEK ? 1 : 32>(fv, ws, foff); }
      if constexpr (PH == 1 && st == G1_K2 - 2) { foff = (fbase2 + lane_id()) * FS; flag_peek<NOPEEK ? 1 : 16>(fv, ws, foff); }
    }
    auto req = [&](auto mc) {
      static_for<PL.nreq(st)>([&](auto jc) {
        constexpr int j = decltype(jc)::v, nx = PL.nx(st), xu = j < nx ? PL.xreq(st, j) / 5 : 0, xi = j < nx ? PL.xreq(st, j) % 5 : 0;
        if constexpr (PL.slot(st, j) == decltype(mc)::v && !(GX & 2)) {
          SB0();
          if constexpr (j < nx && xi < 3) piece(IC<3 * xu + xi>{});
          else if constexpr (j < nx) ldfrag_rot1<2 * xu + xi - 3>(W[2 * xu + xi - 3], wsrc1, rot, lane16);
          else ldfrag_rot<PL.wfirst[st] + j - nx, PH == 1 ? 16384 : 8192>(W[PL.wfirst[st] + j - nx], wsrc, PH == 1 ? rot : 0, lane16);
          SB0();
        }
      });
    };
    req(IC<-1>{});
    u32x4 nw;
    constexpr f32x4 Z = {0, 0, 0, 0};
#define GM(a, b, c) MFMAU(a, b, c)
    if constexpr (GORD == 1 && PH == 1) {
      constexpr int ks = st & 15;
      static_for<24>([&](auto mc) {
        constexpr int m = decltype(mc)::v, n = m < 12 ? m >> 1 : (m - 12) % 6;
        if constexpr (st == 16 && m < 12 && !(m & 1)) sty(n, yoff);
        if constexpr (m < 12 && !(m & 1)) R[n] = GM(W[2 * ks], xc[n], ks == 0 ? Z : R[n]);
        else if constexpr (m < 12) Q[n] = GM(W[2 * ks], xs[n], ks == 0 ? Z : Q[n]);
        else if constexpr (m < 18) R[n] = GM(W[2 * ks + 1], xs[n], R[n]);
        else Q[n] = GM(nw, xc[n], Q[n]);
        if constexpr (m == 0) { tie(W[2 * ks + 1]); nw = W[2 * ks + 1] ^ 0x80008000u; }
        req(mc);
        if constexpr (MORE && m >= 12) {
          constexpr int slot = (st + 1) % RING, ro = n * 2048 + (slot & 3) * SLOT;
          if constexpr (m < 18) xs[n] = *(const u32x4*)(lds + xb[(slot >> 2) * 2 + 1] + ro);
          else xc[n] = *(const u32x4*)(lds + xb[(slot >> 2) * 2] + ro);
        }
        SB0();
      });
    } else
    static_for<6>([&](auto pc) {
      constexpr int pi = decltype(pc)::v, ks = PH == 1 ? st & 15 : 2 * st + pi / 3, n = PH == 1 ? pi : pi % 3;
      if constexpr (PH == 1 && st == 16) sty(n, yoff);
      if constexpr (GORD == 2) {
        if constexpr (n == 0) { tie(W[2 * ks + 1]); }
        R[n] = GM(W[2 * ks + 1], xs[pi], ks == 0 ? Z : R[n]);
        if constexpr (n == 0) { nw = W[2 * ks + 1] ^ 0x80008000u; }
        req(IC<4 * pi>{});
        Q[n] = GM(W[2 * ks], xs[pi], ks == 0 ? Z : Q[n]);
        req(IC<4 * pi + 1>{});
        R[n] = GM(W[2 * ks], xc[pi], R[n]);
        req(IC<4 * pi + 2>{});
        Q[n] = GM(nw, xc[pi], Q[n]);
        req(IC<4 * pi + 3>{});
      } else {
      R[n] = GM(W[2 * ks], xc[pi], ks == 0 ? Z : R[n]);
      if constexpr (n == 0) { tie(W[2 * ks + 1]); nw = W[2 * ks + 1] ^ 0x80008000u; }
      req(IC<4 * pi>{});
      Q[n] = GM(W[2 * ks], xs[pi], ks == 0 ? Z : Q[n]);
      req(IC<4 * pi + 1>{});
      R[n] = GM(W[2 * ks + 1], xs[pi], R[n]);
      req(IC<4 * pi + 2>{});
      Q[n] = GM(nw, xc[pi], Q[n]);
      req(IC<4 * pi + 3>{});
      }
      if constexpr (MORE && !(GX & 4)) ldx(pi, (st + 1) % RING);
      SB0();
    });
  });
  STAMP(PH == 1 ? 10 : (PH == 2 ? 15 : 19));
#pragma unroll
  for (int n = 0; n < NACC; ++n) sty(n, PH == 1 ? yoff + Y1_S : yoff);
  if constexpr ((TRAIL >> PH) & 1) { publish(ws, pflag, tag, w); PRIO(PRIO_TR); }
  if constexpr (PH != 2) static_for<32 - PL.wfirst[NST]>([&](auto jc) {
    constexpr int j = decltype(jc)::v, i = PL.wfirst[NST] + j;
    if constexpr (PH == 1 && j >= TR_PACE) wait_vm<TR_PACE - 1>();
    ldfrag_rot<i, PH == 1 ? 16384 : 8192>(W[i], wsrc, PH == 1 ? rot : 0, lane16);
  });
  if constexpr (!((TRAIL >> PH) & 1)) publish(ws, pflag, tag, w);
}

template <bool FAST> DEVINL float jval(float f, float r) {
  float t = FAST ? __builtin_fmaf(f, -r, 0.5f) : 0.5f - r;
  asm("" : "+v"(t));
  return t;
}
template <bool FAST, int E> DEVINL float jden(float f, float g) {
  float t = __builtin_bit_cast(float, __builtin_amdgcn_mov_dpp(__builtin_bit_cast(int, g), 0x150 + E, 0xf, 0xf, true)) + f;
  if (!FAST) t = 1.0f + __builtin_amdgcn_exp2f(t);
  asm("" : "+v"(t));
  return t;
}
DEVINL float nanmax3(float m, float a, float b) { return __builtin_fmaxf(__builtin_fmaxf(m, __builtin_fabsf(a)), __builtin_fabsf(b)); }
#define OUT_POL "sc1"
#define OUT_AUX 16
#define JX 0
constexpr int J_Y = 32768, J_F = 65536, J_G = J_F + 8192;
constexpr int J_AH = 2;
template <bool FAST> DEVINL void j_make(const f32x4 (&fe)[4], float g, char* lds, int u, int w, int lane) {
  f32x4 t[4];
  static_for<16>([&](auto ec) { constexpr int e = decltype(ec)::v; t[e >> 2][e & 3] = jval<FAST>(fe[e >> 2][e & 3], __builtin_amdgcn_rcpf(jden<FAST, e>(fe[e >> 2][e & 3], g))); });
#pragma unroll
  for (int h = 0; h < 2; ++h) *(u32x4*)(lds + u * 8192 + (2 * w + h) * 1024 + lane * 16) = pack8(t[2 * h], t[2 * h + 1]);
}
#define J_PRE 4
#define JA_SLEEP 0
#define JA_SLEEP2 0
#define JA_POLL 0
#define JA_NP 32
#define U_ROT ((q >> 3) & 1)
#define J_WC 12
#define J_SC 12
#define J_ST0 5
#define J_STD 8
#define J_BAR 28
#define J_LC 2
struct JPlan {
  enum { D, R, V, C, W };
  static constexpr int ST0 = J_ST0, ST1 = J_ST0 + J_STD;
  static constexpr int NV = 96;
  int op[6 * NV], first[130], nop, lev[3] = {};
  static constexpr int tile(int q) { return q < 3 ? 0 : (q > 28 ? 1 : (q - 3) & 1); }
  static constexpr int kstep(int q) { return q < 3 ? q : (q > 28 ? q - 16 : ((q - 3) & 1 ? (q - 3) >> 1 : 3 + ((q - 3) >> 1))); }
  static constexpr int bread(int n) {
    const int p = n >> 5, q = n & 31;
    if (q >= 28) return p < 3 ? 16 * (p + 1) + q - 28 : -1;
    return (q & 1) && q <= 23 ? 16 * p + (q + 7) / 2 : -1;
  }
  static constexpr int fixed(int n) {
    const int p = n >> 5, q = n & 31;
    int c = bread(n) >= 0 ? J_LC : 0;
    if (q == 29 || q == 30) c += 14;
    if (q == 31) c += 17;
    if (p && (q == 0 || q == 4)) c += 8;
    if (p && (q == 1 || q == 2)) c += 14;
    if (p && q == 3) c += 17;
    if (p && (q == ST0 || q == ST1)) c += 2 * J_SC;
    return c;
  }
  constexpr JPlan() : op{}, first{}, nop(0) {
    int cost[6 * NV] = {};
    auto add = [&](int t, int i, int c) { op[nop] = t * 128 + i; cost[nop++] = c; };
    for (int i = 0; i < NV + 4; ++i) {
      if (i < NV) add(D, i, 4);
      if (i >= 1 && i - 1 < NV) add(R, i - 1, 8);
      if (i >= 2 && i - 2 < NV) add(V, i - 2, 4);
      if (i >= 4 && !(i & 1) && i - 3 < NV) add(C, (i - 4) >> 1, 5);
      if (i >= 10 && ((i - 10) & 7) == 0) add(W, (i - 10) >> 3, J_WC);
    }
    int j = 0, n = 0;
    while (op[j] != D * 128 + J_PRE) ++j;
    for (int p = 0; p < 3; ++p) {
      const int n0 = n, j0 = j, n1 = 32 * p + J_BAR - 1;
      int j1 = j0;
      while (op[j1] != W * 128 + 4 * p + 3) ++j1;
      for (int bud = 8; j <= j1; ++bud) {
        int carry = 0;
        j = j0;
        for (n = n0; n < n1; ++n) {
          first[n] = j;
          int left = bud - fixed(n) + carry;
          while (j <= j1 && cost[j] <= left + 2) left -= cost[j++];
          carry = left > 4 ? 4 : (left < -4 ? -4 : left);
        }
        lev[p] = bud;
      }
    }
    for (; n <= 128; ++n) first[n] = j;
  }
};
typedef float f32x16 __attribute__((ext_vector_type(16)));
DEVINL void j_loop(u32x4 (&woa)[32], const f32x4 (&fe)[4], const f32x4 (&g)[2], char* lds, int jw, int jstep, char* out, int row0, int ur, int w, int lane, stv_t& stv) {
  static constexpr JPlan PL;
  static_assert(PL.first[128] == PL.nop, "");
  __amdgpu_buffer_rsrc_t ors = mkrsrc(out + (size_t)row0 * 4096, 65536);
  unsigned ooff = (lane & 15) * 4096 + ur * 2048 + w * 128 + (lane >> 4) * 16;
  const char* jr = lds + ((lane >> 4) & 1) * 8192 + (lane >> 5) * 256 + (lane & 15) * 16;
  auto ldb = [&](int j) { return *(const u32x4*)(jr + (j >> 4) * 16384 + (j & 15) * 512); };
  wait_lds();
  bar();
  STAMP(28);
  u32x4 B[6];
#pragma unroll
  for (int j = 0; j < 4; ++j) B[j] = ldb(j);
  f32x16 acc[2];
  u32x4 X[2], Y[2], jo;
  float d[8];
  auto cvt = [&](int m, int i) {
    if (i & 1) Y[m][i >> 1] = pack_bf16(acc[m][8 + (i & 6)], acc[m][9 + (i & 6)]);
    else X[m][i >> 1] = pack_bf16(acc[m][i], acc[m][i + 1]);
  };
  auto swp = [&](int m, int i) { auto r = __builtin_amdgcn_permlane16_swap(X[m][i], Y[m][i], false, false); X[m][i] = r[0]; Y[m][i] = r[1]; };
  auto emit = [&](auto jc) {
    constexpr int o = PL.op[decltype(jc)::v], t = o >> 7, e = o & 127;
    if constexpr (t == JPlan::D) d[e & 7] = jden<true, e & 15>(fe[(e >> 2) & 3][e & 3], g[(e + 32) >> 6][((e + 32) >> 4) & 3]);
    if constexpr (t == JPlan::R && !(JX & 32)) d[e & 7] = __builtin_amdgcn_rcpf(d[e & 7]);
    if constexpr (t == JPlan::V && !(JX & 64)) d[e & 7] = jval<true>(fe[(e >> 2) & 3][e & 3], d[e & 7]);
    if constexpr (t == JPlan::C) jo[e & 3] = pack_bf16(d[(2 * e) & 7], d[(2 * e + 1) & 7]);
    if constexpr (t == JPlan::W && (JX & 16)) tie(jo);
    if constexpr (t == JPlan::W && !(JX & 16)) {
      *(u32x4*)(lds + jw + (e & 1) * 1024) = jo;
      if constexpr (e & 1) { jw += jstep; asm("" : "+v"(jw)); }
    }
    SB0();
  };
  auto st = [&](auto oc, const u32x4& v) { __builtin_amdgcn_raw_buffer_store_b128(v, ors, ooff + (decltype(oc)::v & 2047), 0, OUT_AUX); };
  SB0();
  if constexpr (!(JX & 4)) static_for<PL.first[0]>([&](auto jc) { emit(jc); });
  static_for<128>([&](auto nc) {
    constexpr int n = decltype(nc)::v, p = n >> 5, q = n & 31, m = PL.tile(q), k = PL.kstep(q);
    if constexpr (n == 0) { wait_vm<16>(); static_for<16>([&](auto c) { tie(woa[decltype(c)::v]); }); }
    if constexpr (n == 13) { wait_vm<0>(); static_for<16>([&](auto c) { tie(woa[16 + decltype(c)::v]); }); }
    constexpr f32x16 Z = {};
    acc[m] = __builtin_amdgcn_mfma_f32_32x32x16_bf16(__builtin_bit_cast(bf16x8, woa[2 * k + m]), __builtin_bit_cast(bf16x8, B[(16 * p + k) % 6]), k ? acc[m] : Z, 0, 0, 0);
    SB0();
    if constexpr (q == J_BAR && p < 3 && !(JX & 2)) { wait_lds(); bar(); }
    if constexpr (PL.bread(n) >= 0) B[PL.bread(n) % 6] = ldb(PL.bread(n));
    if constexpr (q >= 29 || (p && q >= 1 && q <= 3)) {
      constexpr int t = q < 4, s = q < 4 ? q - 1 : q - 29;
      cvt(t, 3 * s); cvt(t, 3 * s + 1);
      if constexpr (s < 2) cvt(t, 3 * s + 2); else { swp(t, 0); swp(t, 1); }
    }
    if constexpr (p && (q == 0 || q == 4)) { swp(q / 4, 2); swp(q / 4, 3); }
    if constexpr (!(JX & 1)) {
      if constexpr (p && q == PL.ST0) { st(IC<(2 * p - 2) * 512>{}, X[0]); st(IC<(2 * p - 2) * 512 + 64>{}, X[1]); }
      if constexpr (p && q == PL.ST1) { st(IC<(2 * p - 1) * 512>{}, Y[0]); st(IC<(2 * p - 1) * 512 + 64>{}, Y[1]); }
      if constexpr (n == 95) { ooff ^= 2048; asm("" : "+v"(ooff)); }
    } else if constexpr (p && (q == 1 || q == 6)) { tie(X[q / 6]); tie(Y[q / 6]); }
    if constexpr (!(JX & 4)) static_for<PL.first[n + 1] - PL.first[n]>([&](auto jc) { emit(IC<PL.first[n] + decltype(jc)::v>{}); });
    SB0();
  });
  swp(0, 2); swp(0, 3);
#pragma unroll
  for (int i = 0; i < 8; ++i) cvt(1, i);
#pragma unroll
  for (int i = 0; i < 4; ++i) swp(1, i);
  st(IC<6 * 512>{}, X[0]);
  st(IC<6 * 512 + 64>{}, X[1]);
  st(IC<7 * 512>{}, Y[0]);
  st(IC<7 * 512 + 64>{}, Y[1]);
}

#define HINT 0
#define TAGLATE 0
#define XLOOP 1
#define ETOUCH 4096
#define RFIRST 1
#define PTOUCH 1
#define FTOUCH 1
#define EARLY_WF 16
#define WF_PACE 4
#define WF_PACE2 8
#define WO_PACE 3
#ifndef M_AH
#define M_AH 3
#endif
#define SG_AT 3
#ifndef M_PEEK
#define M_PEEK 4
#endif
#define LATE_WF 1
#define G_ALL 1
#define T_ALL 1
#define W1_SLEEP 16
#define ROT 1
#define W2_PACE 2
constexpr int M_HS = 96;

template <int MK = 0> DEVINL void x_issue(const u16* __restrict__ x, char* reg, int s, int tp, int par, int ch, bool live, int lane) {
  __amdgpu_buffer_rsrc_t rs = mkrsrc(x + (size_t)s * T0 * 256, T0 * 512);
  const int t0 = 96 * tp - 111 + par + 2 * (lane >> 3);
  const int base = t0 * 512 + ch * 128 + (((lane & 7) ^ (4 * ((lane >> 4) & 1))) * 16);
  unsigned o0 = (tp == 0 || t0 < 0) ? OOB : (unsigned)base, o1 = tp == 0 ? OOB : (unsigned)(base + 8192), so;
  if (live) asm volatile("s_mov_b32 m0, %4\n\ts_mov_b32 %0, 6\n\ts_nop 2\n\tbuffer_load_dwordx4 %2, %3, 0 offen " IN_POL " lds\n"
               "1:\n\ts_add_u32 m0, m0, 1024\n\ts_sub_u32 %0, %0, 1\n\tbuffer_load_dwordx4 %1, %3, 0 offen " IN_POL " lds\n\tv_add_u32 %1, 0x2000, %1\n\ts_cbranch_scc0 1b ;@L7\n;@%5"
               : "=&s"(so), "+v"(o1) : "v"(o0), "s"(rs), "s"((unsigned)(size_t)(lds_void_p)reg), "n"(MK) : "scc", "memory");
}
#define G_GATE 0
#define T_GATE 0
DEVINL void gate(unsigned t0, unsigned n) {
  asm volatile("1:\n\ts_sleep 2\n\ts_memrealtime vcc\n\ts_waitcnt lgkmcnt(0)\n\ts_sub_u32 vcc_lo, vcc_lo, %0\n\ts_cmp_lt_u32 vcc_lo, %1\n\ts_cbranch_scc1 1b"
               :: "s"(t0), "s"(n) : "vcc", "scc", "memory");
}
DEVINL u64 load_tag(const char* ws) {
  u64 tag;
  asm volatile("s_load_dwordx2 %0, %1, 0x0\n\ts_waitcnt lgkmcnt(0)" : "=s"(tag) : "s"(ws + OFF_FLAG + F_EPOCH * FS) : "memory");
  return tag * 6364136223846793005ull + 1442695040888963407ull;
}

extern "C" __global__ __launch_bounds__(256, 2) void k_all(const u16* __restrict__ x, const u16* __restrict__ w1, const u16* __restrict__ w2, const u16* __restrict__ g_,
                                                const u16* __restrict__ wf, const u16* __restrict__ wg, const u16* __restrict__ wo,
                                                char* __restrict__ ws_, char* __restrict__ out) {
  constexpr unsigned pb = 0;
  char* const ws = ws_ + (-(size_t)ws_ & ((2 << 20) - 1));
  __shared__ __attribute__((aligned(16))) char lds[LDS_BYTES];
  int lane = threadIdx.x & 63;
  const int w = __builtin_amdgcn_readfirstlane(threadIdx.x >> 6);
  const int role = blockIdx.x >> 8, xl = blockIdx.x & 7, idx = ((blockIdx.x >> 3) & 31) ^ IDXX;
  const int q = xl * 32 + idx;
  stv_t stv = {};
  STAMP(0);
  u64 tag;

  const int eu = w == EU_A ? idx : ((idx & 3) == EU_I && w == EU_B ? 32 + (idx >> 2) : -1);
  const int tpv[2] = {xl * 21 + (idx >> 1), xl * 21 + 16 + (eu >> 3)}, parv[2] = {idx & 1, (eu >> 2) & 1}, chv[2] = {w, eu & 3};
  const int wsd = WSD;
  u32x4 F[8];
  static_for<4>([&](auto ic) { constexpr int i = decltype(ic)::v; ldfrag<i, false>(F[i], (const char*)TAB + (role ? TAB_F + 4 * wsd : TAB_T) * 1024, lane * 16); });
  asm volatile("s_cmp_eq_u32 %6, 0\n\ts_cbranch_scc1 1f\n\t;@f\n\tglobal_load_dwordx4 %0, %4, %5\n\tglobal_load_dwordx4 %1, %4, %5 offset:1024\n\t"
               "global_load_dwordx4 %2, %4, %5 offset:2048\n\tglobal_load_dwordx4 %3, %4, %5 offset:3072\n1:"
               : "=&v"(F[4]), "=&v"(F[5]), "=&v"(F[6]), "=&v"(F[7]) : "v"(lane * 16), "s"((const char*)TAB + (TAB_F + 4 - 4 * wsd) * 1024), "s"(role) : "scc", "memory");
  asm volatile("" :: "s"(x), "s"(w1), "s"(w2), "s"(g_), "s"(wf), "s"(wg), "s"(wo), "s"(ws_));
  int role2 = role;
  if (role2 == 0) {
    asm volatile(";@g");
    PRIO(PRIO_G0);
    w_issue(w1, lds, (q >> 5) * 8 + ((q >> 3) & 3), q & 3, w, lane);
    w_issue(w1, lds + 16384, (q >> 5) * 8 + 4 + ((q >> 3) & 3), q & 3, w, lane);
    w_issue(w2, lds + 65536, q >> 2, q & 3, w, lane);
  }
  asm volatile("" : "+s"(role2));
  if (role2 != 0) {
    asm volatile(";@t");
    PRIO(PRIO_T0);
    x_issue(x, lds + w * 8192, 0, tpv[0], parv[0], chv[0], true, lane);
    x_issue<1>(x, lds + 65536 + (eu >> 5) * 8192, 0, tpv[1], parv[1], chv[1], eu >= 0, lane);
  }
  if (RFIRST) x_issue<2>(x, lds + 32768 + w * 8192, 1, tpv[role == 0], parv[role == 0], chv[role == 0], role != 0 || eu >= 0, lane);
  unsigned t_start;
  asm volatile("s_memrealtime vcc\n\ts_waitcnt lgkmcnt(0)\n\ts_mov_b32 %0, vcc_lo" : "=s"(t_start) :: "vcc", "memory");
  unsigned per;
  {
    u64 pc;
    unsigned a, b;
    asm volatile("s_getpc_b64 %0\n.Lpe:\n\ts_mov_b32 %1, .Lg_beg-.Lpe\n\ts_mov_b32 %2, .Lt_beg-.Lpe" : "=s"(pc), "=s"(a), "=s"(b));
    const unsigned o = (idx * 4 + w) * 64;
    asm volatile("s_load_dword %0, %1, %2" : "=s"(per) : "s"(pc), "s"(((role ? b : a) + (o < ETOUCH ? o : 0)) & ~3u) : "memory");
  }
  asm volatile("" : "+s"(role2));

  u32x4 W[32];
  if (__builtin_expect(role2 == 0, 1)) {
    asm volatile(".Lg_beg:");
    const unsigned lane16 = lane * 16;
    const int TB = (idx >> 2) & 1, cb = idx & 3;
    const unsigned foff = (lane >= 16 ? F_X + TB * 128 + w * 32 + lane - 16 : F_W1 + cb * 64 + w * 16 + lane) * FS;
    u64 fv = 0;
    {
      const char* probe = ws + OFF_PROBE + (q >> 4) * 16384 + (q & 15) * 512;
      unsigned ct = 0;
      if (RFIRST) {
        u64 pc;
        unsigned len;
        asm volatile("s_getpc_b64 %0\n.Lpc%=:\n\ts_mov_b32 %1, .Lpc%=-k_all" : "=s"(pc), "=s"(len));
        pc -= len;
        asm volatile("s_mov_b32 %0, .Lk_end-k_all\n\ts_max_u32 %0, %0, .Lt_end-k_all" : "=s"(len) :: "scc");
        const unsigned wv = idx * 4 + w, co = wv * 512 + lane * 64, l2 = lane >> 1;
        const char* a = (const char*)pc + (co < len ? co : 0);
        if (l2 >= 4) a = (const char*)(l2 == 4 ? wf : (l2 == 5 ? wg : wo)) + (q * 4 + w) * 128 + (lane & 1) * 64;
        if (l2 == 7) a = (wv < 64 ? (const char*)g_ + wv * 128 : (const char*)TAB + TAB_G * 1024 + (wv - 64) * 128) + (lane & 1) * 64;
        constexpr int NF = FS == 128 ? 4 : (FS >= 16 ? FS / 16 : 1);
        if (lane >= 17) a = ws + OFF_FLAG + ((q * 4 + w) * NF + lane - 17) * (FS == 128 ? 128 : 64);
        if (lane == 16) a = ws + OFF_PROBE + (((q >> 4) * 16384 + 32768) & 0x3ffff) + (q & 15) * 512 + (w & 1) * 8192;
        asm volatile("s_bfm_b64 exec, %2, 0\n\ts_nop 1\n\tglobal_load_dword %0, %1, off sc1\n\ts_mov_b64 exec, -1" : "=v"(ct) : "v"(a), "n"(17 + NF) : "memory");
        if (!TAGLATE) { unsigned d; asm volatile("s_load_dword %0, %1, 0x100" : "=s"(d) : "s"(probe) : "memory"); tag = load_tag(ws); asm volatile("" :: "s"(d)); }
        wait_vm<G_ALL ? 0 : 13>();
        if (eu < 0) { wait_vm<5>(); asm volatile(";@e"); }
      } else wait_vm<0>();
      if (RFIRST) asm volatile(";@Ng2_ge\n;@r"); else asm volatile(";@Ng\n;@r");
      wait_lds();
      asm volatile("" :: "s"(per));
      if (W1_SLEEP) __builtin_amdgcn_s_sleep(W1_SLEEP);
      if (G_GATE) gate(t_start, G_GATE);
      static_for<4>([&](auto ic) { tie(F[decltype(ic)::v]); });
      const bool sd = q & 4;
      const u32x4 Tm[2] = {sd ? F[1] : F[0], sd ? F[3] : F[2]};
      bar();
      STAMP(1);
      w_compute<16>(Tm, ws + OFF_W1F, lds, q >> 3, q & 3, w, lane, pb ^ (sd << 13));
      if (TAGLATE) { unsigned d; asm volatile("s_load_dword %0, %1, 0x100" : "=s"(d) : "s"(probe) : "memory"); tag = load_tag(ws); asm volatile("" :: "s"(d)); }
      int vote = 0;
      if (w == 0) vote = (xl >> 2) ^ near_side(probe, (idx >> 2) & 1);
      publish(ws, F_W1 + (q & 3) * 64 + (q >> 2), tag ^ ((u64)vote << 63), w);
      tie(ct);
      STAMP(2);
      PRIO(PRIO_G0);
      w_compute<16>(Tm, ws + OFF_W1F, lds + 16384, 32 + (q >> 3), q & 3, w, lane, pb ^ (sd << 13));
      if (OPENV & 2) flag_peek<OPENV & 1 ? 48 : 16>(fv, ws, foff);
      publish(ws, F_W1H + (q & 3) * 64 + (q >> 2), tag, w);
      STAMP(31);
    }
    ((int*)(lds + LDS_BYTES - 16))[w] = poll_vote<OPENV & 1 ? 48 : 16>(OPENV & 2 ? fv : ~tag, ws, foff, tag);
    wait_lds();
    bar();
    const u32x4 votes = *(const u32x4*)(lds + LDS_BYTES - 16);
    const int grp = (xl * 4 + (idx >> 3)) ^ (__builtin_amdgcn_readfirstlane(votes[0] + votes[1] + votes[2] + votes[3]) > 32 ? 16 : 0);
    const int gflag = (TB * 4 + cb) * 32 + grp, rot = ROT ? cb + 2 * TB : 0;
    const int gl = grp & 15;
    const unsigned sb = pb ^ ((grp >> 4) << 13);
    const unsigned yoff = (gl >> 2) * 16384 + sb + (gl & 3) * 2048 + (cb * 4 + w) * 128;
    const size_t woff = gl * WF_G + (cb * 4 + w) * 65536 + (sb ^ ((gl & 2) << 12));
    const size_t x1off = gl * X1_G + TB * X1_TB + (sb ^ ((gl & 2) << 12)), x2off = gl * X2_G + TB * X2_TB + (sb ^ ((gl & 4) << 11));
    {
      if (!(OPENV & 1)) {
        STAMP(3);
        PRIO(PRIO_GW);
        static_for<2 * WPRE>([&](auto ic) { constexpr int i = decltype(ic)::v; ldfrag_rot1<i>(W[i], ws + OFF_W1F + woff, rot, lane16); });
        PRIO(PRIO_F);
        poll(ws, F_X + TB * 128 + w * 32 + lane, lanes(32), tag);
        bar();
      }
      STAMP(4);
      PRIO(PRIO_G1);
      g_phase<1>(lds, ws, ws + OFF_X1F + x1off, ws + OFF_W1F + woff, ws + OFF_W2F + woff, W, ws + OFF_Y1F, yoff + TB * 42 * 65536,
                 F_X + 256 + TB * 128 + w * 32, F_W2 + cb * 64 + w * 16, F_G1 + gflag, F_G1 + 256 + gflag, tag, rot, gl & 7, w, stv);
      STAMP(11);
    }
    lane = lane_id();
    if (!EARLY_WF && SG_AT == 6) poll(ws, F_X + 256 + w * 64 + lane, ~0ull, tag);
    poll(ws, F_M + TB * 112 + w * 28 + lane, lanes(28), tag);
    bar();
    STAMP(13);
    PRIO(PRIO_G2A);
    g_phase<2>(lds, ws, ws + OFF_X2F + x2off, nullptr, nullptr, W, ws + OFF_Y2F, yoff + TB * 21 * 65536, 0, 0, 0, F_G2 + gflag, tag, rot, gl & 7, w, stv);
    STAMP(16);
    lane = lane_id();
    poll(ws, lane >= 28 ? F_S + w * 4 + lane - 28 : F_M + 256 + TB * 112 + w * 28 + lane, lanes(32), tag);
    bar();
    STAMP(17);
    PRIO(PRIO_G2B);
    g_phase<3>(lds, ws, ws + OFF_X2F + X2_S + x2off, nullptr, ws + OFF_WFT + w * 32768, W, ws + OFF_Y2F + Y2_S, yoff + TB * 21 * 65536, 0, 0, 0,
               F_G2 + 256 + gflag, tag, rot, gl & 7, w, stv);
    if (TRAIL & 8) wait_vm<0>();
    STAMP(20);
    PRIO(PRIO_JB0);
  } else {
    asm volatile(".Lt_beg:");
    const int G = lane >> 4, rq = (lane & 15) >> 2, p = lane & 3;
    const unsigned lane16 = lane * 16;
    {
      u32x4 Tm[4];
      tag = load_tag(ws);
      asm volatile("" :: "s"(per));
      wait_vm<T_ALL ? 0 : 8 + 8 * RFIRST>();
      if (eu < 0) { wait_vm<8 * RFIRST>(); asm volatile(";@e"); }
      if (RFIRST) asm volatile(";@Nft12_ft2e\n;@r"); else asm volatile(";@Nft1_fte\n;@r");
      tie(F);
      STAMP(1);
      if (XA_SLEEP) __builtin_amdgcn_s_sleep(XA_SLEEP);
      if (T_GATE) gate(t_start, T_GATE);
#pragma unroll 1
      for (int i = 0; i < 8; ++i) {
        const int s = i >> 2, r = s ? (i >> 1) & 1 : i & 1, h = s ? i & 1 : i >> 1;
        if (r == 0 || eu >= 0) {
          if (r && !h) wait_vm<12>();
          const int tl = (r ? tpv[1] : tpv[0]) - 84 * (xl >> 2);
          char* gd = ws + OFF_X1F + s * X1_S + (xl >> 2) * X1_TB + (size_t)(8 * h + (r ? parv[1] * 4 + chv[1] : parv[0] * 4 + chv[0])) * XST;
          const char* reg = lds + (r == s ? w * 8192 : (s ? 32768 + w * 8192 : 65536 + (eu >> 5) * 8192));
          fwd_unit<128, X1_G>(reg + (8 * G + rq) * 128 + (((4 * h + p) ^ (4 * ((rq >> 1) & 1))) * 16), F, gd, x_lane_off<X1_G>(lane, tl, tl, pb ^ (wsd << 13)));
        } else if (i == SG_AT && w == SG_W) {
          u32x4 d;
          s_gather(d, wf, wo, q & 32, q >> 6, q & 31, lane);
          st16(ws + OFF_WFT + (q & 32) * 4096 + (q >> 6) * 32768 + (q & 31) * 1024 + lane16, d);
        }
        if ((0x8a >> i) & 1) {
          publish(ws, (i == 3 ? F_XH : F_X + s * 256) + q, tag, w);
          PRIO(PRIO_T1);
          if (i == 1) STAMP(2);
          if (i == 7) STAMP(4);
        }
        if (i == 3) {
          STAMP(3);
          x_issue(x, lds + w * 8192, 1, tpv[1], parv[1], chv[1], eu >= 0, lane);
          w_issue(w2, lds + 65536, q >> 2, q & 3, w, lane);
          if (XB_SLEEP) __builtin_amdgcn_s_sleep(XB_SLEEP);
        }
      }
      PRIO(PRIO_T2);
      static_for<4>([&](auto ic) { constexpr int i = decltype(ic)::v; ldfrag<TAB_T + i, false>(Tm[i], (const char*)TAB + (i & 1 ? -1024 : 1024) * wsd, lane16); });
      if (SG_AT == 3) poll(ws, F_XH + w * 64 + lane, ~0ull, tag); else wait_vm<0>();
      tie(Tm);
      w_compute<W2_PACE>(Tm, ws + OFF_W2F, lds + 65536, q >> 2, q & 3, w, lane, pb ^ (wsd << 13));
      publish(ws, F_W2 + (q & 3) * 64 + (q >> 2), tag, w);
      STAMP(5);
    }
    u32x4 Gt[6], F2[2];
    static_for<6>([&](auto ic) { constexpr int i = decltype(ic)::v; ldfrag<i & 1, false>(Gt[i], (const char*)TAB + (TAB_G + 2 * ((2 + 5 * w + i / 2) % 3)) * 1024, lane16); });
    static_for<2>([&](auto ic) { constexpr int i = decltype(ic)::v; ldfrag<i, false>(F2[i], (const char*)TAB + (TAB_F + 2 * w) * 1024, lane16); });
    if (EARLY_WF && SG_AT == 6) poll(ws, F_X + 256 + w * 64 + lane, NOPEEK ? 3ull : ~0ull, tag); else wait_vm<0>();
    tie(Gt); tie(F2);
    STAMP(15);
    PRIO(PRIO_T2);
    static_for<EARLY_WF>([&](auto ic) {
      constexpr int i = decltype(ic)::v;
      if constexpr (i >= WF_PACE) wait_vm<WF_PACE - 1>();
      ldfrag<i, true>(W[i], ws + OFF_WFT + w * 32768, lane16);
    });
    wait_vm<0>();
    static_for<EARLY_WF>([&](auto ic) { tie(W[decltype(ic)::v]); });
    PRIO(PRIO_F);
    STAMP(14);
    if (idx < 28) {
      char* reg = lds + w * 8192;
      constexpr int HIM = 32768;
      const int pr = 3 * idx + w, TB2 = idx >= 14;
      unsigned yo[2], la[5][2], xo[3];
#pragma unroll
      for (int i = 0; i < 2; ++i) {
        yo[i] = pr * 65536 + G * 2048 + xl * 256 + (((lane & 15) ^ ((G & 1) << 2) ^ i) * 16);
        const unsigned a0 = (4 * G + (rq >> 1)) * 256 + (((p >> 1) * 8 + 2 * (p & 1) + i) ^ ((rq >> 1) << 2) ^ (G & 1)) * 16 + (rq & 1) * 8;
#pragma unroll
        for (int j = 0; j < 5; ++j) { const int t = (2 + 5 * w + j) / 3; la[j][i] = a0 ^ ((t >> 1) * 8192 + (t & 1) * 64); tie(la[j][i]); }
        tie(yo[i]);
      }
#pragma unroll
      for (int i = 0; i < 3; ++i) { const int tl = 3 * idx - 42 * TB2 + i; xo[i] = x_lane_off<X2_G>(lane, tl, tl + 42 * (xl & 1), pb ^ ((w >> 1) << 13)); tie(xo[i]); }
      unsigned hw = HIM + (32 + 80 * w + (lane & 15)) * M_HS + G * 16, hr = HIM + (33 + 2 * (8 * G + rq)) * M_HS + p * 16;
      unsigned yfl = F_G1 + ((idx >= 14 || lane >= 8 ? 4 : 0) + (xl >> 1)) * 32 + w * 8 + (lane & 7);
      tie(hw); tie(hr); tie(yfl);
      const unsigned keep = pr == 84 ? 0u : ~0u;
      if (lane < 4) *(u32x4*)(lds + HIM + 352 * M_HS + lane16) = (u32x4){0, 0, 0, 0};
      u64 fv = ~tag;
#pragma unroll 1
      for (int s = 0; s < 2; ++s) {
        __amdgpu_buffer_rsrc_t rs = mkrsrc(ws + OFF_Y1F + s * Y1_S, 84 * 65536);
        unsigned ys = ((pr & 32) << 8) ^ pb;
        char* gd = ws + OFF_X2F + s * X2_S + TB2 * X2_TB + (size_t)(xl >> 1) * XST + (size_t)(w & 1) * 8 * X2_G;
        char* gd1 = gd + 4 * XST;
        asm volatile("" : "+s"(rs), "+s"(ys), "+s"(gd), "+s"(gd1));
        poll_from(fv, ws, yfl + s * 256, idx == 13 ? lanes(16) : lanes(8), tag);
        bar();
#pragma unroll
        for (int i = 0; i < 8; ++i) dma16<SC1>(rs, reg + i * 1024, yo[i & 1], (ys ^ ((i >> 2) << 13)) + (i & 3) * 16384);
        if (s == 0) { PRIO(PRIO_MA); } else { PRIO(PRIO_MB); }
        STAMP2(6, 10);
        wait_vm<0>();
        bar();
        STAMP2(7, 11);
        bf16x8 A[2][2][2];
        f32x4 c[2][2];
        auto lda = [&](int j) {
#pragma unroll
          for (int ks = 0; ks < 2; ++ks)
#pragma unroll
            for (int i = 0; i < 2; ++i) A[j & 1][i][ks] = tr_frag(lds + la[j][i] + ks * 4096, 512);
        };
        auto idft = [&](int j, int k) {
          constexpr f32x4 Z = {0, 0, 0, 0};
          c[j & 1][k & 1] = MFMAU(A[j & 1][k & 1][k >> 1], Gt[2 * (j % 3) + (k >> 1)], k < 2 ? Z : c[j & 1][k & 1]);
        };
        lda(0); lda(1);
        SB0();
#pragma unroll
        for (int k = 0; k < 4; ++k) idft(0, k);
        SB0();
        static_for<5>([&](auto jc) {
          constexpr int j = decltype(jc)::v;
          if constexpr (j + 2 < 5) lda(j + 2);
          SB0();
          u32x4 o = gelu_pack8(c[j & 1][0], c[j & 1][1], [&](int k) { if (j + 1 < 5 && k < 4) idft(j + 1, k); });
          if constexpr (j == 0) o &= keep;
          *(u32x4*)(lds + hw + j * 16 * M_HS) = o;
          SB0();
        });
        wait_lds();
        bar();
        STAMP2(8, 12);
        bf16x8 B[6][2][2];
        auto ldb = [&](int u) {
#pragma unroll
          for (int ks = 0; ks < 2; ++ks)
#pragma unroll
            for (int i = 0; i < 2; ++i) B[u][i][ks] = tr_frag(lds + hr + ((u >> 1) * 96 + (u & 1) + 64 * ks) * M_HS + 8 * i, 8 * M_HS);
        };
        f32x4 d[2][2];
        auto fdft = [&](int u) {
          constexpr f32x4 Z = {0, 0, 0, 0};
#pragma unroll
          for (int k = 0; k < 4; ++k) d[u & 1][k & 1] = MFMAU(B[u][k & 1][k >> 1], F2[k >> 1], k < 2 ? Z : d[u & 1][k & 1]);
        };
#pragma unroll
        for (int u = 0; u < M_AH; ++u) ldb(u);
        SB0();
        fdft(0);
#pragma unroll
        for (int u = 0; u < 6; ++u) {
          SB0();
          if (u + M_AH < 6) ldb(u + M_AH);
          if (u + 1 < 6) fdft(u + 1);
          if (u == M_PEEK) flag_peek<16>(fv, ws, (yfl + 256) * FS);
          SB0();
          const u32x4 o = pack8(d[u & 1][0], d[u & 1][1]);
          asm volatile("global_store_dwordx4 %0, %1, %2 " ST_POL "\n\ts_nop 1" ::"v"(xo[u >> 1]), "v"(o), "s"(u & 1 ? gd1 : gd) : "memory");
        }
        publish(ws, F_M + s * 256 + idx * 8 + xl, tag, w);
        asm volatile("" : "+v"(fv));
        STAMP2(9, 13);
      }
    }
    if (idx >= 28 && LATE_WF) poll(ws, F_G1 + 256 + (idx - 24) * 32 + w * 8 + lane, lanes(8), tag);
    if (idx >= 30) {
      if (w == 0) s_ge(g_, wg, (float*)(ws + OFF_GE), lds, (idx - 30) * 8 + xl, lane);
      publish(ws, F_S + (idx - 30) * 8 + xl, tag, w);
    }
    poll(ws, F_S + w * 4 + lane, lanes(4), tag);
    if (!EARLY_WF && SG_AT == 6) poll(ws, F_X + 256 + w * 64 + lane, ~0ull, tag);
    PRIO(PRIO_JA);
    sgpr_settle();
    static_for<32 - EARLY_WF>([&](auto ic) {
      constexpr int i = EARLY_WF + decltype(ic)::v;
      if constexpr (i >= EARLY_WF + WF_PACE2) wait_vm<WF_PACE2 - 1>();
      ldfrag<i, true>(W[i], ws + OFF_WFT + w * 32768, lane16);
    });
    wait_vm<0>();
    asm volatile(".Lt_end:");
  }
  asm volatile(";@Nr");
  tie(W);
  const int s = role ^ 1;
  lane = lane_id();
  const int G = lane >> 4, rq = (lane & 15) >> 2, p = lane & 3;
  const unsigned lane16 = lane * 16;

  const int jb = q;
  if (jb >= 250) {
    if (blockIdx.x == 255 && w == 0) {
#pragma unroll 1
      for (int i = 4; i < 8; ++i) poll(ws, F_G2 + i * 64 + lane, ~0ull, tag);
      set_flag(ws, F_EPOCH, tag, w);
    }
    STAMP_FLUSH();
    return;
  }
  {
    const int tile = jb / 3, vt = jb - 3 * tile, ur = U_ROT;
    u32x4 woa[32], Gt[2];
    sgpr_settle();
    static_for<2>([&](auto ic) { constexpr int i = decltype(ic)::v; ldfrag<i, false>(Gt[i], (const char*)TAB + (TAB_G + 2 * vt) * 1024, lane16); });
    __amdgpu_buffer_rsrc_t grs = mkrsrc(ws + OFF_GE, 32768);
#pragma unroll
    for (int i = 0; i < 2; ++i) dma16<SC1>(grs, lds + J_G + w * 2048 + i * 1024, 16384 + (s * 4 + w) * 2048 + (i ^ ur) * 1024 + lane16, 0);
    static_for<16>([&](auto ic) {
      constexpr int i = decltype(ic)::v;
      if constexpr (WO_PACE < 16 && i >= WO_PACE) asm volatile("s_cmp_eq_u32 %0, 0\n\ts_cbranch_scc1 1f\n\ts_waitcnt vmcnt(%1)\n1:" :: "s"(role), "n"(WO_PACE - 1) : "scc", "memory");
      ldfrag<i, true>(woa[i], ws + OFF_WOT + w * 32768, lane16);
    });
    char* reg = lds + J_Y + w * 8192;
    __amdgpu_buffer_rsrc_t rs = mkrsrc(ws + OFF_Y2F + s * Y2_S + (size_t)(tile >> 1) * 65536, 65536);
    unsigned yo[2], ys[2], la[4];
#pragma unroll
    for (int i = 0; i < 2; ++i) {
      const int c = (lane & 15) ^ ((G & 1) << 3) ^ i;
      yo[i] = G * 2048 + (4 * w + (c >> 2)) * 128 + (tile & 1) * 64 + (c & 3) * 16;
      ys[i] = (i << 13) ^ ((tile & 64) << 7) ^ pb;
    }
#pragma unroll
    for (int i = 0; i < 4; ++i) la[i] = J_Y + w * 8192 + (4 * G + (rq >> 1)) * 256 + ((4 * (i & 2) + 2 * p + (i & 1)) ^ ((rq >> 1) << 3) ^ (G & 1)) * 16 + (rq & 1) * 8;
    asm volatile("" : "+s"(rs), "+s"(ys[0]), "+s"(ys[1]), "+v"(yo[0]), "+v"(yo[1]), "+v"(la[0]), "+v"(la[1]), "+v"(la[2]), "+v"(la[3]));
    PRIO(PRIO_F);
    poll(ws, F_G2 + s * 256 + ((tile >= 42 ? 4 : 0) + w) * 32 + lane, lanes(32), tag);
#pragma unroll
    for (int i = 0; i < 8; ++i) dma16<SC1>(rs, reg + i * 1024, yo[i & 1], ys[i >> 2] + (i & 3) * 16384);
    if (role) { PRIO(PRIO_JA); } else { PRIO(PRIO_JB); }
    tie(Gt);
    STAMP(24);
    {
      bf16x8 A[2][4];
      f32x4 c[4];
      wait_vm<4>();
#pragma unroll
      for (int i = 0; i < 4; ++i) A[0][i] = tr_frag(lds + la[i], 512);
      wait_vm<0>();
      STAMP(25);
#pragma unroll
      for (int i = 0; i < 4; ++i) A[1][i] = tr_frag(lds + la[i] + 4096, 512);
      SB0();
#pragma unroll
      for (int i = 0; i < 4; ++i) c[i] = MFMAU(A[0][i], Gt[0], ((f32x4){0, 0, 0, 0}));
#pragma unroll
      for (int i = 0; i < 4; ++i) c[i] = MFMAU(A[1][i], Gt[1], c[i]);
      SB0();
#pragma unroll
      for (int h = 0; h < 2; ++h) *(u32x4*)(lds + J_F + (2 * w + h) * 1024 + lane16) = gelu_pack8(c[2 * h], c[2 * h + 1]);
    }
    wait_lds();
    bar();
    STAMP(26);
    if (JA_SLEEP && role) __builtin_amdgcn_s_sleep(JA_SLEEP);
    if (JA_POLL == 1 && role) poll(ws, F_G2 + 256 + ((tile >= 42 ? 4 : 0) + w) * 32 + lane, lanes(JA_NP), tag);
    f32x4 fe[4], ex[4], gt[2];
    u32x4 bfr[8];
#pragma unroll
    for (int ks = 0; ks < 8; ++ks) bfr[ks] = *(const u32x4*)(lds + J_F + ks * 1024 + lane16);
    SB0();
    auto femm = [&](auto kc) {
      constexpr int k = decltype(kc)::v, m = (k >> 4) * 2 + (k & 1), ks = (k >> 1) & 7;
      constexpr f32x4 Z = {0, 0, 0, 0};
      fe[m] = MFMAU(W[m * 8 + ks], bfr[ks], ks == 0 ? Z : fe[m]);
      SB0();
      if constexpr (k & 1) ldfrag<16 + (k >> 1), true>(woa[16 + (k >> 1)], ws + OFF_WOT + w * 32768, lane16);
      if constexpr (k == 6 || k == 8) gt[(k - 6) / 2] = *(const f32x4*)(lds + J_G + w * 2048 + (k - 6) * 512 + lane16);
      SB0();
    };
    static_for<18>([&](auto kc) { femm(kc); });
    float big = 0;
    static_for<2>([&](auto hc) {
      constexpr int h = decltype(hc)::v;
      float d[2][8];
      u32x4 jo;
      static_for<78>([&](auto jc) {
        constexpr int j = decltype(jc)::v;
        auto at = [&](f32x4 (&a)[4], int e) { return a[2 * h + (e >> 2)][e & 3]; };
        if constexpr (j < 8) { float t = at(fe, j) * LOG2E2; asm("" : "+v"(t)); fe[2 * h + (j >> 2)][j & 3] = t; }
        else if constexpr (j < 16) ex[2 * h + ((j - 8) >> 2)][j & 3] = __builtin_amdgcn_exp2f(-at(fe, j - 8));
        else if constexpr (j < 20) big = nanmax3(big, at(fe, 2 * (j - 16)), at(fe, 2 * (j - 16) + 1));
        else if constexpr (j < 28) d[0][j - 20] = jden<true, 8 * h + j - 20>(at(ex, j - 20), gt[0][0]);
        else if constexpr (j < 36) d[0][j - 28] = __builtin_amdgcn_rcpf(d[0][j - 28]);
        else if constexpr (j < 44) d[1][j - 36] = jden<true, 8 * h + j - 36>(at(ex, j - 36), gt[0][1]);
        else if constexpr (j < 52) d[0][j - 44] = jval<true>(at(ex, j - 44), d[0][j - 44]);
        else if constexpr (j < 60) d[1][j - 52] = __builtin_amdgcn_rcpf(d[1][j - 52]);
        else if constexpr (j < 64) jo[j - 60] = pack_bf16(d[0][2 * (j - 60)], d[0][2 * (j - 60) + 1]);
        else if constexpr (j == 64) *(u32x4*)(lds + (2 * w + h) * 1024 + lane16) = jo;
        else if constexpr (j < 73) d[1][j - 65] = jval<true>(at(ex, j - 65), d[1][j - 65]);
        else if constexpr (j < 77) jo[j - 73] = pack_bf16(d[1][2 * (j - 73)], d[1][2 * (j - 73) + 1]);
        else *(u32x4*)(lds + 8192 + (2 * w + h) * 1024 + lane16) = jo;
        SB0();
        if constexpr (h == 0 && (j & 3) == 3 && j < 56) femm(IC<18 + (j >> 2)>{});
      });
      if constexpr (h == 0) STAMP(27);
    });
    const bool fast = __all(big <= 32.0f);
    if (!fast) {
      wait_lds();
#pragma unroll
      for (int i = 0; i < 2; ++i) dma16<SC1>(grs, lds + J_G + w * 2048 + i * 1024, (s * 4 + w) * 2048 + (i ^ ur) * 1024 + lane16, 0);
      wait_vm<0>();
#pragma unroll 1
      for (int u = 0; u < 8; ++u) j_make<false>(fe, *(const float*)(lds + J_G + w * 2048 + (u >> 2) * 1024 + lane16 + (u & 3) * 4), lds, u, w, lane);
    }
    if (JA_SLEEP2 && role) __builtin_amdgcn_s_sleep(JA_SLEEP2);
    if (JA_POLL == 2 && role) poll(ws, F_G2 + 256 + ((tile >= 42 ? 4 : 0) + w) * 32 + lane, lanes(JA_NP), tag);
    if (role) { PRIO(PRIO_JAL); } else { PRIO(PRIO_JBL); }
    j_loop(woa, ex, gt, lds, (fast ? J_AH * 8192 : J_F) + 2 * w * 1024 + lane16, fast ? 8192 : 0, out, s * T2 + jb * 16, ur, w, lane, stv);
  }
  asm volatile(".Lk_end:");
  STAMP(29);
}

#define WS_SKIP 0
extern "C" void kernel_launch(void* const* d_in, const int* in_sizes, int n_in, void* d_out, int out_size, void* d_ws,
                              size_t ws_size, hipStream_t stream) {
  k_all<<<512, 256, 0, stream>>>((const u16*)d_in[0], (const u16*)d_in[1], (const u16*)d_in[2], (const u16*)d_in[3], (const u16*)d_in[4],
                                 (const u16*)d_in[5], (const u16*)d_in[6], (char*)d_ws + WS_SKIP, (char*)d_out);
}
```

```cpp
#include <hip/hip_runtime.h>
#include <hip/hip_bf16.h>
#include <utility>
__device__ const unsigned TAB[4608] __attribute__((aligned(128))) = {
0x3f803f80,0x3f803f80,0x3f803f80,0x3f803f80,0x0,0x0,0x0,0x0,
0x3f7f3f80,0x3f753f7b,0x3f613f6c,0x3f443f54,0x3dcc0000,0x3e973e4b,0x3ef53ec7,0x3f253f10,
0x3f7b3f80,0x3f543f6c,0x3f0b3f33,0x3e323ebb,0x3e4b0000,0x3f103ec7,0x3f573f37,0x3f7c3f6e,
0x3f753f80,0x3f203f54,0x3d993ebb,0xbf00be64,0x3e970000,0x3f483f10,0x3f7f3f6e,0x3f5e3f7a,
0x3f6c3f80,0x3ebb3f33,0xbed3bccc,0xbf71bf3c,0x3ec70000,0x3f6e3f37,0x3f693f80,0x3eaf3f2e,
0x3f613f80,0x3d993f0b,0xbf4cbed3,0xbf71bf7d,0x3ef50000,0x3f7f3f57,0x3f1b3f69,0xbeaf3e19,
0x3f543f80,0xbe643ebb,0xbf7dbf3c,0xbf00bf67,0x3f100000,0x3f7a3f6e,0x3e193f2e,0xbf5ebede,
0x3f443f80,0xbf003e32,0xbf71bf71,0x3e32bf00,0x3f250000,0x3f5e3f7c,0xbeaf3eaf,0xbf7cbf5e,
0x3f803f80,0x3f803f80,0x3f803f80,0x3f803f80,0x0,0x0,0x0,0x0,
0x3f203f33,0x3eea3f0b,0x3e8b3ebb,0x3d993e32,0x3f483f37,0x3f643f57,0x3f763f6e,0x3f7f3f7c,
0xbe64bccc,0xbf15bed3,0xbf5abf3c,0xbf7dbf71,0x3f7a3f80,0x3f503f69,0x3f053f2e,0x3e193eaf,
0xbf67bf3c,0xbf7dbf7d,0xbf3cbf67,0xbe64bf00,0x3ede3f2e,0xbe193e19,0xbf2ebede,0xbf7abf5e,
0xbf67bf80,0xbea3bf29,0x3eea3d99,0x3f753f44,0xbedebd4c,0xbf73bf40,0xbf64bf7f,0xbe97bf25,
0xbe64bf29,0x3f333e8b,0x3f7b3f75,0x3ebb3f44,0xbf7abf40,0xbf37bf76,0x3e4bbe97,0x3f6e3f25,
0x3f203d99,0x3f753f75,0x3d993f20,0xbf67bf00,0xbf48bf7f,0x3e97be97,0x3f7f3f48,0x3ede3f5e,
0x3f803f44,0x3e323f44,0xbf71bf00,0xbf00bf71,0xbf25,0x3f7c3f25,0x3eaf3f5e,0xbf5ebeaf,
0x3f803f80,0x3f803f80,0x3f803f80,0x3f803f80,0x0,0x0,0x0,0x0,
0xbdffbccc,0xbea3be64,0xbf00bed3,0xbf29bf15,0x3f7e3f80,0x3f733f7a,0x3f5e3f69,0x3f403f50,
0xbf78bf80,0xbf4cbf67,0xbf00bf29,0xbdffbea3,0xbe7dbd4c,0xbf1bbede,0xbf5ebf40,0xbf7ebf73,
0x3ebb3d99,0x3f543f20,0x3f803f75,0x3f543f75,0xbf6ebf7f,0xbf10bf48,0xbe97,0x3f103e97,
0x3f613f7f,0x3e8b3f20,0xbf00bdff,0xbf78bf4c,0x3ef53dcc,0x3f763f48,0x3f5e3f7e,0x3e7d3f1b,
0xbf15bdff,0xbf80bf67,0xbf00bf5a,0x3eeabccc,0x3f503f7e,0xbd4c3ede,0xbf5ebf05,0xbf64bf80,
0xbf3cbf7d,0x3ebbbe64,0x3f803f54,0x3ebb3f54,0xbf2ebe19,0xbf6ebf7a,0xbf10,0x3f6e3f10,
0x3f443e32,0x3f443f80,0xbf003e32,0xbf71bf71,0xbf25bf7c,0x3f250000,0x3f5e3f7c,0xbeaf3eaf,
0x3f803f80,0x3f803f80,0x3f803f80,0x3f803f80,0x0,0x0,0x0,0x0,
0xbf4cbf3c,0xbf67bf5a,0xbf78bf71,0xbf80bf7d,0x3f1b3f2e,0x3ede3f05,0x3e7d3eaf,0x3d4c3e19,
0x3e8b3d99,0x3f203eea,0x3f613f44,0x3f7f3f75,0xbf76bf7f,0xbf48bf64,0xbef5bf25,0xbdccbe97,
0x3ebb3f20,0xbe643d99,0xbf3cbf00,0xbf7dbf67,0x3f6e3f48,0x3f7a3f7f,0x3f2e3f5e,0x3e193ede,
0xbf5abf7d,0xbe64bf15,0x3f0b3e32,0x3f7b3f54,0xbf05be19,0xbf7abf50,0xbf57bf7c,0xbe4bbf10,
0x3f7f3f54,0x3f203f6c,0xbea33e32,0xbf78bf3c,0xbdccbf10,0x3f483ec7,0x3f733f7c,0x3e7d3f2e,
0xbf3cbe64,0xbf67bf7d,0x3d99bf00,0x3f753f20,0x3f2e3f7a,0xbede3e19,0xbf7fbf5e,0xbe97bf48,
0x3e32bf00,0x3f803f44,0x3e323f44,0xbf71bf00,0xbf7cbf5e,0xbf25,0x3f7c3f25,0x3eaf3f5e,
0x3f803f80,0x3f803f80,0x3f803f80,0x3f803f80,0x0,0x0,0x0,0x0,
0xbf7dbf80,0xbf71bf78,0xbf5abf67,0xbf3cbf4c,0xbe19bd4c,0xbeafbe7d,0xbf05bede,0xbf2ebf1b,
0x3f753f7f,0x3f443f61,0x3eea3f20,0x3d993e8b,0x3e973dcc,0x3f253ef5,0x3f643f48,0x3f7f3f76,
0xbf67bf7d,0xbf00bf3c,0x3d99be64,0x3f203ebb,0xbedebe19,0xbf5ebf2e,0xbf7fbf7a,0xbf48bf6e,
0x3f543f7b,0x3e323f0b,0xbf15be64,0xbf7dbf5a,0x3f103e4b,0x3f7c3f57,0x3f503f7a,0x3e193f05,
0xbf3cbf78,0x3e32bea3,0x3f6c3f20,0x3f543f7f,0xbf2ebe7d,0xbf7cbf73,0xbec7bf48,0x3f103dcc,
0x3f203f75,0xbf003d99,0xbf7dbf67,0xbe64bf3c,0x3f483e97,0x3f5e3f7f,0xbe193ede,0xbf7abf2e,
0xbf00bf71,0x3f443e32,0x3f443f80,0xbf003e32,0xbf5ebeaf,0xbf25bf7c,0x3f250000,0x3f5e3f7c,
0x3f803f80,0x3f803f80,0x3f803f80,0x3f803f80,0x0,0x0,0x0,0x0,
0xbf15bf29,0xbed3bf00,0xbe64bea3,0xbcccbdff,0xbf50bf40,0xbf69bf5e,0xbf7abf73,0xbf80bf7e,
0xbea3bdff,0xbf29bf00,0xbf67bf4c,0xbf80bf78,0x3f733f7e,0x3f403f5e,0x3ede3f1b,0x3d4c3e7d,
0x3f753f54,0x3f753f80,0x3f203f54,0x3d993ebb,0xbe97bf10,0x3e970000,0x3f483f10,0x3f7f3f6e,
0xbf4cbf78,0xbdffbf00,0x3f203e8b,0x3f7f3f61,0xbf1bbe7d,0xbf7ebf5e,0xbf48bf76,0xbdccbef5,
0xbccc3eea,0xbf5abf00,0xbf67bf80,0xbdffbf15,0x3f803f64,0x3f053f5e,0xbede3d4c,0xbf7ebf50,
0x3f543ebb,0x3f543f80,0xbe643ebb,0xbf7dbf3c,0xbf10bf6e,0x3f100000,0x3f7a3f6e,0x3e193f2e,
0xbf71bf71,0x3e32bf00,0x3f803f44,0x3e323f44,0xbeaf3eaf,0xbf7cbf5e,0xbf25,0x3f7c3f25,
0x3f803f80,0x3f803f80,0x3f803f80,0x3f803f80,0x0,0x0,0x0,0x0,
0x3e323d99,0x3ebb3e8b,0x3f0b3eea,0x3f333f20,0xbf7cbf7f,0xbf6ebf76,0xbf57bf64,0xbf37bf48,
0xbf71bf7d,0xbf3cbf5a,0xbed3bf15,0xbcccbe64,0xbeafbe19,0xbf2ebf05,0xbf69bf50,0xbf80bf7a,
0xbf00be64,0xbf67bf3c,0xbf7dbf7d,0xbf3cbf67,0x3f5e3f7a,0x3ede3f2e,0xbe193e19,0xbf2ebede,
0x3f443f75,0x3d993eea,0xbf29bea3,0xbf80bf67,0x3f253e97,0x3f7f3f64,0x3f403f73,0x3d4c3ede,
0x3f443ebb,0x3f753f7b,0x3e8b3f33,0xbf29be64,0xbf25bf6e,0x3e97be4b,0x3f763f37,0x3f403f7a,
0xbf00bf67,0x3f203d99,0x3f753f75,0x3d993f20,0xbf5ebede,0xbf48bf7f,0x3e97be97,0x3f7f3f48,
0xbf71bf00,0xbf00bf71,0x3f443e32,0x3f443f80,0x3eaf3f5e,0xbf5ebeaf,0xbf25bf7c,0x3f250000,
0x3f803f80,0x3f803f80,0x3f803f80,0x3f80,0x0,0x0,0x0,0x0,
0x3f543f44,0x3f6c3f61,0x3f7b3f75,0x3f7f,0xbf10bf25,0xbec7bef5,0xbe4bbe97,0xbdcc,
0x3ebb3e32,0x3f333f0b,0x3f6c3f54,0x3f7b,0xbf6ebf7c,0xbf37bf57,0xbec7bf10,0xbe4b,
0xbe64bf00,0x3ebb3d99,0x3f543f20,0x3f75,0xbf7abf5e,0xbf6ebf7f,0xbf10bf48,0xbe97,
0xbf3cbf71,0xbcccbed3,0x3f333ebb,0x3f6c,0xbf2ebeaf,0xbf80bf69,0xbf37bf6e,0xbec7,
0xbf7dbf71,0xbed3bf4c,0x3f0b3d99,0x3f61,0xbe193eaf,0xbf69bf1b,0xbf57bf7f,0xbef5,
0xbf67bf00,0xbf3cbf7d,0x3ebbbe64,0x3f54,0x3ede3f5e,0xbf2ebe19,0xbf6ebf7a,0xbf10,
0xbf003e32,0xbf71bf71,0x3e32bf00,0x3f44,0x3f5e3f7c,0xbeaf3eaf,0xbf7cbf5e,0xbf25,
0x3f333f80,0xbf3cbccc,0xbf29bf80,0x3f443d99,0x3f370000,0x3f2e3f80,0xbf40bd4c,0xbf25bf7f,
0x3f203f80,0xbf67be64,0xbe64bf67,0x3f803f20,0x3f480000,0x3ede3f7a,0xbf7abede,0xbf48,
0x3f0b3f80,0xbf7dbed3,0x3e8bbf29,0x3f443f75,0x3f570000,0x3e193f69,0xbf76bf40,0x3f25be97,
0x3eea3f80,0xbf7dbf15,0x3f33bea3,0x3e323f75,0x3f640000,0xbe193f50,0xbf37bf73,0x3f7c3e97,
0x3ebb3f80,0xbf67bf3c,0x3f753d99,0xbf003f20,0x3f6e0000,0xbede3f2e,0xbe97bf7f,0x3f5e3f48,
0x3e8b3f80,0xbf3cbf5a,0x3f7b3eea,0xbf713d99,0x3f760000,0xbf2e3f05,0x3e4bbf64,0x3eaf3f7f,
0x3e323f80,0xbf00bf71,0x3f443f44,0xbf71bf00,0x3f7c0000,0xbf5e3eaf,0x3f25bf25,0xbeaf3f5e,
0x3d993f80,0xbe64bf7d,0x3ebb3f75,0xbf00bf67,0x3f7f0000,0xbf7a3e19,0x3f6ebe97,0xbf5e3ede,
0x3f203f7f,0xbf4cbdff,0xbf15bf7d,0x3f543e32,0x3f483dcc,0x3f1b3f7e,0xbf50be19,0xbf10bf7c,
0xbe643f20,0xbf67bf67,0x3f20be64,0x3f203f80,0x3f7a3f48,0xbede3ede,0xbf48bf7a,0x3f480000,
0xbf67bdff,0xbcccbf5a,0x3f6c3f54,0xbf3c3e32,0x3ede3f7e,0xbf80bf05,0x3ec7bf10,0x3f2e3f7c,
0xbf67bf4c,0x3f61bccc,0xbdff3f54,0xbf3cbf71,0xbede3f1b,0xbef5bf80,0x3f7e3f10,0xbf2e3eaf,
0xbe64bf7d,0x3f543f54,0xbf7dbe64,0x3f20bf00,0xbf7abe19,0x3f10bf10,0x3e193f7a,0xbf48bf5e,
0x3f20bf15,0xbdff3f6c,0xbed3bf7d,0x3f543f44,0xbf48bf50,0x3f7e3ec7,0xbf693e19,0x3f10bf25,
0x3f803e32,0xbf713e32,0x3f44bf00,0xbf003f44,0xbf7c,0x3eaf3f7c,0xbf25bf5e,0x3f5e3f25,
0x3f203f54,0xbf3cbf3c,0x3f543f20,0xbf67bf00,0x3f48bf10,0xbf2e3f2e,0x3f10bf48,0xbede3f5e,
0x3f0b3f7b,0xbf5abe64,0xbf00bf78,0x3f613e8b,0x3f573e4b,0x3f053f7a,0xbf5ebe7d,0xbef5bf76,
0xbf67be64,0xbe64bf67,0x3f803f20,0xbe643f20,0x3ede3f7a,0xbf7abede,0xbf48,0x3f7a3f48,
0xbea3bf78,0x3f7f3f20,0xbf003eea,0xbf15bf80,0xbf73be7d,0x3dccbf48,0x3f5e3f64,0xbf503d4c,
0x3f7b3e8b,0xbed33f20,0xbf00bf80,0x3f7f3f0b,0xbe4bbf76,0x3f693f48,0xbf5e3d4c,0x3dccbf57,
0x3d993f75,0xbf3cbf67,0x3f803ebb,0xbf3c3ebb,0x3f7f3e97,0xbf2e3ede,0xbf6e,0x3f2e3f6e,
0xbf80bea3,0x3f61be64,0xbf003f33,0xbcccbf78,0xbd4c3f73,0xbef5bf7a,0x3f5e3f37,0xbf80be7d,
0x3e32bf71,0x3e323f80,0xbf00bf71,0x3f443f44,0xbf7cbeaf,0x3f7c0000,0xbf5e3eaf,0x3f25bf25,
0x3f753ebb,0xbf7dbe64,0x3f803d99,0xbf7d3d99,0x3e97bf6e,0xbe193f7a,0xbf7f,0x3e193f7f,
0x3eea3f75,0xbf67bea3,0xbed3bf71,0x3f6c3ebb,0x3f643e97,0x3ede3f73,0xbf69beaf,0xbec7bf6e,
0xbf67bf67,0x3f20be64,0x3f203f80,0xbf67be64,0xbede3ede,0xbf48bf7a,0x3f480000,0x3ede3f7a,
0x3f7b3ebb,0xbe643f33,0xbf4cbf71,0x3f613d99,0xbe4bbf6e,0x3f7a3f37,0xbf1b3eaf,0xbef5bf7f,
0xbf293ebb,0xbe64bf78,0x3f6c3f44,0xbf5a3d99,0x3f403f6e,0xbf7abe7d,0x3ec7bf25,0x3f053f7f,
0x3d99bf67,0x3f203f75,0xbf7dbf00,0x3f54be64,0xbf7fbede,0x3f48be97,0xbe193f5e,0xbf10bf7a,
0x3f0b3f75,0xbf67bf29,0x3f7f3e32,0xbf4c3ebb,0x3f57be97,0xbede3f40,0xbdccbf7c,0x3f1b3f6e,
0xbf71bf00,0x3f803e32,0xbf713e32,0x3f44bf00,0xbeaf3f5e,0xbf7c,0x3eaf3f7c,0xbf25bf5e,
0x3f75be64,0xbf673ebb,0x3f54bf00,0xbf3c3f20,0xbe97bf7a,0x3ede3f6e,0xbf10bf5e,0x3f2e3f48,
0x3ebb3f6c,0xbf71bed3,0xbea3bf67,0x3f753eea,0x3f6e3ec7,0x3eaf3f69,0xbf73bede,0xbe97bf64,
0xbe64bf67,0x3f803f20,0xbe643f20,0xbf67bf67,0xbf7abede,0xbf48,0x3f7a3f48,0xbede3ede,
0x3d993f61,0xbf71bf4c,0x3f33be64,0x3ebb3f7b,0x3f7f3ef5,0xbeaf3f1b,0xbf37bf7a,0x3f6e3e4b,
0x3d99bf5a,0x3f443f6c,0xbf78be64,0x3ebbbf29,0xbf7fbf05,0x3f25bec7,0x3e7d3f7a,0xbf6ebf40,
0xbe643f54,0xbf00bf7d,0x3f753f20,0xbf673d99,0x3f7a3f10,0xbf5e3e19,0x3e97bf48,0x3ede3f7f,
0x3ebbbf4c,0x3e323f7f,0xbf29bf67,0x3f753f0b,0xbf6ebf1b,0x3f7c3dcc,0xbf403ede,0x3e97bf57,
0xbf003f44,0x3e32bf71,0x3e323f80,0xbf00bf71,0x3f5e3f25,0xbf7cbeaf,0x3f7c0000,0xbf5e3eaf,
0x3f20bf3c,0xbf003f54,0x3ebbbf67,0xbe643f75,0xbf48bf2e,0x3f5e3f10,0xbf6ebede,0x3f7a3e97,
0x3e8b3f61,0xbf78bf00,0xbe64bf5a,0x3f7b3f0b,0x3f763ef5,0x3e7d3f5e,0xbf7abf05,0xbe4bbf57,
0x3f20be64,0x3f203f80,0xbf67be64,0xbe64bf67,0xbf48bf7a,0x3f480000,0x3ede3f7a,0xbf7abede,
0xbf80bf15,0x3eeabf00,0x3f203f7f,0xbf78bea3,0xbd4c3f50,0xbf64bf5e,0x3f48bdcc,0x3e7d3f73,
0x3f0b3f7f,0xbf80bf00,0x3f20bed3,0x3e8b3f7b,0x3f57bdcc,0xbd4c3f5e,0xbf48bf69,0x3f763e4b,
0x3ebbbf3c,0x3ebb3f80,0xbf67bf3c,0x3f753d99,0xbf6ebf2e,0x3f6e0000,0xbede3f2e,0xbe97bf7f,
0xbf78bccc,0x3f33bf00,0xbe643f61,0xbea3bf80,0x3e7d3f80,0xbf37bf5e,0x3f7a3ef5,0xbf733d4c,
0x3f443f44,0xbf71bf00,0x3f803e32,0xbf713e32,0x3f25bf25,0xbeaf3f5e,0xbf7c,0x3eaf3f7c,
0x3d99bf7d,0x3d993f80,0xbe64bf7d,0x3ebb3f75,0xbf7fbe19,0x3f7f0000,0xbf7a3e19,0x3f6ebe97,
0x3e323f54,0xbf7dbf15,0xbdffbf4c,0x3f7f3f20,0x3f7c3f10,0x3e193f50,0xbf7ebf1b,0xbdccbf48,
0x3f803f20,0xbe643f20,0xbf67bf67,0x3f20be64,0xbf48,0x3f7a3f48,0xbede3ede,0xbf48bf7a,
0x3e32bf3c,0x3f543f6c,0xbf5abccc,0xbdffbf67,0xbf7cbf2e,0x3f10bec7,0x3f053f80,0xbf7ebede,
0xbf71bf3c,0x3f54bdff,0xbccc3f61,0xbf4cbf67,0xbeaf3f2e,0xbf10bf7e,0x3f803ef5,0xbf1b3ede,
0xbf003f20,0xbe64bf7d,0x3f543f54,0xbf7dbe64,0x3f5e3f48,0xbf7abe19,0x3f10bf10,0x3e193f7a,
0x3f443f54,0xbf7dbed3,0x3f6cbdff,0xbf153f20,0x3f25bf10,0xbe193f69,0xbec7bf7e,0x3f503f48,
0x3f44bf00,0xbf003f44,0x3e32bf71,0x3e323f80,0xbf25bf5e,0x3f5e3f25,0xbf7cbeaf,0x3f7c0000,
0xbf00bf67,0x3f203f54,0xbf3cbf3c,0x3f543f20,0xbf5e3ede,0x3f48bf10,0xbf2e3f2e,0x3f10bf48,
0x3d993f44,0xbf80bf29,0xbcccbf3c,0x3f33,0x3f7f3f25,0x3d4c3f40,0xbf80bf2e,0xbf37,
0x3f203f80,0xbf67be64,0xbe64bf67,0x3f20,0x3f480000,0x3ede3f7a,0xbf7abede,0xbf48,
0x3f753f44,0xbf293e8b,0xbed3bf7d,0x3f0b,0x3e97bf25,0x3f403f76,0xbf69be19,0xbf57,
0x3f753e32,0xbea33f33,0xbf15bf7d,0x3eea,0xbe97bf7c,0x3f733f37,0xbf503e19,0xbf64,
0x3f20bf00,0x3d993f75,0xbf3cbf67,0x3ebb,0xbf48bf5e,0x3f7f3e97,0xbf2e3ede,0xbf6e,
0x3d99bf71,0x3eea3f7b,0xbf5abf3c,0x3e8b,0xbf7fbeaf,0x3f64be4b,0xbf053f2e,0xbf76,
0xbf00bf71,0x3f443f44,0xbf71bf00,0x3e32,0xbf5e3eaf,0x3f25bf25,0xbeaf3f5e,0xbf7c,
0xbf67bf00,0x3f753ebb,0xbf7dbe64,0x3d99,0xbede3f5e,0x3e97bf6e,0xbe193f7a,0xbf7f,
0xbccc3f80,0x3d99bf80,0xbdff3f7f,0x3e32bf7d,0x3f800000,0xbf7fbd4c,0x3f7e3dcc,0xbf7cbe19,
0xbdff3f80,0x3ebbbf78,0xbf153f61,0x3f44bf3c,0x3f7e0000,0xbf6ebe7d,0x3f503ef5,0xbf25bf2e,
0xbe643f80,0x3f20bf67,0xbf673f20,0x3f80be64,0x3f7a0000,0xbf48bede,0x3ede3f48,0xbf7a,
0xbea33f80,0x3f54bf4c,0xbf803e8b,0x3f443ebb,0x3f730000,0xbf10bf1b,0xbd4c3f76,0x3f25bf6e,
0xbed33f80,0x3f75bf29,0xbf5abdff,0x3e323f54,0x3f690000,0xbe97bf40,0xbf053f7e,0x3f7cbf10,
0xbf003f80,0x3f80bf00,0xbf00bf00,0xbf003f80,0x3f5e0000,0xbf5e,0xbf5e3f5e,0x3f5e0000,
0xbf153f80,0x3f75bea3,0xbcccbf4c,0xbf713f54,0x3f500000,0x3e97bf73,0xbf803f1b,0x3eaf3f10,
0xbf293f80,0x3f54bdff,0x3eeabf78,0xbf713ebb,0x3f400000,0x3f10bf7e,0xbf643e7d,0xbeaf3f6e,
0xbe643f7b,0x3e8bbf78,0xbea33f75,0x3ebbbf71,0x3f7a3e4b,0xbf76be7d,0x3f733e97,0xbf6ebeaf,
0xbf673f0b,0x3f7bbea3,0xbf803d99,0x3f753e32,0x3ede3f57,0xbe4bbf73,0xbd4c3f7f,0x3e97bf7c,
0xbf67be64,0x3f203f20,0xbe64bf67,0xbe643f80,0xbede3f7a,0x3f48bf48,0xbf7a3ede,0x3f7a0000,
0xbe64bf5a,0xbed33f7f,0x3f61bf3c,0xbf7d3e32,0xbf7a3f05,0x3f693dcc,0xbef5bf2e,0xbe193f7c,
0x3f20bf78,0xbf803eea,0x3f333ebb,0x3d99bf71,0xbf48be7d,0x3d4c3f64,0x3f37bf6e,0xbf7f3eaf,
0x3f80bf00,0xbf00bf00,0xbf003f80,0x3f80bf00,0xbf5e,0xbf5e3f5e,0x3f5e0000,0xbf5e,
0x3f203e8b,0x3f0bbf80,0xbf783ebb,0x3d993f44,0x3f48bf76,0xbf573d4c,0xbe7d3f6e,0x3f7fbf25,
0xbe643f61,0x3f7fbf15,0xbcccbf3c,0xbf7d3f44,0x3f7abef5,0x3dccbf50,0xbf803f2e,0x3e193f25,
0xbed33f6c,0x3eeabf67,0xbf003f61,0x3f0bbf5a,0x3f693ec7,0xbf64bede,0x3f5e3ef5,0xbf57bf05,
0xbf5abed3,0x3f333f20,0xbf00bf4c,0x3e8b3f6c,0xbf053f69,0x3f37bf48,0xbf5e3f1b,0x3f76bec7,
0x3f20bf67,0xbf673f20,0x3f80be64,0xbf67be64,0xbf48bede,0x3ede3f48,0xbf7a,0xbede3f7a,
0x3f333eea,0xbdffbf67,0xbf003f7b,0x3f6cbf29,0x3f37bf64,0xbf7e3ede,0x3f5e3e4b,0xbec7bf40,
0xbf4c3f61,0x3f7bbe64,0xbf00bf15,0xbea33f7f,0x3f1b3ef5,0x3e4bbf7a,0xbf5e3f50,0x3f73bdcc,
0xbf00bf00,0xbf003f80,0x3f80bf00,0xbf00bf00,0xbf5e3f5e,0x3f5e0000,0xbf5e,0xbf5e3f5e,
0x3f6cbf5a,0xbf29be64,0xbf003f7f,0x3f7bbed3,0xbec7bf05,0xbf403f7a,0x3f5ebdcc,0x3e4bbf69,
0x3e8b3f0b,0x3f6cbf67,0xbf00bea3,0xbf4c3f7b,0x3f76bf57,0xbec7bede,0xbf5e3f73,0x3f1b3e4b,
0xbf153f54,0x3f20bf4c,0xbf293f44,0x3f33bf3c,0x3f503f10,0xbf48bf1b,0x3f403f25,0xbf37bf2e,
0xbcccbf7d,0xbe643f7f,0x3eeabf71,0xbf293f54,0xbf803e19,0x3f7a3dcc,0xbf64beaf,0x3f403f10,
0x3f203f20,0xbe64bf67,0xbe643f80,0x3f20bf67,0x3f48bf48,0xbf7a3ede,0x3f7a0000,0xbf48bede,
0xbf783d99,0x3f203f0b,0xbcccbf71,0xbf153f75,0xbe7d3f7f,0x3f48bf57,0xbf803eaf,0x3f503e97,
0x3f6cbf3c,0xbf67bccc,0x3e8b3f44,0x3f0bbf7d,0xbec7bf2e,0xbede3f80,0x3f76bf25,0xbf57be19,
0xbf003f80,0x3f80bf00,0xbf00bf00,0xbf003f80,0x3f5e0000,0xbf5e,0xbf5e3f5e,0x3f5e0000,
0xbdffbf3c,0xbf673f61,0x3f333e32,0x3eeabf7d,0xbf7e3f2e,0x3ede3ef5,0x3f37bf7c,0xbf643e19,
0x3f333d99,0x3f20bf80,0xbf5a3e32,0xbed33f75,0x3f37bf7f,0xbf483d4c,0xbf053f7c,0x3f69be97,
0xbf3c3f33,0x3f44bf29,0xbf4c3f20,0x3f54bf15,0x3f2e3f37,0xbf25bf40,0x3f1b3f48,0xbf10bf50,
0x3f54bf29,0xbf713eea,0x3f7fbe64,0xbf7dbccc,0xbf10bf40,0x3eaf3f64,0xbdccbf7a,0xbe193f80,
0xbf673f20,0x3f80be64,0xbf67be64,0x3f203f20,0x3ede3f48,0xbf7a,0xbede3f7a,0x3f48bf48,
0x3f75bf15,0xbf71bccc,0x3f0b3f20,0x3d99bf78,0xbe97bf50,0xbeaf3f80,0x3f57bf48,0xbf7f3e7d,
0xbf7d3f0b,0x3f443e8b,0xbcccbf67,0xbf3c3f6c,0x3e193f57,0x3f25bf76,0xbf803ede,0x3f2e3ec7,
0x3f80bf00,0xbf00bf00,0xbf003f80,0x3f80bf00,0xbf5e,0xbf5e3f5e,0x3f5e0000,0xbf5e,
0xbf7d3eea,0x3e323f33,0x3f61bf67,0xbf3cbdff,0xbe193f64,0x3f7cbf37,0xbef5bede,0xbf2e3f7e,
0x3f75bed3,0x3e32bf5a,0xbf803f20,0x3d993f33,0x3e97bf69,0xbf7c3f05,0xbd4c3f48,0x3f7fbf37,
0xbf5a3f0b,0x3f61bf00,0xbf673eea,0x3f6cbed3,0x3f053f57,0xbef5bf5e,0x3ede3f64,0xbec7bf69,
0x3f6c3e8b,0xbf4cbf00,0x3f203f33,0xbed3bf5a,0x3ec7bf76,0xbf1b3f5e,0x3f48bf37,0xbf693f05,
0xbe64bf67,0xbe643f80,0x3f20bf67,0xbf673f20,0xbf7a3ede,0x3f7a0000,0xbf48bede,0x3ede3f48,
0xbf293f6c,0x3f7bbf00,0xbf67bdff,0x3eea3f33,0x3f403ec7,0xbe4bbf5e,0xbede3f7e,0x3f64bf37,
0x3f7fbea3,0xbf15bf00,0xbe643f7b,0x3f61bf4c,0x3dccbf73,0xbf503f5e,0x3f7abe4b,0xbef5bf1b,
0xbf00bf00,0xbf003f80,0x3f80bf00,0xbf00bf00,0xbf5e3f5e,0x3f5e0000,0xbf5e,0xbf5e3f5e,
0xbed33f7b,0x3f7fbf00,0xbe64bf29,0xbf5a3f6c,0x3f69be4b,0x3dccbf5e,0xbf7a3f40,0x3f053ec7,
0x3f7bbf4c,0xbea3bf00,0xbf673f6c,0x3f0b3e8b,0xbe4bbf1b,0xbf733f5e,0x3ede3ec7,0x3f57bf76,
0xbf713ebb,0x3f75bea3,0xbf783e8b,0x3f7bbe64,0x3eaf3f6e,0xbe97bf73,0x3e7d3f76,0xbe4bbf7a,
0x3e323f75,0x3d99bf80,0xbea33f7b,0x3f0bbf67,0x3f7cbe97,0xbf7f3d4c,0x3f733e4b,0xbf57bede,
0x3f80be64,0xbf67be64,0x3f203f20,0xbe64bf67,0xbf7a,0xbede3f7a,0x3f48bf48,0xbf7a3ede,
0x3e32bf7d,0xbf3c3f61,0x3f7fbed3,0xbf5abe64,0xbf7c3e19,0x3f2e3ef5,0xbdccbf69,0xbf053f7a,
0xbf713d99,0x3ebb3f33,0x3eeabf80,0xbf783f20,0xbeaf3f7f,0x3f6ebf37,0xbf64bd4c,0x3e7d3f48,
0xbf003f80,0x3f80bf00,0xbf00bf00,0xbf003f80,0x3f5e0000,0xbf5e,0xbf5e3f5e,0x3f5e0000,
0x3f443d99,0x3ebbbf78,0xbf803f0b,0x3e8b3f20,0x3f25bf7f,0xbf6e3e7d,0xbd4c3f57,0x3f76bf48,
0x3f44bf7d,0xbf3cbccc,0xbf153f7f,0x3f61be64,0xbf25be19,0xbf2e3f80,0x3f50bdcc,0x3ef5bf7a,
0xbf7d3e32,0x3f7fbdff,0xbf803d99,0xbccc,0x3e193f7c,0xbdccbf7e,0x3d4c3f7f,0xbf80,
0xbf3c3f44,0x3f61bf15,0xbf783ebb,0xbdff,0x3f2e3f25,0xbef5bf50,0x3e7d3f6e,0xbf7e,
0xbe643f80,0x3f20bf67,0xbf673f20,0xbe64,0x3f7a0000,0xbf48bede,0x3ede3f48,0xbf7a,
0x3ebb3f44,0x3e8bbf80,0xbf4c3f54,0xbea3,0x3f6ebf25,0xbf763d4c,0x3f1b3f10,0xbf73,
0x3f543e32,0xbdffbf5a,0xbf293f75,0xbed3,0x3f10bf7c,0xbf7e3f05,0x3f403e97,0xbf69,
0x3f80bf00,0xbf00bf00,0xbf003f80,0xbf00,0xbf5e,0xbf5e3f5e,0x3f5e0000,0xbf5e,
0x3f54bf71,0xbf4cbccc,0xbea33f75,0xbf15,0xbf10beaf,0xbf1b3f80,0x3f73be97,0xbf50,
0x3ebbbf71,0xbf783eea,0xbdff3f54,0xbf29,0xbf6e3eaf,0xbe7d3f64,0x3f7ebf10,0xbf40,
0xbf3c3f80,0x3f203d99,0x3f54bf7d,0xbf00be64,0x3f2e0000,0x3f48bf7f,0xbf10be19,0xbf5e3f7a,
0xbf4c3f80,0x3ebb3e8b,0x3f7fbf5a,0x3e32bf3c,0x3f1b0000,0x3f6ebf76,0xbdccbf05,0xbf7c3f2e,
0xbf5a3f80,0x3d993eea,0x3f6cbf15,0x3f44bf7d,0x3f050000,0x3f7fbf64,0x3ec7bf50,0xbf253e19,
0xbf673f80,0xbe643f20,0x3f20be64,0x3f80bf67,0x3ede0000,0x3f7abf48,0x3f48bf7a,0xbede,
0xbf713f80,0xbf003f44,0x3e323e32,0x3f44bf00,0x3eaf0000,0x3f5ebf25,0x3f7cbf7c,0x3f25bf5e,
0xbf783f80,0xbf3c3f61,0xbea33f0b,0x3e323d99,0x3e7d0000,0x3f2ebef5,0x3f73bf57,0x3f7cbf7f,
0xbf7d3f80,0xbf673f75,0xbf3c3f54,0xbf003f20,0x3e190000,0x3edebe97,0x3f2ebf10,0x3f5ebf48,
0xbf803f80,0xbf7d3f7f,0xbf783f7b,0xbf713f75,0x3d4c0000,0x3e19bdcc,0x3e7dbe4b,0x3eafbe97,
0xbf673f75,0x3ebb3ebb,0x3f75bf67,0xbe64bf00,0x3ede3e97,0x3f6ebf6e,0xbe97bede,0xbf7a3f5e,
0xbf673eea,0xbf293f7b,0x3f0b3d99,0x3f75bf71,0xbede3f64,0x3f40be4b,0x3f57bf7f,0xbe97beaf,
0xbe64bea3,0xbf783f33,0xbf293f75,0x3ebb3e32,0xbf7a3f73,0xbe7d3f37,0x3f40be97,0x3f6ebf7c,
0x3f20bf67,0xbe64be64,0xbf673f20,0xbf673f80,0xbf483ede,0xbf7a3f7a,0xbede3f48,0x3ede0000,
0x3f80bf71,0x3f44bf71,0x3e32bf00,0xbf003e32,0xbeaf,0xbf253eaf,0xbf7c3f5e,0xbf5e3f7c,
0x3f20bed3,0x3f6cbf4c,0x3f7fbf7d,0x3f54bf71,0x3f48bf69,0x3ec7bf1b,0xbdccbe19,0xbf103eaf,
0xbe643ebb,0x3d993d99,0x3ebbbe64,0x3f20bf00,0x3f7abf6e,0x3f7fbf7f,0x3f6ebf7a,0x3f48bf5e,
0xbf673f6c,0xbf5a3f61,0xbf4c3f54,0xbf3c3f44,0x3edebec7,0x3f05bef5,0x3f1bbf10,0x3f2ebf25,
0xbf7d3f54,0x3d993f20,0x3f80bf3c,0x3d99bf3c,0x3e193f10,0x3f7fbf48,0xbf2e,0xbf7f3f2e,
0xbcccbf15,0xbf783f20,0xbf003f6c,0x3f33bdff,0xbf803f50,0xbe7d3f48,0x3f5ebec7,0x3f37bf7e,
0x3f7fbf4c,0x3f0bbf67,0xbf00bccc,0xbf803f61,0x3dccbf1b,0xbf573ede,0xbf5e3f80,0x3d4c3ef5,
0xbe643f20,0x3f20be64,0x3f80bf67,0x3f20bf67,0x3f7abf48,0x3f48bf7a,0xbede,0xbf483ede,
0xbf713f44,0xbf713f80,0xbf003f44,0x3e323e32,0xbeaf3f25,0x3eaf0000,0x3f5ebf25,0x3f7cbf7c,
0x3eeabf29,0xbcccbe64,0xbf003e8b,0xbf5a3f33,0xbf643f40,0xbf803f7a,0xbf5e3f76,0xbf053f37,
0x3f54bf3c,0x3f75bf67,0x3f80bf7d,0x3f75bf7d,0x3f10bf2e,0x3e97bede,0xbe19,0xbe973e19,
0xbf293f33,0xbf153f20,0xbf003f0b,0xbed33eea,0x3f40bf37,0x3f50bf48,0x3f5ebf57,0x3f69bf64,
0xbf7d3f20,0xbe643f54,0x3f75bf00,0x3ebbbf67,0xbe193f48,0x3f7abf10,0x3e97bf5e,0xbf6e3ede,
0x3f61bf7d,0xbe64bed3,0xbf803f44,0xbea33f54,0xbef5be19,0xbf7a3f69,0xbd4c3f25,0x3f73bf10,
0xbed33f54,0x3f20bdff,0x3f7bbf71,0x3e8bbf3c,0x3f69bf10,0x3f48bf7e,0xbe4bbeaf,0xbf763f2e,
0xbe64be64,0xbf673f20,0xbf673f80,0xbe643f20,0xbf7a3f7a,0xbede3f48,0x3ede0000,0x3f7abf48,
0x3f44bf00,0x3f80bf71,0x3f44bf71,0x3e32bf00,0x3f25bf5e,0xbeaf,0xbf253eaf,0xbf7c3f5e,
0xbf803f75,0xbf673f7b,0xbf153f44,0xbdff3ebb,0xbd4c3e97,0x3edebe4b,0x3f50bf25,0x3f7ebf6e,
0x3f54bf67,0x3f20bf3c,0x3ebbbf00,0x3d99be64,0xbf103ede,0xbf483f2e,0xbf6e3f5e,0xbf7f3f7a,
0xbea33ebb,0xbe643e8b,0xbdff3e32,0xbccc3d99,0x3f73bf6e,0x3f7abf76,0x3f7ebf7c,0x3f80bf7f,
0xbf673ebb,0xbf003f75,0x3f54be64,0x3f20bf7d,0xbede3f6e,0x3f5ebe97,0x3f10bf7a,0xbf483e19,
0x3f54bea3,0x3f44bf80,0xbed3be64,0xbf7d3f61,0x3f10bf73,0xbf253d4c,0xbf693f7a,0x3e193ef5,
0xbf3c3e8b,0xbf713f7b,0xbdff3f20,0x3f54bed3,0xbf2e3f76,0x3eaf3e4b,0x3f7ebf48,0x3f10bf69,
0x3f20be64,0x3f80bf67,0x3f20bf67,0xbe64be64,0x3f48bf7a,0xbede,0xbf483ede,0xbf7a3f7a,
0xbf003e32,0xbf713f44,0xbf713f80,0xbf003f44,0xbf5e3f7c,0xbeaf3f25,0x3eaf0000,0x3f5ebf25,
0x3ebbbdff,0x3f44bf15,0x3f7bbf67,0x3f75bf80,0x3f6ebf7e,0x3f25bf50,0x3e4bbede,0xbe973d4c,
0xbe643d99,0xbf003ebb,0xbf3c3f20,0xbf673f54,0xbf7a3f7f,0xbf5e3f6e,0xbf2e3f48,0xbede3f10,
0x3d99bccc,0x3e32bdff,0x3e8bbe64,0x3ebbbea3,0x3f7fbf80,0x3f7cbf7e,0x3f76bf7a,0x3f6ebf73,
0xbf3c3d99,0xbf3c3f80,0x3f203d99,0x3f54bf7d,0xbf2e3f7f,0x3f2e0000,0x3f48bf7f,0xbf10be19,
0xbdff3f33,0x3f6cbf00,0x3f20bf78,0xbf15bccc,0x3f7ebf37,0x3ec7bf5e,0xbf483e7d,0xbf503f80,
0x3f61bf80,0xbcccbf00,0xbf673f0b,0xbf4c3f7f,0xbef5bd4c,0xbf803f5e,0xbede3f57,0x3f1bbdcc,
0xbf673f20,0xbf673f80,0xbe643f20,0x3f20be64,0xbede3f48,0x3ede0000,0x3f7abf48,0x3f48bf7a,
0x3e323e32,0x3f44bf00,0x3f80bf71,0x3f44bf71,0x3f7cbf7c,0x3f25bf5e,0xbeaf,0xbf253eaf,
0x3f33bf5a,0x3e8bbf00,0xbe64bccc,0xbf293eea,0xbf373f05,0xbf763f5e,0xbf7a3f80,0xbf403f64,
0xbf7d3f75,0xbf7d3f80,0xbf673f75,0xbf3c3f54,0xbe193e97,0x3e190000,0x3edebe97,0x3f2ebf10,
0x3eeabed3,0x3f0bbf00,0x3f20bf15,0x3f33bf29,0x3f64bf69,0x3f57bf5e,0x3f48bf50,0x3f37bf40,
0xbf00be64,0xbf673f75,0x3ebb3ebb,0x3f75bf67,0xbf5e3f7a,0x3ede3e97,0x3f6ebf6e,0xbe97bede,
0xbf713f75,0x3d993f0b,0x3f7bbf29,0x3eeabf67,0x3eaf3e97,0x3f7fbf57,0x3e4bbf40,0xbf643ede,
0x3e323ebb,0x3f75bf29,0x3f33bf78,0xbea3be64,0x3f7cbf6e,0x3e97bf40,0xbf373e7d,0xbf733f7a,
0x3f80bf67,0x3f20bf67,0xbe64be64,0xbf673f20,0xbede,0xbf483ede,0xbf7a3f7a,0xbede3f48,
0x3e32bf00,0xbf003e32,0xbf713f44,0xbf713f80,0xbf7c3f5e,0xbf5e3f7c,0xbeaf3f25,0x3eaf0000,
0xbf713f54,0xbf7d3f7f,0xbf4c3f6c,0xbed33f20,0xbeaf3f10,0x3e193dcc,0x3f1bbec7,0x3f69bf48,
0xbf003f20,0xbe643ebb,0x3d993d99,0x3ebbbe64,0x3f5ebf48,0x3f7abf6e,0x3f7fbf7f,0x3f6ebf7a,
0x3f44bf3c,0x3f54bf4c,0x3f61bf5a,0x3f6cbf67,0x3f25bf2e,0x3f10bf1b,0x3ef5bf05,0x3ec7bede,
0xbe64bf00,0xbf7d3f54,0x3d993f20,0xbf3c,0xbf7a3f5e,0x3e193f10,0x3f7fbf48,0xbf2e,
0xbf3c3e32,0xbf5a3f7f,0x3e8b3ebb,0xbf4c,0xbf2e3f7c,0x3f053dcc,0x3f76bf6e,0xbf1b,
0xbf7d3f44,0xbf153f6c,0x3eea3d99,0xbf5a,0xbe193f25,0x3f50bec7,0x3f64bf7f,0xbf05,
0xbf673f80,0xbe643f20,0x3f20be64,0xbf67,0x3ede0000,0x3f7abf48,0x3f48bf7a,0xbede,
0xbf003f44,0x3e323e32,0x3f44bf00,0xbf71,0x3f5ebf25,0x3f7cbf7c,0x3f25bf5e,0xbeaf,
0x3d993e32,0x3f0bbea3,0x3f61bf3c,0xbf78,0x3f7fbf7c,0x3f57bf73,0x3ef5bf2e,0xbe7d,
0x3f20bf00,0x3f54bf3c,0x3f75bf67,0xbf7d,0x3f48bf5e,0x3f10bf2e,0x3e97bede,0xbe19,
0x3f75bf71,0x3f7bbf78,0x3f7fbf7d,0xbf80,0x3e97beaf,0x3e4bbe7d,0x3dccbe19,0xbd4c,
0x3bf4,0x3c74,0x3c74,0x3c74,0x3bf4,0x3ac23c73,0x3b413c6f,0x3b903c69,
0x3bf4,0x3b413c6f,0x3bbd3c61,0x3c093c49,0x3bf4,0x3b903c69,0x3c093c49,0x3c3f3c18,
0x3bf4,0x3bbd3c61,0x3c2f3c2a,0x3c633bb2,0x3bf4,0x3be93c56,0x3c4d3c04,0x3c733a92,
0x3bf4,0x3c093c49,0x3c633bb2,0x3c6ebb59,0x3bf4,0x3c1d3c3b,0x3c703b29,0x3c53bbf4,
0x3bf4,0x3c2f3c2a,0x3c74b9c2,0x3c26bc33,0x3bf4,0x3c3f3c18,0x3c6ebb59,0x3bd4bc5c,
0x3bf4,0x3c4d3c04,0x3c5ebbc9,0x3b11bc71,0x3bf4,0x3c593bde,0x3c46bc0e,0xbb11bc71,
0x3bf4,0x3c633bb2,0x3c26bc33,0xbbd4bc5c,0x3bf4,0x3c6b3b84,0x3bfebc50,0xbc26bc33,
0x3bf4,0x3c703b29,0x3ba7bc65,0xbc53bbf4,0x3bf4,0x3c733a92,0x3b11bc71,0xbc6ebb59,
0x3c74,0x3c74,0x3c74,0x3c74,0x3bbd3c61,0x3be93c56,0x3c093c49,0x3c1d3c3b,
0x3c2f3c2a,0x3c4d3c04,0x3c633bb2,0x3c703b29,0x3c633bb2,0x3c733a92,0x3c6ebb59,0x3c53bbf4,
0x3c74b9c2,0x3c5ebbc9,0x3c26bc33,0x3ba7bc65,0x3c5ebbc9,0x3c13bc42,0x3b11bc71,0xbba7bc65,
0x3c26bc33,0x3b11bc71,0xbbd4bc5c,0xbc53bbf4,0x3ba7bc65,0xbba7bc65,0xbc53bbf4,0xbc703b29,
0xba42bc73,0xbc37bc21,0xbc733a92,0xbc1d3c3b,0xbbd4bc5c,0xbc6ebb59,0xbc3f3c18,0x3c74,
0xbc37bc21,0xbc6b3b84,0xbb903c69,0x3c1d3c3b,0xbc67bb9b,0xbc2f3c2a,0x3b903c69,0x3c703b29,
0xbc733a92,0xbb903c69,0x3c3f3c18,0x3c53bbf4,0xbc593bde,0x3b413c6f,0x3c733a92,0x3ba7bc65,
0xbc1d3c3b,0x3c1d3c3b,0x3c53bbf4,0xbba7bc65,0xbb903c69,0x3c633bb2,0x3bd4bc5c,0xbc53bbf4,
0x3c74,0x3c74,0x3c74,0x3c74,0x3c2f3c2a,0x3c3f3c18,0x3c4d3c04,0x3c593bde,
0x3c74b9c2,0x3c6ebb59,0x3c5ebbc9,0x3c46bc0e,0x3c26bc33,0x3bd4bc5c,0x3b11bc71,0xbb11bc71,
0xba42bc73,0xbbd4bc5c,0xbc37bc21,0xbc67bb9b,0xbc37bc21,0xbc6ebb59,0xbc6b3b84,0xbc2f3c2a,
0xbc733a92,0xbc3f3c18,0xbb903c69,0x3b903c69,0xbc1d3c3b,0x3c74,0x3c1d3c3b,0x3c703b29,
0x3ac23c73,0x3c3f3c18,0x3c72baf2,0x3c13bc42,0x3c3f3c18,0x3c6ebb59,0x3bd4bc5c,0xbbd4bc5c,
0x3c72baf2,0x3bd4bc5c,0xbbfebc50,0xbc74b9c2,0x3c13bc42,0xbbd4bc5c,0xbc74b9c2,0xbbe93c56,
0xbb11bc71,0xbc6ebb59,0xbc093c49,0x3c093c49,0xbc46bc0e,0xbc3f3c18,0x3bbd3c61,0x3c72baf2,
0xbc703b29,0x3c74,0x3c703b29,0x3ba7bc65,0xbc093c49,0x3c3f3c18,0x3c26bc33,0xbc26bc33,
0x3c74,0x3c74,0x3c74,0x3c74,0x3c633bb2,0x3c6b3b84,0x3c703b29,0x3c733a92,
0x3c26bc33,0x3bfebc50,0x3ba7bc65,0x3b11bc71,0xbbd4bc5c,0xbc26bc33,0xbc53bbf4,0xbc6ebb59,
0xbc733a92,0xbc593bde,0xbc1d3c3b,0xbb903c69,0xbb903c69,0x3b413c6f,0x3c1d3c3b,0x3c633bb2,
0x3c3f3c18,0x3c733a92,0x3c53bbf4,0x3bd4bc5c,0x3c53bbf4,0x3ba7bc65,0xbba7bc65,0xbc53bbf4,
0xbb11bc71,0xbc46bc0e,0xbc703b29,0xbc093c49,0xbc6ebb59,0xbc3f3c18,0x3c74,0x3c3f3c18,
0xbc093c49,0x3bbd3c61,0x3c703b29,0x3c26bc33,0x3c093c49,0x3c72baf2,0x3ba7bc65,0xbc26bc33,
0x3c6ebb59,0x3b11bc71,0xbc53bbf4,0xbc3f3c18,0x3b11bc71,0xbc5ebbc9,0xbc1d3c3b,0x3c093c49,
0xbc53bbf4,0xbc1d3c3b,0x3c1d3c3b,0x3c53bbf4,0xbc3f3c18,0x3c093c49,0x3c53bbf4,0xbbd4bc5c,
0x3c74,0x3c74,0x3c74,0x3c74,0x3c74b9c2,0x3c72baf2,0x3c6ebb59,0x3c67bb9b,
0xba42bc73,0xbb71bc6c,0xbbd4bc5c,0xbc13bc42,0xbc733a92,0xbc633bb2,0xbc3f3c18,0xbc093c49,
0x3ac23c73,0x3be93c56,0x3c3f3c18,0x3c6b3b84,0x3c72baf2,0x3c46bc0e,0x3bd4bc5c,0xba42bc73,
0xbb11bc71,0xbc26bc33,0xbc6ebb59,0xbc633bb2,0xbc703b29,0xbc1d3c3b,0x3c74,0x3c1d3c3b,
0x3b413c6f,0x3c4d3c04,0x3c6ebb59,0x3bfebc50,0x3c6ebb59,0x3bd4bc5c,0xbbd4bc5c,0xbc6ebb59,
0xbb71bc6c,0xbc67bb9b,0xbc3f3c18,0x3ac23c73,0xbc6b3b84,0xbb413c6f,0x3c3f3c18,0x3c5ebbc9,
0x3b903c69,0x3c733a92,0x3bd4bc5c,0xbc26bc33,0x3c67bb9b,0xba42bc73,0xbc6ebb59,0xbbe93c56,
0xbba7bc65,0xbc703b29,0x3c74,0x3c703b29,0xbc633bb2,0x3b903c69,0x3c6ebb59,0xbb11bc71,
0x3c74,0x3c74,0x3c74,0x3c74,0x3c5ebbc9,0x3c53bbf4,0x3c46bc0e,0x3c37bc21,
0xbc37bc21,0xbc53bbf4,0xbc67bb9b,0xbc72baf2,0xbb903c69,0x3c74,0x3b903c69,0x3c093c49,
0x3c72baf2,0x3c53bbf4,0x3c13bc42,0x3b71bc6c,0xbbfebc50,0xbc53bbf4,0xbc74b9c2,0xbc593bde,
0xbc093c49,0x3c74,0x3c093c49,0x3c633bb2,0x3c703b29,0x3c53bbf4,0x3ba7bc65,0xbba7bc65,
0xbb71bc6c,0xbc53bbf4,0xbc6b3b84,0xbbe93c56,0xbc3f3c18,0x3c74,0x3c3f3c18,0x3c6ebb59,
0x3c593bde,0x3c53bbf4,0x3a42bc73,0xbc46bc0e,0x3a42bc73,0xbc53bbf4,0xbc4d3c04,0x3ac23c73,
0xbc633bb2,0x3c74,0x3c633bb2,0x3c26bc33,0x3c2f3c2a,0x3c53bbf4,0xbb71bc6c,0xbc74b9c2,
0x3ba7bc65,0xbc53bbf4,0xbc1d3c3b,0x3c1d3c3b,0xbc733a92,0x3c74,0x3c733a92,0x3b11bc71,
0x3c74,0x3c74,0x3c74,0x3c74,0x3c26bc33,0x3c13bc42,0x3bfebc50,0x3bd4bc5c,
0xbc733a92,0xbc6b3b84,0xbc593bde,0xbc3f3c18,0x3c3f3c18,0x3c633bb2,0x3c733a92,0x3c6ebb59,
0xbb11bc71,0xbbfebc50,0xbc46bc0e,0xbc6ebb59,0xbc093c49,0xbac23c73,0x3bbd3c61,0x3c3f3c18,
0x3c6ebb59,0x3c26bc33,0x3b11bc71,0xbbd4bc5c,0xbc53bbf4,0xbc703b29,0xbc1d3c3b,0x3c74,
0x3b903c69,0x3c593bde,0x3c67bb9b,0x3bd4bc5c,0x3bd4bc5c,0xbbd4bc5c,0xbc6ebb59,0xbc3f3c18,
0xbc633bb2,0xbb413c6f,0x3c2f3c2a,0x3c6ebb59,0x3c633bb2,0x3c37bc21,0xbb71bc6c,0xbc6ebb59,
0xbbd4bc5c,0xbc733a92,0xbb903c69,0x3c3f3c18,0xbb903c69,0x3c4d3c04,0x3c37bc21,0xbbd4bc5c,
0x3c53bbf4,0xbba7bc65,0xbc703b29,0x3c74,0xbc6ebb59,0xbb903c69,0x3c633bb2,0x3bd4bc5c,
0x3c74,0x3c74,0x3c74,0x3c74,0x3ba7bc65,0x3b71bc6c,0x3b11bc71,0x3a42bc73,
0xbc1d3c3b,0xbbe93c56,0xbb903c69,0xbac23c73,0x3c53bbf4,0x3c26bc33,0x3bd4bc5c,0x3b11bc71,
0xbc703b29,0xbc4d3c04,0xbc093c49,0xbb413c6f,0x3c703b29,0x3c67bb9b,0x3c26bc33,0x3b71bc6c,
0xbc53bbf4,0xbc733a92,0xbc3f3c18,0xbb903c69,0x3c1d3c3b,0x3c703b29,0x3c53bbf4,0x3ba7bc65,
0xbba7bc65,0xbc5ebbc9,0xbc633bb2,0xbbbd3c61,0x3c74,0x3c3f3c18,0x3c6ebb59,0x3bd4bc5c,
0x3ba7bc65,0xbc13bc42,0xbc733a92,0xbbe93c56,0xbc1d3c3b,0x3bbd3c61,0x3c733a92,0x3bfebc50,
0x3c53bbf4,0xbb11bc71,0xbc6ebb59,0xbc093c49,0xbc703b29,0xbac23c73,0x3c633bb2,0x3c13bc42,
0x3c703b29,0x3ba7bc65,0xbc53bbf4,0xbc1d3c3b,0xbc53bbf4,0xbc093c49,0x3c3f3c18,0x3c26bc33,
0x3bf4,0x3c74b9c2,0xba42bc73,0xbc733a92,0x3bf4,0x3c72baf2,0xbb71bc6c,0xbc633bb2,
0x3bf4,0x3c6ebb59,0xbbd4bc5c,0xbc3f3c18,0x3bf4,0x3c67bb9b,0xbc13bc42,0xbc093c49,
0x3bf4,0x3c5ebbc9,0xbc37bc21,0xbb903c69,0x3bf4,0x3c53bbf4,0xbc53bbf4,0x3c74,
0x3bf4,0x3c46bc0e,0xbc67bb9b,0x3b903c69,0x3bf4,0x3c37bc21,0xbc72baf2,0x3c093c49,
0x3bf4,0x3c26bc33,0xbc733a92,0x3c3f3c18,0x3bf4,0x3c13bc42,0xbc6b3b84,0x3c633bb2,
0x3bf4,0x3bfebc50,0xbc593bde,0x3c733a92,0x3bf4,0x3bd4bc5c,0xbc3f3c18,0x3c6ebb59,
0x3bf4,0x3ba7bc65,0xbc1d3c3b,0x3c53bbf4,0x3bf4,0x3b71bc6c,0xbbe93c56,0x3c26bc33,
0x3bf4,0x3b11bc71,0xbb903c69,0x3bd4bc5c,0x3bf4,0x3a42bc73,0xbac23c73,0x3b11bc71,
0x3ac23c73,0x3c72baf2,0xbb11bc71,0xbc703b29,0x3be93c56,0x3c46bc0e,0xbc26bc33,0xbc1d3c3b,
0x3c3f3c18,0x3bd4bc5c,0xbc6ebb59,0x3c74,0x3c6b3b84,0xba42bc73,0xbc633bb2,0x3c1d3c3b,
0x3c72baf2,0xbbfebc50,0xbc093c49,0x3c703b29,0x3c53bbf4,0xbc53bbf4,0x3c74,0x3c53bbf4,
0x3c13bc42,0xbc74b9c2,0x3c093c49,0x3ba7bc65,0x3b71bc6c,0xbc593bde,0x3c633bb2,0xbba7bc65,
0xbb11bc71,0xbc093c49,0x3c6ebb59,0xbc53bbf4,0xbbfebc50,0xbac23c73,0x3c26bc33,0xbc703b29,
0xbc46bc0e,0x3bbd3c61,0x3b11bc71,0xbc1d3c3b,0xbc6ebb59,0x3c3f3c18,0xbbd4bc5c,0x3c74,
0xbc703b29,0x3c703b29,0xbc53bbf4,0x3c1d3c3b,0xbc4d3c04,0x3c67bb9b,0xbc733a92,0x3c703b29,
0xbc093c49,0x3c26bc33,0xbc3f3c18,0x3c53bbf4,0xbb413c6f,0x3b71bc6c,0xbb903c69,0x3ba7bc65,
0x3b413c6f,0x3c6ebb59,0xbb71bc6c,0xbc6b3b84,0x3c4d3c04,0x3bd4bc5c,0xbc67bb9b,0xbb413c6f,
0x3c6ebb59,0xbbd4bc5c,0xbc3f3c18,0x3c3f3c18,0x3bfebc50,0xbc6ebb59,0x3ac23c73,0x3c5ebbc9,
0xbb71bc6c,0xbc3f3c18,0x3c593bde,0x3a42bc73,0xbc53bbf4,0x3c74,0x3c53bbf4,0xbc53bbf4,
0xbc6b3b84,0x3c3f3c18,0x3a42bc73,0xbc4d3c04,0xbbe93c56,0x3c6ebb59,0xbc46bc0e,0x3ac23c73,
0x3b903c69,0x3bd4bc5c,0xbc633bb2,0x3c633bb2,0x3c593bde,0xbbd4bc5c,0xbb413c6f,0x3c37bc21,
0x3c67bb9b,0xbc6ebb59,0x3c2f3c2a,0xbb71bc6c,0x3bd4bc5c,0xbc3f3c18,0x3c6ebb59,0xbc6ebb59,
0xbba7bc65,0x3c74,0x3ba7bc65,0xbc1d3c3b,0xbc5ebbc9,0x3c3f3c18,0xbc13bc42,0x3bbd3c61,
0xbc633bb2,0x3c6ebb59,0xbc733a92,0x3c733a92,0xbbbd3c61,0x3bd4bc5c,0xbbe93c56,0x3bfebc50,
0x3b903c69,0x3c67bb9b,0xbba7bc65,0xbc633bb2,0x3c733a92,0xba42bc73,0xbc703b29,0x3b903c69,
0x3bd4bc5c,0xbc6ebb59,0x3c74,0x3c6ebb59,0xbc26bc33,0xbbe93c56,0x3c703b29,0xbb11bc71,
0xbc633bb2,0x3c2f3c2a,0x3ba7bc65,0xbc733a92,0x3c74,0x3c53bbf4,0xbc53bbf4,0x3c74,
0x3c633bb2,0xbb71bc6c,0xbc1d3c3b,0x3c733a92,0x3c26bc33,0xbc74b9c2,0x3c1d3c3b,0x3b11bc71,
0xbbd4bc5c,0xbb903c69,0x3c53bbf4,0xbc6ebb59,0xbc733a92,0x3c4d3c04,0xbba7bc65,0xbb903c69,
0xbb903c69,0x3c37bc21,0xbc703b29,0x3c633bb2,0x3c3f3c18,0xbbd4bc5c,0x3c74,0x3bd4bc5c,
0x3c53bbf4,0xbc703b29,0x3c703b29,0xbc53bbf4,0xbb11bc71,0xbac23c73,0x3ba7bc65,0xbc093c49,
0xbc6ebb59,0x3c633bb2,0xbc53bbf4,0x3c3f3c18,0xbc093c49,0x3c13bc42,0xbc1d3c3b,0x3c26bc33,
0x3bbd3c61,0x3c5ebbc9,0xbbd4bc5c,0xbc593bde,0x3c5ebbc9,0xbbfebc50,0xbc3f3c18,0x3c2f3c2a,
0xbbd4bc5c,0xbc3f3c18,0x3c3f3c18,0x3bd4bc5c,0xbc593bde,0x3c2f3c2a,0x3bd4bc5c,0xbc72baf2,
0x3be93c56,0x3c13bc42,0xbc6ebb59,0x3b413c6f,0x3c53bbf4,0xbc53bbf4,0x3c74,0x3c53bbf4,
0xbbfebc50,0xbbbd3c61,0x3c6ebb59,0xbc37bc21,0xbc4d3c04,0x3c6b3b84,0xbbd4bc5c,0xbbbd3c61,
0x3c093c49,0x3b11bc71,0xbc3f3c18,0x3c733a92,0x3c46bc0e,0xbc74b9c2,0x3c3f3c18,0xbb71bc6c,
0xbc13bc42,0x3ac23c73,0x3bd4bc5c,0xbc4d3c04,0xbc3f3c18,0x3c6ebb59,0xbc6ebb59,0x3c3f3c18,
0x3c1d3c3b,0xbba7bc65,0x3c74,0x3ba7bc65,0x3c37bc21,0xbc593bde,0x3c6ebb59,0xbc74b9c2,
0xbc26bc33,0x3c093c49,0xbbd4bc5c,0x3b903c69,0xbc2f3c2a,0x3c37bc21,0xbc3f3c18,0x3c46bc0e,
0x3be93c56,0x3c53bbf4,0xbbfebc50,0xbc4d3c04,0x3c13bc42,0xbc53bbf4,0xbbbd3c61,0x3c6b3b84,
0xbc6ebb59,0x3c74,0x3c6ebb59,0xbbd4bc5c,0x3b413c6f,0x3c53bbf4,0xbc37bc21,0xbbbd3c61,
0x3c46bc0e,0xbc53bbf4,0xbac23c73,0x3c67bb9b,0xbc53bbf4,0x3c74,0x3c53bbf4,0xbc53bbf4,
0xbac23c73,0x3c53bbf4,0xbc5ebbc9,0x3b413c6f,0x3c67bb9b,0xbc53bbf4,0x3b413c6f,0x3c13bc42,
0xbc26bc33,0x3c74,0x3c26bc33,0xbc733a92,0xbbbd3c61,0x3c53bbf4,0xbc72baf2,0x3c2f3c2a,
0x3c74b9c2,0xbc53bbf4,0x3be93c56,0x3a42bc73,0xbbd4bc5c,0x3c74,0x3bd4bc5c,0xbc3f3c18,
0xbc1d3c3b,0x3c53bbf4,0xbc703b29,0x3c703b29,0x3c6b3b84,0xbc53bbf4,0x3c2f3c2a,0xbbfebc50,
0xbb11bc71,0x3c74,0x3b11bc71,0xbb903c69,0xbc4d3c04,0x3c53bbf4,0xbc593bde,0x3c5ebbc9,
0x3c093c49,0x3c46bc0e,0xbc13bc42,0xbc3f3c18,0x3b11bc71,0xbc74b9c2,0x3ac23c73,0x3c6ebb59,
0xbc3f3c18,0x3c3f3c18,0x3bd4bc5c,0xbc6ebb59,0x3c733a92,0xbb71bc6c,0xbc4d3c04,0x3c3f3c18,
0xbc26bc33,0xbbbd3c61,0x3c74b9c2,0xbbd4bc5c,0x3c74,0x3c53bbf4,0xbc53bbf4,0x3c74,
0x3c26bc33,0xbc72baf2,0x3be93c56,0x3bd4bc5c,0xbc733a92,0x3c2f3c2a,0x3a42bc73,0xbc3f3c18,
0x3c3f3c18,0xbb11bc71,0xbc093c49,0x3c6ebb59,0xbb11bc71,0xbbe93c56,0x3c5ebbc9,0xbc6ebb59,
0xbc093c49,0x3c5ebbc9,0xbc72baf2,0x3c3f3c18,0x3c6ebb59,0xbc6ebb59,0x3c3f3c18,0xbbd4bc5c,
0xbc53bbf4,0x3c1d3c3b,0xbba7bc65,0x3c74,0x3b903c69,0xba42bc73,0xbb413c6f,0x3bd4bc5c,
0x3bd4bc5c,0xbc093c49,0x3c26bc33,0xbc3f3c18,0xbc633bb2,0x3c67bb9b,0xbc6b3b84,0x3c6ebb59,
0x3c1d3c3b,0x3c37bc21,0xbc26bc33,0xbc2f3c2a,0xbba7bc65,0xbc593bde,0x3c093c49,0x3c37bc21,
0x3c74,0x3c6ebb59,0xbbd4bc5c,0xbc3f3c18,0x3ba7bc65,0xbc74b9c2,0x3b903c69,0x3c46bc0e,
0xbc1d3c3b,0x3c6b3b84,0xbb11bc71,0xbc4d3c04,0x3c53bbf4,0xbc53bbf4,0x3c74,0x3c53bbf4,
0xbc703b29,0x3c2f3c2a,0x3b11bc71,0xbc593bde,0x3c703b29,0xbbfebc50,0xbb903c69,0x3c5ebbc9,
0xbc53bbf4,0x3b903c69,0x3bd4bc5c,0xbc633bb2,0x3c1d3c3b,0xba42bc73,0xbc093c49,0x3c67bb9b,
0xbba7bc65,0xbb413c6f,0x3c26bc33,0xbc6b3b84,0x3c74,0x3bd4bc5c,0xbc3f3c18,0x3c6ebb59,
0x3ba7bc65,0xbc1d3c3b,0x3c53bbf4,0xbc703b29,0xbc1d3c3b,0x3c46bc0e,0xbc633bb2,0x3c72baf2,
0x3c53bbf4,0xbc633bb2,0x3c6ebb59,0xbc733a92,0xbc703b29,0x3c72baf2,0xbc733a92,0x3c74b9c2,
0x3bf4,0xba42bc73,0x3ac23c73,0xbb11bc71,0x3bf4,0xbb11bc71,0x3b903c69,0xbbd4bc5c,
0x3bf4,0xbb71bc6c,0x3be93c56,0xbc26bc33,0x3bf4,0xbba7bc65,0x3c1d3c3b,0xbc53bbf4,
0x3bf4,0xbbd4bc5c,0x3c3f3c18,0xbc6ebb59,0x3bf4,0xbbfebc50,0x3c593bde,0xbc733a92,
0x3bf4,0xbc13bc42,0x3c6b3b84,0xbc633bb2,0x3bf4,0xbc26bc33,0x3c733a92,0xbc3f3c18,
0x3bf4,0xbc37bc21,0x3c72baf2,0xbc093c49,0x3bf4,0xbc46bc0e,0x3c67bb9b,0xbb903c69,
0x3bf4,0xbc53bbf4,0x3c53bbf4,0x3c74,0x3bf4,0xbc5ebbc9,0x3c37bc21,0x3b903c69,
0x3bf4,0xbc67bb9b,0x3c13bc42,0x3c093c49,0x3bf4,0xbc6ebb59,0x3bd4bc5c,0x3c3f3c18,
0x3bf4,0xbc72baf2,0x3b71bc6c,0x3c633bb2,0x3bf4,0xbc74b9c2,0x3a42bc73,0x3c733a92,
0x3b413c6f,0xbb71bc6c,0x3b903c69,0xbba7bc65,0x3c093c49,0xbc26bc33,0x3c3f3c18,0xbc53bbf4,
0x3c4d3c04,0xbc67bb9b,0x3c733a92,0xbc703b29,0x3c703b29,0xbc703b29,0x3c53bbf4,0xbc1d3c3b,
0x3c6ebb59,0xbc3f3c18,0x3bd4bc5c,0x3c74,0x3c46bc0e,0xbbbd3c61,0xbb11bc71,0x3c1d3c3b,
0x3bfebc50,0x3ac23c73,0xbc26bc33,0x3c703b29,0x3b11bc71,0x3c093c49,0xbc6ebb59,0x3c53bbf4,
0xbb71bc6c,0x3c593bde,0xbc633bb2,0x3ba7bc65,0xbc13bc42,0x3c74b9c2,0xbc093c49,0xbba7bc65,
0xbc53bbf4,0x3c53bbf4,0x3c74,0xbc53bbf4,0xbc72baf2,0x3bfebc50,0x3c093c49,0xbc703b29,
0xbc6b3b84,0x3a42bc73,0x3c633bb2,0xbc1d3c3b,0xbc3f3c18,0xbbd4bc5c,0x3c6ebb59,0x3c74,
0xbbe93c56,0xbc46bc0e,0x3c26bc33,0x3c1d3c3b,0xbac23c73,0xbc72baf2,0x3b11bc71,0x3c703b29,
0x3bbd3c61,0xbbd4bc5c,0x3be93c56,0xbbfebc50,0x3c633bb2,0xbc6ebb59,0x3c733a92,0xbc733a92,
0x3c5ebbc9,0xbc3f3c18,0x3c13bc42,0xbbbd3c61,0x3ba7bc65,0x3c74,0xbba7bc65,0x3c1d3c3b,
0xbbd4bc5c,0x3c3f3c18,0xbc6ebb59,0x3c6ebb59,0xbc67bb9b,0x3c6ebb59,0xbc2f3c2a,0x3b71bc6c,
0xbc593bde,0x3bd4bc5c,0x3b413c6f,0xbc37bc21,0xbb903c69,0xbbd4bc5c,0x3c633bb2,0xbc633bb2,
0x3be93c56,0xbc6ebb59,0x3c46bc0e,0xbac23c73,0x3c6b3b84,0xbc3f3c18,0xba42bc73,0x3c4d3c04,
0x3c53bbf4,0x3c74,0xbc53bbf4,0x3c53bbf4,0x3b71bc6c,0x3c3f3c18,0xbc593bde,0xba42bc73,
0xbbfebc50,0x3c6ebb59,0xbac23c73,0xbc5ebbc9,0xbc6ebb59,0x3bd4bc5c,0x3c3f3c18,0xbc3f3c18,
0xbc4d3c04,0xbbd4bc5c,0x3c67bb9b,0x3b413c6f,0xbb413c6f,0xbc6ebb59,0x3b71bc6c,0x3c6b3b84,
0x3c093c49,0xbc13bc42,0x3c1d3c3b,0xbc26bc33,0x3c6ebb59,0xbc633bb2,0x3c53bbf4,0xbc3f3c18,
0x3b11bc71,0x3ac23c73,0xbba7bc65,0x3c093c49,0xbc53bbf4,0x3c703b29,0xbc703b29,0x3c53bbf4,
0xbc3f3c18,0x3bd4bc5c,0x3c74,0xbbd4bc5c,0x3b903c69,0xbc37bc21,0x3c703b29,0xbc633bb2,
0x3c733a92,0xbc4d3c04,0x3ba7bc65,0x3b903c69,0x3bd4bc5c,0x3b903c69,0xbc53bbf4,0x3c6ebb59,
0xbc26bc33,0x3c74b9c2,0xbc1d3c3b,0xbb11bc71,0xbc633bb2,0x3b71bc6c,0x3c1d3c3b,0xbc733a92,
0x3c74,0xbc53bbf4,0x3c53bbf4,0x3c74,0x3c633bb2,0xbc2f3c2a,0xbba7bc65,0x3c733a92,
0x3c26bc33,0x3be93c56,0xbc703b29,0x3b11bc71,0xbbd4bc5c,0x3c6ebb59,0x3c74,0xbc6ebb59,
0xbc733a92,0x3a42bc73,0x3c703b29,0xbb903c69,0xbb903c69,0xbc67bb9b,0x3ba7bc65,0x3c633bb2,
0x3c2f3c2a,0xbc37bc21,0x3c3f3c18,0xbc46bc0e,0x3c26bc33,0xbc093c49,0x3bd4bc5c,0xbb903c69,
0xbc37bc21,0x3c593bde,0xbc6ebb59,0x3c74b9c2,0xbc1d3c3b,0x3ba7bc65,0x3c74,0xbba7bc65,
0x3c3f3c18,0xbc6ebb59,0x3c6ebb59,0xbc3f3c18,0x3c13bc42,0xbac23c73,0xbbd4bc5c,0x3c4d3c04,
0xbc46bc0e,0x3c74b9c2,0xbc3f3c18,0x3b71bc6c,0xbc093c49,0xbb11bc71,0x3c3f3c18,0xbc733a92,
0x3c4d3c04,0xbc6b3b84,0x3bd4bc5c,0x3bbd3c61,0x3bfebc50,0x3bbd3c61,0xbc6ebb59,0x3c37bc21,
0xbc53bbf4,0x3c53bbf4,0x3c74,0xbc53bbf4,0xbbe93c56,0xbc13bc42,0x3c6ebb59,0xbb413c6f,
0x3c593bde,0xbc2f3c2a,0xbbd4bc5c,0x3c72baf2,0x3bd4bc5c,0x3c3f3c18,0xbc3f3c18,0xbbd4bc5c,
0xbc5ebbc9,0x3bfebc50,0x3c3f3c18,0xbc2f3c2a,0xbbbd3c61,0xbc5ebbc9,0x3bd4bc5c,0x3c593bde,
0x3c4d3c04,0xbc53bbf4,0x3c593bde,0xbc5ebbc9,0x3b11bc71,0x3c74,0xbb11bc71,0x3b903c69,
0xbc6b3b84,0x3c53bbf4,0xbc2f3c2a,0x3bfebc50,0x3c1d3c3b,0xbc53bbf4,0x3c703b29,0xbc703b29,
0x3bd4bc5c,0x3c74,0xbbd4bc5c,0x3c3f3c18,0xbc74b9c2,0x3c53bbf4,0xbbe93c56,0xba42bc73,
0x3bbd3c61,0xbc53bbf4,0x3c72baf2,0xbc2f3c2a,0x3c26bc33,0x3c74,0xbc26bc33,0x3c733a92,
0xbc67bb9b,0x3c53bbf4,0xbb413c6f,0xbc13bc42,0x3ac23c73,0xbc53bbf4,0x3c5ebbc9,0xbb413c6f,
0x3c53bbf4,0x3c74,0xbc53bbf4,0x3c53bbf4,0xbc46bc0e,0x3c53bbf4,0x3ac23c73,0xbc67bb9b,
0xbb413c6f,0xbc53bbf4,0x3c37bc21,0x3bbd3c61,0x3c6ebb59,0x3c74,0xbc6ebb59,0x3bd4bc5c,
0xbc13bc42,0x3c53bbf4,0x3bbd3c61,0xbc6b3b84,0xbbe93c56,0xbc53bbf4,0x3bfebc50,0x3c4d3c04,
0x3c633bb2,0xbc67bb9b,0x3c6b3b84,0xbc6ebb59,0xbbd4bc5c,0x3c093c49,0xbc26bc33,0x3c3f3c18,
0xbb903c69,0x3a42bc73,0x3b413c6f,0xbbd4bc5c,0x3c53bbf4,0xbc1d3c3b,0x3ba7bc65,0x3c74,
0xbc6ebb59,0x3c6ebb59,0xbc3f3c18,0x3bd4bc5c,0x3c093c49,0xbc5ebbc9,0x3c72baf2,0xbc3f3c18,
0x3b11bc71,0x3be93c56,0xbc5ebbc9,0x3c6ebb59,0xbc3f3c18,0x3b11bc71,0x3c093c49,0xbc6ebb59,
0x3c733a92,0xbc2f3c2a,0xba42bc73,0x3c3f3c18,0xbc26bc33,0x3c72baf2,0xbbe93c56,0xbbd4bc5c,
0x3c74,0xbc53bbf4,0x3c53bbf4,0x3c74,0x3c26bc33,0x3bbd3c61,0xbc74b9c2,0x3bd4bc5c,
0xbc733a92,0x3b71bc6c,0x3c4d3c04,0xbc3f3c18,0x3c3f3c18,0xbc3f3c18,0xbbd4bc5c,0x3c6ebb59,
0xbb11bc71,0x3c74b9c2,0xbac23c73,0xbc6ebb59,0xbc093c49,0xbc46bc0e,0x3c13bc42,0x3c3f3c18,
0x3c703b29,0xbc72baf2,0x3c733a92,0xbc74b9c2,0xbc53bbf4,0x3c633bb2,0xbc6ebb59,0x3c733a92,
0x3c1d3c3b,0xbc46bc0e,0x3c633bb2,0xbc72baf2,0xbba7bc65,0x3c1d3c3b,0xbc53bbf4,0x3c703b29,
0x3c74,0xbbd4bc5c,0x3c3f3c18,0xbc6ebb59,0x3ba7bc65,0x3b413c6f,0xbc26bc33,0x3c6b3b84,
0xbc1d3c3b,0x3a42bc73,0x3c093c49,0xbc67bb9b,0x3c53bbf4,0xbb903c69,0xbbd4bc5c,0x3c633bb2,
0xbc703b29,0x3bfebc50,0x3b903c69,0xbc5ebbc9,0x3c703b29,0xbc2f3c2a,0xbb11bc71,0x3c593bde,
0xbc53bbf4,0x3c53bbf4,0x3c74,0xbc53bbf4,0x3c1d3c3b,0xbc6b3b84,0x3b11bc71,0x3c4d3c04,
0xbba7bc65,0x3c74b9c2,0xbb903c69,0xbc46bc0e,0x3c74,0xbc6ebb59,0x3bd4bc5c,0x3c3f3c18,
0x3ba7bc65,0x3c593bde,0xbc093c49,0xbc37bc21,0xbc1d3c3b,0xbc37bc21,0x3c26bc33,0x3c2f3c2a,
0x3f803f80,0x3f803f80,0x3f803f80,0x3f803f80,0x3f7f3f80,0x3f753f7b,0x3f613f6c,0x3f443f54,
0x3f7b3f80,0x3f543f6c,0x3f0b3f33,0x3e323ebb,0x3f753f80,0x3f203f54,0x3d993ebb,0xbf00be64,
0x3f6c3f80,0x3ebb3f33,0xbed3bccc,0xbf71bf3c,0x3f613f80,0x3d993f0b,0xbf4cbed3,0xbf71bf7d,
0x3f543f80,0xbe643ebb,0xbf7dbf3c,0xbf00bf67,0x3f443f80,0xbf003e32,0xbf71bf71,0x3e32bf00,
0x3f333f80,0xbf3cbccc,0xbf29bf80,0x3f443d99,0x3f203f80,0xbf67be64,0xbe64bf67,0x3f803f20,
0x3f0b3f80,0xbf7dbed3,0x3e8bbf29,0x3f443f75,0x3eea3f80,0xbf7dbf15,0x3f33bea3,0x3e323f75,
0x3ebb3f80,0xbf67bf3c,0x3f753d99,0xbf003f20,0x3e8b3f80,0xbf3cbf5a,0x3f7b3eea,0xbf713d99,
0x3e323f80,0xbf00bf71,0x3f443f44,0xbf71bf00,0x3d993f80,0xbe64bf7d,0x3ebb3f75,0xbf00bf67,
0x3f803f80,0x3f803f80,0x3f803f80,0x3f803f80,0x3f203f33,0x3eea3f0b,0x3e8b3ebb,0x3d993e32,
0xbe64bccc,0xbf15bed3,0xbf5abf3c,0xbf7dbf71,0xbf67bf3c,0xbf7dbf7d,0xbf3cbf67,0xbe64bf00,
0xbf67bf80,0xbea3bf29,0x3eea3d99,0x3f753f44,0xbe64bf29,0x3f333e8b,0x3f7b3f75,0x3ebb3f44,
0x3f203d99,0x3f753f75,0x3d993f20,0xbf67bf00,0x3f803f44,0x3e323f44,0xbf71bf00,0xbf00bf71,
0x3f203f7f,0xbf4cbdff,0xbf15bf7d,0x3f543e32,0xbe643f20,0xbf67bf67,0x3f20be64,0x3f203f80,
0xbf67bdff,0xbcccbf5a,0x3f6c3f54,0xbf3c3e32,0xbf67bf4c,0x3f61bccc,0xbdff3f54,0xbf3cbf71,
0xbe64bf7d,0x3f543f54,0xbf7dbe64,0x3f20bf00,0x3f20bf15,0xbdff3f6c,0xbed3bf7d,0x3f543f44,
0x3f803e32,0xbf713e32,0x3f44bf00,0xbf003f44,0x3f203f54,0xbf3cbf3c,0x3f543f20,0xbf67bf00,
0x0,0x0,0x0,0x0,0xba8b0000,0xbabfb99b,0xba33b9d1,0x39dcbaf7,
0xb99b0000,0xbaf7b9d1,0xb9deba81,0xb93d38e0,0xbabf0000,0xbac6baf7,0x37c138e0,0xa580390e,
0xb9d10000,0x38e0ba81,0x3a58b7ee,0x3ae13aad,0xba330000,0x37c1b9de,0xb9873a58,0x3ae1ba10,
0xbaf70000,0x390e38e0,0xba103aad,0x25e03ab4,0x39dc0000,0xa580b93d,0x3ae13ae1,0xb93d25e0,
0xba810000,0x3aadb7ee,0xbac83aa3,0x39dc37c1,0xbac60000,0x3ab4390e,0x390e3ab4,0xbac6,
0xb9de0000,0xba103a58,0xba29bac8,0x39dcbabf,0xba570000,0xba10bae0,0xba81b905,0xb93dbabf,
0x38e00000,0x3ab43aad,0xbabf37c1,0xa580bac6,0xba290000,0x3aadbae3,0xb99bba57,0x3ae137c1,
0xb93d0000,0x25e03ae1,0x39dc39dc,0x3ae1a580,0x37c10000,0x390eba10,0x38e0babf,0x25e03ab4,
0x0,0x0,0x0,0x0,0xbac6ba81,0xba57b9de,0xba2938e0,0x37c1b93d,
0x390eb7ee,0xbae03a58,0xbae33aad,0xba103ae1,0x3ab43aad,0xba10ba10,0x3aad3ab4,0x390e25e0,
0x3ab43aa3,0xb905bac8,0xba5737c1,0xbabf39dc,0x390ebac8,0xba81ba29,0xb99bbabf,0x38e039dc,
0xbac637c1,0xbabfbabf,0x37c1bac6,0x3ab4a580,0x39dc,0xb93d39dc,0x3ae1a580,0x25e03ae1,
0xbac6ba8b,0xb9873930,0xbae0ba10,0xbaf7b93d,0x390ebac6,0x3ab43ab4,0xbac6390e,0xbac60000,
0x3ab43930,0xb7eebae3,0xb9d1baf7,0x3aadb93d,0x3ab4b987,0xba33b7ee,0x3930baf7,0x3aad3ae1,
0x390eba10,0xbaf7baf7,0xba10390e,0xbac625e0,0xbac6bae0,0x3930b9d1,0x3a58ba10,0xbaf739dc,
0xb93d,0x3ae1b93d,0x39dc25e0,0xa58039dc,0xbac6baf7,0x3aad3aad,0xbaf7bac6,0x3ab4a580,
0xbccc3f80,0x3d99bf80,0xbdff3f7f,0x3e32bf7d,0xbdff3f80,0x3ebbbf78,0xbf153f61,0x3f44bf3c,
0xbe643f80,0x3f20bf67,0xbf673f20,0x3f80be64,0xbea33f80,0x3f54bf4c,0xbf803e8b,0x3f443ebb,
0xbed33f80,0x3f75bf29,0xbf5abdff,0x3e323f54,0xbf003f80,0x3f80bf00,0xbf00bf00,0xbf003f80,
0xbf153f80,0x3f75bea3,0xbcccbf4c,0xbf713f54,0xbf293f80,0x3f54bdff,0x3eeabf78,0xbf713ebb,
0xbf3c3f80,0x3f203d99,0x3f54bf7d,0xbf00be64,0xbf4c3f80,0x3ebb3e8b,0x3f7fbf5a,0x3e32bf3c,
0xbf5a3f80,0x3d993eea,0x3f6cbf15,0x3f44bf7d,0xbf673f80,0xbe643f20,0x3f20be64,0x3f80bf67,
0xbf713f80,0xbf003f44,0x3e323e32,0x3f44bf00,0xbf783f80,0xbf3c3f61,0xbea33f0b,0x3e323d99,
0xbf7d3f80,0xbf673f75,0xbf3c3f54,0xbf003f20,0xbf803f80,0xbf7d3f7f,0xbf783f7b,0xbf713f75,
0xbe643f7b,0x3e8bbf78,0xbea33f75,0x3ebbbf71,0xbf673f0b,0x3f7bbea3,0xbf803d99,0x3f753e32,
0xbf67be64,0x3f203f20,0xbe64bf67,0xbe643f80,0xbe64bf5a,0xbed33f7f,0x3f61bf3c,0xbf7d3e32,
0x3f20bf78,0xbf803eea,0x3f333ebb,0x3d99bf71,0x3f80bf00,0xbf00bf00,0xbf003f80,0x3f80bf00,
0x3f203e8b,0x3f0bbf80,0xbf783ebb,0x3d993f44,0xbe643f61,0x3f7fbf15,0xbcccbf3c,0xbf7d3f44,
0xbf673f75,0x3ebb3ebb,0x3f75bf67,0xbe64bf00,0xbf673eea,0xbf293f7b,0x3f0b3d99,0x3f75bf71,
0xbe64bea3,0xbf783f33,0xbf293f75,0x3ebb3e32,0x3f20bf67,0xbe64be64,0xbf673f20,0xbf673f80,
0x3f80bf71,0x3f44bf71,0x3e32bf00,0xbf003e32,0x3f20bed3,0x3f6cbf4c,0x3f7fbf7d,0x3f54bf71,
0xbe643ebb,0x3d993d99,0x3ebbbe64,0x3f20bf00,0xbf673f6c,0xbf5a3f61,0xbf4c3f54,0xbf3c3f44,
0xb7ee0000,0x37c13aa3,0x3930ba8b,0xb93dba10,0x39300000,0x38e0b9ac,0xbae0ba33,0x39dc3aad,
0x390e0000,0xbac63ab4,0x3ab4bac6,0x390e,0xb9050000,0xbaf7b987,0x3aa3ba29,0x39dc38e0,
0x3a580000,0xbabfbac8,0xbae33930,0xb93dbaf7,0xa5800000,0x25e0,0x25e0a580,0xa5800000,
0xbae00000,0xbabfb905,0xb7eeb987,0x3ae1baf7,0xbac80000,0xbaf73930,0xba57b9ac,0x3ae138e0,
0x3aad0000,0xbac637c1,0xbaf7ba10,0x25e0390e,0xb9870000,0x38e0ba29,0xba8bbae3,0xb93d3aad,
0xbae30000,0x37c1ba57,0xb9d1bae0,0x39dcba10,0x3ab40000,0x390ebac6,0xbac6390e,0x3ab4,
0x3ae10000,0xa58039dc,0xb93db93d,0x39dc25e0,0xb9ac0000,0x3aadba33,0xb905b9de,0xb93d37c1,
0xba100000,0x3ab4babf,0x3aadbaf7,0xa580bac6,0x3aa30000,0xba10ba8b,0xb9acb99b,0x3ae1babf,
0x390eb99b,0xba29b9ac,0xb905babf,0x38e03ae1,0x3ab4b9de,0xb99bb905,0x3aa337c1,0xbabfb93d,
0x3ab4390e,0xbac6bac6,0x390e3ab4,0x390e0000,0x390ebae3,0x3a58ba8b,0xba333aad,0xba10b93d,
0xbac6b9ac,0x3aa3ba57,0xba8138e0,0x37c13ae1,0x25e0,0x25e0a580,0xa5800000,0x25e0,
0xbac6ba29,0xb9de3aa3,0xb9ac38e0,0x37c139dc,0x390eba33,0xba8bbae0,0xb7ee3aad,0xba1039dc,
0x3ab4babf,0x38e038e0,0xbabf3ab4,0x390ea580,0x3ab4ba57,0xbac8b99b,0xb9de37c1,0xbabf3ae1,
0x390eb905,0xb9acba81,0xbac8babf,0x38e0b93d,0xbac63ab4,0x390e390e,0x3ab4bac6,0x3ab40000,
0x3ae1,0x39dc3ae1,0xb93da580,0x25e0b93d,0xbac63a58,0xb9d1b987,0xba8bba10,0xbaf73ae1,
0x390e38e0,0x37c137c1,0x38e0390e,0xbac625e0,0x3ab4b9d1,0xbae3ba33,0xb987baf7,0x3aad39dc,
0x0,0x0,0x0,0x0,0x3dcc0000,0x3e973e4b,0x3ef53ec7,0x3f253f10,
0x3e4b0000,0x3f103ec7,0x3f573f37,0x3f7c3f6e,0x3e970000,0x3f483f10,0x3f7f3f6e,0x3f5e3f7a,
0x3ec70000,0x3f6e3f37,0x3f693f80,0x3eaf3f2e,0x3ef50000,0x3f7f3f57,0x3f1b3f69,0xbeaf3e19,
0x3f100000,0x3f7a3f6e,0x3e193f2e,0xbf5ebede,0x3f250000,0x3f5e3f7c,0xbeaf3eaf,0xbf7cbf5e,
0x3f370000,0x3f2e3f80,0xbf40bd4c,0xbf25bf7f,0x3f480000,0x3ede3f7a,0xbf7abede,0xbf48,
0x3f570000,0x3e193f69,0xbf76bf40,0x3f25be97,0x3f640000,0xbe193f50,0xbf37bf73,0x3f7c3e97,
0x3f6e0000,0xbede3f2e,0xbe97bf7f,0x3f5e3f48,0x3f760000,0xbf2e3f05,0x3e4bbf64,0x3eaf3f7f,
0x3f7c0000,0xbf5e3eaf,0x3f25bf25,0xbeaf3f5e,0x3f7f0000,0xbf7a3e19,0x3f6ebe97,0xbf5e3ede,
0x0,0x0,0x0,0x0,0x3f483f37,0x3f643f57,0x3f763f6e,0x3f7f3f7c,
0x3f7a3f80,0x3f503f69,0x3f053f2e,0x3e193eaf,0x3ede3f2e,0xbe193e19,0xbf2ebede,0xbf7abf5e,
0xbedebd4c,0xbf73bf40,0xbf64bf7f,0xbe97bf25,0xbf7abf40,0xbf37bf76,0x3e4bbe97,0x3f6e3f25,
0xbf48bf7f,0x3e97be97,0x3f7f3f48,0x3ede3f5e,0xbf25,0x3f7c3f25,0x3eaf3f5e,0xbf5ebeaf,
0x3f483dcc,0x3f1b3f7e,0xbf50be19,0xbf10bf7c,0x3f7a3f48,0xbede3ede,0xbf48bf7a,0x3f480000,
0x3ede3f7e,0xbf80bf05,0x3ec7bf10,0x3f2e3f7c,0xbede3f1b,0xbef5bf80,0x3f7e3f10,0xbf2e3eaf,
0xbf7abe19,0x3f10bf10,0x3e193f7a,0xbf48bf5e,0xbf48bf50,0x3f7e3ec7,0xbf693e19,0x3f10bf25,
0xbf7c,0x3eaf3f7c,0xbf25bf5e,0x3f5e3f25,0x3f48bf10,0xbf2e3f2e,0x3f10bf48,0xbede3f5e,
0x0,0x0,0x0,0x0,0xb82e0000,0xb92fb8c9,0xb989b979,0xbae53a57,
0xb8c90000,0x3a57b979,0x393f3a86,0x39e33a9b,0xb92f0000,0x3a183a57,0x3a913a9b,0xba98bad6,
0xb9790000,0x3a9b3a86,0x3ab1b9a3,0x396a39fe,0xb9890000,0x3a91393f,0xbada3ab1,0xb96ab9c3,
0x3a570000,0xbad63a9b,0xb9c339fe,0x3a98b998,0xbae50000,0xba9839e3,0xb96a396a,0xb9e33a98,
0x3a860000,0x39feb9a3,0x3965b82d,0x3ae5ba91,0x3a180000,0x3998bad6,0x3ad6b998,0xba18,
0x393f0000,0xb9c33ab1,0xbadd3965,0xbae5392f,0xba450000,0x39c3ba13,0xba863aa9,0x39e3b92f,
0x3a9b0000,0xb99839fe,0x392fba91,0xba983a18,0x3add0000,0xb9fe3afa,0xb8c93a45,0x396a3a91,
0x39e30000,0x3a98396a,0xbae53ae5,0xb96aba98,0x3a910000,0x3ad6b9c3,0x3a9b392f,0x3a983998,
0x0,0x0,0x0,0x0,0x3a183a86,0xba45393f,0x3add3a9b,0x3a9139e3,
0xbad6b9a3,0xba133ab1,0x3afa39fe,0xb9c3396a,0x399839fe,0x39c3b9c3,0xb9feb998,0x3ad63a98,
0xb998b82d,0x3aa93965,0x3a45ba91,0x392f3ae5,0x3ad63965,0xba86badd,0xb8c9392f,0x3a9bbae5,
0xba18ba91,0xb92f392f,0x3a913a18,0x3998ba98,0x3ae5,0x39e3bae5,0x396aba98,0x3a98b96a,
0x3a18b82e,0xbada3859,0x3a1339c3,0xba57b9e3,0xbad63a18,0xb9983998,0xba183ad6,0x3a180000,
0x39983859,0x39a3bafa,0xb979ba57,0x39fe39e3,0xb998bada,0x398939a3,0x38593a57,0xb9fe396a,
0x3ad639c3,0x3a57ba57,0xb9c3bad6,0xba183a98,0xba183a13,0x3859b979,0xbab1b9c3,0x3a573ae5,
0xb9e3,0x396a39e3,0x3ae53a98,0xba98bae5,0x3a18ba57,0xb9fe39fe,0x3a57ba18,0xb998ba98,
0x3f800000,0xbf7fbd4c,0x3f7e3dcc,0xbf7cbe19,0x3f7e0000,0xbf6ebe7d,0x3f503ef5,0xbf25bf2e,
0x3f7a0000,0xbf48bede,0x3ede3f48,0xbf7a,0x3f730000,0xbf10bf1b,0xbd4c3f76,0x3f25bf6e,
0x3f690000,0xbe97bf40,0xbf053f7e,0x3f7cbf10,0x3f5e0000,0xbf5e,0xbf5e3f5e,0x3f5e0000,
0x3f500000,0x3e97bf73,0xbf803f1b,0x3eaf3f10,0x3f400000,0x3f10bf7e,0xbf643e7d,0xbeaf3f6e,
0x3f2e0000,0x3f48bf7f,0xbf10be19,0xbf5e3f7a,0x3f1b0000,0x3f6ebf76,0xbdccbf05,0xbf7c3f2e,
0x3f050000,0x3f7fbf64,0x3ec7bf50,0xbf253e19,0x3ede0000,0x3f7abf48,0x3f48bf7a,0xbede,
0x3eaf0000,0x3f5ebf25,0x3f7cbf7c,0x3f25bf5e,0x3e7d0000,0x3f2ebef5,0x3f73bf57,0x3f7cbf7f,
0x3e190000,0x3edebe97,0x3f2ebf10,0x3f5ebf48,0x3d4c0000,0x3e19bdcc,0x3e7dbe4b,0x3eafbe97,
0x3f7a3e4b,0xbf76be7d,0x3f733e97,0xbf6ebeaf,0x3ede3f57,0xbe4bbf73,0xbd4c3f7f,0x3e97bf7c,
0xbede3f7a,0x3f48bf48,0xbf7a3ede,0x3f7a0000,0xbf7a3f05,0x3f693dcc,0xbef5bf2e,0xbe193f7c,
0xbf48be7d,0x3d4c3f64,0x3f37bf6e,0xbf7f3eaf,0xbf5e,0xbf5e3f5e,0x3f5e0000,0xbf5e,
0x3f48bf76,0xbf573d4c,0xbe7d3f6e,0x3f7fbf25,0x3f7abef5,0x3dccbf50,0xbf803f2e,0x3e193f25,
0x3ede3e97,0x3f6ebf6e,0xbe97bede,0xbf7a3f5e,0xbede3f64,0x3f40be4b,0x3f57bf7f,0xbe97beaf,
0xbf7a3f73,0xbe7d3f37,0x3f40be97,0x3f6ebf7c,0xbf483ede,0xbf7a3f7a,0xbede3f48,0x3ede0000,
0xbeaf,0xbf253eaf,0xbf7c3f5e,0xbf5e3f7c,0x3f48bf69,0x3ec7bf1b,0xbdccbe19,0xbf103eaf,
0x3f7abf6e,0x3f7fbf7f,0x3f6ebf7a,0x3f48bf5e,0x3edebec7,0x3f05bef5,0x3f1bbf10,0x3f2ebf25,
0xb9a30000,0xba91b82d,0x3859b82e,0xb9e339c3,0x38590000,0xba9b39a4,0xba13b989,0x3ae5b9fe,
0xbad60000,0xba18b998,0x39983a18,0x3ad6,0xbaa90000,0xba573ada,0xb82d3add,0xbae5ba9b,
0x3ab10000,0x392f3965,0xbafa3859,0x39e3ba57,0xba980000,0x3a98,0x3a98ba98,0xba980000,
0xba130000,0xb92f3aa9,0x39a3bada,0x396a3a57,0xb9650000,0x3a57b859,0x3a45b9a4,0xb96a3a9b,
0x39fe0000,0x3a18ba91,0xba5739c3,0x3a98bad6,0xbada0000,0x3a9bbadd,0x382ebafa,0xb9e339fe,
0x3afa0000,0x3a913a45,0xb9793a13,0x3ae5b9c3,0x39980000,0xbad6ba18,0x3a183ad6,0xb998,
0x396a0000,0xba983ae5,0x39e3b9e3,0xbae53a98,0xb9a40000,0x39fe3989,0xbaa9b93f,0x39e3ba91,
0xb9c30000,0x3998392f,0x39feba57,0xba98ba18,0x382d0000,0xb9c3382e,0xb9a438c9,0x396a392f,
0xbad6b8c9,0xbadd39a4,0xbaa9b92f,0xba9bb96a,0x3998393f,0x38c93aa9,0xb82d3a91,0xb92fb9e3,
0xb998bad6,0x3a18ba18,0x3ad63998,0xbad60000,0x3ad63afa,0x3ab1b82e,0x3989b9fe,0x39c339e3,
0xba1839a4,0x382dba45,0x3a86ba9b,0xba91396a,0x3a98,0x3a98ba98,0xba980000,0x3a98,
0x3a18badd,0xb93f382d,0x39a43a9b,0x3a913ae5,0xbad63989,0xb82e3a13,0x39a339fe,0xb9c3bae5,
0x3998b92f,0x3a9bba9b,0x392fb998,0x3ad6ba98,0xb998ba45,0xb96538c9,0x393fba91,0x392fb96a,
0x3ad6baa9,0x39a43a86,0xb965392f,0x3a9bb9e3,0xba183998,0x3ad6bad6,0xb9983a18,0x39980000,
0xb96a,0x3ae5396a,0xb9e3ba98,0x3a9839e3,0x3a18bab1,0xb9793ada,0x382e39c3,0xba57396a,
0xbad6ba9b,0x3a91ba91,0x3a9b3ad6,0x3a183a98,0x39983979,0x3afa3989,0xbadaba57,0x39fe3ae5,
};

typedef unsigned short u16;
typedef unsigned long long u64;
typedef __attribute__((ext_vector_type(8))) short bf16x8;
typedef __attribute__((ext_vector_type(4))) short s16x4;
typedef __attribute__((ext_vector_type(4))) float f32x4;
typedef __attribute__((ext_vector_type(2))) float f32x2;
typedef __attribute__((ext_vector_type(4))) unsigned u32x4;
typedef __attribute__((ext_vector_type(2))) __bf16 bf16x2_t;
typedef __attribute__((address_space(3))) s16x4* lds_s16x4_p;
typedef __attribute__((address_space(3))) void* lds_void_p;
#define DEVINL __device__ __forceinline__

#define STAMP(i)
#define STAMP_FLUSH()
#define STAMP2(a, b) do { if (s == 0) { STAMP(a); } else { STAMP(b); } } while (0)
typedef unsigned stv_t;

#define IN_POL ""
#define ST_POL "sc1"
#define MFMA16(a, b, c) __builtin_amdgcn_mfma_f32_16x16x32_bf16((a), (b), (c), 0, 0, 0)
#define MFMAU(a, b, c) MFMA16(__builtin_bit_cast(bf16x8, a), __builtin_bit_cast(bf16x8, b), c)
#define SB0() __builtin_amdgcn_sched_barrier(0)
constexpr unsigned OOB = 0x7ffffff0u;
constexpr int SC1 = 16;

template <int N> struct IC { static constexpr int v = N; };
template <int... I, class F> DEVINL void static_for_impl(std::integer_sequence<int, I...>, F&& f) { (f(IC<I>{}), ...); }
template <int N, class F> DEVINL void static_for(F&& f) { static_for_impl(std::make_integer_sequence<int, N>{}, f); }
DEVINL void bar() { asm volatile("" ::: "memory"); __builtin_amdgcn_s_barrier(); asm volatile("" ::: "memory"); }
template <int N> DEVINL void wait_vm() { asm volatile("s_waitcnt vmcnt(%0)" ::"n"(N) : "memory"); }
DEVINL void wait_lds() { __builtin_amdgcn_s_waitcnt(0xc07f); }

constexpr int T0 = 16000, T2 = 4000;
constexpr int NT1 = 167;
constexpr int XST = 32768;
constexpr size_t X1_TB = 16 * XST, X1_G = 2 * X1_TB, X1_S = 16 * X1_G;
constexpr size_t X2_TB = 8 * XST, X2_G = 2 * X2_TB, X2_S = 16 * X2_G;
constexpr size_t WF_G = 1 << 20, WF_BYTES = 16 * WF_G;
constexpr size_t Y1_S = (size_t)128 * 65536, Y2_S = (size_t)64 * 65536;
constexpr size_t OFF_FLAG = 0;
constexpr int F_X = 0, F_W1 = 768, F_W2 = 1024, F_G1 = 1280, F_M = 1792, F_G2 = 2304, F_S = 2816, F_EPOCH = 2840, F_XH = 512, F_W1H = 2848, F_END = 3104;
constexpr size_t OFF_PROBE = 1 << 20;
constexpr size_t OFF_WFT = OFF_FLAG + 4096 * 128;
constexpr size_t OFF_WOT = OFF_WFT + 128 * 1024;
constexpr size_t OFF_GE = OFF_WOT + 128 * 1024;
constexpr size_t OFF_X1F = 4 << 20;
constexpr size_t OFF_W1F = OFF_X1F + 2 * X1_S;
constexpr size_t OFF_Y1F = OFF_W1F + WF_BYTES;
constexpr size_t OFF_X2F = OFF_Y1F + 2 * Y1_S;
constexpr size_t OFF_W2F = OFF_X2F + 2 * X2_S;
constexpr size_t OFF_Y2F = OFF_W2F + WF_BYTES;
constexpr size_t WS_USED = OFF_Y2F + 2 * Y2_S;
constexpr int TAB_F = 0, TAB_G = 8, TAB_T = 14;
constexpr int LDS_BYTES = 81920;

DEVINL unsigned pack_bf16(float a, float b) {
  f32x2 v = {a, b};
  bf16x2_t r = __builtin_convertvector(v, bf16x2_t);
  return __builtin_bit_cast(unsigned, r);
}
DEVINL u32x4 pack8(const f32x4& a, const f32x4& b) {
  return (u32x4){pack_bf16(a[0], a[1]), pack_bf16(a[2], a[3]), pack_bf16(b[0], b[1]), pack_bf16(b[2], b[3])};
}
DEVINL s16x4 tr_read(const char* p) { return __builtin_amdgcn_ds_read_tr16_b64_v4i16((lds_s16x4_p)(p)); }
DEVINL bf16x8 cat8(s16x4 a, s16x4 b) { return __builtin_shufflevector(a, b, 0, 1, 2, 3, 4, 5, 6, 7); }
DEVINL bf16x8 tr_frag(const char* p, int second) { return cat8(tr_read(p), tr_read(p + second)); }
template <int AUX> DEVINL void dma16(__amdgpu_buffer_rsrc_t rs, char* lds_dst, unsigned voff, unsigned soff) {
  const unsigned m = (unsigned)(size_t)(lds_void_p)lds_dst;
  if constexpr (AUX == 0) asm volatile("s_mov_b32 m0, %0\n\ts_nop 0\n\tbuffer_load_dwordx4 %1, %2, %3 offen " IN_POL " lds" :: "s"(m), "v"(voff), "s"(rs), "s"(soff) : "memory");
  else asm volatile("s_mov_b32 m0, %0\n\ts_nop 0\n\tbuffer_load_dwordx4 %1, %2, %3 offen sc1 lds" :: "s"(m), "v"(voff), "s"(rs), "s"(soff) : "memory");
}
DEVINL __amdgpu_buffer_rsrc_t mkrsrc(const void* p, unsigned bytes) { return __builtin_amdgcn_make_buffer_rsrc((void*)p, 0, bytes, 0x00020000); }
DEVINL void st16(void* p, u32x4 v) { asm volatile("global_store_dwordx4 %0, %1, off " ST_POL "\n\ts_nop 1" ::"v"(p), "v"(v) : "memory"); }
template <int I, bool DEV> DEVINL void ldfrag(u32x4& d, const char* base, unsigned lane16) {
  const char* b = base + (I >> 3) * 8192 + 4096;
  if constexpr (DEV) asm volatile("global_load_dwordx4 %0, %1, %2 offset:%3 sc1" : "=v"(d) : "v"(lane16), "s"(b), "n"((I & 7) * 1024 - 4096) : "memory");
  else asm volatile("global_load_dwordx4 %0, %1, %2 offset:%3" : "=v"(d) : "v"(lane16), "s"(b), "n"((I & 7) * 1024 - 4096) : "memory");
}
template <int I, int BS = 16384> DEVINL void ldfrag_rot(u32x4& d, const char* base, int rot, unsigned lane16) {
  ldfrag<I & 7, true>(d, base + (((I >> 3) + rot) & 3) * BS, lane16);
}
template <int I> DEVINL void ldfrag_rot1(u32x4& d, const char* base, int rot, unsigned lane16) {
  const int b = ((I >> 2) + rot) & 3;
  ldfrag<I & 3, true>(d, base + (I >> 4) * 32768 + (b >> 1) * 16384 + (b & 1) * 4096, lane16);
}
DEVINL void tie(u32x4& a) { asm volatile("" : "+v"(a)); }
template <int N> DEVINL void tie(u32x4 (&a)[N]) {
#pragma unroll
  for (int i = 0; i < N; ++i) tie(a[i]);
}
DEVINL int lane_id() { int l; asm volatile("v_mbcnt_lo_u32_b32 %0, -1, 0\n\tv_mbcnt_hi_u32_b32 %0, -1, %0" : "=v"(l)); return l; }
DEVINL void sgpr_settle() { asm volatile("s_nop 4" ::: "memory"); }

#define CT_STEP 64
DEVINL void code_touch(unsigned& d, int slice) {
  u64 pc;
  unsigned len, off = slice * CT_STEP;
  asm volatile("s_getpc_b64 %0\n.Lpc%=:\n\ts_mov_b32 %1, .Lpc%=-k_all" : "=s"(pc), "=s"(len));
  pc -= len;
  asm volatile("s_mov_b32 %1, .Lk_end-k_all\n\ts_max_u32 %1, %1, .Lt_end-k_all\n"
               "1:\n\ts_load_dword %0, %3, %2\n\ts_add_u32 %2, %2, %4\n\ts_cmp_lt_u32 %2, %1\n\ts_cbranch_scc1 1b"
               : "=&s"(d), "=&s"(len), "+s"(off) : "s"(pc), "n"(128 * CT_STEP) : "scc", "memory");
}
DEVINL void tie(unsigned& a) { asm volatile("" : "+v"(a)); }

#define PRIO(x) __builtin_amdgcn_s_setprio(x)
#define PRIO_JB0 0
#define PRIO_JAL PRIO_JA
#define PRIO_JBL PRIO_JB
#define PRIO_F 3
#define PRIO_G0 2
#define PRIO_GW 0
#define PRIO_G1 0
#define PRIO_G2A 0
#define PRIO_G2B 0
#define PRIO_JB 0
#define PRIO_T0 1
#define PRIO_T1 1
#define PRIO_T2 0
#define PRIO_MA 0
#define PRIO_MB 1
#define PRIO_JA 0
#define FS 128
#define FLAG_LANES (FS >= 64 ? 8 : FS / 8)
DEVINL void set_flag(char* ws, int flag, u64 tag, int w) {
  u64 d;
  unsigned o;
#if FLAG_LANES > 1
  asm volatile("s_cmp_lg_u32 %2, 0\n\ts_cbranch_scc1 1f\n\ts_bfm_b64 exec, %6, 0\n\ts_nop 1\n\tv_mbcnt_lo_u32_b32 %1, -1, 0\n\tv_mov_b64 %0, %5\n\tv_lshl_add_u32 %1, %1, 3, %3\n\t"
               "global_store_dwordx2 %1, %0, %4 sc1\n\ts_nop 1\n\ts_mov_b64 exec, -1\n1:"
               : "=&v"(d), "=&v"(o) : "s"(w), "s"(flag * FS), "s"(ws + OFF_FLAG), "s"(tag), "n"(FLAG_LANES) : "scc", "memory");
  return;
#endif
  asm volatile("s_cmp_lg_u32 %2, 0\n\ts_cbranch_scc1 1f\n\ts_mov_b64 exec, 1\n\ts_nop 1\n\tv_mov_b32 %1, %3\n\tv_mov_b64 %0, %5\n\t"
               "global_store_dwordx2 %1, %0, %4 sc1\n\ts_nop 1\n\ts_mov_b64 exec, -1\n1:"
               : "=&v"(d), "=&v"(o) : "s"(w), "s"(flag * FS), "s"(ws + OFF_FLAG), "s"(tag) : "scc", "memory");
}
template <int N = 0> DEVINL void publish(char* ws, int flag, u64 tag, int w) { wait_vm<N>(); PRIO(PRIO_F); bar(); set_flag(ws, flag, tag, w); }
#define POLL_SLEEP 1
DEVINL void poll(const char* ws, int flag, u64 mask, u64 tag) {
  const unsigned off = flag * FS;
  u64 v;
  unsigned cnt;
  asm volatile("s_mov_b64 exec, %6\n\ts_nop 4\n\ts_mov_b32 %1, 40000\n"
               "1:\n\tglobal_load_dwordx2 %0, %2, %3 sc1\n\ts_waitcnt vmcnt(0)\n\tv_cmp_ne_u64 vcc, %4, %0\n\ts_nop 1\n\ts_cbranch_vccz 2f\n\t"
               "s_cmp_eq_u64 vcc, %6\n\ts_cbranch_scc1 3f\n\t"
               "s_mov_b64 exec, vcc\n\ts_sub_u32 %1, %1, 1\n\ts_cbranch_scc1 2f\n\ts_sleep %5\n\ts_branch 1b\n"
               "3:\n\ts_mov_b64 exec, 1\n"
               "4:\n\ts_sleep %5\n\tglobal_load_dwordx2 %0, %2, %3 sc1\n\ts_waitcnt vmcnt(0)\n\tv_cmp_ne_u64 vcc, %4, %0\n\ts_sub_u32 %1, %1, 1\n\ts_cbranch_scc1 2f\n\ts_cbranch_vccnz 4b\n\t"
               "s_andn2_b64 exec, %6, 1\n\ts_branch 1b\n"
               "2:\n\ts_mov_b64 exec, -1"
               : "=&v"(v), "=&s"(cnt) : "v"(off), "s"(ws + OFF_FLAG), "s"(tag), "n"(POLL_SLEEP), "s"(mask) : "vcc", "scc", "memory");
}
DEVINL void poll_from(u64 v, const char* ws, int flag, u64 mask, u64 tag) {
  const unsigned off = flag * FS;
  unsigned cnt;
  asm volatile("s_mov_b64 exec, %6\n\ts_nop 4\n\ts_mov_b32 %1, 40000\n\ts_branch 5f\n"
               "1:\n\tglobal_load_dwordx2 %0, %2, %3 sc1\n\ts_waitcnt vmcnt(0)\n5:\n\tv_cmp_ne_u64 vcc, %4, %0\n\ts_nop 1\n\ts_cbranch_vccz 2f\n\t"
               "s_cmp_eq_u64 vcc, %6\n\ts_cbranch_scc1 3f\n\t"
               "s_mov_b64 exec, vcc\n\ts_sub_u32 %1, %1, 1\n\ts_cbranch_scc1 2f\n\ts_sleep %5\n\ts_branch 1b\n"
               "3:\n\ts_mov_b64 exec, 1\n"
               "4:\n\ts_sleep %5\n\tglobal_load_dwordx2 %0, %2, %3 sc1\n\ts_waitcnt vmcnt(0)\n\tv_cmp_ne_u64 vcc, %4, %0\n\ts_sub_u32 %1, %1, 1\n\ts_cbranch_scc1 2f\n\ts_cbranch_vccnz 4b\n\t"
               "s_andn2_b64 exec, %6, 1\n\ts_branch 1b\n"
               "2:\n\ts_mov_b64 exec, -1"
               : "+v"(v), "=&s"(cnt) : "v"(off), "s"(ws + OFF_FLAG), "s"(tag), "n"(POLL_SLEEP), "s"(mask) : "vcc", "scc", "memory");
}
constexpr u64 lanes(int n) { return (1ull << n) - 1; }
template <int N> DEVINL int poll_vote(u64 v, const char* ws, unsigned off, u64 tag) {
  u64 t;
  unsigned cnt;
  asm volatile("s_bfm_b64 exec, %8, 0\n\ts_nop 4\n\ts_mov_b32 %1, 40000\n\ts_branch 5f\n"
               "1:\n\tglobal_load_dwordx2 %0, %3, %4 sc1\n\ts_waitcnt vmcnt(0)\n5:\n\tv_lshlrev_b64 %2, 1, %0\n\tv_cmp_ne_u64 vcc, %5, %2\n\ts_nop 1\n\ts_cbranch_vccz 2f\n\t"
               "s_mov_b64 exec, vcc\n\ts_sub_u32 %1, %1, 1\n\ts_cbranch_scc1 2f\n\ts_sleep %6\n\ts_branch 1b\n"
               "2:\n\ts_bfm_b64 exec, %8, 0\n\ts_nop 1\n\tv_cmp_ne_u64 vcc, %7, %0\n\ts_mov_b64 exec, -1\n\ts_nop 1\n\ts_bcnt1_i32_b64 %1, vcc"
               : "+v"(v), "=&s"(cnt), "=&v"(t) : "v"(off), "s"(ws + OFF_FLAG), "s"(tag << 1), "n"(POLL_SLEEP), "s"(tag), "n"(N) : "vcc", "scc", "memory");
  return cnt;
}
DEVINL int near_side(const char* p, int first) {
  u64 t0, t1, t2;
  unsigned d;
  asm volatile("s_memtime %0\n\ts_waitcnt lgkmcnt(0)\n\ts_load_dword %3, %4, 0x0\n\ts_waitcnt lgkmcnt(0)\n\t"
               "s_memtime %1\n\ts_waitcnt lgkmcnt(0)\n\ts_load_dword %3, %5, 0x0\n\ts_waitcnt lgkmcnt(0)\n\ts_memtime %2\n\ts_waitcnt lgkmcnt(0)"
               : "=&s"(t0), "=&s"(t1), "=&s"(t2), "=&s"(d) : "s"(p + first * 8192), "s"(p + (first ^ 1) * 8192) : "memory");
  return first ^ ((unsigned)t2 - (unsigned)t1 < (unsigned)t1 - (unsigned)t0);
}

template <int N> DEVINL void flag_peek(u64& v, const char* ws, unsigned off) {
  asm volatile("s_bfm_b64 exec, %3, 0\n\ts_nop 1\n\tglobal_load_dwordx2 %0, %1, %2 sc1\n\ts_mov_b64 exec, -1" : "=v"(v) : "v"(off), "s"(ws + OFF_FLAG), "n"(N) : "memory");
}
template <int N> DEVINL void flag_check(u64& v, const char* ws, unsigned off, u64 tag) {
  unsigned cnt;
  asm volatile("s_bfm_b64 exec, %5, 0\n\ts_mov_b32 %1, 40000\n"
               "1:\n\tv_cmp_ne_u64 vcc, %4, %0\n\ts_nop 1\n\ts_cbranch_vccz 2f\n\t"
               "s_mov_b64 exec, vcc\n\ts_sub_u32 %1, %1, 1\n\ts_cbranch_scc1 2f\n\tglobal_load_dwordx2 %0, %2, %3 sc1\n\ts_waitcnt vmcnt(0)\n\ts_branch 1b\n"
               "2:\n\ts_mov_b64 exec, -1"
               : "+v"(v), "=&s"(cnt) : "v"(off), "s"(ws + OFF_FLAG), "s"(tag), "n"(N) : "vcc", "scc", "memory");
}

constexpr float GC = 0.468668693f, GK = 4.91222954f;
template <class H> DEVINL u32x4 gelu_pack8(const f32x4& a, const f32x4& b, H&& between) {
  float y[8] = {a[0], a[1], a[2], a[3], b[0], b[1], b[2], b[3]}, t[8];
#pragma unroll
  for (int i = 0; i < 8; ++i) { t[i] = __builtin_fmaf(y[i], y[i], GK); asm("" : "+v"(t[i])); }
  SB0(); between(0); SB0();
#pragma unroll
  for (int i = 0; i < 8; ++i) { t[i] *= y[i]; asm("" : "+v"(t[i])); }
  SB0(); between(1); SB0();
#pragma unroll
  for (int i = 0; i < 8; ++i) t[i] = __builtin_amdgcn_exp2f(-t[i]);
  SB0(); between(2); SB0();
#pragma unroll
  for (int i = 0; i < 8; ++i) { t[i] = __builtin_fmaf(t[i], GC, GC); asm("" : "+v"(t[i])); }
  SB0(); between(3); SB0();
#pragma unroll
  for (int i = 0; i < 8; ++i) t[i] = __builtin_amdgcn_rcpf(t[i]);
  SB0();
#pragma unroll
  for (int i = 0; i < 8; ++i) { t[i] *= y[i]; asm("" : "+v"(t[i])); }
  SB0();
  return (u32x4){pack_bf16(t[0], t[1]), pack_bf16(t[2], t[3]), pack_bf16(t[4], t[5]), pack_bf16(t[6], t[7])};
}
DEVINL u32x4 gelu_pack8(const f32x4& a, const f32x4& b) { return gelu_pack8(a, b, [](int) {}); }

#define X_PACE 16
#define WL_PACE 32
#define XB_SLEEP 0
#define XA_SLEEP 16
template <int RS, size_t XG>
DEVINL void fwd_unit(const char* la, const u32x4 (&F)[8], char* gd, unsigned lo) {
  bf16x8 A[2][2];
#pragma unroll
  for (int i = 0; i < 2; ++i)
#pragma unroll
    for (int ks = 0; ks < 2; ++ks) A[i][ks] = tr_frag(la + 8 * i + 32 * RS * ks, 4 * RS);
#pragma unroll
  for (int nt = 0; nt < 4; ++nt) {
    f32x4 c0 = {0, 0, 0, 0}, c1 = {0, 0, 0, 0};
#pragma unroll
    for (int ks = 0; ks < 2; ++ks) { c0 = MFMAU(A[0][ks], F[2 * nt + ks], c0); c1 = MFMAU(A[1][ks], F[2 * nt + ks], c1); }
    if constexpr (X_PACE < 16) wait_vm<X_PACE - 1>();
    st16(gd + (nt & 1) * 8 * XG + (lo ^ ((nt >> 1) << 13)), pack8(c0, c1));
  }
}
#define IDXX 0
#define EU_A (idx & 3)
#define EU_B 2
#define EU_I 0
#define SG_W ((idx & 3) ^ 1)
#define SG_PRE 1
#define GE_EARLY 0
#define WSD 0
#define XSW 1
template <size_t XG> DEVINL unsigned x_lane_off(int lane, int tl, int row, unsigned pb) {
  const unsigned go = ((lane & 15) >> 1) * XG;
  return go + (((go >> 8) & 8192) ^ pb) + (row >> 6) * 16384 + (((row & 63) * 128) ^ (XSW ? (lane & 14) << 9 : 0)) + ((((lane & 1) * 4 + (lane >> 4)) ^ ((tl >> 1) & 7)) * 16);
}

DEVINL void w_issue(const u16* __restrict__ wsrc, char* reg, int co, int cb, int w, int lane) {
  const int par = co >> 5, c0 = (co & 31) * 8;
  __amdgpu_buffer_rsrc_t rs = mkrsrc(wsrc, 31 * 65536 * 2);
#pragma unroll
  for (int i = 0; i < 4; ++i) {
    const int cl = 2 * w + (i >> 1), tap = 8 * (i & 1) + (lane >> 3);
    const unsigned off = ((2 * tap + par) * 256 + c0 + cl) * 512 + cb * 128 + (((lane & 7) ^ (2 * (((tap >> 1) & 1) + 2 * (i & 1)))) * 16);
    dma16<0>(rs, reg + (w * 4 + i) * 1024, off, 0);
  }
}
template <int PACE, int NM>
DEVINL void w_compute(const u32x4 (&Tm)[NM], char* __restrict__ wf, const char* reg, int co, int cb, int w, int lane, unsigned pb) {
  const int G = lane >> 4, rq = (lane & 15) >> 2, p = lane & 3;
  char* dst = wf + (cb * 4 + w) * 65536 + (co >> 4) * 16384 + ((co >> 2) & 3) * 2048 + (co & 3) * 256 + (unsigned)((lane & 15) * 16 + 4 * G * WF_G);
  const char* la = reg + (8 * (G & 1) + rq) * 128 + (((2 * w + (p >> 1)) ^ (2 * ((rq >> 1) + 2 * (G & 1)))) * 16) + 8 * (p & 1);
  bf16x8 B[8];
#pragma unroll
  for (int cl = 0; cl < 8; ++cl) B[cl] = tr_frag(la + cl * 2048, 4 * 128);
#pragma unroll
  for (int mt = 0; mt < NM; ++mt) {
    f32x4 d[8];
#pragma unroll
    for (int cl = 0; cl < 8; ++cl) d[cl] = MFMAU(Tm[mt], B[cl], ((f32x4){0, 0, 0, 0}));
#pragma unroll
    for (int r = 0; r < 4; ++r) {
      u32x4 o = {pack_bf16(d[0][r], d[1][r]), pack_bf16(d[2][r], d[3][r]), pack_bf16(d[4][r], d[5][r]), pack_bf16(d[6][r], d[7][r])};
      if constexpr (PACE < 16) wait_vm<PACE>();
      st16(dst + (pb ^ ((((NM == 4) & mt) ^ (r >> 1)) << 13)) + r * WF_G + (NM == 4 ? mt >> 1 : mt) * 1024, o);
    }
  }
}

constexpr float LOG2E2 = 2.0f * 1.4426950408889634f;
DEVINL void s_gather(unsigned (&t)[8], const u16* __restrict__ wf, const u16* __restrict__ wo, bool o, int wv, int f, int lane, int key, int go) {
  const int mt = f >> 3;
  const unsigned offf = ((f & 7) * 32 + 8 * (lane >> 4)) * 512 + ((mt >> 1) * 32 + ((lane >> 2) & 3) * 8 + (mt & 1) * 4 + (lane & 3)) * 2;
  const unsigned offo = ((f >> 1) * 16 + 8 * (lane >> 5)) * 512 + ((f & 1) * 32 + ((lane >> 2) & 1) * 16 + ((lane >> 3) & 3) * 4 + (lane & 3)) * 2;
  asm volatile("s_cmp_lg_u32 %10, %11\n\ts_cbranch_scc1 1f\n\t"
               "global_load_ushort %0, %8, %9\n\tglobal_load_ushort %1, %8, %9 offset:512\n\tglobal_load_ushort %2, %8, %9 offset:1024\n\tglobal_load_ushort %3, %8, %9 offset:1536\n\t"
               "global_load_ushort %4, %8, %9 offset:2048\n\tglobal_load_ushort %5, %8, %9 offset:2560\n\tglobal_load_ushort %6, %8, %9 offset:3072\n\tglobal_load_ushort %7, %8, %9 offset:3584\n1:"
               : "+v"(t[0]), "+v"(t[1]), "+v"(t[2]), "+v"(t[3]), "+v"(t[4]), "+v"(t[5]), "+v"(t[6]), "+v"(t[7])
               : "v"((o ? offo : offf) + wv * 128), "s"(o ? wo : wf), "s"(key), "s"(go) : "scc", "memory");
}
DEVINL u32x4 s_pack(const unsigned (&t_)[8], bool o) {
  unsigned t[8];
#pragma unroll
  for (int j = 0; j < 8; ++j) asm volatile("v_mov_b32 %0, %1" : "=v"(t[j]) : "v"(t_[j]));
#pragma unroll
  for (int j = 0; j < 8; ++j) if (o) t[j] = __builtin_bit_cast(unsigned, 2.0f * __builtin_bit_cast(float, t[j] << 16)) >> 16;
  return (u32x4){t[0] | t[1] << 16, t[2] | t[3] << 16, t[4] | t[5] << 16, t[6] | t[7] << 16};
}
DEVINL void s_ge(const u16* __restrict__ g, const u16* __restrict__ wg, float* __restrict__ ge, char* lds, int eb, int lane) {
  __amdgpu_buffer_rsrc_t rs = mkrsrc(wg, 131072);
#pragma unroll
  for (int i = 0; i < 8; ++i) dma16<0>(rs, lds + i * 1024, (i * 32 + (lane >> 1)) * 512 + eb * 32 + (lane & 1) * 16, 0);
  bf16x8 gb[8];
#pragma unroll
  for (int ks = 0; ks < 8; ++ks) gb[ks] = *(const bf16x8*)(g + (lane & 15) * 256 + ks * 32 + (lane >> 4) * 8);
  wait_vm<0>();
  const int G = lane >> 4, rq = (lane & 15) >> 2, p = lane & 3;
  f32x4 acc = {0, 0, 0, 0};
#pragma unroll
  for (int ks = 0; ks < 8; ++ks) acc = MFMA16(gb[ks], tr_frag(lds + (ks * 32 + 8 * G + rq) * 32 + 8 * p, 4 * 32), acc);
  acc *= LOG2E2;
  f32x4 ex;
#pragma unroll
  for (int r = 0; r < 4; ++r) ex[r] = __builtin_amdgcn_exp2f(acc[r] > 64.f ? 64.f : (acc[r] < -64.f ? -64.f : acc[r]));
  const int e = (eb & 3) * 16 + (lane & 15);
  float* d = ge + ((G >> 1) * 4 + (eb >> 2)) * 512 + (G & 1) * 256 + (((e >> 3) & 3) * 16 + (e >> 5) * 8 + (e & 7)) * 4;
  st16(d, __builtin_bit_cast(u32x4, acc));
  st16(d + 4096, __builtin_bit_cast(u32x4, ex));
}

#define G1_K 13
#define G1_P 18
#define G1_K2 27
#define GORD 0
#define GZ 0
#define GX 0
#define SPREAD 1
#define TRAIL 10
#define TR_PACE 32
#define PRIO_TR 0
#define G3_WR 4
#define G1_WR 6
#define OPENV 0
#define WPRE (OPENV & 1 ? 0 : 8)
#define NOPEEK 0
#define G1_K0 3
#define PRO1 7
#define PRO2 7
#define RAMP 2
constexpr int RING = 7, SLOT = 11264, RING_DUMMY = RING * SLOT;
template <int PH> struct GPlan {
  static constexpr int NST = PH == 1 ? 32 : 8, PRO = PH == 1 ? PRO1 : PRO2;
  int first[NST + 1];
  int wfirst[NST + 1];
  int before[NST];
  int pos[NST];
  int posF0, posF, posF2, posY;
  constexpr GPlan() : first{}, wfirst{}, before{}, pos{}, posF0(0), posF(0), posF2(0), posY(0) {
    int n = 0, u = 0, k = 0;
    for (; u < PRO; ++u) pos[u] = (n += 3 + w1(u));
    for (int t = 0; t < NST; ++t) {
      before[t] = n;
      first[t] = u;
      wfirst[t] = k;
      if (PH == 1 && t == G1_K0 - 2) posF0 = ++n;
      if (PH == 1 && t == G1_K - 2) posF = ++n;
      if (PH == 1 && t == G1_K2 - 2) posF2 = ++n;
      if (PH == 1 && t == 16) n += 6;
      for (; u <= t + RING - 1 && u < NST && (PH != 1 || ((u < 8 || t >= G1_K0) && (u < 16 || t >= G1_K))) && (u >= 16 || u < first[t] + RAMP); ++u) pos[u] = (n += 3 + w1(u));
      if (PH == 1 && t == 16) posY = n;
      for (int i = 0; i < G1_WR && PH == 1 && t >= G1_K2 && k < 2 * (t - 16); ++i) { ++k; ++n; }
      for (; PH == 3 && k < G3_WR * t; ++k) ++n;
    }
    first[NST] = u;
    wfirst[NST] = k;
  }
  static constexpr int w1(int u) { return PH == 1 && u >= WPRE && u < 16 ? 2 : 0; }
  constexpr int nx(int t) const { int n = 0; for (int u = first[t]; u < first[t + 1]; ++u) n += 3 + w1(u); return n; }
  constexpr int nreq(int t) const { return nx(t) + wfirst[t + 1] - wfirst[t]; }
  constexpr int xreq(int t, int j) const { int u = first[t]; while (j >= 3 + w1(u)) { j -= 3 + w1(u); ++u; } return 5 * u + j; }
  constexpr int slot(int t, int j) const {
    const int n = nreq(t);
    if (!SPREAD) return -1;
    if (n <= 6) return 4 * (j * 6 / n) + 1;
    if (n <= 12) return 2 * (j * 12 / n);
    return j * 24 / n;
  }
  constexpr int wait(int t) const {
    int need = pos[t + 1];
    if (PH == 1 && t == G1_K0 && posF0 > need) need = posF0;
    if (PH == 1 && t == G1_K && posF > need) need = posF;
    if (PH == 1 && t == G1_K2 && posF2 > need) need = posF2;
    if (PH == 1 && t == G1_P && posY > need) need = posY;
    return before[t] - need;
  }
};
template <int PH>
DEVINL void g_phase(char* lds, char* ws, const char* xsrc, const char* wsrc1, const char* wsrc, u32x4 (&W)[32], char* ydst, unsigned yoff,
                    int fbase, int fbase2, int yflag, int pflag, u64 tag, int rot, int g7, int w, stv_t& stv) {
  const int lane = lane_id();
  constexpr GPlan<PH> PL;
  constexpr int NST = PL.NST, NACC = PH == 1 ? 6 : 3, ROWS = PH == 1 ? 84 : 42;
  __amdgpu_buffer_rsrc_t xrs = mkrsrc(xsrc, PH == 1 ? X1_S + 16 * XST : 8 * XST);
  __amdgpu_buffer_rsrc_t yrs = mkrsrc(ydst, PH == 1 ? 2 * Y1_S : Y2_S);
  unsigned xo[3];
  int xd[3];
#pragma unroll
  for (int i = 0; i < 3; ++i) {
    const int piece = i * 4 + w, row = piece * 8 + (lane >> 3);
    xo[i] = row < 84 ? ((piece & 7) ^ (XSW ? g7 : 0)) * 1024 + (i == 2 ? 16384 : 0) + lane * 16 : OOB;
    xd[i] = piece * 1024;
  }
  const unsigned lane16 = lane * 16;
  const bool dummy = (w == 3);
  unsigned xq[4];
#pragma unroll
  for (int i = 0; i < 4; ++i) xq[i] = ((i + rot) & 3) * (2 * XST);
  auto piece = [&](auto jc) {
    constexpr int u = decltype(jc)::v / 3, i = decltype(jc)::v % 3, slot = u % RING;
    const unsigned so = xq[(u >> 1) & 3] + (u >= 16 ? X1_S : 0) + (u & 9) * XST;
    dma16<SC1>(xrs, lds + (i == 2 && dummy ? RING_DUMMY : slot * SLOT + xd[i]), xo[i], so);
  };
  sgpr_settle();
  static_for<PL.PRO>([&](auto uc) {
    constexpr int u = decltype(uc)::v;
    static_for<3>([&](auto ic) { piece(IC<3 * u + decltype(ic)::v>{}); });
    static_for<PL.w1(u)>([&](auto ic) { constexpr int i = 2 * u + decltype(ic)::v; ldfrag_rot1<i>(W[i], wsrc1, rot, lane16); });
  });
  const int xa = (lane & 15) * 128 + (((lane >> 4) ^ ((lane & 15) >> 1)) * 16);
  int xb[4] = {xa, xa ^ 64, xa + 4 * SLOT, (xa ^ 64) + 4 * SLOT};
  asm volatile("" : "+v"(xb[2]), "+v"(xb[3]));
  const int za = (lane & 8) ? (xa & 255) + 512 + ((lane & 2) << 7) : xa;
  int xz[4] = {za, za ^ 64, za + 4 * SLOT, (za ^ 64) + 4 * SLOT};
  if (GZ) asm volatile("" : "+v"(xz[2]), "+v"(xz[3]));
  u32x4 xc[6], xs[6];
  auto ldx = [&](int pi, int slot) {
    const int ro = (PH == 1 ? pi * 16 : (pi / 3) * 42 + (pi % 3) * 16) * 128 + (slot & 3) * SLOT;
    if (GZ && PH == 1 && pi == 5) {
      xc[pi] = *(const u32x4*)(lds + xz[(slot >> 2) * 2] + ro);
      xs[pi] = *(const u32x4*)(lds + xz[(slot >> 2) * 2 + 1] + ro);
    } else {
      xc[pi] = *(const u32x4*)(lds + xb[(slot >> 2) * 2] + ro);
      xs[pi] = *(const u32x4*)(lds + xb[(slot >> 2) * 2 + 1] + ro);
    }
  };
  f32x4 R[NACC], Q[NACC];
  auto sty = [&](int n, unsigned base) {
    const int l = lane_id();
    const int tl = n * 16 + (l & 15);
    unsigned off = (tl >> 1) * 65536 + base + (tl & 1) * 64 + (l >> 4) * 16;
    off ^= (off >> 8) & 8192;
    __builtin_amdgcn_raw_buffer_store_b128(pack8(R[n], Q[n]), yrs, tl < ROWS ? off : OOB, 0, SC1);
  };
  wait_vm<PL.before[0] - PL.pos[0]>();
  static_for<PH == 1 ? (WPRE ? 2 * WPRE : 2) : 32>([&](auto ic) { tie(W[decltype(ic)::v]); });
  bar();
  STAMP(PH == 1 ? 5 : (PH == 2 ? 14 : 18));
#pragma unroll
  for (int pi = 0; pi < 6; ++pi) ldx(pi, 0);
  u64 fv;
  unsigned foff;
  static_for<NST>([&](auto ic) {
    constexpr int st = decltype(ic)::v;
    constexpr bool MORE = st + 1 < NST;
    if constexpr (MORE) {
      wait_vm<PL.wait(st)>();
      if constexpr (PH == 1 && st + 1 >= WPRE && st + 1 < 16) { tie(W[2 * st + 2]); tie(W[2 * st + 3]); }
      if constexpr (PH == 1 && st == G1_K0) { STAMP(12); flag_check<NOPEEK ? 1 : 48>(fv, ws, foff, tag); STAMP(23); }
      if constexpr (PH == 1 && st == G1_K) { STAMP(8); flag_check<NOPEEK ? 1 : 32>(fv, ws, foff, tag); }
      if constexpr (PH == 1 && st == G1_K2) { STAMP(21); flag_check<NOPEEK ? 1 : 16>(fv, ws, foff, tag); STAMP(22); }
      if constexpr (!(GX & 1) && !((GX & 8) && (st & 1))) bar();
      if constexpr (PH == 1 && st == G1_K) STAMP(9);
      if constexpr (PH == 1 && st == 16) STAMP(6);
      if constexpr (PH == 1 && st == G1_P) { PRIO(PRIO_F); set_flag(ws, yflag, tag, w); PRIO(PRIO_G1); STAMP(7); }
      SB0();
      if constexpr (PH == 1 && st == G1_K0 - 2) {
        const int l = lane_id();
        foff = (l + (l < 32 ? fbase + F_XH - F_X - 256 : fbase2 + F_W1H - F_W2 - 32)) * FS;
        flag_peek<NOPEEK ? 1 : 48>(fv, ws, foff);
      }
      if constexpr (PH == 1 && st == G1_K - 2) { foff = (fbase + lane_id()) * FS; flag_peek<NOPEEK ? 1 : 32>(fv, ws, foff); }
      if constexpr (PH == 1 && st == G1_K2 - 2) { foff = (fbase2 + lane_id()) * FS; flag_peek<NOPEEK ? 1 : 16>(fv, ws, foff); }
    }
    auto req = [&](auto mc) {
      static_for<PL.nreq(st)>([&](auto jc) {
        constexpr int j = decltype(jc)::v, nx = PL.nx(st), xu = j < nx ? PL.xreq(st, j) / 5 : 0, xi = j < nx ? PL.xreq(st, j) % 5 : 0;
        if constexpr (PL.slot(st, j) == decltype(mc)::v && !(GX & 2)) {
          SB0();
          if constexpr (j < nx && xi < 3) piece(IC<3 * xu + xi>{});
          else if constexpr (j < nx) ldfrag_rot1<2 * xu + xi - 3>(W[2 * xu + xi - 3], wsrc1, rot, lane16);
          else ldfrag_rot<PL.wfirst[st] + j - nx, PH == 1 ? 16384 : 8192>(W[PL.wfirst[st] + j - nx], wsrc, PH == 1 ? rot : 0, lane16);
          SB0();
        }
      });
    };
    req(IC<-1>{});
    u32x4 nw;
    constexpr f32x4 Z = {0, 0, 0, 0};
#define GM(a, b, c) MFMAU(a, b, c)
    if constexpr (GORD == 1 && PH == 1) {
      constexpr int ks = st & 15;
      static_for<24>([&](auto mc) {
        constexpr int m = decltype(mc)::v, n = m < 12 ? m >> 1 : (m - 12) % 6;
        if constexpr (st == 16 && m < 12 && !(m & 1)) sty(n, yoff);
        if constexpr (m < 12 && !(m & 1)) R[n] = GM(W[2 * ks], xc[n], ks == 0 ? Z : R[n]);
        else if constexpr (m < 12) Q[n] = GM(W[2 * ks], xs[n], ks == 0 ? Z : Q[n]);
        else if constexpr (m < 18) R[n] = GM(W[2 * ks + 1], xs[n], R[n]);
        else Q[n] = GM(nw, xc[n], Q[n]);
        if constexpr (m == 0) { tie(W[2 * ks + 1]); nw = W[2 * ks + 1] ^ 0x80008000u; }
        req(mc);
        if constexpr (MORE && m >= 12) {
          constexpr int slot = (st + 1) % RING, ro = n * 2048 + (slot & 3) * SLOT;
          if constexpr (m < 18) xs[n] = *(const u32x4*)(lds + xb[(slot >> 2) * 2 + 1] + ro);
          else xc[n] = *(const u32x4*)(lds + xb[(slot >> 2) * 2] + ro);
        }
        SB0();
      });
    } else
    static_for<6>([&](auto pc) {
      constexpr int pi = decltype(pc)::v, ks = PH == 1 ? st & 15 : 2 * st + pi / 3, n = PH == 1 ? pi : pi % 3;
      if constexpr (PH == 1 && st == 16) sty(n, yoff);
      if constexpr (GORD == 2) {
        if constexpr (n == 0) { tie(W[2 * ks + 1]); }
        R[n] = GM(W[2 * ks + 1], xs[pi], ks == 0 ? Z : R[n]);
        if constexpr (n == 0) { nw = W[2 * ks + 1] ^ 0x80008000u; }
        req(IC<4 * pi>{});
        Q[n] = GM(W[2 * ks], xs[pi], ks == 0 ? Z : Q[n]);
        req(IC<4 * pi + 1>{});
        R[n] = GM(W[2 * ks], xc[pi], R[n]);
        req(IC<4 * pi + 2>{});
        Q[n] = GM(nw, xc[pi], Q[n]);
        req(IC<4 * pi + 3>{});
      } else {
      R[n] = GM(W[2 * ks], xc[pi], ks == 0 ? Z : R[n]);
      if constexpr (n == 0) { tie(W[2 * ks + 1]); nw = W[2 * ks + 1] ^ 0x80008000u; }
      req(IC<4 * pi>{});
      Q[n] = GM(W[2 * ks], xs[pi], ks == 0 ? Z : Q[n]);
      req(IC<4 * pi + 1>{});
      R[n] = GM(W[2 * ks + 1], xs[pi], R[n]);
      req(IC<4 * pi + 2>{});
      Q[n] = GM(nw, xc[pi], Q[n]);
      req(IC<4 * pi + 3>{});
      }
      if constexpr (MORE && !(GX & 4)) ldx(pi, (st + 1) % RING);
      SB0();
    });
  });
  STAMP(PH == 1 ? 10 : (PH == 2 ? 15 : 19));
#pragma unroll
  for (int n = 0; n < NACC; ++n) sty(n, PH == 1 ? yoff + Y1_S : yoff);
  if constexpr ((TRAIL >> PH) & 1) { publish(ws, pflag, tag, w); PRIO(PRIO_TR); }
  if constexpr (PH != 2) static_for<32 - PL.wfirst[NST]>([&](auto jc) {
    constexpr int j = decltype(jc)::v, i = PL.wfirst[NST] + j;
    if constexpr (PH == 1 && j >= TR_PACE) wait_vm<TR_PACE - 1>();
    ldfrag_rot<i, PH == 1 ? 16384 : 8192>(W[i], wsrc, PH == 1 ? rot : 0, lane16);
  });
  if constexpr (!((TRAIL >> PH) & 1)) publish(ws, pflag, tag, w);
}

template <bool FAST> DEVINL float jval(float f, float r) {
  float t = FAST ? __builtin_fmaf(f, -r, 0.5f) : 0.5f - r;
  asm("" : "+v"(t));
  return t;
}
template <bool FAST, int E> DEVINL float jden(float f, float g) {
  float t = __builtin_bit_cast(float, __builtin_amdgcn_mov_dpp(__builtin_bit_cast(int, g), 0x150 + E, 0xf, 0xf, true)) + f;
  if (!FAST) t = 1.0f + __builtin_amdgcn_exp2f(t);
  asm("" : "+v"(t));
  return t;
}
DEVINL float nanmax3(float m, float a, float b) { return __builtin_fmaxf(__builtin_fmaxf(m, __builtin_fabsf(a)), __builtin_fabsf(b)); }
#define OUT_POL "sc1"
#define OUT_AUX 16
#define JX 0
constexpr int J_Y = 32768, J_F = 65536, J_G = J_F + 8192;
constexpr int J_AH = 2;
template <bool FAST> DEVINL void j_make(const f32x4 (&fe)[4], float g, char* lds, int u, int w, int lane) {
  f32x4 t[4];
  static_for<16>([&](auto ec) { constexpr int e = decltype(ec)::v; t[e >> 2][e & 3] = jval<FAST>(fe[e >> 2][e & 3], __builtin_amdgcn_rcpf(jden<FAST, e>(fe[e >> 2][e & 3], g))); });
#pragma unroll
  for (int h = 0; h < 2; ++h) *(u32x4*)(lds + u * 8192 + (2 * w + h) * 1024 + lane * 16) = pack8(t[2 * h], t[2 * h + 1]);
}
#define J_PRE 4
#define JA_SLEEP 0
#define JA_SLEEP2 0
#define JA_POLL 0
#define JA_NP 32
#define U_ROT ((q >> 3) & 1)
#define J_WC 12
#define J_SC 12
#define J_ST0 5
#define J_STD 8
#define J_BAR 28
#define J_LC 2
struct JPlan {
  enum { D, R, V, C, W };
  static constexpr int ST0 = J_ST0, ST1 = J_ST0 + J_STD;
  static constexpr int NV = 96;
  int op[6 * NV], first[130], nop, lev[3] = {};
  static constexpr int tile(int q) { return q < 3 ? 0 : (q > 28 ? 1 : (q - 3) & 1); }
  static constexpr int kstep(int q) { return q < 3 ? q : (q > 28 ? q - 16 : ((q - 3) & 1 ? (q - 3) >> 1 : 3 + ((q - 3) >> 1))); }
  static constexpr int bread(int n) {
    const int p = n >> 5, q = n & 31;
    if (q >= 28) return p < 3 ? 16 * (p + 1) + q - 28 : -1;
    return (q & 1) && q <= 23 ? 16 * p + (q + 7) / 2 : -1;
  }
  static constexpr int fixed(int n) {
    const int p = n >> 5, q = n & 31;
    int c = bread(n) >= 0 ? J_LC : 0;
    if (q == 29 || q == 30) c += 14;
    if (q == 31) c += 17;
    if (p && (q == 0 || q == 4)) c += 8;
    if (p && (q == 1 || q == 2)) c += 14;
    if (p && q == 3) c += 17;
    if (p && (q == ST0 || q == ST1)) c += 2 * J_SC;
    return c;
  }
  constexpr JPlan() : op{}, first{}, nop(0) {
    int cost[6 * NV] = {};
    auto add = [&](int t, int i, int c) { op[nop] = t * 128 + i; cost[nop++] = c; };
    for (int i = 0; i < NV + 4; ++i) {
      if (i < NV) add(D, i, 4);
      if (i >= 1 && i - 1 < NV) add(R, i - 1, 8);
      if (i >= 2 && i - 2 < NV) add(V, i - 2, 4);
      if (i >= 4 && !(i & 1) && i - 3 < NV) add(C, (i - 4) >> 1, 5);
      if (i >= 10 && ((i - 10) & 7) == 0) add(W, (i - 10) >> 3, J_WC);
    }
    int j = 0, n = 0;
    while (op[j] != D * 128 + J_PRE) ++j;
    for (int p = 0; p < 3; ++p) {
      const int n0 = n, j0 = j, n1 = 32 * p + J_BAR - 1;
      int j1 = j0;
      while (op[j1] != W * 128 + 4 * p + 3) ++j1;
      for (int bud = 8; j <= j1; ++bud) {
        int carry = 0;
        j = j0;
        for (n = n0; n < n1; ++n) {
          first[n] = j;
          int left = bud - fixed(n) + carry;
          while (j <= j1 && cost[j] <= left + 2) left -= cost[j++];
          carry = left > 4 ? 4 : (left < -4 ? -4 : left);
        }
        lev[p] = bud;
      }
    }
    for (; n <= 128; ++n) first[n] = j;
  }
};
typedef float f32x16 __attribute__((ext_vector_type(16)));
DEVINL void j_loop(u32x4 (&woa)[32], const f32x4 (&fe)[4], const f32x4 (&g)[2], char* lds, int jw, int jstep, char* out, int row0, int ur, int w, int lane, stv_t& stv) {
  static constexpr JPlan PL;
  static_assert(PL.first[128] == PL.nop, "");
  __amdgpu_buffer_rsrc_t ors = mkrsrc(out + (size_t)row0 * 4096, 65536);
  unsigned ooff = (lane & 15) * 4096 + ur * 2048 + w * 128 + (lane >> 4) * 16;
  const char* jr = lds + ((lane >> 4) & 1) * 8192 + (lane >> 5) * 256 + (lane & 15) * 16;
  auto ldb = [&](int j) { return *(const u32x4*)(jr + (j >> 4) * 16384 + (j & 15) * 512); };
  wait_lds();
  bar();
  STAMP(28);
  u32x4 B[6];
#pragma unroll
  for (int j = 0; j < 4; ++j) B[j] = ldb(j);
  f32x16 acc[2];
  u32x4 X[2], Y[2], jo;
  float d[8];
  auto cvt = [&](int m, int i) {
    if (i & 1) Y[m][i >> 1] = pack_bf16(acc[m][8 + (i & 6)], acc[m][9 + (i & 6)]);
    else X[m][i >> 1] = pack_bf16(acc[m][i], acc[m][i + 1]);
  };
  auto swp = [&](int m, int i) { auto r = __builtin_amdgcn_permlane16_swap(X[m][i], Y[m][i], false, false); X[m][i] = r[0]; Y[m][i] = r[1]; };
  auto emit = [&](auto jc) {
    constexpr int o = PL.op[decltype(jc)::v], t = o >> 7, e = o & 127;
    if constexpr (t == JPlan::D) d[e & 7] = jden<true, e & 15>(fe[(e >> 2) & 3][e & 3], g[(e + 32) >> 6][((e + 32) >> 4) & 3]);
    if constexpr (t == JPlan::R && !(JX & 32)) d[e & 7] = __builtin_amdgcn_rcpf(d[e & 7]);
    if constexpr (t == JPlan::V && !(JX & 64)) d[e & 7] = jval<true>(fe[(e >> 2) & 3][e & 3], d[e & 7]);
    if constexpr (t == JPlan::C) jo[e & 3] = pack_bf16(d[(2 * e) & 7], d[(2 * e + 1) & 7]);
    if constexpr (t == JPlan::W && (JX & 16)) tie(jo);
    if constexpr (t == JPlan::W && !(JX & 16)) {
      *(u32x4*)(lds + jw + (e & 1) * 1024) = jo;
      if constexpr (e & 1) { jw += jstep; asm("" : "+v"(jw)); }
    }
    SB0();
  };
  auto st = [&](auto oc, const u32x4& v) { __builtin_amdgcn_raw_buffer_store_b128(v, ors, ooff + (decltype(oc)::v & 2047), 0, OUT_AUX); };
  SB0();
  if constexpr (!(JX & 4)) static_for<PL.first[0]>([&](auto jc) { emit(jc); });
  static_for<128>([&](auto nc) {
    constexpr int n = decltype(nc)::v, p = n >> 5, q = n & 31, m = PL.tile(q), k = PL.kstep(q);
    if constexpr (n == 0) { wait_vm<16>(); static_for<16>([&](auto c) { tie(woa[decltype(c)::v]); }); }
    if constexpr (n == 13) { wait_vm<0>(); static_for<16>([&](auto c) { tie(woa[16 + decltype(c)::v]); }); }
    constexpr f32x16 Z = {};
    acc[m] = __builtin_amdgcn_mfma_f32_32x32x16_bf16(__builtin_bit_cast(bf16x8, woa[2 * k + m]), __builtin_bit_cast(bf16x8, B[(16 * p + k) % 6]), k ? acc[m] : Z, 0, 0, 0);
    SB0();
    if constexpr (q == J_BAR && p < 3 && !(JX & 2)) { wait_lds(); bar(); }
    if constexpr (PL.bread(n) >= 0) B[PL.bread(n) % 6] = ldb(PL.bread(n));
    if constexpr (q >= 29 || (p && q >= 1 && q <= 3)) {
      constexpr int t = q < 4, s = q < 4 ? q - 1 : q - 29;
      cvt(t, 3 * s); cvt(t, 3 * s + 1);
      if constexpr (s < 2) cvt(t, 3 * s + 2); else { swp(t, 0); swp(t, 1); }
    }
    if constexpr (p && (q == 0 || q == 4)) { swp(q / 4, 2); swp(q / 4, 3); }
    if constexpr (!(JX & 1)) {
      if constexpr (p && q == PL.ST0) { st(IC<(2 * p - 2) * 512>{}, X[0]); st(IC<(2 * p - 2) * 512 + 64>{}, X[1]); }
      if constexpr (p && q == PL.ST1) { st(IC<(2 * p - 1) * 512>{}, Y[0]); st(IC<(2 * p - 1) * 512 + 64>{}, Y[1]); }
      if constexpr (n == 95) { ooff ^= 2048; asm("" : "+v"(ooff)); }
    } else if constexpr (p && (q == 1 || q == 6)) { tie(X[q / 6]); tie(Y[q / 6]); }
    if constexpr (!(JX & 4)) static_for<PL.first[n + 1] - PL.first[n]>([&](auto jc) { emit(IC<PL.first[n] + decltype(jc)::v>{}); });
    SB0();
  });
  swp(0, 2); swp(0, 3);
#pragma unroll
  for (int i = 0; i < 8; ++i) cvt(1, i);
#pragma unroll
  for (int i = 0; i < 4; ++i) swp(1, i);
  st(IC<6 * 512>{}, X[0]);
  st(IC<6 * 512 + 64>{}, X[1]);
  st(IC<7 * 512>{}, Y[0]);
  st(IC<7 * 512 + 64>{}, Y[1]);
}

#define HINT 0
#define TAGLATE 0
#define XLOOP 1
#define ETOUCH 4096
#define RFIRST 1
#define PTOUCH 1
#define FTOUCH 1
#define EARLY_WF 16
#define WF_PACE 4
#define WF_PACE2 8
#define WO_PACE 3
#ifndef M_AH
#define M_AH 3
#endif
#define SG_AT 3
#ifndef M_PEEK
#define M_PEEK 4
#endif
#define LATE_WF 1
#define G_ALL 1
#define T_ALL 1
#define W1_SLEEP 16
#define ROT 1
#define W2_PACE 2
constexpr int M_HS = 96;

template <int MK = 0> DEVINL void x_issue(const u16* __restrict__ x, char* reg, int s, int tp, int par, int ch, bool live, int lane) {
  __amdgpu_buffer_rsrc_t rs = mkrsrc(x + (size_t)s * T0 * 256, T0 * 512);
  const int t0 = 96 * tp - 111 + par + 2 * (lane >> 3);
  const int base = t0 * 512 + ch * 128 + (((lane & 7) ^ (4 * ((lane >> 4) & 1))) * 16);
  unsigned o0 = (tp == 0 || t0 < 0) ? OOB : (unsigned)base, o1 = tp == 0 ? OOB : (unsigned)(base + 8192), so;
  if (live) asm volatile("s_mov_b32 m0, %4\n\ts_mov_b32 %0, 6\n\ts_nop 2\n\tbuffer_load_dwordx4 %2, %3, 0 offen " IN_POL " lds\n"
               "1:\n\ts_add_u32 m0, m0, 1024\n\ts_sub_u32 %0, %0, 1\n\tbuffer_load_dwordx4 %1, %3, 0 offen " IN_POL " lds\n\tv_add_u32 %1, 0x2000, %1\n\ts_cbranch_scc0 1b ;@L7\n;@%5"
               : "=&s"(so), "+v"(o1) : "v"(o0), "s"(rs), "s"((unsigned)(size_t)(lds_void_p)reg), "n"(MK) : "scc", "memory");
}
#define G_GATE 0
#define T_GATE 0
DEVINL void gate(unsigned t0, unsigned n) {
  asm volatile("1:\n\ts_sleep 2\n\ts_memrealtime vcc\n\ts_waitcnt lgkmcnt(0)\n\ts_sub_u32 vcc_lo, vcc_lo, %0\n\ts_cmp_lt_u32 vcc_lo, %1\n\ts_cbranch_scc1 1b"
               :: "s"(t0), "s"(n) : "vcc", "scc", "memory");
}
DEVINL u64 load_tag(const char* ws) {
  u64 tag;
  asm volatile("s_load_dwordx2 %0, %1, 0x0\n\ts_waitcnt lgkmcnt(0)" : "=s"(tag) : "s"(ws + OFF_FLAG + F_EPOCH * FS) : "memory");
  return tag * 6364136223846793005ull + 1442695040888963407ull;
}

extern "C" __global__ __launch_bounds__(256, 2) void k_all(const u16* __restrict__ x, const u16* __restrict__ w1, const u16* __restrict__ w2, const u16* __restrict__ g_,
                                                const u16* __restrict__ wf, const u16* __restrict__ wg, const u16* __restrict__ wo,
                                                char* __restrict__ ws_, char* __restrict__ out) {
  constexpr unsigned pb = 0;
  char* const ws = ws_ + (-(size_t)ws_ & ((2 << 20) - 1));
  __shared__ __attribute__((aligned(16))) char lds[LDS_BYTES];
  int lane = threadIdx.x & 63;
  const int w = __builtin_amdgcn_readfirstlane(threadIdx.x >> 6);
  const int role = blockIdx.x >> 8, xl = blockIdx.x & 7, idx = ((blockIdx.x >> 3) & 31) ^ IDXX;
  const int q = xl * 32 + idx;
  stv_t stv = {};
  STAMP(0);
  u64 tag;

  const int eu = w == EU_A ? idx : ((idx & 3) == EU_I && w == EU_B ? 32 + (idx >> 2) : -1);
  const int tpv[2] = {xl * 21 + (idx >> 1), xl * 21 + 16 + (eu >> 3)}, parv[2] = {idx & 1, (eu >> 2) & 1}, chv[2] = {w, eu & 3};
  const int wsd = WSD;
  u32x4 F[8];
  static_for<4>([&](auto ic) { constexpr int i = decltype(ic)::v; ldfrag<i, false>(F[i], (const char*)TAB + (role ? TAB_F + 4 * wsd : TAB_T) * 1024, lane * 16); });
  asm volatile("s_cmp_eq_u32 %6, 0\n\ts_cbranch_scc1 1f\n\t;@f\n\tglobal_load_dwordx4 %0, %4, %5\n\tglobal_load_dwordx4 %1, %4, %5 offset:1024\n\t"
               "global_load_dwordx4 %2, %4, %5 offset:2048\n\tglobal_load_dwordx4 %3, %4, %5 offset:3072\n1:"
               : "=&v"(F[4]), "=&v"(F[5]), "=&v"(F[6]), "=&v"(F[7]) : "v"(lane * 16), "s"((const char*)TAB + (TAB_F + 4 - 4 * wsd) * 1024), "s"(role) : "scc", "memory");
  asm volatile("" :: "s"(x), "s"(w1), "s"(w2), "s"(g_), "s"(wf), "s"(wg), "s"(wo), "s"(ws_));
  int role2 = role;
  if (role2 == 0) {
    asm volatile(";@g");
    PRIO(PRIO_G0);
    w_issue(w1, lds, (q >> 5) * 8 + ((q >> 3) & 3), q & 3, w, lane);
    w_issue(w1, lds + 16384, (q >> 5) * 8 + 4 + ((q >> 3) & 3), q & 3, w, lane);
    w_issue(w2, lds + 65536, q >> 2, q & 3, w, lane);
  }
  asm volatile("" : "+s"(role2));
  if (role2 != 0) {
    asm volatile(";@t");
    PRIO(PRIO_T0);
    x_issue(x, lds + w * 8192, 0, tpv[0], parv[0], chv[0], true, lane);
    x_issue<1>(x, lds + 65536 + (eu >> 5) * 8192, 0, tpv[1], parv[1], chv[1], eu >= 0, lane);
  }
  if (RFIRST) x_issue<2>(x, lds + 32768 + w * 8192, 1, tpv[role == 0], parv[role == 0], chv[role == 0], role != 0 || eu >= 0, lane);
  unsigned t_start;
  asm volatile("s_memrealtime vcc\n\ts_waitcnt lgkmcnt(0)\n\ts_mov_b32 %0, vcc_lo" : "=s"(t_start) :: "vcc", "memory");
  unsigned per;
  {
    u64 pc;
    unsigned a, b;
    asm volatile("s_getpc_b64 %0\n.Lpe:\n\ts_mov_b32 %1, .Lg_beg-.Lpe\n\ts_mov_b32 %2, .Lt_beg-.Lpe" : "=s"(pc), "=s"(a), "=s"(b));
    const unsigned o = (idx * 4 + w) * 64;
    asm volatile("s_load_dword %0, %1, %2" : "=s"(per) : "s"(pc), "s"(((role ? b : a) + (o < ETOUCH ? o : 0)) & ~3u) : "memory");
  }
  asm volatile("" : "+s"(role2));

  u32x4 W[32];
  if (__builtin_expect(role2 == 0, 1)) {
    asm volatile(".Lg_beg:");
    const unsigned lane16 = lane * 16;
    const int TB = (idx >> 2) & 1, cb = idx & 3;
    const unsigned foff = (lane >= 16 ? F_X + TB * 128 + w * 32 + lane - 16 : F_W1 + cb * 64 + w * 16 + lane) * FS;
    u64 fv = 0;
    {
      const char* probe = ws + OFF_PROBE + (q >> 4) * 16384 + (q & 15) * 512;
      unsigned ct = 0;
      if (RFIRST) {
        u64 pc;
        unsigned len;
        asm volatile("s_getpc_b64 %0\n.Lpc%=:\n\ts_mov_b32 %1, .Lpc%=-k_all" : "=s"(pc), "=s"(len));
        pc -= len;
        asm volatile("s_mov_b32 %0, .Lk_end-k_all\n\ts_max_u32 %0, %0, .Lt_end-k_all" : "=s"(len) :: "scc");
        const unsigned wv = idx * 4 + w, co = wv * 512 + lane * 64, l2 = lane >> 1;
        const char* a = (const char*)pc + (co < len ? co : 0);
        if (l2 >= 4) a = (const char*)(l2 == 4 ? wf : (l2 == 5 ? wg : wo)) + (q * 4 + w) * 128 + (lane & 1) * 64;
        if (l2 == 7) a = (wv < 64 ? (const char*)g_ + wv * 128 : (const char*)TAB + TAB_G * 1024 + (wv - 64) * 128) + (lane & 1) * 64;
        constexpr int NF = FS == 128 ? 4 : (FS >= 16 ? FS / 16 : 1);
        if (lane >= 17) a = ws + OFF_FLAG + ((q * 4 + w) * NF + lane - 17) * (FS == 128 ? 128 : 64);
        if (lane == 16) a = ws + OFF_PROBE + (((q >> 4) * 16384 + 32768) & 0x3ffff) + (q & 15) * 512 + (w & 1) * 8192;
        asm volatile("s_bfm_b64 exec, %2, 0\n\ts_nop 1\n\tglobal_load_dword %0, %1, off sc1\n\ts_mov_b64 exec, -1" : "=v"(ct) : "v"(a), "n"(17 + NF) : "memory");
        if (!TAGLATE) { unsigned d; asm volatile("s_load_dword %0, %1, 0x100" : "=s"(d) : "s"(probe) : "memory"); tag = load_tag(ws); asm volatile("" :: "s"(d)); }
        wait_vm<G_ALL ? 0 : 13>();
        if (eu < 0) { wait_vm<5>(); asm volatile(";@e"); }
      } else wait_vm<0>();
      if (RFIRST) asm volatile(";@Ng2_ge\n;@r"); else asm volatile(";@Ng\n;@r");
      wait_lds();
      asm volatile("" :: "s"(per));
      if (W1_SLEEP) __builtin_amdgcn_s_sleep(W1_SLEEP);
      if (G_GATE) gate(t_start, G_GATE);
      static_for<4>([&](auto ic) { tie(F[decltype(ic)::v]); });
      const bool sd = q & 4;
      const u32x4 Tm[2] = {sd ? F[1] : F[0], sd ? F[3] : F[2]};
      bar();
      STAMP(1);
      w_compute<16>(Tm, ws + OFF_W1F, lds, q >> 3, q & 3, w, lane, pb ^ (sd << 13));
      if (TAGLATE) { unsigned d; asm volatile("s_load_dword %0, %1, 0x100" : "=s"(d) : "s"(probe) : "memory"); tag = load_tag(ws); asm volatile("" :: "s"(d)); }
      int vote = 0;
      if (w == 0) vote = (xl >> 2) ^ near_side(probe, (idx >> 2) & 1);
      publish(ws, F_W1 + (q & 3) * 64 + (q >> 2), tag ^ ((u64)vote << 63), w);
      tie(ct);
      STAMP(2);
      PRIO(PRIO_G0);
      w_compute<16>(Tm, ws + OFF_W1F, lds + 16384, 32 + (q >> 3), q & 3, w, lane, pb ^ (sd << 13));
      if (OPENV & 2) flag_peek<OPENV & 1 ? 48 : 16>(fv, ws, foff);
      publish(ws, F_W1H + (q & 3) * 64 + (q >> 2), tag, w);
      STAMP(31);
    }
    ((int*)(lds + LDS_BYTES - 16))[w] = poll_vote<OPENV & 1 ? 48 : 16>(OPENV & 2 ? fv : ~tag, ws, foff, tag);
    wait_lds();
    bar();
    const u32x4 votes = *(const u32x4*)(lds + LDS_BYTES - 16);
    const int grp = (xl * 4 + (idx >> 3)) ^ (__builtin_amdgcn_readfirstlane(votes[0] + votes[1] + votes[2] + votes[3]) > 32 ? 16 : 0);
    const int gflag = (TB * 4 + cb) * 32 + grp, rot = ROT ? cb + 2 * TB : 0;
    const int gl = grp & 15;
    const unsigned sb = pb ^ ((grp >> 4) << 13);
    const unsigned yoff = (gl >> 2) * 16384 + sb + (gl & 3) * 2048 + (cb * 4 + w) * 128;
    const size_t woff = gl * WF_G + (cb * 4 + w) * 65536 + (sb ^ ((gl & 2) << 12));
    const size_t x1off = gl * X1_G + TB * X1_TB + (sb ^ ((gl & 2) << 12)), x2off = gl * X2_G + TB * X2_TB + (sb ^ ((gl & 4) << 11));
    {
      if (!(OPENV & 1)) {
        STAMP(3);
        PRIO(PRIO_GW);
        static_for<2 * WPRE>([&](auto ic) { constexpr int i = decltype(ic)::v; ldfrag_rot1<i>(W[i], ws + OFF_W1F + woff, rot, lane16); });
        PRIO(PRIO_F);
        poll(ws, F_X + TB * 128 + w * 32 + lane, lanes(32), tag);
        bar();
      }
      STAMP(4);
      PRIO(PRIO_G1);
      g_phase<1>(lds, ws, ws + OFF_X1F + x1off, ws + OFF_W1F + woff, ws + OFF_W2F + woff, W, ws + OFF_Y1F, yoff + TB * 42 * 65536,
                 F_X + 256 + TB * 128 + w * 32, F_W2 + cb * 64 + w * 16, F_G1 + gflag, F_G1 + 256 + gflag, tag, rot, gl & 7, w, stv);
      STAMP(11);
    }
    lane = lane_id();
    if (!EARLY_WF && SG_AT == 6) poll(ws, F_X + 256 + w * 64 + lane, ~0ull, tag);
    poll(ws, F_M + TB * 112 + w * 28 + lane, lanes(28), tag);
    bar();
    STAMP(13);
    PRIO(PRIO_G2A);
    g_phase<2>(lds, ws, ws + OFF_X2F + x2off, nullptr, nullptr, W, ws + OFF_Y2F, yoff + TB * 21 * 65536, 0, 0, 0, F_G2 + gflag, tag, rot, gl & 7, w, stv);
    STAMP(16);
    lane = lane_id();
    poll(ws, lane >= 28 ? F_S + w * 4 + lane - 28 : F_M + 256 + TB * 112 + w * 28 + lane, lanes(32), tag);
    bar();
    STAMP(17);
    PRIO(PRIO_G2B);
    g_phase<3>(lds, ws, ws + OFF_X2F + X2_S + x2off, nullptr, ws + OFF_WFT + w * 32768, W, ws + OFF_Y2F + Y2_S, yoff + TB * 21 * 65536, 0, 0, 0,
               F_G2 + 256 + gflag, tag, rot, gl & 7, w, stv);
    if (TRAIL & 8) wait_vm<0>();
    STAMP(20);
    PRIO(PRIO_JB0);
  } else {
    asm volatile(".Lt_beg:");
    const int G = lane >> 4, rq = (lane & 15) >> 2, p = lane & 3;
    const unsigned lane16 = lane * 16;
    {
      u32x4 Tm[4];
      tag = load_tag(ws);
      asm volatile("" :: "s"(per));
      wait_vm<T_ALL ? 0 : 8 + 8 * RFIRST>();
      if (eu < 0) { wait_vm<8 * RFIRST>(); asm volatile(";@e"); }
      if (RFIRST) asm volatile(";@Nft12_ft2e\n;@r"); else asm volatile(";@Nft1_fte\n;@r");
      tie(F);
      STAMP(1);
      if (XA_SLEEP) __builtin_amdgcn_s_sleep(XA_SLEEP);
      if (T_GATE) gate(t_start, T_GATE);
      unsigned sg[8] = {};
#pragma unroll 1
      for (int i = 0; i < 8; ++i) {
        const int s = i >> 2, r = s ? (i >> 1) & 1 : i & 1, h = s ? i & 1 : i >> 1;
        s_gather(sg, wf, wo, q & 32, q >> 6, q & 31, lane, 4 * i + w, 4 * (SG_AT - SG_PRE) + SG_W);
        if (r == 0 || eu >= 0) {
          if (r && !h) wait_vm<12>();
          const int tl = (r ? tpv[1] : tpv[0]) - 84 * (xl >> 2);
          char* gd = ws + OFF_X1F + s * X1_S + (xl >> 2) * X1_TB + (size_t)(8 * h + (r ? parv[1] * 4 + chv[1] : parv[0] * 4 + chv[0])) * XST;
          const char* reg = lds + (r == s ? w * 8192 : (s ? 32768 + w * 8192 : 65536 + (eu >> 5) * 8192));
          fwd_unit<128, X1_G>(reg + (8 * G + rq) * 128 + (((4 * h + p) ^ (4 * ((rq >> 1) & 1))) * 16), F, gd, x_lane_off<X1_G>(lane, tl, tl, pb ^ (wsd << 13)));
        } else if (i == SG_AT && w == SG_W) {
          wait_vm<SG_PRE ? 4 : 0>();
          st16(ws + OFF_WFT + (q & 32) * 4096 + (q >> 6) * 32768 + (q & 31) * 1024 + lane16, s_pack(sg, q & 32));
        }
        if ((0x8a >> i) & 1) {
          publish(ws, (i == 3 ? F_XH : F_X + s * 256) + q, tag, w);
          PRIO(PRIO_T1);
          if (i == 1) STAMP(2);
          if (i == 7) STAMP(4);
        }
        if (i == 3) {
          STAMP(3);
          x_issue(x, lds + w * 8192, 1, tpv[1], parv[1], chv[1], eu >= 0, lane);
          w_issue(w2, lds + 65536, q >> 2, q & 3, w, lane);
          if (XB_SLEEP) __builtin_amdgcn_s_sleep(XB_SLEEP);
        }
      }
      PRIO(PRIO_T2);
      static_for<4>([&](auto ic) { constexpr int i = decltype(ic)::v; ldfrag<TAB_T + i, false>(Tm[i], (const char*)TAB + (i & 1 ? -1024 : 1024) * wsd, lane16); });
      if (SG_AT == 3) poll(ws, F_XH + w * 64 + lane, ~0ull, tag); else wait_vm<0>();
      tie(Tm);
      w_compute<W2_PACE>(Tm, ws + OFF_W2F, lds + 65536, q >> 2, q & 3, w, lane, pb ^ (wsd << 13));
      publish(ws, F_W2 + (q & 3) * 64 + (q >> 2), tag, w);
      STAMP(5);
    }
    u32x4 Gt[6], F2[2];
    static_for<6>([&](auto ic) { constexpr int i = decltype(ic)::v; ldfrag<i & 1, false>(Gt[i], (const char*)TAB + (TAB_G + 2 * ((2 + 5 * w + i / 2) % 3)) * 1024, lane16); });
    static_for<2>([&](auto ic) { constexpr int i = decltype(ic)::v; ldfrag<i, false>(F2[i], (const char*)TAB + (TAB_F + 2 * w) * 1024, lane16); });
    if (EARLY_WF && SG_AT == 6) poll(ws, F_X + 256 + w * 64 + lane, NOPEEK ? 3ull : ~0ull, tag); else wait_vm<0>();
    tie(Gt); tie(F2);
    STAMP(15);
    PRIO(PRIO_T2);
    static_for<EARLY_WF>([&](auto ic) {
      constexpr int i = decltype(ic)::v;
      if constexpr (i >= WF_PACE) wait_vm<WF_PACE - 1>();
      ldfrag<i, true>(W[i], ws + OFF_WFT + w * 32768, lane16);
    });
    wait_vm<0>();
    static_for<EARLY_WF>([&](auto ic) { tie(W[decltype(ic)::v]); });
    PRIO(PRIO_F);
    STAMP(14);
    if (idx < 28) {
      char* reg = lds + w * 8192;
      constexpr int HIM = 32768;
      const int pr = 3 * idx + w, TB2 = idx >= 14;
      unsigned yo[2], la[5][2], xo[3];
#pragma unroll
      for (int i = 0; i < 2; ++i) {
        yo[i] = pr * 65536 + G * 2048 + xl * 256 + (((lane & 15) ^ ((G & 1) << 2) ^ i) * 16);
        const unsigned a0 = (4 * G + (rq >> 1)) * 256 + (((p >> 1) * 8 + 2 * (p & 1) + i) ^ ((rq >> 1) << 2) ^ (G & 1)) * 16 + (rq & 1) * 8;
#pragma unroll
        for (int j = 0; j < 5; ++j) { const int t = (2 + 5 * w + j) / 3; la[j][i] = a0 ^ ((t >> 1) * 8192 + (t & 1) * 64); tie(la[j][i]); }
        tie(yo[i]);
      }
#pragma unroll
      for (int i = 0; i < 3; ++i) { const int tl = 3 * idx - 42 * TB2 + i; xo[i] = x_lane_off<X2_G>(lane, tl, tl + 42 * (xl & 1), pb ^ ((w >> 1) << 13)); tie(xo[i]); }
      unsigned hw = HIM + (32 + 80 * w + (lane & 15)) * M_HS + G * 16, hr = HIM + (33 + 2 * (8 * G + rq)) * M_HS + p * 16;
      unsigned yfl = F_G1 + ((idx >= 14 || lane >= 8 ? 4 : 0) + (xl >> 1)) * 32 + w * 8 + (lane & 7);
      tie(hw); tie(hr); tie(yfl);
      const unsigned keep = pr == 84 ? 0u : ~0u;
      if (lane < 4) *(u32x4*)(lds + HIM + 352 * M_HS + lane16) = (u32x4){0, 0, 0, 0};
      u64 fv = ~tag;
#pragma unroll 1
      for (int s = 0; s < 2; ++s) {
        __amdgpu_buffer_rsrc_t rs = mkrsrc(ws + OFF_Y1F + s * Y1_S, 84 * 65536);
        unsigned ys = ((pr & 32) << 8) ^ pb;
        char* gd = ws + OFF_X2F + s * X2_S + TB2 * X2_TB + (size_t)(xl >> 1) * XST + (size_t)(w & 1) * 8 * X2_G;
        char* gd1 = gd + 4 * XST;
        asm volatile("" : "+s"(rs), "+s"(ys), "+s"(gd), "+s"(gd1));
        poll_from(fv, ws, yfl + s * 256, idx == 13 ? lanes(16) : lanes(8), tag);
        bar();
#pragma unroll
        for (int i = 0; i < 8; ++i) dma16<SC1>(rs, reg + i * 1024, yo[i & 1], (ys ^ ((i >> 2) << 13)) + (i & 3) * 16384);
        if (s == 0) { PRIO(PRIO_MA); } else { PRIO(PRIO_MB); }
        STAMP2(6, 10);
        wait_vm<0>();
        bar();
        STAMP2(7, 11);
        bf16x8 A[2][2][2];
        f32x4 c[2][2];
        auto lda = [&](int j) {
#pragma unroll
          for (int ks = 0; ks < 2; ++ks)
#pragma unroll
            for (int i = 0; i < 2; ++i) A[j & 1][i][ks] = tr_frag(lds + la[j][i] + ks * 4096, 512);
        };
        auto idft = [&](int j, int k) {
          constexpr f32x4 Z = {0, 0, 0, 0};
          c[j & 1][k & 1] = MFMAU(A[j & 1][k & 1][k >> 1], Gt[2 * (j % 3) + (k >> 1)], k < 2 ? Z : c[j & 1][k & 1]);
        };
        lda(0); lda(1);
        SB0();
#pragma unroll
        for (int k = 0; k < 4; ++k) idft(0, k);
        SB0();
        static_for<5>([&](auto jc) {
          constexpr int j = decltype(jc)::v;
          if constexpr (j + 2 < 5) lda(j + 2);
          SB0();
          u32x4 o = gelu_pack8(c[j & 1][0], c[j & 1][1], [&](int k) { if (j + 1 < 5 && k < 4) idft(j + 1, k); });
          if constexpr (j == 0) o &= keep;
          *(u32x4*)(lds + hw + j * 16 * M_HS) = o;
          SB0();
        });
        wait_lds();
        bar();
        STAMP2(8, 12);
        bf16x8 B[6][2][2];
        auto ldb = [&](int u) {
#pragma unroll
          for (int ks = 0; ks < 2; ++ks)
#pragma unroll
            for (int i = 0; i < 2; ++i) B[u][i][ks] = tr_frag(lds + hr + ((u >> 1) * 96 + (u & 1) + 64 * ks) * M_HS + 8 * i, 8 * M_HS);
        };
        f32x4 d[2][2];
        auto fdft = [&](int u) {
          constexpr f32x4 Z = {0, 0, 0, 0};
#pragma unroll
          for (int k = 0; k < 4; ++k) d[u & 1][k & 1] = MFMAU(B[u][k & 1][k >> 1], F2[k >> 1], k < 2 ? Z : d[u & 1][k & 1]);
        };
#pragma unroll
        for (int u = 0; u < M_AH; ++u) ldb(u);
        SB0();
        fdft(0);
#pragma unroll
        for (int u = 0; u < 6; ++u) {
          SB0();
          if (u + M_AH < 6) ldb(u + M_AH);
          if (u + 1 < 6) fdft(u + 1);
          if (u == M_PEEK) flag_peek<16>(fv, ws, (yfl + 256) * FS);
          SB0();
          const u32x4 o = pack8(d[u & 1][0], d[u & 1][1]);
          asm volatile("global_store_dwordx4 %0, %1, %2 " ST_POL "\n\ts_nop 1" ::"v"(xo[u >> 1]), "v"(o), "s"(u & 1 ? gd1 : gd) : "memory");
        }
        publish(ws, F_M + s * 256 + idx * 8 + xl, tag, w);
        asm volatile("" : "+v"(fv));
        STAMP2(9, 13);
      }
    }
    if (!GE_EARLY && idx >= 28 && LATE_WF) poll(ws, F_G1 + 256 + (idx - 24) * 32 + w * 8 + lane, lanes(8), tag);
    if (idx >= 30) {
      if (w == 0) s_ge(g_, wg, (float*)(ws + OFF_GE), lds, (idx - 30) * 8 + xl, lane);
      publish(ws, F_S + (idx - 30) * 8 + xl, tag, w);
    }
    if (GE_EARLY && idx >= 28 && LATE_WF) poll(ws, F_G1 + 256 + (idx - 24) * 32 + w * 8 + lane, lanes(8), tag);
    poll(ws, F_S + w * 4 + lane, lanes(4), tag);
    if (!EARLY_WF && SG_AT == 6) poll(ws, F_X + 256 + w * 64 + lane, ~0ull, tag);
    PRIO(PRIO_JA);
    sgpr_settle();
    static_for<32 - EARLY_WF>([&](auto ic) {
      constexpr int i = EARLY_WF + decltype(ic)::v;
      if constexpr (i >= EARLY_WF + WF_PACE2) wait_vm<WF_PACE2 - 1>();
      ldfrag<i, true>(W[i], ws + OFF_WFT + w * 32768, lane16);
    });
    wait_vm<0>();
    asm volatile(".Lt_end:");
  }
  asm volatile(";@Nr");
  tie(W);
  const int s = role ^ 1;
  lane = lane_id();
  const int G = lane >> 4, rq = (lane & 15) >> 2, p = lane & 3;
  const unsigned lane16 = lane * 16;

  const int jb = q;
  if (jb >= 250) {
    if (blockIdx.x == 255 && w == 0) {
#pragma unroll 1
      for (int i = 4; i < 8; ++i) poll(ws, F_G2 + i * 64 + lane, ~0ull, tag);
      set_flag(ws, F_EPOCH, tag, w);
    }
    STAMP_FLUSH();
    return;
  }
  {
    const int tile = jb / 3, vt = jb - 3 * tile, ur = U_ROT;
    u32x4 woa[32], Gt[2];
    sgpr_settle();
    static_for<2>([&](auto ic) { constexpr int i = decltype(ic)::v; ldfrag<i, false>(Gt[i], (const char*)TAB + (TAB_G + 2 * vt) * 1024, lane16); });
    __amdgpu_buffer_rsrc_t grs = mkrsrc(ws + OFF_GE, 32768);
#pragma unroll
    for (int i = 0; i < 2; ++i) dma16<SC1>(grs, lds + J_G + w * 2048 + i * 1024, 16384 + (s * 4 + w) * 2048 + (i ^ ur) * 1024 + lane16, 0);
    static_for<16>([&](auto ic) {
      constexpr int i = decltype(ic)::v;
      if constexpr (WO_PACE < 16 && i >= WO_PACE) asm volatile("s_cmp_eq_u32 %0, 0\n\ts_cbranch_scc1 1f\n\ts_waitcnt vmcnt(%1)\n1:" :: "s"(role), "n"(WO_PACE - 1) : "scc", "memory");
      ldfrag<i, true>(woa[i], ws + OFF_WOT + w * 32768, lane16);
    });
    char* reg = lds + J_Y + w * 8192;
    __amdgpu_buffer_rsrc_t rs = mkrsrc(ws + OFF_Y2F + s * Y2_S + (size_t)(tile >> 1) * 65536, 65536);
    unsigned yo[2], ys[2], la[4];
#pragma unroll
    for (int i = 0; i < 2; ++i) {
      const int c = (lane & 15) ^ ((G & 1) << 3) ^ i;
      yo[i] = G * 2048 + (4 * w + (c >> 2)) * 128 + (tile & 1) * 64 + (c & 3) * 16;
      ys[i] = (i << 13) ^ ((tile & 64) << 7) ^ pb;
    }
#pragma unroll
    for (int i = 0; i < 4; ++i) la[i] = J_Y + w * 8192 + (4 * G + (rq >> 1)) * 256 + ((4 * (i & 2) + 2 * p + (i & 1)) ^ ((rq >> 1) << 3) ^ (G & 1)) * 16 + (rq & 1) * 8;
    asm volatile("" : "+s"(rs), "+s"(ys[0]), "+s"(ys[1]), "+v"(yo[0]), "+v"(yo[1]), "+v"(la[0]), "+v"(la[1]), "+v"(la[2]), "+v"(la[3]));
    PRIO(PRIO_F);
    poll(ws, F_G2 + s * 256 + ((tile >= 42 ? 4 : 0) + w) * 32 + lane, lanes(32), tag);
#pragma unroll
    for (int i = 0; i < 8; ++i) dma16<SC1>(rs, reg + i * 1024, yo[i & 1], ys[i >> 2] + (i & 3) * 16384);
    if (role) { PRIO(PRIO_JA); } else { PRIO(PRIO_JB); }
    tie(Gt);
    STAMP(24);
    {
      bf16x8 A[2][4];
      f32x4 c[4];
      wait_vm<4>();
#pragma unroll
      for (int i = 0; i < 4; ++i) A[0][i] = tr_frag(lds + la[i], 512);
      wait_vm<0>();
      STAMP(25);
#pragma unroll
      for (int i = 0; i < 4; ++i) A[1][i] = tr_frag(lds + la[i] + 4096, 512);
      SB0();
#pragma unroll
      for (int i = 0; i < 4; ++i) c[i] = MFMAU(A[0][i], Gt[0], ((f32x4){0, 0, 0, 0}));
#pragma unroll
      for (int i = 0; i < 4; ++i) c[i] = MFMAU(A[1][i], Gt[1], c[i]);
      SB0();
#pragma unroll
      for (int h = 0; h < 2; ++h) *(u32x4*)(lds + J_F + (2 * w + h) * 1024 + lane16) = gelu_pack8(c[2 * h], c[2 * h + 1]);
    }
    wait_lds();
    bar();
    STAMP(26);
    if (JA_SLEEP && role) __builtin_amdgcn_s_sleep(JA_SLEEP);
    if (JA_POLL == 1 && role) poll(ws, F_G2 + 256 + ((tile >= 42 ? 4 : 0) + w) * 32 + lane, lanes(JA_NP), tag);
    f32x4 fe[4], ex[4], gt[2];
    u32x4 bfr[8];
#pragma unroll
    for (int ks = 0; ks < 8; ++ks) bfr[ks] = *(const u32x4*)(lds + J_F + ks * 1024 + lane16);
    SB0();
    auto femm = [&](auto kc) {
      constexpr int k = decltype(kc)::v, m = (k >> 4) * 2 + (k & 1), ks = (k >> 1) & 7;
      constexpr f32x4 Z = {0, 0, 0, 0};
      fe[m] = MFMAU(W[m * 8 + ks], bfr[ks], ks == 0 ? Z : fe[m]);
      SB0();
      if constexpr (k & 1) ldfrag<16 + (k >> 1), true>(woa[16 + (k >> 1)], ws + OFF_WOT + w * 32768, lane16);
      if constexpr (k == 6 || k == 8) gt[(k - 6) / 2] = *(const f32x4*)(lds + J_G + w * 2048 + (k - 6) * 512 + lane16);
      SB0();
    };
    static_for<18>([&](auto kc) { femm(kc); });
    float big = 0;
    static_for<2>([&](auto hc) {
      constexpr int h = decltype(hc)::v;
      float d[2][8];
      u32x4 jo;
      static_for<78>([&](auto jc) {
        constexpr int j = decltype(jc)::v;
        auto at = [&](f32x4 (&a)[4], int e) { return a[2 * h + (e >> 2)][e & 3]; };
        if constexpr (j < 8) { float t = at(fe, j) * LOG2E2; asm("" : "+v"(t)); fe[2 * h + (j >> 2)][j & 3] = t; }
        else if constexpr (j < 16) ex[2 * h + ((j - 8) >> 2)][j & 3] = __builtin_amdgcn_exp2f(-at(fe, j - 8));
        else if constexpr (j < 20) big = nanmax3(big, at(fe, 2 * (j - 16)), at(fe, 2 * (j - 16) + 1));
        else if constexpr (j < 28) d[0][j - 20] = jden<true, 8 * h + j - 20>(at(ex, j - 20), gt[0][0]);
        else if constexpr (j < 36) d[0][j - 28] = __builtin_amdgcn_rcpf(d[0][j - 28]);
        else if constexpr (j < 44) d[1][j - 36] = jden<true, 8 * h + j - 36>(at(ex, j - 36), gt[0][1]);
        else if constexpr (j < 52) d[0][j - 44] = jval<true>(at(ex, j - 44), d[0][j - 44]);
        else if constexpr (j < 60) d[1][j - 52] = __builtin_amdgcn_rcpf(d[1][j - 52]);
        else if constexpr (j < 64) jo[j - 60] = pack_bf16(d[0][2 * (j - 60)], d[0][2 * (j - 60) + 1]);
        else if constexpr (j == 64) *(u32x4*)(lds + (2 * w + h) * 1024 + lane16) = jo;
        else if constexpr (j < 73) d[1][j - 65] = jval<true>(at(ex, j - 65), d[1][j - 65]);
        else if constexpr (j < 77) jo[j - 73] = pack_bf16(d[1][2 * (j - 73)], d[1][2 * (j - 73) + 1]);
        else *(u32x4*)(lds + 8192 + (2 * w + h) * 1024 + lane16) = jo;
        SB0();
        if constexpr (h == 0 && (j & 3) == 3 && j < 56) femm(IC<18 + (j >> 2)>{});
      });
      if constexpr (h == 0) STAMP(27);
    });
    const bool fast = __all(big <= 32.0f);
    if (!fast) {
      wait_lds();
#pragma unroll
      for (int i = 0; i < 2; ++i) dma16<SC1>(grs, lds + J_G + w * 2048 + i * 1024, (s * 4 + w) * 2048 + (i ^ ur) * 1024 + lane16, 0);
      wait_vm<0>();
#pragma unroll 1
      for (int u = 0; u < 8; ++u) j_make<false>(fe, *(const float*)(lds + J_G + w * 2048 + (u >> 2) * 1024 + lane16 + (u & 3) * 4), lds, u, w, lane);
    }
    if (JA_SLEEP2 && role) __builtin_amdgcn_s_sleep(JA_SLEEP2);
    if (JA_POLL == 2 && role) poll(ws, F_G2 + 256 + ((tile >= 42 ? 4 : 0) + w) * 32 + lane, lanes(JA_NP), tag);
    if (role) { PRIO(PRIO_JAL); } else { PRIO(PRIO_JBL); }
    j_loop(woa, ex, gt, lds, (fast ? J_AH * 8192 : J_F) + 2 * w * 1024 + lane16, fast ? 8192 : 0, out, s * T2 + jb * 16, ur, w, lane, stv);
  }
  asm volatile(".Lk_end:");
  STAMP(29);
}

#define WS_SKIP 0
extern "C" void kernel_launch(void* const* d_in, const int* in_sizes, int n_in, void* d_out, int out_size, void* d_ws,
                              size_t ws_size, hipStream_t stream) {
  k_all<<<512, 256, 0, stream>>>((const u16*)d_in[0], (const u16*)d_in[1], (const u16*)d_in[2], (const u16*)d_in[3], (const u16*)d_in[4],
                                 (const u16*)d_in[5], (const u16*)d_in[6], (char*)d_ws + WS_SKIP, (char*)d_out);
}
```
